```python
import torch

def _softmax(x, axis=-1):
    return torch.softmax(x, dim=axis)

def _gelu(x):
    return torch.nn.functional.gelu(x, approximate='tanh')

def _silu(x):
    return torch.nn.functional.silu(x)

def _sigmoid(x):
    return torch.sigmoid(x)

def _layernorm(x, axis=-1, eps=1e-05):
    m = x.mean(dim=axis, keepdim=True)
    v = x.var(dim=axis, keepdim=True, unbiased=False)
    return (x - m) / torch.sqrt(v + eps)

def _scatter_add(tgt, idx, src, axis=0):
    return tgt.clone().index_add_(axis, idx, src)

def _segment_sum(src, seg, n):
    out = torch.zeros((n,) + src.shape[1:], dtype=src.dtype)
    return out.index_add_(0, seg, src)

def _pad(x, pw):
    flat = tuple((v for pair in reversed(pw) for v in pair))
    return torch.nn.functional.pad(x, flat)
kernel_name = 'standalone__kv_cache_quant_pre_rope_vs_post_rope__decode'

def setup_inputs():

    _w = lambda a: (lambda t: t.to(torch.bfloat16) if t.is_floating_point() else t)(torch.as_tensor(a))
    B, Sq, Skv = (1, 1, 16384)
    H, Dh0 = (32, 192)
    G = -1 if -1 > 0 else Dh0
    G = max(2, G // 2 * 2)
    Dh = max(G, Dh0 // G * G)
    Skv = max(Skv, Sq)
    ng = Dh // G
    bits = 3 if 3 > 1 else 4
    bits = max(2, min(8, bits))
    qmax = (1 << bits) - 1
    half = Dh // 2
    inv = 1.0 / 10000.0 ** (torch.arange(half) / max(1, half))
    ang_k = torch.arange(Skv)[:, None] * inv[None, :]
    ang_q = torch.arange(Skv - Sq, Skv)[:, None] * inv[None, :]
    kc = torch.cat([torch.cos(ang_k), torch.cos(ang_k)], dim=-1)
    ks = torch.cat([torch.sin(ang_k), torch.sin(ang_k)], dim=-1)
    qc = torch.cat([torch.cos(ang_q), torch.cos(ang_q)], dim=-1)
    qs = torch.cat([torch.sin(ang_q), torch.sin(ang_q)], dim=-1)
    qk_i = torch.randint(0, qmax + 1, (B, H, Skv, Dh)).to(torch.int32)
    qv_i = torch.randint(0, qmax + 1, (B, H, Skv, Dh)).to(torch.int32)
    ksc = torch.abs((lambda *s: torch.randn(*s, dtype=torch.bfloat16) * 0.4)(B, H, Skv, ng)) * 0.1 + 0.01
    kzp = torch.full((B, H, Skv, ng), qmax / 2.0)
    vsc = torch.abs((lambda *s: torch.randn(*s, dtype=torch.bfloat16) * 0.4)(B, H, Skv, ng)) * 0.1 + 0.01
    vzp = torch.full((B, H, Skv, ng), qmax / 2.0)
    return {'q': _w((lambda *s: torch.randn(*s, dtype=torch.bfloat16) * 6.4)(B, H, Sq, Dh) * 0.5), 'qk': _w(qk_i), 'k_scales': _w(ksc), 'k_zeros': _w(kzp), 'qv': _w(qv_i), 'v_scales': _w(vsc), 'v_zeros': _w(vzp), 'q_cos': _w(qc), 'q_sin': _w(qs), 'k_cos': _w(kc), 'k_sin': _w(ks)}

def reference(q, qk, k_scales, k_zeros, qv, v_scales, v_zeros, q_cos, q_sin, k_cos, k_sin):
    B, H, Sq, Dh = q.shape
    ng = k_scales.shape[-1]
    G = Dh // ng
    half = Dh // 2
    ks = (lambda x, n, axis: torch.repeat_interleave(x, n, dim=axis))(k_scales, G, axis=-1)
    kz = (lambda x, n, axis: torch.repeat_interleave(x, n, dim=axis))(k_zeros, G, axis=-1)
    k = ((lambda x, dt: x.to(dt))(qk, q.dtype) - kz) * ks
    vs = (lambda x, n, axis: torch.repeat_interleave(x, n, dim=axis))(v_scales, G, axis=-1)
    vz = (lambda x, n, axis: torch.repeat_interleave(x, n, dim=axis))(v_zeros, G, axis=-1)
    v = ((lambda x, dt: x.to(dt))(qv, q.dtype) - vz) * vs
    k1 = k[..., :half].clone()
    k2 = k[..., half:].clone()
    krh = torch.concatenate([-k2, k1], axis=-1)
    kr = k * k_cos[None, None, :, :] + krh * k_sin[None, None, :, :]
    q1 = q[..., :half].clone()
    q2 = q[..., half:].clone()
    qrh = torch.concatenate([-q2, q1], axis=-1)
    qr = q * q_cos[None, None, :, :] + qrh * q_sin[None, None, :, :]
    logits = torch.einsum('bhqd,bhkd->bhqk', qr, kr) * float(Dh) ** (-0.5)
    attn = _softmax(logits, axis=-1)
    out = torch.einsum('bhqk,bhkd->bhqd', attn, v)
    return out
```

```cpp
#include <hip/hip_runtime.h>
#include <hip/hip_bf16.h>
#include <cstdint>


using bf16 = __hip_bfloat16;
using i32x4 = __attribute__((ext_vector_type(4))) int;
using u32x4 = __attribute__((ext_vector_type(4))) unsigned;
using u32x2 = __attribute__((ext_vector_type(2))) unsigned;
using f32x2 = __attribute__((ext_vector_type(2))) float;
using f32x4 = __attribute__((ext_vector_type(4))) float;

constexpr int DH = 192;
constexpr int HALF = 96;
constexpr int NH = 32;
constexpr int SKV = 16384;
constexpr int NWG = 256;
constexpr unsigned ROWB = DH * 4;
constexpr unsigned HEADB = SKV * ROWB;
constexpr unsigned KVB = NH * HEADB;
constexpr unsigned CSB = SKV * DH * 2;
constexpr unsigned OOB = 0x80000000u;

__device__ __forceinline__ float bflo(unsigned u) { return __uint_as_float(u << 16); }
__device__ __forceinline__ float bfhi(unsigned u) { return __uint_as_float(u & 0xffff0000u); }
__device__ __forceinline__ float rbf(float x) { return __bfloat162float(__float2bfloat16(x)); }

template <int CTRL> __device__ __forceinline__ float dpp(float x) {
    return __builtin_bit_cast(float, __builtin_amdgcn_mov_dpp(__builtin_bit_cast(int, x), CTRL, 0xf, 0xf, true));
}
__device__ __forceinline__ float row16_allsum(float x) {
    x += dpp<0x128>(x);
    x += dpp<0x124>(x);
    x += dpp<0x122>(x);
    x += dpp<0x121>(x);
    return x;
}
__device__ __forceinline__ float xrow_max(float x) {
    auto s = __builtin_amdgcn_permlane16_swap(__float_as_uint(x), __float_as_uint(x), false, false);
    x = fmaxf(__uint_as_float(s[0]), __uint_as_float(s[1]));
    auto t = __builtin_amdgcn_permlane32_swap(__float_as_uint(x), __float_as_uint(x), false, false);
    return fmaxf(__uint_as_float(t[0]), __uint_as_float(t[1]));
}
__device__ __forceinline__ float xrow_sum(float x) {
    auto s = __builtin_amdgcn_permlane16_swap(__float_as_uint(x), __float_as_uint(x), false, false);
    x = __uint_as_float(s[0]) + __uint_as_float(s[1]);
    auto t = __builtin_amdgcn_permlane32_swap(__float_as_uint(x), __float_as_uint(x), false, false);
    return __uint_as_float(t[0]) + __uint_as_float(t[1]);
}
__device__ __forceinline__ float swap32_add(float a, float b) {
    auto t = __builtin_amdgcn_permlane32_swap(__float_as_uint(a), __float_as_uint(b), false, false);
    return __uint_as_float(t[0]) + __uint_as_float(t[1]);
}
__device__ __forceinline__ float swap16_add(float a, float b) {
    auto t = __builtin_amdgcn_permlane16_swap(__float_as_uint(a), __float_as_uint(b), false, false);
    return __uint_as_float(t[0]) + __uint_as_float(t[1]);
}

#define PIN() do { asm volatile("" ::: "memory"); __builtin_amdgcn_sched_barrier(0); } while (0)


template <int NG, int R, int W, int UNPK>
__global__ __launch_bounds__(256) void kv_sweep_kernel(
    const unsigned short* q, const int* qk, const unsigned short* ksc,
    const unsigned short* kzp, const int* qv, const unsigned short* vsc,
    const unsigned short* vzp, const unsigned short* q_cos, const unsigned short* q_sin,
    const unsigned short* k_cos, const unsigned short* k_sin, float* part, float* pml, float scale_log2)
{
    constexpr int NW = 4;
    constexpr int HPG = NH / NG;
    constexpr int WPG = NWG / NG;
    constexpr int TPH = 4 * NG;
    constexpr int NLD = 3 * TPH;
    constexpr int NT = NW * 64;
    static_assert(NLD % R == 0 && W <= R, "ring");

    __shared__ __attribute__((aligned(16))) float s_q[HPG * DH];
    __shared__ __attribute__((aligned(16))) unsigned short s_sc[4 * NH * 64];
    __shared__ __attribute__((aligned(16))) float s_pw[NW * HPG * 4 * TPH];
    __shared__ __attribute__((aligned(16))) float s_ml[HPG * NW * 4];
    __shared__ __attribute__((aligned(16))) float s_part[HPG * NW * DH];

    const int tid = threadIdx.x;
    const int lane = tid & 63;
    const int wave = __builtin_amdgcn_readfirstlane(tid >> 6);
    const int r = lane >> 4;
    const int c = lane & 15;
    const int bid = blockIdx.x;
    const int g = (bid >> 3) % NG;
    const int m = (bid & 7) * (32 / NG) + (bid >> 3) / NG;

    auto blk_row = [&](int bi) { return ((bi + g) % NG) * (SKV / NG) + m * 64; };

    constexpr int QIT = HPG * 24;
    constexpr int QPT = (QIT + NT - 1) / NT;
    u32x4 lq[QPT], lp[QPT], lc[QPT], ls[QPT];
#pragma unroll
    for (int i = 0; i < QPT; ++i) {
        const int e = (tid + i * NT) % QIT;
        const int hh = e / 24, d8 = e % 24;
        const unsigned short* qh = q + (hh * NG + g) * DH;
        lq[i] = *reinterpret_cast<const u32x4*>(qh + d8 * 8);
        lp[i] = *reinterpret_cast<const u32x4*>(qh + (d8 < 12 ? d8 + 12 : d8 - 12) * 8);
        lc[i] = *reinterpret_cast<const u32x4*>(q_cos + d8 * 8);
        ls[i] = *reinterpret_cast<const u32x4*>(q_sin + d8 * 8);
    }
    u32x4 lsc[4];
#pragma unroll
    for (int i = 0; i < 4; ++i) {
        const int hb = tid >> 3, pc = tid & 7;
        const int hh = hb / NG, bi = hb % NG;
        const unsigned short* base = i == 0 ? ksc : i == 1 ? kzp : i == 2 ? vsc : vzp;
        lsc[i] = __builtin_nontemporal_load(reinterpret_cast<const u32x4*>(base + (size_t)(hh * NG + g) * SKV + blk_row(bi) + pc * 8));
    }
    PIN();

    const auto rc = __builtin_amdgcn_make_buffer_rsrc((void*)k_cos, 0, CSB, 0x00020000);
    const auto rs = __builtin_amdgcn_make_buffer_rsrc((void*)k_sin, 0, CSB, 0x00020000);
    u32x2 cs[TPH][3], sn[TPH][3];
    {
        const unsigned co = r * 384 + c * 8;
        const unsigned so1 = r * 384 + (c < 8 ? c * 8 + 320 : c * 8 - 64);
#pragma unroll
        for (int t = 0; t < TPH; ++t) {
            const unsigned rb = (blk_row(t / 4) + wave * 16 + (t % 4) * 4) * 384u;
            cs[t][0] = __builtin_amdgcn_raw_buffer_load_b64(rc, co, rb, 2);
            cs[t][1] = __builtin_amdgcn_raw_buffer_load_b64(rc, co + 128, rb, 2);
            cs[t][2] = __builtin_amdgcn_raw_buffer_load_b64(rc, co + 256, rb, 2);
            sn[t][0] = __builtin_amdgcn_raw_buffer_load_b64(rs, co + 192, rb, 2);
            sn[t][1] = __builtin_amdgcn_raw_buffer_load_b64(rs, so1, rb, 2);
            sn[t][2] = __builtin_amdgcn_raw_buffer_load_b64(rs, co + 64, rb, 2);
        }
    }
    PIN();

    const auto rk = __builtin_amdgcn_make_buffer_rsrc((void*)qk, 0, KVB, 0x00020000);
    const auto rv = __builtin_amdgcn_make_buffer_rsrc((void*)qv, 0, KVB, 0x00020000);
    const unsigned voff = r * ROWB + c * 16;
    unsigned toff[TPH];
#pragma unroll
    for (int t = 0; t < TPH; ++t) toff[t] = (blk_row(t / 4) + wave * 16 + (t % 4) * 4) * ROWB;
    u32x4 ring[R];
    auto ld = [&](__amdgpu_buffer_rsrc_t rr, int n, unsigned hoff) {
        return __builtin_amdgcn_raw_buffer_load_b128(rr, voff + (n % 3) * 256, hoff + toff[n / 3], 2);
    };
#pragma unroll
    for (int n = 0; n < W; ++n) ring[n] = ld(rk, n, g * HEADB);
    PIN();

#pragma unroll
    for (int i = 0; i < QPT; ++i) {
        const int e = (tid + i * NT) % QIT;
        const unsigned sg = (e % 24) < 12 ? 0x80008000u : 0u;
        float o[8];
#pragma unroll
        for (int k = 0; k < 4; ++k) {
            const unsigned pp = lp[i][k] ^ sg;
            o[2 * k] = rbf(rbf(bflo(lq[i][k]) * bflo(lc[i][k])) + rbf(bflo(pp) * bflo(ls[i][k])));
            o[2 * k + 1] = rbf(rbf(bfhi(lq[i][k]) * bfhi(lc[i][k])) + rbf(bfhi(pp) * bfhi(ls[i][k])));
        }
        *reinterpret_cast<f32x4*>(s_q + e * 8) = f32x4{o[0], o[1], o[2], o[3]};
        *reinterpret_cast<f32x4*>(s_q + e * 8 + 4) = f32x4{o[4], o[5], o[6], o[7]};
    }
#pragma unroll
    for (int i = 0; i < 4; ++i) *reinterpret_cast<u32x4*>(s_sc + (tid + i * NT) * 8) = lsc[i];
    asm volatile("s_waitcnt lgkmcnt(0)" ::: "memory");
    __builtin_amdgcn_s_barrier();
    PIN();
    {
        const unsigned sg1 = c < 8 ? 0u : 0x80008000u;
#pragma unroll
        for (int t = 0; t < TPH; ++t) {
            sn[t][1] ^= sg1;
            sn[t][2] ^= 0x80008000u;
        }
    }

    const int qi0 = 4 * c, qi1 = c < 8 ? 160 + 4 * c : 4 * c - 32;
    f32x4 qa[3], qb[3], qan[3], qbn[3];
    auto load_q = [&](int hi) {
        const float* qh = s_q + hi * DH;
        qan[0] = *reinterpret_cast<const f32x4*>(qh + qi0);
        qan[1] = *reinterpret_cast<const f32x4*>(qh + qi0 + 64);
        qan[2] = *reinterpret_cast<const f32x4*>(qh + qi0 + 128);
        qbn[0] = *reinterpret_cast<const f32x4*>(qh + qi0 + 96);
        qbn[1] = *reinterpret_cast<const f32x4*>(qh + qi1);
        qbn[2] = *reinterpret_cast<const f32x4*>(qh + qi0 + 32);
    };
    load_q(0);
    const int srow = wave * 16 + r;
    float* pwp = s_pw + (wave * HPG * 4 + r) * TPH;

    for (int hi = 0; hi < HPG; ++hi) {
        const unsigned hoff = (hi * NG + g) * HEADB;
        const bool last = hi + 1 == HPG;
        const unsigned hnext = last ? g * HEADB : hoff + NG * HEADB;
        const auto rn = __builtin_amdgcn_make_buffer_rsrc((void*)(last ? qv : qk), 0, KVB, 0x00020000);
        const unsigned short* sc = s_sc + hi * NG * 64 + srow;
#pragma unroll
        for (int j = 0; j < 3; ++j) { qa[j] = qan[j]; qb[j] = qbn[j]; }
        load_q(last ? hi : hi + 1);
        float ks[TPH], kz[TPH], vs[TPH], vz[TPH], s[TPH];
#pragma unroll
        for (int t = 0; t < TPH; ++t) {
            const int so = (t / 4) * 64 + (t % 4) * 4;
            ks[t] = bflo(sc[so]);
            kz[t] = bflo(sc[NH * 64 + so]);
            f32x2 dot = {0.f, 0.f}, ws = {0.f, 0.f};
#pragma unroll
            for (int j = 0; j < 3; ++j) {
                const int n = t * 3 + j;
                u32x2 cc = cs[t][j], ss = sn[t][j];
                if (!UNPK) asm volatile("" : "+v"(cc), "+v"(ss));
                const f32x2 c01 = {bflo(cc[0]), bfhi(cc[0])}, c23 = {bflo(cc[1]), bfhi(cc[1])};
                const f32x2 s01 = {bflo(ss[0]), bfhi(ss[0])}, s23 = {bflo(ss[1]), bfhi(ss[1])};
                f32x2 w01 = qa[j].xy * c01 + qb[j].xy * s01;
                f32x2 w23 = qa[j].zw * c23 + qb[j].zw * s23;
                ws += w01;
                ws += w23;
                asm volatile("" : "+v"(w01), "+v"(w23), "+v"(ws), "+v"(dot));
                __builtin_amdgcn_sched_barrier(0);
                const u32x4 kk = ring[n % R];
                f32x2 k01 = {(float)(int)kk[0], (float)(int)kk[1]}, k23 = {(float)(int)kk[2], (float)(int)kk[3]};
                asm volatile("" : "+v"(k01), "+v"(k23));
                __builtin_amdgcn_sched_barrier(0);
                ring[(n + W) % R] = n + W < NLD ? ld(rk, n + W, hoff) : ld(rn, n + W - NLD, hnext);
                PIN();
                dot += k01 * w01;
                dot += k23 * w23;
            }
            vs[t] = bflo(sc[2 * NH * 64 + so]);
            vz[t] = bflo(sc[3 * NH * 64 + so]);
            const float a = (dot[0] + dot[1]) - kz[t] * (ws[0] + ws[1]);
            s[t] = row16_allsum(a) * (ks[t] * scale_log2);
        }
        float mx = s[0];
#pragma unroll
        for (int t = 1; t < TPH; ++t) mx = fmaxf(mx, s[t]);
        mx = xrow_max(mx);
        float l = 0.f, z = 0.f;
#pragma unroll
        for (int t = 0; t < TPH; ++t) {
            const float p = __builtin_amdgcn_exp2f(s[t] - mx);
            l += p;
            s[t] = p * vs[t];
            z += s[t] * vz[t];
        }
        l = xrow_sum(l);
        z = xrow_sum(z);
        if (c == 0) {
#pragma unroll
            for (int t = 0; t < TPH; t += 4) *reinterpret_cast<f32x4*>(pwp + hi * 4 * TPH + t) = f32x4{s[t], s[t + 1], s[t + 2], s[t + 3]};
            *reinterpret_cast<f32x4*>(s_ml + (hi * NW + wave) * 4) = f32x4{mx, l, z, 0.f};
        }
        PIN();
    }

    f32x4 pwn[TPH / 4];
#pragma unroll
    for (int t = 0; t < TPH / 4; ++t) pwn[t] = *reinterpret_cast<const f32x4*>(pwp + 4 * t);
    for (int hi = 0; hi < HPG; ++hi) {
        const unsigned hoff = (hi * NG + g) * HEADB;
        const bool last = hi + 1 == HPG;
        const unsigned hnext = last ? OOB : hoff + NG * HEADB;
        f32x4 pw[TPH / 4];
#pragma unroll
        for (int t = 0; t < TPH / 4; ++t) pw[t] = pwn[t];
#pragma unroll
        for (int t = 0; t < TPH / 4; ++t) pwn[t] = *reinterpret_cast<const f32x4*>(pwp + (last ? hi : hi + 1) * 4 * TPH + 4 * t);
        f32x2 acc[3][2];
#pragma unroll
        for (int t = 0; t < TPH; ++t) {
            const f32x2 p2 = {pw[t / 4][t % 4], pw[t / 4][t % 4]};
#pragma unroll
            for (int j = 0; j < 3; ++j) {
                const int n = t * 3 + j;
                const u32x4 vv = ring[n % R];
                f32x2 v01 = {(float)(int)vv[0], (float)(int)vv[1]}, v23 = {(float)(int)vv[2], (float)(int)vv[3]};
                asm volatile("" : "+v"(v01), "+v"(v23));
                __builtin_amdgcn_sched_barrier(0);
                ring[(n + W) % R] = n + W < NLD ? ld(rv, n + W, hoff) : ld(rv, n + W - NLD, hnext);
                PIN();
                if (t == 0) { acc[j][0] = p2 * v01; acc[j][1] = p2 * v23; }
                else { acc[j][0] += p2 * v01; acc[j][1] += p2 * v23; }
                asm volatile("" : "+v"(acc[j][0]), "+v"(acc[j][1]));
                __builtin_amdgcn_sched_barrier(0);
            }
        }
        float* sl = s_part + (hi * NW + wave) * DH;
#pragma unroll
        for (int j = 0; j < 3; ++j) {
            const float x = swap32_add(acc[j][0][0], acc[j][1][0]);
            const float y = swap32_add(acc[j][0][1], acc[j][1][1]);
            sl[64 * j + 4 * c + r] = swap16_add(x, y);
        }
        PIN();
    }
    __syncthreads();

#pragma unroll
    for (int it0 = 0; it0 < HPG * 48; it0 += NT) {
        const int it = it0 + tid;
        if (HPG * 48 % NT != 0 && it >= HPG * 48) break;
        const int hi = it / 48, c4 = it % 48;
        f32x4 ml[NW];
#pragma unroll
        for (int w = 0; w < NW; ++w) ml[w] = *reinterpret_cast<const f32x4*>(s_ml + (hi * NW + w) * 4);
        float M = ml[0][0];
#pragma unroll
        for (int w = 1; w < NW; ++w) M = fmaxf(M, ml[w][0]);
        f32x4 o = {0.f, 0.f, 0.f, 0.f};
        float L = 0.f;
#pragma unroll
        for (int w = 0; w < NW; ++w) {
            const float f = __builtin_amdgcn_exp2f(ml[w][0] - M);
            o += f * (*reinterpret_cast<const f32x4*>(s_part + (hi * NW + w) * DH + 4 * c4) - ml[w][2]);
            L += f * ml[w][1];
        }
        const int h = hi * NG + g;
        *reinterpret_cast<f32x4*>(part + (((size_t)h * 6 + (c4 >> 3)) * WPG + m) * 32 + (c4 & 7) * 4) = o;
        if (c4 == 0) *reinterpret_cast<f32x2*>(pml + ((size_t)h * WPG + m) * 2) = f32x2{M, L};
    }
}

template <int NP, int NWV>
__global__ __launch_bounds__(NWV * 64) void kv_merge_kernel(const float* __restrict__ part, const float* __restrict__ pml, bf16* __restrict__ out)
{
    constexpr int PER = NP / (8 * NWV);
    __shared__ float s_o[NWV][36];
    const int h = blockIdx.x / 6, dg = blockIdx.x % 6;
    const int lane = threadIdx.x & 63, wave = threadIdx.x >> 6;
    const int sub = lane & 7, pr = (lane >> 3) + wave * 8;
    f32x4 o[PER];
    f32x2 ml[PER];
#pragma unroll
    for (int i = 0; i < PER; ++i) {
        const int p = pr + i * 8 * NWV;
        ml[i] = *reinterpret_cast<const f32x2*>(pml + ((size_t)h * NP + p) * 2);
        o[i] = *reinterpret_cast<const f32x4*>(part + (((size_t)h * 6 + dg) * NP + p) * 32 + sub * 4);
    }
    float M = ml[0][0];
#pragma unroll
    for (int i = 1; i < PER; ++i) M = fmaxf(M, ml[i][0]);
    M = fmaxf(M, dpp<0x128>(M));
    M = xrow_max(M);
    f32x4 a = {0.f, 0.f, 0.f, 0.f};
    float L = 0.f;
#pragma unroll
    for (int i = 0; i < PER; ++i) {
        const float f = __builtin_amdgcn_exp2f(ml[i][0] - M);
        a += f * o[i];
        L += f * ml[i][1];
    }
    L += dpp<0x128>(L);
    L = xrow_sum(L);
#pragma unroll
    for (int e = 0; e < 4; ++e) { a[e] += dpp<0x128>(a[e]); a[e] = xrow_sum(a[e]); }
    if constexpr (NWV > 1) {
        if (lane < 8) { *reinterpret_cast<f32x4*>(&s_o[wave][4 * sub]) = a; }
        if (lane == 0) { s_o[wave][32] = M; s_o[wave][33] = L; }
        __syncthreads();
        if (threadIdx.x < 32) {
            float MM = s_o[0][32];
#pragma unroll
            for (int w = 1; w < NWV; ++w) MM = fmaxf(MM, s_o[w][32]);
            float LL = 0.f, oo = 0.f;
#pragma unroll
            for (int w = 0; w < NWV; ++w) {
                const float f = __builtin_amdgcn_exp2f(s_o[w][32] - MM);
                LL += f * s_o[w][33];
                oo += f * s_o[w][threadIdx.x];
            }
            out[h * DH + dg * 32 + threadIdx.x] = __float2bfloat16(oo / LL);
        }
    } else {
        if (lane < 8) {
            const float inv = 1.f / L;
#pragma unroll
            for (int e = 0; e < 4; ++e) out[h * DH + dg * 32 + sub * 4 + e] = __float2bfloat16(a[e] * inv);
        }
    }
}

template <int NG, int R, int W, int UNPK>
static void launch_cfg(void* const* d_in, void* d_out, void* d_ws, hipStream_t stream) {
    constexpr int WPG = NWG / NG;
    float* part = (float*)d_ws;
    float* pml = part + (size_t)NH * 6 * WPG * 32;
    const float scale_log2 = 1.4426950408889634f / sqrtf((float)DH);
    kv_sweep_kernel<NG, R, W, UNPK><<<NWG, 256, 0, stream>>>((const unsigned short*)d_in[0], (const int*)d_in[1], (const unsigned short*)d_in[2],
        (const unsigned short*)d_in[3], (const int*)d_in[4], (const unsigned short*)d_in[5], (const unsigned short*)d_in[6],
        (const unsigned short*)d_in[7], (const unsigned short*)d_in[8], (const unsigned short*)d_in[9], (const unsigned short*)d_in[10], part, pml, scale_log2);
    constexpr int NWV = WPG >= 256 ? 4 : WPG >= 128 ? 2 : 1;
    kv_merge_kernel<WPG, NWV><<<NH * 6, NWV * 64, 0, stream>>>(part, pml, (bf16*)d_out);
}

extern "C" void kernel_launch(void* const* d_in, const int* in_sizes, int n_in,
                              void* d_out, int out_size, void* d_ws, size_t ws_size,
                              hipStream_t stream) {
    launch_cfg<1, 6, 6, 1>(d_in, d_out, d_ws, stream);
}
```

```cpp
#include <hip/hip_runtime.h>
#include <hip/hip_bf16.h>
#include <cstdint>
#include <type_traits>


using bf16 = __hip_bfloat16;
using i32x4 = __attribute__((ext_vector_type(4))) int;
using u32x4 = __attribute__((ext_vector_type(4))) unsigned;
using u32x2 = __attribute__((ext_vector_type(2))) unsigned;
using f32x2 = __attribute__((ext_vector_type(2))) float;
using f32x4 = __attribute__((ext_vector_type(4))) float;

constexpr int DH = 192;
constexpr int HALF = 96;
constexpr int NH = 32;
constexpr int SKV = 16384;
constexpr int NWG = 256;
constexpr unsigned ROWB = DH * 4;
constexpr unsigned HEADB = SKV * ROWB;
constexpr unsigned KVB = NH * HEADB;
constexpr unsigned CSB = SKV * DH * 2;
constexpr unsigned OOB = 0x80000000u;

__device__ __forceinline__ float bflo(unsigned u) { return __uint_as_float(u << 16); }
__device__ __forceinline__ float bfhi(unsigned u) { return __uint_as_float(u & 0xffff0000u); }
__device__ __forceinline__ float rbf(float x) { return __bfloat162float(__float2bfloat16(x)); }

template <int CTRL> __device__ __forceinline__ float dpp(float x) {
    return __builtin_bit_cast(float, __builtin_amdgcn_mov_dpp(__builtin_bit_cast(int, x), CTRL, 0xf, 0xf, true));
}
__device__ __forceinline__ float row16_allsum(float x) {
    x += dpp<0x128>(x);
    x += dpp<0x124>(x);
    x += dpp<0x122>(x);
    x += dpp<0x121>(x);
    return x;
}
__device__ __forceinline__ float xrow_max(float x) {
    auto s = __builtin_amdgcn_permlane16_swap(__float_as_uint(x), __float_as_uint(x), false, false);
    x = fmaxf(__uint_as_float(s[0]), __uint_as_float(s[1]));
    auto t = __builtin_amdgcn_permlane32_swap(__float_as_uint(x), __float_as_uint(x), false, false);
    return fmaxf(__uint_as_float(t[0]), __uint_as_float(t[1]));
}
__device__ __forceinline__ float xrow_sum(float x) {
    auto s = __builtin_amdgcn_permlane16_swap(__float_as_uint(x), __float_as_uint(x), false, false);
    x = __uint_as_float(s[0]) + __uint_as_float(s[1]);
    auto t = __builtin_amdgcn_permlane32_swap(__float_as_uint(x), __float_as_uint(x), false, false);
    return __uint_as_float(t[0]) + __uint_as_float(t[1]);
}
__device__ __forceinline__ float swap32_add(float a, float b) {
    auto t = __builtin_amdgcn_permlane32_swap(__float_as_uint(a), __float_as_uint(b), false, false);
    return __uint_as_float(t[0]) + __uint_as_float(t[1]);
}
__device__ __forceinline__ float swap16_add(float a, float b) {
    auto t = __builtin_amdgcn_permlane16_swap(__float_as_uint(a), __float_as_uint(b), false, false);
    return __uint_as_float(t[0]) + __uint_as_float(t[1]);
}

#define PIN() do { asm volatile("" ::: "memory"); __builtin_amdgcn_sched_barrier(0); } while (0)


typedef volatile __attribute__((address_space(3))) unsigned vlds_u32;
typedef volatile __attribute__((address_space(3))) u32x4 vlds_u32x4;

struct Pace {
    unsigned step;
    unsigned stepv;
    int start;
    int offset;
    int debt;
    int maxadj;
};

template <int HS, int W, int FLUSH, int STAUX, int SLP>
__global__ __launch_bounds__(256 * HS) void kv_sweep_kernel(
    const unsigned short* q, const int* qk, const unsigned short* ksc,
    const unsigned short* kzp, const int* qv, const unsigned short* vsc,
    const unsigned short* vzp, const unsigned short* q_cos, const unsigned short* q_sin,
    const unsigned short* k_cos, const unsigned short* k_sin, float* part, float* pml, Pace pace, float scale_log2)
{
    constexpr int R = 12;
    constexpr int NT = 256 * HS;
    constexpr int NI = NH / HS;
    constexpr int TPW = 4 / HS;

    __shared__ __attribute__((aligned(16))) float s_big[NH * DH + NH * 64 * 4 + 64 * DH];
    __shared__ __attribute__((aligned(16))) float s_pw[NH * 64];
    __shared__ __attribute__((aligned(16))) float s_ml[NH * 4 * 4];
    __shared__ __attribute__((aligned(16))) unsigned s_done[4 * HS];
    float* const s_q = s_big;
    float* const s_sc = s_big + NH * DH;
    u32x2* const s_x = reinterpret_cast<u32x2*>(s_big + NH * DH + NH * 64 * 4);
    float* const s_part = s_big;

    const unsigned tstart = (unsigned)__builtin_amdgcn_s_memrealtime();
    const int tid = threadIdx.x;
    const int lane = tid & 63;
    const int wave = __builtin_amdgcn_readfirstlane(tid >> 6);
    const int rw = wave & 3, hp = wave >> 2;
    const int r = lane >> 4;
    const int c = lane & 15;
    const int bid = blockIdx.x;
    const int m = (bid & 7) * 32 + (bid >> 3);
    const int row0 = m * 64 + rw * 16;

    unsigned tgt = (tstart << 8) + pace.start + ((pace.step * (unsigned)(hp * 1024 + m * 4 + rw)) >> 10);
    unsigned nom = tgt;
    unsigned step = pace.step * HS;
    auto pace_wait = [&]() {
        unsigned now = (unsigned)__builtin_amdgcn_s_memrealtime() << 8;
        if ((int)(now - tgt) > pace.debt) tgt = now - pace.debt;
        while ((int)(tgt - now) > 0) { if (SLP) __builtin_amdgcn_s_sleep(SLP); now = (unsigned)__builtin_amdgcn_s_memrealtime() << 8; }
        tgt += step - min((int)(tgt - nom), pace.maxadj);
        nom += step;
    };

    constexpr int QPT = (384 + NT - 1) / NT;
    u32x4 lq[QPT][2], lc[QPT][2], ls[QPT][2];
#pragma unroll
    for (int i = 0; i < QPT; ++i) {
        const int e = min(tid + i * NT, 383);
        const int hh = e / 12, d8 = e % 12;
#pragma unroll
        for (int k = 0; k < 2; ++k) {
            lq[i][k] = *reinterpret_cast<const u32x4*>(q + hh * DH + k * HALF + d8 * 8);
            lc[i][k] = *reinterpret_cast<const u32x4*>(q_cos + k * HALF + d8 * 8);
            ls[i][k] = *reinterpret_cast<const u32x4*>(q_sin + k * HALF + d8 * 8);
        }
    }
    constexpr int SPT = 512 / NT;
    u32x2 lsc[SPT][4];
#pragma unroll
    for (int i = 0; i < SPT; ++i)
#pragma unroll
        for (int a = 0; a < 4; ++a) {
            const int e = tid + i * NT;
            const unsigned short* base = a == 0 ? ksc : a == 1 ? kzp : a == 2 ? vsc : vzp;
            lsc[i][a] = __builtin_nontemporal_load(reinterpret_cast<const u32x2*>(base + (size_t)(e >> 4) * SKV + m * 64 + (e & 15) * 4));
        }
    PIN();

    const auto rc = __builtin_amdgcn_make_buffer_rsrc((void*)k_cos, 0, CSB, 0x00020000);
    const auto rs = __builtin_amdgcn_make_buffer_rsrc((void*)k_sin, 0, CSB, 0x00020000);
    u32x2 raw[TPW][6];
    auto load_cs = [&](int t) {
        const unsigned co = r * 384 + c * 8;
        const unsigned so1 = r * 384 + (c < 8 ? c * 8 + 320 : c * 8 - 64);
        const unsigned rb = (row0 + (hp * TPW + t) * 4) * 384u;
        raw[t][0] = __builtin_amdgcn_raw_buffer_load_b64(rc, co, rb, 2);
        raw[t][1] = __builtin_amdgcn_raw_buffer_load_b64(rc, co + 128, rb, 2);
        raw[t][2] = __builtin_amdgcn_raw_buffer_load_b64(rc, co + 256, rb, 2);
        raw[t][3] = __builtin_amdgcn_raw_buffer_load_b64(rs, co + 192, rb, 2);
        raw[t][4] = __builtin_amdgcn_raw_buffer_load_b64(rs, so1, rb, 2);
        raw[t][5] = __builtin_amdgcn_raw_buffer_load_b64(rs, co + 64, rb, 2);
        PIN();
    };
    const auto rk = __builtin_amdgcn_make_buffer_rsrc((void*)qk, 0, KVB, 0x00020000);
    const auto rv = __builtin_amdgcn_make_buffer_rsrc((void*)qv, 0, KVB, 0x00020000);
    const unsigned voff = r * ROWB + c * 16;
    const unsigned woff = row0 * ROWB + hp * HEADB;
    u32x4 ring[R];
    auto ld = [&](__amdgpu_buffer_rsrc_t rr, int n, unsigned hoff) {
        return __builtin_amdgcn_raw_buffer_load_b128(rr, voff + (n % 3) * 256, hoff + (n / 3) * (4 * ROWB), 2);
    };
    auto load_k0 = [&](int t) {
        if (3 * t < W) {
            pace_wait();
#pragma unroll
            for (int j = 0; j < 3; ++j) if (3 * t + j < W) ring[3 * t + j] = ld(rk, 3 * t + j, woff);
            PIN();
        }
    };
    load_cs(0);
#pragma unroll
    for (int t = 1; t < TPW; ++t) { pace_wait(); load_cs(t); }
#pragma unroll
    for (int i = 0; i < QPT; ++i) {
        const int e = min(tid + i * NT, 383);
        float* dst = s_q + (e / 12) * DH + (e % 12) * 8;
#pragma unroll
        for (int k = 0; k < 2; ++k) {
            float o[8];
#pragma unroll
            for (int u = 0; u < 4; ++u) {
                const unsigned pp = lq[i][k ^ 1][u] ^ (k == 0 ? 0x80008000u : 0u);
                o[2 * u] = rbf(rbf(bflo(lq[i][k][u]) * bflo(lc[i][k][u])) + rbf(bflo(pp) * bflo(ls[i][k][u])));
                o[2 * u + 1] = rbf(rbf(bfhi(lq[i][k][u]) * bfhi(lc[i][k][u])) + rbf(bfhi(pp) * bfhi(ls[i][k][u])));
            }
            *reinterpret_cast<f32x4*>(dst + k * HALF) = f32x4{o[0], o[1], o[2], o[3]};
            *reinterpret_cast<f32x4*>(dst + k * HALF + 4) = f32x4{o[4], o[5], o[6], o[7]};
        }
    }
#pragma unroll
    for (int i = 0; i < SPT; ++i) {
        float* dst = s_sc + (tid + i * NT) * 16;
#pragma unroll
        for (int u = 0; u < 2; ++u) {
            *reinterpret_cast<f32x4*>(dst + 8 * u) = f32x4{bflo(lsc[i][0][u]) * scale_log2, bflo(lsc[i][1][u]), bflo(lsc[i][2][u]), bflo(lsc[i][3][u])};
            *reinterpret_cast<f32x4*>(dst + 8 * u + 4) = f32x4{bfhi(lsc[i][0][u]) * scale_log2, bfhi(lsc[i][1][u]), bfhi(lsc[i][2][u]), bfhi(lsc[i][3][u])};
        }
    }
    if (tid < 4 * HS) s_done[tid] = 0;
    PIN();
    load_k0(0);
    if (HS == 1) load_k0(1);
    if (HS > 1) {
#pragma unroll
        for (int t = 0; t < TPW; ++t)
#pragma unroll
            for (int k = 0; k < 6; ++k) s_x[((rw * 4 + hp * TPW + t) * 6 + k) * 64 + lane] = raw[t][k];
    }
    asm volatile("s_waitcnt lgkmcnt(0)" ::: "memory");
    __builtin_amdgcn_s_barrier();
    PIN();
    nom += pace.offset;
    if ((int)(nom - tgt) > 0) tgt = nom;
    f32x2 cf[4][3][2], sf[4][3][2];
    {
        const unsigned sg1 = c < 8 ? 0u : 0x80008000u;
#pragma unroll
        for (int t = 0; t < 4; ++t) {
            u32x2 x[6];
#pragma unroll
            for (int k = 0; k < 6; ++k) x[k] = HS > 1 ? s_x[((rw * 4 + t) * 6 + k) * 64 + lane] : raw[t % TPW][k];
            x[4] ^= sg1;
            x[5] ^= 0x80008000u;
#pragma unroll
            for (int j = 0; j < 3; ++j)
#pragma unroll
                for (int k = 0; k < 2; ++k) {
                    cf[t][j][k] = f32x2{bflo(x[j][k]), bfhi(x[j][k])};
                    sf[t][j][k] = f32x2{bflo(x[3 + j][k]), bfhi(x[3 + j][k])};
                }
        }
    }

    const int qi0 = 4 * c, qi1 = c < 8 ? 160 + 4 * c : 4 * c - 32;
    f32x4 qa[3], qb[3];
    auto load_q = [&](int h, int j) {
        const float* qh = s_q + h * DH;
        qa[j] = *reinterpret_cast<const f32x4*>(qh + qi0 + 64 * j);
        qb[j] = *reinterpret_cast<const f32x4*>(qh + (j == 0 ? qi0 + 96 : j == 1 ? qi1 : qi0 + 32));
    };
    load_q(hp, 0); load_q(hp, 1); load_q(hp, 2);
    PIN();
#pragma unroll
    for (int t = (HS == 1 ? 2 : 1); t < 4; ++t) load_k0(t);

    for (int i = 0; i < NI; ++i) {
        const int h = i * HS + hp;
        const unsigned hoff = i * (HS * HEADB) + woff;
        const bool last = i + 1 == NI;
        const unsigned hnext = last ? woff : hoff + HS * HEADB;
        const auto rn = __builtin_amdgcn_make_buffer_rsrc((void*)(last ? qv : qk), 0, KVB, 0x00020000);
        f32x4 sc[4];
#pragma unroll
        for (int t = 0; t < 4; ++t) sc[t] = *reinterpret_cast<const f32x4*>(s_sc + (h * 64 + rw * 16 + 4 * t + r) * 4);
        float s[4];
#pragma unroll
        for (int t = 0; t < 4; ++t) {
            f32x2 dot, ws;
#pragma unroll
            for (int j = 0; j < 3; ++j) {
                const int n = t * 3 + j;
                f32x2 w01 = qa[j].xy * cf[t][j][0] + qb[j].xy * sf[t][j][0];
                f32x2 w23 = qa[j].zw * cf[t][j][1] + qb[j].zw * sf[t][j][1];
                if (j == 0) ws = w01 + w23; else { ws += w01; ws += w23; }
                asm volatile("" : "+v"(w01), "+v"(w23), "+v"(ws));
                if (t == 3) load_q(last ? h : h + HS, j);
                __builtin_amdgcn_sched_barrier(0);
                const u32x4 kk = ring[n];
                f32x2 k01 = {(float)(int)kk[0], (float)(int)kk[1]}, k23 = {(float)(int)kk[2], (float)(int)kk[3]};
                asm volatile("" : "+v"(k01), "+v"(k23));
                __builtin_amdgcn_sched_barrier(0);
                if ((n + W) % 3 == 0) pace_wait();
                ring[(n + W) % R] = n + W < R ? ld(rk, n + W, hoff) : ld(rn, n + W - R, hnext);
                PIN();
                if (j == 0) dot = k01 * w01; else dot += k01 * w01;
                dot += k23 * w23;
            }
            const float a = (dot[0] + dot[1]) - sc[t][1] * (ws[0] + ws[1]);
            s[t] = row16_allsum(a) * sc[t][0];
        }
        const float mx = xrow_max(fmaxf(fmaxf(s[0], s[1]), fmaxf(s[2], s[3])));
        float l = 0.f, z = 0.f;
#pragma unroll
        for (int t = 0; t < 4; ++t) {
            const float p = __builtin_amdgcn_exp2f(s[t] - mx);
            l += p;
            s[t] = p * sc[t][2];
            z += s[t] * sc[t][3];
        }
        l = xrow_sum(l);
        z = xrow_sum(z);
        if (c == 0) {
            *reinterpret_cast<f32x4*>(s_pw + ((h * 4 + rw) * 4 + r) * 4) = f32x4{s[0], s[1], s[2], s[3]};
            *reinterpret_cast<f32x4*>(s_ml + (h * 4 + rw) * 4) = f32x4{mx, l, z, 0.f};
        }
        PIN();
    }
    asm volatile("s_waitcnt lgkmcnt(0)" ::: "memory");
    __builtin_amdgcn_s_barrier();
    PIN();

    const auto rp = __builtin_amdgcn_make_buffer_rsrc((void*)part, 0, NH * 6 * NWG * 128, 0x00020000);
    const auto rm = __builtin_amdgcn_make_buffer_rsrc((void*)pml, 0, NH * NWG * 8, 0x00020000);
    auto flush = [&](int h, int c4) {
        f32x4 ml[4];
#pragma unroll
        for (int w = 0; w < 4; ++w) ml[w] = *reinterpret_cast<const f32x4*>(s_ml + (h * 4 + w) * 4);
        const float M = fmaxf(fmaxf(ml[0][0], ml[1][0]), fmaxf(ml[2][0], ml[3][0]));
        f32x4 o = {0.f, 0.f, 0.f, 0.f};
        float L = 0.f;
#pragma unroll
        for (int w = 0; w < 4; ++w) {
            const float f = __builtin_amdgcn_exp2f(ml[w][0] - M);
            o += f * (*reinterpret_cast<const f32x4*>(s_part + (h * 4 + w) * DH + 4 * c4) - ml[w][2]);
            L += f * ml[w][1];
        }
        __builtin_amdgcn_raw_buffer_store_b128(__builtin_bit_cast(u32x4, o), rp, ((h * 6 + (c4 >> 3)) * NWG + m) * 128 + (c4 & 7) * 16, 0, STAUX);
        if (c4 == 0) __builtin_amdgcn_raw_buffer_store_b64(__builtin_bit_cast(u32x2, f32x2{M, L}), rm, (h * NWG + m) * 8, 0, STAUX);
    };

    step = pace.stepv * HS;
    const float* pwp = s_pw + (rw * 4 + r) * 4;
    f32x4 pwn = *reinterpret_cast<const f32x4*>(pwp + hp * 64);
    for (int i = 0; i < NI; ++i) {
        const int h = i * HS + hp;
        const unsigned hoff = i * (HS * HEADB) + woff;
        const bool last = i + 1 == NI;
        const unsigned hnext = last ? OOB : hoff + HS * HEADB;
        const f32x4 pw = pwn;
        pwn = *reinterpret_cast<const f32x4*>(pwp + (last ? h : h + HS) * 64);
        f32x2 acc[3][2];
#pragma unroll
        for (int t = 0; t < 4; ++t) {
            const f32x2 p2 = {pw[t], pw[t]};
#pragma unroll
            for (int j = 0; j < 3; ++j) {
                const int n = t * 3 + j;
                const u32x4 vv = ring[n];
                f32x2 v01 = {(float)(int)vv[0], (float)(int)vv[1]}, v23 = {(float)(int)vv[2], (float)(int)vv[3]};
                asm volatile("" : "+v"(v01), "+v"(v23));
                __builtin_amdgcn_sched_barrier(0);
                if ((n + W) % 3 == 0 && !(last && n + W >= R)) pace_wait();
                ring[(n + W) % R] = n + W < R ? ld(rv, n + W, hoff) : ld(rv, n + W - R, hnext);
                PIN();
                if (t == 0) { acc[j][0] = p2 * v01; acc[j][1] = p2 * v23; }
                else { acc[j][0] += p2 * v01; acc[j][1] += p2 * v23; }
                asm volatile("" : "+v"(acc[j][0]), "+v"(acc[j][1]));
                __builtin_amdgcn_sched_barrier(0);
            }
        }
        float* sl = s_part + (h * 4 + rw) * DH;
#pragma unroll
        for (int j = 0; j < 3; ++j) {
            const float x = swap32_add(acc[j][0][0], acc[j][1][0]);
            const float y = swap32_add(acc[j][0][1], acc[j][1][1]);
            sl[64 * j + 4 * c + r] = swap16_add(x, y);
        }
        PIN();
        if constexpr (FLUSH) {
            if (lane == 0) *(vlds_u32*)(s_done + wave) = i + 1;
            const int fi = i - FLUSH;
            if (fi >= 0 && (fi & 3) == rw && !last) {
                for (;;) {
                    const u32x4 dn = *(vlds_u32x4*)(s_done + hp * 4);
                    const unsigned mn = min(min(dn[0], dn[1]), min(dn[2], dn[3]));
                    if (__builtin_amdgcn_readfirstlane(mn) > (unsigned)fi) break;
                    __builtin_amdgcn_s_sleep(1);
                }
                asm volatile("" ::: "memory");
                if (lane < 48) flush(fi * HS + hp, lane);
            }
            PIN();
        }
    }
    __syncthreads();

    if constexpr (FLUSH) {
        static_assert(FLUSH + 1 <= 4, "tail heads are written one per wave");
        if (rw <= FLUSH && lane < 48) flush((NI - 1 - rw) * HS + hp, lane);
    } else {
#pragma unroll
        for (int it0 = 0; it0 < NH * 48; it0 += NT) flush((it0 + tid) / 48, (it0 + tid) % 48);
    }
}

template <int NP, int NWV, int LDAUX>
__global__ __launch_bounds__(NWV * 64) void kv_merge_kernel(const float* __restrict__ part, const float* __restrict__ pml, bf16* __restrict__ out)
{
    constexpr int PER = NP / (8 * NWV);
    __shared__ float s_o[NWV][36];
    const int h = blockIdx.x / 6, dg = blockIdx.x % 6;
    const int lane = threadIdx.x & 63, wave = threadIdx.x >> 6;
    const int sub = lane & 7, pr = (lane >> 3) + wave * 8;
    const auto rp = __builtin_amdgcn_make_buffer_rsrc((void*)part, 0, NH * 6 * NP * 128, 0x00020000);
    const auto rm = __builtin_amdgcn_make_buffer_rsrc((void*)pml, 0, NH * NP * 8, 0x00020000);
    f32x4 o[PER];
    f32x2 ml[PER];
#pragma unroll
    for (int i = 0; i < PER; ++i) {
        const int p = pr + i * 8 * NWV;
        ml[i] = __builtin_bit_cast(f32x2, __builtin_amdgcn_raw_buffer_load_b64(rm, (h * NP + p) * 8, 0, LDAUX));
        o[i] = __builtin_bit_cast(f32x4, __builtin_amdgcn_raw_buffer_load_b128(rp, ((h * 6 + dg) * NP + p) * 128 + sub * 16, 0, LDAUX));
    }
    float M = ml[0][0];
#pragma unroll
    for (int i = 1; i < PER; ++i) M = fmaxf(M, ml[i][0]);
    M = fmaxf(M, dpp<0x128>(M));
    M = xrow_max(M);
    f32x4 a = {0.f, 0.f, 0.f, 0.f};
    float L = 0.f;
#pragma unroll
    for (int i = 0; i < PER; ++i) {
        const float f = __builtin_amdgcn_exp2f(ml[i][0] - M);
        a += f * o[i];
        L += f * ml[i][1];
    }
    L += dpp<0x128>(L);
    L = xrow_sum(L);
#pragma unroll
    for (int e = 0; e < 4; ++e) { a[e] += dpp<0x128>(a[e]); a[e] = xrow_sum(a[e]); }
    if constexpr (NWV > 1) {
        if (lane < 8) { *reinterpret_cast<f32x4*>(&s_o[wave][4 * sub]) = a; }
        if (lane == 0) { s_o[wave][32] = M; s_o[wave][33] = L; }
        __syncthreads();
        if (threadIdx.x < 32) {
            float MM = s_o[0][32];
#pragma unroll
            for (int w = 1; w < NWV; ++w) MM = fmaxf(MM, s_o[w][32]);
            float LL = 0.f, oo = 0.f;
#pragma unroll
            for (int w = 0; w < NWV; ++w) {
                const float f = __builtin_amdgcn_exp2f(s_o[w][32] - MM);
                LL += f * s_o[w][33];
                oo += f * s_o[w][threadIdx.x];
            }
            out[h * DH + dg * 32 + threadIdx.x] = __float2bfloat16(oo / LL);
        }
    } else {
        if (lane < 8) {
            const float inv = 1.f / L;
#pragma unroll
            for (int e = 0; e < 4; ++e) out[h * DH + dg * 32 + sub * 4 + e] = __float2bfloat16(a[e] * inv);
        }
    }
}

template <int HS, int W, int FLUSH, int STAUX, int LDAUX>
static void launch_cfg(void* const* d_in, void* d_out, void* d_ws, hipStream_t stream, float t_us, float tv_us, float start_us, float off_us, float debt_us, float adj_pct) {
    float* part = (float*)d_ws;
    float* pml = part + (size_t)NH * 6 * NWG * 32;
    const float scale_log2 = 1.4426950408889634f / sqrtf((float)DH);
    Pace pace;
    pace.step = (unsigned)(t_us * 25600.f / 256.f);
    pace.stepv = (unsigned)(tv_us * 25600.f / 256.f);
    pace.start = (int)(start_us * 25600.f);
    pace.offset = (int)(off_us * 25600.f);
    pace.debt = (int)(debt_us * 25600.f);
    pace.maxadj = (int)(pace.step * HS * adj_pct * 0.01f);
    kv_sweep_kernel<HS, W, FLUSH, STAUX, 1><<<NWG, 256 * HS, 0, stream>>>((const unsigned short*)d_in[0], (const int*)d_in[1], (const unsigned short*)d_in[2],
        (const unsigned short*)d_in[3], (const int*)d_in[4], (const unsigned short*)d_in[5], (const unsigned short*)d_in[6],
        (const unsigned short*)d_in[7], (const unsigned short*)d_in[8], (const unsigned short*)d_in[9], (const unsigned short*)d_in[10], part, pml, pace, scale_log2);
    kv_merge_kernel<NWG, 4, LDAUX><<<NH * 6, 256, 0, stream>>>(part, pml, (bf16*)d_out);
}

extern "C" void kernel_launch(void* const* d_in, const int* in_sizes, int n_in,
                              void* d_out, int out_size, void* d_ws, size_t ws_size,
                              hipStream_t stream) {
    launch_cfg<2, 3, 0, 0, 0>(d_in, d_out, d_ws, stream, 103.5f, 103.5f, 0.5f, 2.0f, 1000.f, 0.f);
}
```

```cpp
#include <hip/hip_runtime.h>
#include <hip/hip_bf16.h>
#include <cstdint>
#include <type_traits>

using bf16 = __hip_bfloat16;
using u32x4 = __attribute__((ext_vector_type(4))) unsigned;
using u32x2 = __attribute__((ext_vector_type(2))) unsigned;
using f32x2 = __attribute__((ext_vector_type(2))) float;
using f32x4 = __attribute__((ext_vector_type(4))) float;
using bf16x2 = __attribute__((ext_vector_type(2))) __bf16;

constexpr int DH = 192;
constexpr int HALF = 96;
constexpr int NH = 32;
constexpr int SKV = 16384;
constexpr int NWG = 256;
constexpr int NI = 16;
constexpr unsigned ROWB = DH * 4;
constexpr unsigned HEADB = SKV * ROWB;
constexpr unsigned KVB = NH * HEADB;
constexpr unsigned CSB = SKV * DH * 2;
constexpr unsigned OOB = 0x80000000u;
constexpr float SCALE_LOG2 = 1.4426950408889634f * 0.07216878364870322f;
constexpr unsigned SPIN_LIMIT = 20000000u;

constexpr unsigned UNITB = NWG * 64;
constexpr unsigned WS_PART = 0;
constexpr unsigned WS_EPOCH = WS_PART + NWG * (UNITB + 1024);
constexpr unsigned WS_BYTES = WS_EPOCH + 128;

__device__ __forceinline__ float bflo(unsigned u) { return __uint_as_float(u << 16); }
__device__ __forceinline__ float bfhi(unsigned u) { return __uint_as_float(u & 0xffff0000u); }
__device__ __forceinline__ float rbf(float x) { return __bfloat162float(__float2bfloat16(x)); }
__device__ __forceinline__ unsigned pk_bf16(f32x2 a) { return __builtin_bit_cast(unsigned, __builtin_convertvector(a, bf16x2)); }
__device__ __forceinline__ void dot_cvt(const u32x4 q, const u32x4 t, const u32x4 ki, f32x2& w01, f32x2& w23, f32x2& k01, f32x2& k23) {
    float w0, w1, w2, w3;
    float k0 = __uint_as_float(ki[0]), k1 = __uint_as_float(ki[1]), k2 = __uint_as_float(ki[2]), k3 = __uint_as_float(ki[3]);
    asm("v_dot2_f32_bf16 %0, %8, %12, 0\n\tv_dot2_f32_bf16 %1, %9, %13, 0\n\tv_dot2_f32_bf16 %2, %10, %14, 0\n\tv_dot2_f32_bf16 %3, %11, %15, 0\n\t"
        "v_cvt_f32_i32 %4, %4\n\tv_cvt_f32_i32 %5, %5\n\tv_cvt_f32_i32 %6, %6\n\tv_cvt_f32_i32 %7, %7"
        : "=&v"(w0), "=&v"(w1), "=&v"(w2), "=&v"(w3), "+v"(k0), "+v"(k1), "+v"(k2), "+v"(k3)
        : "v"(q[0]), "v"(q[1]), "v"(q[2]), "v"(q[3]), "v"(t[0]), "v"(t[1]), "v"(t[2]), "v"(t[3]));
    w01 = f32x2{w0, w1}; w23 = f32x2{w2, w3}; k01 = f32x2{k0, k1}; k23 = f32x2{k2, k3};
}

template <int CTRL> __device__ __forceinline__ float dpp(float x) {
    return __builtin_bit_cast(float, __builtin_amdgcn_mov_dpp(__builtin_bit_cast(int, x), CTRL, 0xf, 0xf, true));
}
__device__ __forceinline__ float row16_allsum(float x) {
    x += dpp<0x128>(x);
    x += dpp<0x124>(x);
    x += dpp<0x122>(x);
    x += dpp<0x121>(x);
    return x;
}
__device__ __forceinline__ float row16_allmax(float x) {
    x = fmaxf(x, dpp<0x128>(x));
    x = fmaxf(x, dpp<0x124>(x));
    x = fmaxf(x, dpp<0x122>(x));
    x = fmaxf(x, dpp<0x121>(x));
    return x;
}
__device__ __forceinline__ float xrow_max(float x) {
    auto s = __builtin_amdgcn_permlane16_swap(__float_as_uint(x), __float_as_uint(x), false, false);
    x = fmaxf(__uint_as_float(s[0]), __uint_as_float(s[1]));
    auto t = __builtin_amdgcn_permlane32_swap(__float_as_uint(x), __float_as_uint(x), false, false);
    return fmaxf(__uint_as_float(t[0]), __uint_as_float(t[1]));
}
__device__ __forceinline__ float xrow_sum(float x) {
    auto s = __builtin_amdgcn_permlane16_swap(__float_as_uint(x), __float_as_uint(x), false, false);
    x = __uint_as_float(s[0]) + __uint_as_float(s[1]);
    auto t = __builtin_amdgcn_permlane32_swap(__float_as_uint(x), __float_as_uint(x), false, false);
    return __uint_as_float(t[0]) + __uint_as_float(t[1]);
}
__device__ __forceinline__ float swap32_add(float a, float b) {
    auto t = __builtin_amdgcn_permlane32_swap(__float_as_uint(a), __float_as_uint(b), false, false);
    return __uint_as_float(t[0]) + __uint_as_float(t[1]);
}
__device__ __forceinline__ float swap16_add(float a, float b) {
    auto t = __builtin_amdgcn_permlane16_swap(__float_as_uint(a), __float_as_uint(b), false, false);
    return __uint_as_float(t[0]) + __uint_as_float(t[1]);
}
__device__ __forceinline__ unsigned now32() { return (unsigned)__builtin_amdgcn_s_memrealtime(); }

#define NSTAMP 12
#define STAMP(i)
#define TRACE(i)
#define PIN() do { asm volatile("" ::: "memory"); __builtin_amdgcn_sched_barrier(0); } while (0)
#define BARRIER() do { asm volatile("s_waitcnt lgkmcnt(0)" ::: "memory"); __builtin_amdgcn_s_barrier(); PIN(); } while (0)
#define VMCNT(n) asm volatile("s_waitcnt vmcnt(%0)" :: "i"(n) : "memory")

typedef volatile __attribute__((address_space(3))) unsigned vlds_u32;
typedef volatile __attribute__((address_space(3))) u32x4 vlds_u32x4;

struct Pace {
    unsigned step;
    int start;
    int offset;
    int debt;
    int maxadj;
    int pub;
};

template <int X, int PRO_, int GATE, int STAG_, int SLP>
__global__ __launch_bounds__(576) void kv_sweep_kernel(
    const unsigned short* q, const int* qk, const unsigned short* ksc,
    const unsigned short* kzp, const int* qv, const unsigned short* vsc,
    const unsigned short* vzp, const unsigned short* q_cos, const unsigned short* q_sin,
    const unsigned short* k_cos, const unsigned short* k_sin, unsigned char* ws, bf16* out, Pace pace)
{
    __shared__ __attribute__((aligned(16))) unsigned s_q[NH * DH];
    __shared__ __attribute__((aligned(16))) float s_sc[NH * 64 * 4];
    __shared__ __attribute__((aligned(16))) float s_part[8 * NI * DH];
    __shared__ __attribute__((aligned(16))) float s_red[8 * 28];
    __shared__ __attribute__((aligned(16))) float s_f[NH * 8];
    __shared__ __attribute__((aligned(16))) unsigned s_done[8];
    __shared__ __attribute__((aligned(16))) unsigned s_sync[8];

    constexpr int PRO = PRO_ % 10, PF = PRO_ / 10, STAG = STAG_ % 10, LAY = STAG_ / 10;
    constexpr unsigned PSTR = LAY == 0 ? 64 : LAY == 1 ? UNITB + 256 : LAY == 2 ? UNITB + 128 : UNITB;
    constexpr unsigned USTR = LAY == 0 ? UNITB : 64;
    STAMP(0);
    unsigned long long pc0;
    asm volatile("s_getpc_b64 %0" : "=s"(pc0));
    const unsigned tstart = now32();
    const int tid = threadIdx.x;
    const int lane = tid & 63;
    const int wave = __builtin_amdgcn_readfirstlane(tid >> 6);
    const int r = lane >> 4;
    const int c = lane & 15;
    const int bid = blockIdx.x;
    const int m = (bid & 7) * 32 + (bid >> 3);
    const auto rw_ = __builtin_amdgcn_make_buffer_rsrc((void*)ws, 0, WS_BYTES, 0x00020000);

    auto merge = [&](int i, int hp, int c6, unsigned tag) {
        const f32x4 f = *reinterpret_cast<const f32x4*>(s_f + (2 * i + hp) * 8);
        const f32x2 zl = *reinterpret_cast<const f32x2*>(s_f + (2 * i + hp) * 8 + 4);
        const float zs = zl[0];
        f32x2 a[3];
#pragma unroll
        for (int k = 0; k < 3; ++k) a[k] = f32x2{-zs, -zs};
#pragma unroll
        for (int w = 0; w < 4; ++w) {
            const float* p = s_part + ((hp * 4 + w) * NI + i) * DH + c6 * 6;
#pragma unroll
            for (int k = 0; k < 3; ++k) a[k] += f[w] * *reinterpret_cast<const f32x2*>(p + 2 * k);
        }
        return u32x4{pk_bf16(a[0]), pk_bf16(a[1]), pk_bf16(a[2]), tag | ((__float_as_uint(zl[1]) >> (16 * (c6 & 1))) & 0xffffu)};
    };
    auto publish = [&](u32x4 v, int i, int hp, int c6, bool on) {
        __builtin_amdgcn_raw_buffer_store_b128(v, rw_, on ? (hp * 8 + (c6 >> 2)) * USTR + (c6 & 3) * 16 : OOB, WS_PART + i * 16 * USTR + m * PSTR, 16);
    };

    if (wave == 8) {
        unsigned long long pc1;
        asm volatile("s_getpc_b64 %0" : "=s"(pc1));
        pc0 &= ~255ull;
        const auto rcode = __builtin_amdgcn_make_buffer_rsrc((void*)pc0, 0, (unsigned)(pc1 - pc0), 0x00020000);
        auto warm_code = [&]() {
            unsigned x[2];
#pragma unroll
            for (int k = 0; k < 2; ++k) x[k] = __builtin_amdgcn_raw_buffer_load_b32(rcode, lane * 128 + k * 8192, 0, 0);
            asm volatile("s_waitcnt vmcnt(0)" :: "v"(x[0]), "v"(x[1]) : "memory");
        };
        if (PF) warm_code();
        const unsigned tag = ((unsigned)__builtin_amdgcn_raw_buffer_load_b16(rw_, (lane & 3) * 2, WS_EPOCH, 16) + 1u) << 16;
        if (lane < 8) { s_done[lane] = 0; s_sync[lane] = lane < 4 ? 0 : tag; }
        PIN();
        BARRIER();
        const int mh = lane >> 5, mc = lane & 31;
        if (PF >= 2) {
            while ((int)(now32() - tstart) < pace.pub / 2 - 300) __builtin_amdgcn_s_sleep(64);
            warm_code();
        }
#pragma nounroll
        for (int i = 0; i < NI - 1; ++i) {
            for (;;) {
                const u32x4 a = *(vlds_u32x4*)s_done, b = *(vlds_u32x4*)(s_done + 4);
                const unsigned mn = min(min(min(a[0], a[1]), min(a[2], a[3])), min(min(b[0], b[1]), min(b[2], b[3])));
                if (__builtin_amdgcn_readfirstlane(mn) > (unsigned)i) break;
                __builtin_amdgcn_s_sleep(4);
            }
            PIN();
            if (i == 0) {
                f32x4 ml[4], f;
#pragma unroll
                for (int w = 0; w < 4; ++w) ml[w] = *reinterpret_cast<const f32x4*>(s_sc + (mc * 4 + w) * 64 + 16);
                const float M = fmaxf(fmaxf(ml[0][0], ml[1][0]), fmaxf(ml[2][0], ml[3][0]));
                float L = 0.f, zs = 0.f;
#pragma unroll
                for (int w = 0; w < 4; ++w) { f[w] = __builtin_amdgcn_exp2f(ml[w][0] - M); L += f[w] * ml[w][1]; zs += f[w] * ml[w][2]; }
                const float inv = 1.f / L;
                *reinterpret_cast<f32x4*>(s_f + mc * 8) = f * inv;
                *reinterpret_cast<f32x2*>(s_f + mc * 8 + 4) = f32x2{zs * inv, M + __builtin_amdgcn_logf(L)};
                PIN();
                if (lane == 0) *(vlds_u32*)(s_sync + 3) = 1;
                PIN();
            }
            if (PF >= 2 && i == NI - 3) warm_code();
            const u32x4 v = merge(i, mh, mc, tag);
            PIN();
            *reinterpret_cast<u32x4*>(s_part + (mh * 4 * NI + i) * DH + mc * 4) = v;
            PIN();
        }
        u32x4 v[NI - 1];
#pragma unroll
        for (int i = 0; i < NI - 1; ++i) { v[i] = *reinterpret_cast<const u32x4*>(s_part + (mh * 4 * NI + i) * DH + mc * 4); asm volatile("" : "+v"(v[i])); }
        STAMP(5);
        while ((int)(now32() - tstart) < pace.pub) __builtin_amdgcn_s_sleep(1);
        PIN();
        STAMP(6);
#pragma unroll
        for (int i = 0; i < NI - 1; ++i) publish(v[i], i, mh, mc, true);
        STAMP(7);
        return;
    }

    const int rw = wave & 3, hp = wave >> 2;
    const int row0 = m * 64 + rw * 16;

    const unsigned sidx = STAG == 0 ? hp * 1024 + m * 4 + rw : STAG == 1 ? m * 8 + wave : (hp * 1024 + (m & 31) * 32 + (m >> 5) * 4 + rw);
    unsigned tgt = (tstart << 8) + pace.start + ((pace.step * sidx) >> 11);
    unsigned nom = tgt;
    const unsigned step = pace.step;
    auto pace_wait = [&]() {
        unsigned now = now32() << 8;
        if ((int)(now - tgt) > pace.debt) tgt = now - pace.debt;
        while ((int)(tgt - now) > 0) { if (SLP) __builtin_amdgcn_s_sleep(SLP); now = now32() << 8; }
        tgt += step - min((int)(tgt - nom), pace.maxadj);
        nom += step;
    };

    const int qe = min(tid, 383);
    const int qh = qe / 12, qd = (qe % 12) * 8;
    u32x4 lq[2], lc[2], ls[2];
#pragma unroll
    for (int k = 0; k < 2; ++k) {
        lq[k] = *reinterpret_cast<const u32x4*>(q + qh * DH + k * HALF + qd);
        lc[k] = *reinterpret_cast<const u32x4*>(q_cos + k * HALF + qd);
        ls[k] = *reinterpret_cast<const u32x4*>(q_sin + k * HALF + qd);
    }
    u32x2 lsc[4];
#pragma unroll
    for (int a = 0; a < 4; ++a) {
        const unsigned short* base = a == 0 ? ksc : a == 1 ? kzp : a == 2 ? vsc : vzp;
        lsc[a] = __builtin_nontemporal_load(reinterpret_cast<const u32x2*>(base + (size_t)(tid >> 4) * SKV + m * 64 + (tid & 15) * 4));
    }
    PIN();

    const auto rc = __builtin_amdgcn_make_buffer_rsrc((void*)k_cos, 0, CSB, 0x00020000);
    const auto rs = __builtin_amdgcn_make_buffer_rsrc((void*)k_sin, 0, CSB, 0x00020000);
    u32x2 raw[2][6];
    auto load_cs = [&](int t) {
        const unsigned co = r * 384 + c * 8;
        const unsigned so1 = r * 384 + (c < 8 ? c * 8 + 320 : c * 8 - 64);
        const unsigned rb = (row0 + (t + 2 * hp) * 4) * 384u;
        raw[t][0] = __builtin_amdgcn_raw_buffer_load_b64(rc, co, rb, 2);
        raw[t][1] = __builtin_amdgcn_raw_buffer_load_b64(rc, co + 128, rb, 2);
        raw[t][2] = __builtin_amdgcn_raw_buffer_load_b64(rc, co + 256, rb, 2);
        raw[t][3] = __builtin_amdgcn_raw_buffer_load_b64(rs, co + 192, rb, 2);
        raw[t][4] = __builtin_amdgcn_raw_buffer_load_b64(rs, so1, rb, 2);
        raw[t][5] = __builtin_amdgcn_raw_buffer_load_b64(rs, co + 64, rb, 2);
        PIN();
    };
    u32x4 tab[4][3];
    auto pack_cs = [&](int t) {
#pragma unroll
        for (int j = 0; j < 3; ++j) {
#pragma unroll
            for (int k = 0; k < 2; ++k) {
                tab[t][j][2 * k] = __builtin_amdgcn_perm(raw[t][3 + j][k], raw[t][j][k], 0x05040100);
                tab[t][j][2 * k + 1] = __builtin_amdgcn_perm(raw[t][3 + j][k], raw[t][j][k], 0x07060302);
            }
            *reinterpret_cast<u32x4*>(s_part + (wave ^ 4) * (NI * DH) + ((t * 3 + j) * 64 + lane) * 4) = tab[t][j];
        }
        PIN();
    };
    const auto rk = __builtin_amdgcn_make_buffer_rsrc((void*)qk, 0, KVB, 0x00020000);
    const auto rv = __builtin_amdgcn_make_buffer_rsrc((void*)qv, 0, KVB, 0x00020000);
    const unsigned voff = r * ROWB + c * 16;
    const unsigned woff = row0 * ROWB + hp * HEADB;
    auto toff = [&](int t) { return (unsigned)(t ^ (2 * hp)) * (4 * ROWB); };
    u32x4 ring[4][3];
    auto ld = [&](int slot, int j, __amdgpu_buffer_rsrc_t rr, unsigned off) {
        ring[slot][j] = __builtin_amdgcn_raw_buffer_load_b128(rr, voff + j * 256, off, 2);
        PIN();
    };
    auto ld_tile = [&](int slot, __amdgpu_buffer_rsrc_t rr, unsigned off) { ld(slot, 0, rr, off); ld(slot, 1, rr, off); ld(slot, 2, rr, off); };
    auto refill = [&](int slot, int j, __amdgpu_buffer_rsrc_t rr, unsigned off, bool on) {
        if (GATE == 2) {
            if (on) { VMCNT(X); if (j == 0) pace_wait(); }
            ld(slot, j, rr, off);
        } else if (j == 2) {
            if (GATE == 0) {
                if (on) { VMCNT(X); pace_wait(); }
                ld_tile(slot, rr, off);
            } else {
                if (on) { VMCNT(X); pace_wait(); }
                ld(slot, 0, rr, off);
                if (on) VMCNT(X);
                ld(slot, 1, rr, off);
                if (on) VMCNT(X);
                ld(slot, 2, rr, off);
            }
        }
    };
    auto small_to_lds = [&]() {
        float o[2][8];
#pragma unroll
        for (int k = 0; k < 2; ++k)
#pragma unroll
            for (int u = 0; u < 4; ++u) {
                const unsigned pp = lq[k ^ 1][u] ^ (k == 0 ? 0x80008000u : 0u);
                o[k][2 * u] = rbf(rbf(bflo(lq[k][u]) * bflo(lc[k][u])) + rbf(bflo(pp) * bflo(ls[k][u])));
                o[k][2 * u + 1] = rbf(rbf(bfhi(lq[k][u]) * bfhi(lc[k][u])) + rbf(bfhi(pp) * bfhi(ls[k][u])));
            }
#pragma unroll
        for (int v = 0; v < 2; ++v) {
            u32x4 lo, hi;
#pragma unroll
            for (int e = 0; e < 4; ++e) {
                lo[e] = __builtin_amdgcn_perm(__float_as_uint(o[1][4 * v + e]), __float_as_uint(o[0][4 * v + e]), 0x07060302);
                hi[e] = __builtin_amdgcn_perm(__float_as_uint(o[0][4 * v + e]) ^ 0x80000000u, __float_as_uint(o[1][4 * v + e]), 0x07060302);
            }
            *reinterpret_cast<u32x4*>(s_q + qh * DH + qd + 4 * v) = lo;
            *reinterpret_cast<u32x4*>(s_q + qh * DH + HALF + qd + 4 * v) = hi;
        }
        float* dst = s_sc + tid * 16;
#pragma unroll
        for (int u = 0; u < 2; ++u) {
            *reinterpret_cast<f32x4*>(dst + 8 * u) = f32x4{bflo(lsc[0][u]) * SCALE_LOG2, bflo(lsc[1][u]), bflo(lsc[2][u]), bflo(lsc[3][u])};
            *reinterpret_cast<f32x4*>(dst + 8 * u + 4) = f32x4{bfhi(lsc[0][u]) * SCALE_LOG2, bfhi(lsc[1][u]), bfhi(lsc[2][u]), bfhi(lsc[3][u])};
        }
        PIN();
    };

    load_cs(0);
    pace_wait();
    load_cs(1);
    if (PRO == 0) small_to_lds();
    STAMP(1);
    pace_wait(); ld_tile(0, rk, woff + toff(0));
    if (PRO == 0) pack_cs(0);
    if (PRO == 1) small_to_lds();
    pace_wait(); ld_tile(1, rk, woff + toff(1));
    if (PRO == 0) pack_cs(1);
    if (PRO == 1) pack_cs(0);
    if (PRO == 2) small_to_lds();
    if (PRO == 0) VMCNT(3);
    pace_wait(); ld_tile(2, rk, woff + toff(2));
    if (PRO == 1) pack_cs(1);
    if (PRO == 2) { pack_cs(0); pack_cs(1); }
    STAMP(2);
    if (PRO == 0) VMCNT(3);
    if (PRO == 1) VMCNT(6);
    pace_wait(); ld_tile(3, rk, woff + toff(3));
    BARRIER();
    nom += pace.offset;
    if ((int)(nom - tgt) > 0) tgt = nom;
    STAMP(3);

    u32x4 qp[3];
    auto load_q = [&](int h, int j) { qp[j] = *reinterpret_cast<const u32x4*>(s_q + h * DH + 64 * j + 4 * c); };
    load_q(hp, 0); load_q(hp, 1); load_q(hp, 2);
#pragma unroll
    for (int t = 0; t < 2; ++t)
#pragma unroll
        for (int j = 0; j < 3; ++j) tab[2 + t][j] = *reinterpret_cast<const u32x4*>(s_part + wave * (NI * DH) + ((t * 3 + j) * 64 + lane) * 4);

    for (int i = 0; i < NI; ++i) {
        const bool last = i + 1 == NI;
        const int h = 2 * i + hp;
        const unsigned hnext = (i + 1) * (2 * HEADB) + woff;
        const auto rn = __builtin_amdgcn_make_buffer_rsrc((void*)(last ? qv : qk), 0, KVB, 0x00020000);
        float* scp = s_sc + (h * 4 + rw) * 64;
        f32x4 sc[4];
#pragma unroll
        for (int t = 0; t < 4; ++t) sc[t] = *reinterpret_cast<const f32x4*>(scp + (4 * (t ^ (2 * hp)) + r) * 4);
        float s[4];
#pragma unroll
        for (int t = 0; t < 4; ++t) {
            f32x2 dot, wsum;
#pragma unroll
            for (int j = 0; j < 3; ++j) {
                f32x2 w01, w23, k01, k23;
                dot_cvt(qp[j], tab[t][j], ring[t][j], w01, w23, k01, k23);
                if (j == 0) { wsum = w01 + w23; dot = k01 * w01; } else { wsum += w01; wsum += w23; dot += k01 * w01; }
                dot += k23 * w23;
                asm volatile("" : "+v"(dot), "+v"(wsum));
                PIN();
                if (t == 3) load_q(last ? h : h + 2, j);
                if (t == 0 || t == 3) refill(t, j, rn, last ? woff + toff(t == 3 ? 1 : 0) : hnext + toff(t), true);
                else refill(t, j, rk, last ? OOB : hnext + toff(t), !last);
                if (t == 0 && j == (GATE == 2 ? 0 : 2)) TRACE(i);
            }
            const float a = (dot[0] + dot[1]) - sc[t][1] * (wsum[0] + wsum[1]);
            s[t] = row16_allsum(a) * sc[t][0];
        }
        const float mx = xrow_max(fmaxf(fmaxf(s[0], s[1]), fmaxf(s[2], s[3])));
        float l = 0.f, z = 0.f;
#pragma unroll
        for (int t = 0; t < 4; ++t) {
            const float p = __builtin_amdgcn_exp2f(s[t] - mx);
            l += p;
            s[t] = p * sc[t][2];
            z += s[t] * sc[t][3];
        }
        l = xrow_sum(l);
        z = xrow_sum(z);
        if (c == 0) {
            *reinterpret_cast<f32x4*>(scp + r * 4) = f32x4{s[0], s[1], s[2], s[3]};
            *reinterpret_cast<f32x4*>(scp + 16) = f32x4{mx, l, z, 0.f};
        }
        PIN();
    }
    STAMP(4);

    const float* pwp = s_sc + (hp * 4 + rw) * 64 + r * 4;
    f32x4 pw = *reinterpret_cast<const f32x4*>(pwp);
    for (int i = 0; i < NI; ++i) {
        const bool last = i + 1 == NI;
        const unsigned hoff = i * (2 * HEADB) + woff;
        f32x2 acc[3][2];
#pragma unroll
        for (int t = 0; t < 4; ++t) {
            const f32x2 p2 = {pw[t], pw[t]};
#pragma unroll
            for (int j = 0; j < 3; ++j) {
                const u32x4 vv = ring[(t & 1) * 3][j];
                const f32x2 v01 = {(float)(int)vv[0], (float)(int)vv[1]}, v23 = {(float)(int)vv[2], (float)(int)vv[3]};
                if (t == 0) { acc[j][0] = p2 * v01; acc[j][1] = p2 * v23; }
                else { acc[j][0] += p2 * v01; acc[j][1] += p2 * v23; }
                asm volatile("" : "+v"(acc[j][0]), "+v"(acc[j][1]));
                PIN();
                if (t == 3 && j == 2) pw = *reinterpret_cast<const f32x4*>(pwp + (last ? i : i + 1) * 512);
                if (t < 2) refill((t & 1) * 3, j, rv, hoff + toff(t + 2), true);
                else refill((t & 1) * 3, j, rv, last ? OOB : hoff + 2 * HEADB + toff(t - 2), !last);
                if (t == 0 && j == (GATE == 2 ? 0 : 2)) TRACE(16 + i);
            }
        }
        float* sl = s_part + (wave * NI + i) * DH;
#pragma unroll
        for (int j = 0; j < 3; ++j) {
            const float x = swap32_add(acc[j][0][0], acc[j][1][0]);
            const float y = swap32_add(acc[j][0][1], acc[j][1][1]);
            sl[64 * j + 4 * c + r] = swap16_add(x, y);
        }
        PIN();
        if (lane == 0) *(vlds_u32*)(s_done + wave) = i + 1;
        PIN();
    }
    STAMP(6);
    unsigned old = 0;
    if (lane == 0) old = __hip_atomic_fetch_add(s_sync + hp, 1u, __ATOMIC_RELAXED, __HIP_MEMORY_SCOPE_WORKGROUP);
    PIN();
    const unsigned tag = s_sync[4 + (lane & 3)];
    if (__builtin_amdgcn_readfirstlane(old) == 3) {
        while (__builtin_amdgcn_readfirstlane(*(vlds_u32*)(s_sync + 3)) == 0) __builtin_amdgcn_s_sleep(1);
        PIN();
        publish(merge(NI - 1, hp, lane & 31, tag), NI - 1, hp, lane & 31, lane < 32);
        PIN();
    }
    STAMP(7);
    STAMP(8);
    {
        u32x4 x, y;
        unsigned xo = (tid >> 2) * PSTR + (tid & 3) * 16, yo = xo + 128 * PSTR;
        for (;;) {
            const u32x4 xn = __builtin_amdgcn_raw_buffer_load_b128(rw_, xo, WS_PART + bid * USTR, 16);
            const u32x4 yn = __builtin_amdgcn_raw_buffer_load_b128(rw_, yo, WS_PART + bid * USTR, 16);
            unsigned bad = (xo != OOB && (xn[3] ^ tag) > 0xffffu ? 1u : 0u) | (yo != OOB && (yn[3] ^ tag) > 0xffffu ? 2u : 0u);
            bad |= __builtin_amdgcn_mov_dpp(bad, 0xb1, 0xf, 0xf, true);
            bad |= __builtin_amdgcn_mov_dpp(bad, 0x4e, 0xf, 0xf, true);
            const bool tx = xo != OOB, ty = yo != OOB;
#pragma unroll
            for (int k = 0; k < 4; ++k) { x[k] = tx ? xn[k] : x[k]; y[k] = ty ? yn[k] : y[k]; }
            xo = bad & 1 ? xo : OOB;
            yo = bad & 2 ? yo : OOB;
            if (__builtin_amdgcn_ballot_w64(bad != 0) == 0 || now32() - tstart > SPIN_LIMIT) break;
            PIN();
        }
        STAMP(9);
        const unsigned sel = lane & 1 ? 0x01000504u : 0x05040100u;
        const float lx = __uint_as_float(__builtin_amdgcn_perm(__builtin_amdgcn_mov_dpp(x[3], 0xb1, 0xf, 0xf, true), x[3], sel));
        const float ly = __uint_as_float(__builtin_amdgcn_perm(__builtin_amdgcn_mov_dpp(y[3], 0xb1, 0xf, 0xf, true), y[3], sel));
        float M = fmaxf(lx, ly);
        M = fmaxf(M, dpp<0x128>(M));
        M = fmaxf(M, dpp<0x124>(M));
        M = xrow_max(M);
        const float w0 = __builtin_amdgcn_exp2f(lx - M), w1 = __builtin_amdgcn_exp2f(ly - M);
        float a[8];
#pragma unroll
        for (int k = 0; k < 3; ++k) {
            a[2 * k] = w0 * bflo(x[k]) + w1 * bflo(y[k]);
            a[2 * k + 1] = w0 * bfhi(x[k]) + w1 * bfhi(y[k]);
        }
        a[6] = a[7] = w0 + w1;
        float b[4], d[2];
#pragma unroll
        for (int k = 0; k < 4; ++k) b[k] = swap32_add(a[k], a[4 + k]);
#pragma unroll
        for (int k = 0; k < 2; ++k) d[k] = swap16_add(b[k], b[2 + k]);
#pragma unroll
        for (int k = 0; k < 2; ++k) { d[k] += dpp<0x128>(d[k]); d[k] += dpp<0x124>(d[k]); }
        if (c < 4) *reinterpret_cast<f32x2*>(s_red + wave * 28 + (r < 3 ? c * 6 + r * 2 : 24)) = f32x2{d[0], r < 3 ? d[1] : M};
        STAMP(10);
        BARRIER();
        if (tid < 24) {
            f32x2 sm[8];
            float ov[8];
#pragma unroll
            for (int w = 0; w < 8; ++w) { sm[w] = *reinterpret_cast<const f32x2*>(s_red + w * 28 + 24); ov[w] = s_red[w * 28 + tid]; }
            float MM = sm[0][1];
#pragma unroll
            for (int w = 1; w < 8; ++w) MM = fmaxf(MM, sm[w][1]);
            float S = 0.f, o = 0.f;
#pragma unroll
            for (int w = 0; w < 8; ++w) {
                const float ff = __builtin_amdgcn_exp2f(sm[w][1] - MM);
                S += ff * sm[w][0];
                o += ff * ov[w];
            }
            out[bid * 24 + tid] = __float2bfloat16(o * __builtin_amdgcn_rcpf(S));
        }
        if (bid == 0 && wave == 1) __builtin_amdgcn_raw_buffer_store_b16((unsigned short)(tag >> 16), rw_, lane < 4 ? lane * 2 : OOB, WS_EPOCH, 16);
    }
}

template <int X, int PRO, int GATE, int STAG>
static void launch_cfg(void* const* d_in, void* d_out, void* d_ws, hipStream_t stream, float t_us, float start_us, float off_us, float debt_us, float adj_pct, float pub_us) {
    Pace pace;
    pace.step = (unsigned)(t_us * 25600.f / 128.f);
    pace.start = (int)(start_us * 25600.f);
    pace.offset = (int)(off_us * 25600.f);
    pace.debt = (int)(debt_us * 25600.f);
    pace.maxadj = (int)(pace.step * adj_pct * 0.01f);
    pace.pub = (int)((start_us + off_us + t_us * (129.f / 128.f) + pub_us) * 100.f);
    kv_sweep_kernel<X, PRO, GATE, STAG, 1><<<NWG, 576, 0, stream>>>((const unsigned short*)d_in[0], (const int*)d_in[1], (const unsigned short*)d_in[2],
        (const unsigned short*)d_in[3], (const int*)d_in[4], (const unsigned short*)d_in[5], (const unsigned short*)d_in[6],
        (const unsigned short*)d_in[7], (const unsigned short*)d_in[8], (const unsigned short*)d_in[9], (const unsigned short*)d_in[10],
        (unsigned char*)d_ws, (bf16*)d_out, pace);
}

#define CFG 2, 20, 2, 0
#define PRM 103.5f, 0.5f, 2.0f, 1000.f, 0.f, -1.f
extern "C" void kernel_launch(void* const* d_in, const int* in_sizes, int n_in,
                              void* d_out, int out_size, void* d_ws, size_t ws_size,
                              hipStream_t stream) {
    launch_cfg<CFG>(d_in, d_out, d_ws, stream, PRM);
}
```

```cpp
#include <hip/hip_runtime.h>
#include <hip/hip_bf16.h>
#include <cstdint>
#include <type_traits>

using bf16 = __hip_bfloat16;
using u32x4 = __attribute__((ext_vector_type(4))) unsigned;
using u32x2 = __attribute__((ext_vector_type(2))) unsigned;
using f32x2 = __attribute__((ext_vector_type(2))) float;
using f32x4 = __attribute__((ext_vector_type(4))) float;
using bf16x2 = __attribute__((ext_vector_type(2))) __bf16;

constexpr int DH = 192;
constexpr int HALF = 96;
constexpr int NH = 32;
constexpr int SKV = 16384;
constexpr int NWG = 256;
constexpr int NI = 16;
constexpr unsigned ROWB = DH * 4;
constexpr unsigned HEADB = SKV * ROWB;
constexpr unsigned KVB = NH * HEADB;
constexpr unsigned CSB = SKV * DH * 2;
constexpr unsigned OOB = 0x80000000u;
constexpr float SCALE_LOG2 = 1.4426950408889634f * 0.07216878364870322f;
constexpr unsigned SPIN_LIMIT = 20000000u;

constexpr unsigned UNITB = NWG * 64;
constexpr unsigned WS_PART = 0;
constexpr unsigned WS_EPOCH = WS_PART + NWG * (UNITB + 1024);
constexpr unsigned WS_REP = WS_EPOCH + 256;
constexpr unsigned WS_BYTES = WS_REP + 256 * 256;

__device__ __forceinline__ float bflo(unsigned u) { return __uint_as_float(u << 16); }
__device__ __forceinline__ float bfhi(unsigned u) { return __uint_as_float(u & 0xffff0000u); }
__device__ __forceinline__ float rbf(float x) { return __bfloat162float(__float2bfloat16(x)); }
__device__ __forceinline__ unsigned pk_bf16(f32x2 a) { return __builtin_bit_cast(unsigned, __builtin_convertvector(a, bf16x2)); }
__device__ __forceinline__ void dot_cvt(const u32x4 q, const u32x4 t, const u32x4 ki, f32x2& w01, f32x2& w23, f32x2& k01, f32x2& k23) {
    float w0, w1, w2, w3;
    float k0 = __uint_as_float(ki[0]), k1 = __uint_as_float(ki[1]), k2 = __uint_as_float(ki[2]), k3 = __uint_as_float(ki[3]);
    asm("v_dot2_f32_bf16 %0, %8, %12, 0\n\tv_dot2_f32_bf16 %1, %9, %13, 0\n\tv_dot2_f32_bf16 %2, %10, %14, 0\n\tv_dot2_f32_bf16 %3, %11, %15, 0\n\t"
        "v_cvt_f32_i32 %4, %4\n\tv_cvt_f32_i32 %5, %5\n\tv_cvt_f32_i32 %6, %6\n\tv_cvt_f32_i32 %7, %7"
        : "=&v"(w0), "=&v"(w1), "=&v"(w2), "=&v"(w3), "+v"(k0), "+v"(k1), "+v"(k2), "+v"(k3)
        : "v"(q[0]), "v"(q[1]), "v"(q[2]), "v"(q[3]), "v"(t[0]), "v"(t[1]), "v"(t[2]), "v"(t[3]));
    w01 = f32x2{w0, w1}; w23 = f32x2{w2, w3}; k01 = f32x2{k0, k1}; k23 = f32x2{k2, k3};
}

template <int CTRL> __device__ __forceinline__ float dpp(float x) {
    return __builtin_bit_cast(float, __builtin_amdgcn_mov_dpp(__builtin_bit_cast(int, x), CTRL, 0xf, 0xf, true));
}
__device__ __forceinline__ float row16_allsum(float x) {
    x += dpp<0x128>(x);
    x += dpp<0x124>(x);
    x += dpp<0x122>(x);
    x += dpp<0x121>(x);
    return x;
}
__device__ __forceinline__ float row16_allmax(float x) {
    x = fmaxf(x, dpp<0x128>(x));
    x = fmaxf(x, dpp<0x124>(x));
    x = fmaxf(x, dpp<0x122>(x));
    x = fmaxf(x, dpp<0x121>(x));
    return x;
}
__device__ __forceinline__ float xrow_max(float x) {
    auto s = __builtin_amdgcn_permlane16_swap(__float_as_uint(x), __float_as_uint(x), false, false);
    x = fmaxf(__uint_as_float(s[0]), __uint_as_float(s[1]));
    auto t = __builtin_amdgcn_permlane32_swap(__float_as_uint(x), __float_as_uint(x), false, false);
    return fmaxf(__uint_as_float(t[0]), __uint_as_float(t[1]));
}
__device__ __forceinline__ float xrow_sum(float x) {
    auto s = __builtin_amdgcn_permlane16_swap(__float_as_uint(x), __float_as_uint(x), false, false);
    x = __uint_as_float(s[0]) + __uint_as_float(s[1]);
    auto t = __builtin_amdgcn_permlane32_swap(__float_as_uint(x), __float_as_uint(x), false, false);
    return __uint_as_float(t[0]) + __uint_as_float(t[1]);
}
__device__ __forceinline__ float swap32_add(float a, float b) {
    auto t = __builtin_amdgcn_permlane32_swap(__float_as_uint(a), __float_as_uint(b), false, false);
    return __uint_as_float(t[0]) + __uint_as_float(t[1]);
}
__device__ __forceinline__ float swap16_add(float a, float b) {
    auto t = __builtin_amdgcn_permlane16_swap(__float_as_uint(a), __float_as_uint(b), false, false);
    return __uint_as_float(t[0]) + __uint_as_float(t[1]);
}
__device__ __forceinline__ int row16_sum(int x) {
    x += __builtin_amdgcn_mov_dpp(x, 0x128, 0xf, 0xf, true);
    x += __builtin_amdgcn_mov_dpp(x, 0x124, 0xf, 0xf, true);
    x += __builtin_amdgcn_mov_dpp(x, 0x122, 0xf, 0xf, true);
    x += __builtin_amdgcn_mov_dpp(x, 0x121, 0xf, 0xf, true);
    return x;
}
__device__ __forceinline__ unsigned now32() { return (unsigned)__builtin_amdgcn_s_memrealtime(); }

#define NSTAMP 12
#define STAMP(i)
#define TRACE(i)
#define PIN() do { asm volatile("" ::: "memory"); __builtin_amdgcn_sched_barrier(0); } while (0)
#define BARRIER() do { asm volatile("s_waitcnt lgkmcnt(0)" ::: "memory"); __builtin_amdgcn_s_barrier(); PIN(); } while (0)
#define VMCNT(n) asm volatile("s_waitcnt vmcnt(%0)" :: "i"(n) : "memory")

typedef volatile __attribute__((address_space(3))) unsigned vlds_u32;
typedef volatile __attribute__((address_space(3))) u32x2 vlds_u32x2;
typedef volatile __attribute__((address_space(3))) u32x4 vlds_u32x4;

struct Pace {
    unsigned step;
    int start;
    int offset;
    int bias;
    int rd;
    int pub;
};

template <int X, int PRO_, int GATE, int STAG_, int SLP>
__global__ __launch_bounds__(576) void kv_sweep_kernel(
    const unsigned short* q, const int* qk, const unsigned short* ksc,
    const unsigned short* kzp, const int* qv, const unsigned short* vsc,
    const unsigned short* vzp, const unsigned short* q_cos, const unsigned short* q_sin,
    const unsigned short* k_cos, const unsigned short* k_sin, unsigned char* ws, bf16* out, Pace pace)
{
    __shared__ __attribute__((aligned(16))) unsigned s_q[NH * DH];
    __shared__ __attribute__((aligned(16))) float s_sc[NH * 64 * 4];
    __shared__ __attribute__((aligned(16))) float s_part[8 * NI * DH];
    __shared__ __attribute__((aligned(16))) float s_red[8 * 28];
    __shared__ __attribute__((aligned(16))) float s_f[NH * 8];
    __shared__ __attribute__((aligned(16))) unsigned s_done[8];
    __shared__ __attribute__((aligned(16))) unsigned s_sync[8];
    __shared__ __attribute__((aligned(16))) unsigned s_act[16];

    constexpr bool TRABS = true;
    constexpr int ORD = GATE % 10, WID = GATE / 10;
    constexpr int PRO = PRO_ % 10, PF = PRO_ / 10, STAG = STAG_ % 10, LAY = STAG_ / 10;
    constexpr unsigned PSTR = LAY == 0 ? 64 : LAY == 1 ? UNITB + 256 : LAY == 2 ? UNITB + 128 : UNITB;
    constexpr unsigned USTR = LAY == 0 ? UNITB : 64;
    STAMP(0);
    unsigned long long pc0;
    asm volatile("s_getpc_b64 %0" : "=s"(pc0));
    const unsigned tstart = now32();
    const int tid = threadIdx.x;
    const int lane = tid & 63;
    const int wave = __builtin_amdgcn_readfirstlane(tid >> 6);
    const int r = lane >> 4;
    const int c = lane & 15;
    const int bid = blockIdx.x;
    const int m = (bid & 7) * 32 + (bid >> 3);
    const auto rw_ = __builtin_amdgcn_make_buffer_rsrc((void*)ws, 0, WS_BYTES, 0x00020000);

    auto merge = [&](int i, int hp, int c6, unsigned tag) {
        const f32x4 f = *reinterpret_cast<const f32x4*>(s_f + (2 * i + hp) * 8);
        const f32x2 zl = *reinterpret_cast<const f32x2*>(s_f + (2 * i + hp) * 8 + 4);
        const float zs = zl[0];
        f32x2 a[3];
#pragma unroll
        for (int k = 0; k < 3; ++k) a[k] = f32x2{-zs, -zs};
#pragma unroll
        for (int w = 0; w < 4; ++w) {
            const float* p = s_part + ((hp * 4 + w) * NI + i) * DH + c6 * 6;
#pragma unroll
            for (int k = 0; k < 3; ++k) a[k] += f[w] * *reinterpret_cast<const f32x2*>(p + 2 * k);
        }
        return u32x4{pk_bf16(a[0]), pk_bf16(a[1]), pk_bf16(a[2]), tag | ((__float_as_uint(zl[1]) >> (16 * (c6 & 1))) & 0xffffu)};
    };
    auto publish = [&](u32x4 v, int i, int hp, int c6, bool on) {
        __builtin_amdgcn_raw_buffer_store_b128(v, rw_, on ? (hp * 8 + (c6 >> 2)) * USTR + (c6 & 3) * 16 : OOB, WS_PART + i * 16 * USTR + m * PSTR, 16);
        asm volatile("s_nop 1" :: "v"(v) : "memory");
    };

    if (wave == 8) {
        unsigned long long pc1;
        asm volatile("s_getpc_b64 %0" : "=s"(pc1));
        pc0 &= ~255ull;
        const auto rcode = __builtin_amdgcn_make_buffer_rsrc((void*)pc0, 0, (unsigned)(pc1 - pc0), 0x00020000);
        auto warm_code = [&]() {
            unsigned x[2];
#pragma unroll
            for (int k = 0; k < 2; ++k) x[k] = __builtin_amdgcn_raw_buffer_load_b32(rcode, lane * 128 + k * 8192, 0, 0);
            asm volatile("s_waitcnt vmcnt(0)" :: "v"(x[0]), "v"(x[1]) : "memory");
        };
        if (PF) warm_code();
        const unsigned tag = ((unsigned)__builtin_amdgcn_raw_buffer_load_b16(rw_, (lane & 3) * 2, WS_EPOCH, 16) + 1u) << 16;
        unsigned origin = (tstart << 8) + pace.offset;
        if (lane < 8) { s_done[lane] = 0; s_sync[lane] = lane == 2 ? origin : lane < 4 ? 0 : tag; }
        PIN();
        BARRIER();
        const int mh = lane >> 5, mc = lane & 31;
        const unsigned rtag = __builtin_amdgcn_readlane(tag, 0) | __builtin_amdgcn_readlane(tag, 1) >> 16;
        constexpr int RSH = X / 10, RLAG = X % 10;
        const int rid = ((bid >> 3) & 15) == 5 ? (bid >> 7) * 8 + (bid & 7) : -1;
        unsigned round = now32() >> RSH;
        const unsigned round0 = round;
        bool warmed = false;
        int i = 0;
        while (i < NI - 1) {
            const unsigned now = now32();
            if (RSH && (now >> RSH) != round && i < NI - 2) {
                round = now >> RSH;
                const u32x2 a = *(vlds_u32x2*)(s_act + 2 * (lane & 7));
                const int late = max((int)(a[0] - origin), (int)((now << 8) - a[1] - origin));
                const unsigned act = origin + (__builtin_amdgcn_readfirstlane(row16_sum(late)) >> 4);
                if (rid >= 0 && (now & ((1u << RSH) - 1)) < 32)
                    __builtin_amdgcn_raw_buffer_store_b64(u32x2{act, rtag}, rw_, lane == 0 ? rid * 8 : OOB, WS_REP + (round & 255) * 256, 16);
                if (round - round0 > RLAG) {
                    const u32x2 x = __builtin_amdgcn_raw_buffer_load_b64(rw_, c * 8, WS_REP + ((round - RLAG) & 255) * 256, 16);
                    const int d = (int)(x[0] - origin);
                    if (__builtin_amdgcn_ballot_w64(x[1] != rtag || d > (32 << 16) || d < -(32 << 16)) == 0) {
                        origin += (__builtin_amdgcn_readfirstlane(row16_sum(d)) >> 4) - pace.bias;
                        if (lane == 0) *(vlds_u32*)(s_sync + 2) = origin;
                    }
                }
                PIN();
                continue;
            }
            if (PF >= 2 && !warmed && (int)((now << 8) - origin) > 60 * (int)pace.step) { warm_code(); warmed = true; continue; }
            const u32x4 a = *(vlds_u32x4*)s_done, b = *(vlds_u32x4*)(s_done + 4);
            const unsigned mn = min(min(min(a[0], a[1]), min(a[2], a[3])), min(min(b[0], b[1]), min(b[2], b[3])));
            if (__builtin_amdgcn_readfirstlane(mn) <= (unsigned)i) { __builtin_amdgcn_s_sleep(2); continue; }
            PIN();
            if (i == 0) {
                f32x4 ml[4], f;
#pragma unroll
                for (int w = 0; w < 4; ++w) ml[w] = *reinterpret_cast<const f32x4*>(s_sc + (mc * 4 + w) * 64 + 16);
                const float M = fmaxf(fmaxf(ml[0][0], ml[1][0]), fmaxf(ml[2][0], ml[3][0]));
                float L = 0.f, zs = 0.f;
#pragma unroll
                for (int w = 0; w < 4; ++w) { f[w] = __builtin_amdgcn_exp2f(ml[w][0] - M); L += f[w] * ml[w][1]; zs += f[w] * ml[w][2]; }
                const float inv = 1.f / L;
                *reinterpret_cast<f32x4*>(s_f + mc * 8) = f * inv;
                *reinterpret_cast<f32x2*>(s_f + mc * 8 + 4) = f32x2{zs * inv, M + __builtin_amdgcn_logf(L)};
                PIN();
                if (lane == 0) *(vlds_u32*)(s_sync + 3) = 1;
                PIN();
            }
            if (PF >= 2 && i == NI - 3) warm_code();
            const u32x4 v = merge(i, mh, mc, tag);
            PIN();
            if (pace.pub < 0) publish(v, i, mh, mc, true);
            else *reinterpret_cast<u32x4*>(s_part + (mh * 4 * NI + i) * DH + mc * 4) = v;
            PIN();
            ++i;
        }
        u32x4 v[NI - 1];
#pragma unroll
        for (int i = 0; i < NI - 1; ++i) { v[i] = *reinterpret_cast<const u32x4*>(s_part + (mh * 4 * NI + i) * DH + mc * 4); asm volatile("" : "+v"(v[i])); }
        STAMP(5);
        while ((int)((now32() << 8) - origin) < pace.pub) __builtin_amdgcn_s_sleep(1);
        PIN();
        STAMP(6);
#pragma unroll
        for (int i = 0; i < NI - 1; ++i) publish(v[i], i, mh, mc, pace.pub >= 0);
        STAMP(7);
        return;
    }

    const int rw = wave & 3, hp = wave >> 2;
    const int row0 = m * 64 + rw * 16;

    const unsigned sidx = STAG == 0 ? hp * 1024 + m * 4 + rw : STAG == 1 ? m * 8 + wave : (hp * 1024 + (m & 31) * 32 + (m >> 5) * 4 + rw);
    unsigned off = pace.start + ((pace.step * sidx) >> 11);
    unsigned origin = tstart << 8, kept = 0;
    bool wide = false;
    const unsigned step = pace.step;
    auto pace_wait = [&]() {
        const unsigned tgt = __builtin_amdgcn_readfirstlane(origin) + off;
        unsigned now = now32() << 8;
        kept = ((int)(now - tgt) > 0 ? now : tgt) - off;
        wide = (int)(now - tgt) > 30 * 256;
        while ((int)(tgt - now) > 0) { if (SLP) __builtin_amdgcn_s_sleep(SLP); now = now32() << 8; }
        off += step;
    };
    auto report = [&]() {
        if (lane == 0) *(vlds_u32x2*)(s_act + 2 * wave) = u32x2{kept, off};
        origin = *(vlds_u32*)(s_sync + 2);
        PIN();
    };
#define TRV (TRABS ? kept - (tstart << 8) : kept - __builtin_amdgcn_readfirstlane(origin))

    const int qe = min(tid, 383);
    const int qh = qe / 12, qd = (qe % 12) * 8;
    u32x4 lq[2], lc[2], ls[2];
#pragma unroll
    for (int k = 0; k < 2; ++k) {
        lq[k] = *reinterpret_cast<const u32x4*>(q + qh * DH + k * HALF + qd);
        lc[k] = *reinterpret_cast<const u32x4*>(q_cos + k * HALF + qd);
        ls[k] = *reinterpret_cast<const u32x4*>(q_sin + k * HALF + qd);
    }
    u32x2 lsc[4];
#pragma unroll
    for (int a = 0; a < 4; ++a) {
        const unsigned short* base = a == 0 ? ksc : a == 1 ? kzp : a == 2 ? vsc : vzp;
        lsc[a] = __builtin_nontemporal_load(reinterpret_cast<const u32x2*>(base + (size_t)(tid >> 4) * SKV + m * 64 + (tid & 15) * 4));
    }
    PIN();

    const auto rc = __builtin_amdgcn_make_buffer_rsrc((void*)k_cos, 0, CSB, 0x00020000);
    const auto rs = __builtin_amdgcn_make_buffer_rsrc((void*)k_sin, 0, CSB, 0x00020000);
    u32x2 raw[2][6];
    auto load_cs = [&](int t) {
        const unsigned co = r * 384 + c * 8;
        const unsigned so1 = r * 384 + (c < 8 ? c * 8 + 320 : c * 8 - 64);
        const unsigned rb = (row0 + (t + 2 * hp) * 4) * 384u;
        raw[t][0] = __builtin_amdgcn_raw_buffer_load_b64(rc, co, rb, 2);
        raw[t][1] = __builtin_amdgcn_raw_buffer_load_b64(rc, co + 128, rb, 2);
        raw[t][2] = __builtin_amdgcn_raw_buffer_load_b64(rc, co + 256, rb, 2);
        raw[t][3] = __builtin_amdgcn_raw_buffer_load_b64(rs, co + 192, rb, 2);
        raw[t][4] = __builtin_amdgcn_raw_buffer_load_b64(rs, so1, rb, 2);
        raw[t][5] = __builtin_amdgcn_raw_buffer_load_b64(rs, co + 64, rb, 2);
        PIN();
    };
    u32x4 tab[4][3];
    auto pack_cs = [&](int t) {
#pragma unroll
        for (int j = 0; j < 3; ++j) {
#pragma unroll
            for (int k = 0; k < 2; ++k) {
                tab[t][j][2 * k] = __builtin_amdgcn_perm(raw[t][3 + j][k], raw[t][j][k], 0x05040100);
                tab[t][j][2 * k + 1] = __builtin_amdgcn_perm(raw[t][3 + j][k], raw[t][j][k], 0x07060302);
            }
            *reinterpret_cast<u32x4*>(s_part + (wave ^ 4) * (NI * DH) + ((t * 3 + j) * 64 + lane) * 4) = tab[t][j];
        }
        PIN();
    };
    const auto rk = __builtin_amdgcn_make_buffer_rsrc((void*)qk, 0, KVB, 0x00020000);
    const auto rv = __builtin_amdgcn_make_buffer_rsrc((void*)qv, 0, KVB, 0x00020000);
    const unsigned voff = r * ROWB + c * 16;
    const unsigned woff = row0 * ROWB + hp * HEADB;
    auto toff = [&](int t) { return (unsigned)(t ^ (2 * hp)) * (4 * ROWB); };
    u32x4 ring[4][3];
    auto ld = [&](int slot, int j, __amdgpu_buffer_rsrc_t rr, unsigned off) {
        ring[slot][j] = __builtin_amdgcn_raw_buffer_load_b128(rr, voff + j * 256, off, 2);
        PIN();
    };
    auto ld_tile = [&](int slot, __amdgpu_buffer_rsrc_t rr, unsigned off) { ld(slot, 0, rr, off); ld(slot, 1, rr, off); ld(slot, 2, rr, off); };
    auto refill = [&](int slot, int j, __amdgpu_buffer_rsrc_t rr, unsigned goff, bool on) {
        if (ORD == 0) {
            if (on) { if (WID && wide) VMCNT(2 + WID); else VMCNT(2); if (j == 0) pace_wait(); }
            ld(slot, j, rr, goff);
        } else {
            if (on) { if (j == 0) pace_wait(); if (WID && wide) VMCNT(2 + WID); else VMCNT(2); }
            ld(slot, j, rr, goff);
            if (on && j == 0) { const unsigned t = (now32() << 8) - 1024 - (off - step); if ((int)(t - kept) > 0) kept = t; }
        }
        if (j == 2) report();
    };
    auto small_to_lds = [&]() {
        float o[2][8];
#pragma unroll
        for (int k = 0; k < 2; ++k)
#pragma unroll
            for (int u = 0; u < 4; ++u) {
                const unsigned pp = lq[k ^ 1][u] ^ (k == 0 ? 0x80008000u : 0u);
                o[k][2 * u] = rbf(rbf(bflo(lq[k][u]) * bflo(lc[k][u])) + rbf(bflo(pp) * bflo(ls[k][u])));
                o[k][2 * u + 1] = rbf(rbf(bfhi(lq[k][u]) * bfhi(lc[k][u])) + rbf(bfhi(pp) * bfhi(ls[k][u])));
            }
#pragma unroll
        for (int v = 0; v < 2; ++v) {
            u32x4 lo, hi;
#pragma unroll
            for (int e = 0; e < 4; ++e) {
                lo[e] = __builtin_amdgcn_perm(__float_as_uint(o[1][4 * v + e]), __float_as_uint(o[0][4 * v + e]), 0x07060302);
                hi[e] = __builtin_amdgcn_perm(__float_as_uint(o[0][4 * v + e]) ^ 0x80000000u, __float_as_uint(o[1][4 * v + e]), 0x07060302);
            }
            *reinterpret_cast<u32x4*>(s_q + qh * DH + qd + 4 * v) = lo;
            *reinterpret_cast<u32x4*>(s_q + qh * DH + HALF + qd + 4 * v) = hi;
        }
        float* dst = s_sc + tid * 16;
#pragma unroll
        for (int u = 0; u < 2; ++u) {
            *reinterpret_cast<f32x4*>(dst + 8 * u) = f32x4{bflo(lsc[0][u]) * SCALE_LOG2, bflo(lsc[1][u]), bflo(lsc[2][u]), bflo(lsc[3][u])};
            *reinterpret_cast<f32x4*>(dst + 8 * u + 4) = f32x4{bfhi(lsc[0][u]) * SCALE_LOG2, bfhi(lsc[1][u]), bfhi(lsc[2][u]), bfhi(lsc[3][u])};
        }
        PIN();
    };

    load_cs(0);
    pace_wait();
    load_cs(1);
    if (PRO == 0) small_to_lds();
    STAMP(1);
    pace_wait(); ld_tile(0, rk, woff + toff(0));
    if (PRO == 0) pack_cs(0);
    if (PRO == 1) small_to_lds();
    pace_wait(); ld_tile(1, rk, woff + toff(1));
    if (PRO == 0) pack_cs(1);
    if (PRO == 1) pack_cs(0);
    if (PRO == 2) small_to_lds();
    if (PRO == 0) VMCNT(3);
    pace_wait(); ld_tile(2, rk, woff + toff(2));
    if (PRO == 1) pack_cs(1);
    if (PRO == 2) { pack_cs(0); pack_cs(1); }
    STAMP(2);
    if (PRO == 0) VMCNT(3);
    if (PRO == 1) VMCNT(6);
    pace_wait(); ld_tile(3, rk, woff + toff(3));
    if (lane == 0) *(vlds_u32x2*)(s_act + 2 * wave) = u32x2{kept, off};
    BARRIER();
    origin = *(vlds_u32*)(s_sync + 2);
    STAMP(3);

    u32x4 qp[3];
    auto load_q = [&](int h, int j) { qp[j] = *reinterpret_cast<const u32x4*>(s_q + h * DH + 64 * j + 4 * c); };
    load_q(hp, 0); load_q(hp, 1); load_q(hp, 2);
#pragma unroll
    for (int t = 0; t < 2; ++t)
#pragma unroll
        for (int j = 0; j < 3; ++j) tab[2 + t][j] = *reinterpret_cast<const u32x4*>(s_part + wave * (NI * DH) + ((t * 3 + j) * 64 + lane) * 4);

    for (int i = 0; i < NI; ++i) {
        const bool last = i + 1 == NI;
        const int h = 2 * i + hp;
        const unsigned hnext = (i + 1) * (2 * HEADB) + woff;
        const auto rn = __builtin_amdgcn_make_buffer_rsrc((void*)(last ? qv : qk), 0, KVB, 0x00020000);
        float* scp = s_sc + (h * 4 + rw) * 64;
        f32x4 sc[4];
#pragma unroll
        for (int t = 0; t < 4; ++t) sc[t] = *reinterpret_cast<const f32x4*>(scp + (4 * (t ^ (2 * hp)) + r) * 4);
        float s[4];
#pragma unroll
        for (int t = 0; t < 4; ++t) {
            f32x2 dot, wsum;
#pragma unroll
            for (int j = 0; j < 3; ++j) {
                f32x2 w01, w23, k01, k23;
                dot_cvt(qp[j], tab[t][j], ring[t][j], w01, w23, k01, k23);
                if (j == 0) { wsum = w01 + w23; dot = k01 * w01; } else { wsum += w01; wsum += w23; dot += k01 * w01; }
                dot += k23 * w23;
                asm volatile("" : "+v"(dot), "+v"(wsum));
                PIN();
                if (t == 3) load_q(last ? h : h + 2, j);
                if (t == 0 || t == 3) refill(t, j, rn, last ? woff + toff(t == 3 ? 1 : 0) : hnext + toff(t), true);
                else refill(t, j, rk, last ? OOB : hnext + toff(t), !last);
                if (t == 0 && j == 0) TRACE(i);
            }
            const float a = (dot[0] + dot[1]) - sc[t][1] * (wsum[0] + wsum[1]);
            s[t] = row16_allsum(a) * sc[t][0];
        }
        const float mx = xrow_max(fmaxf(fmaxf(s[0], s[1]), fmaxf(s[2], s[3])));
        float l = 0.f, z = 0.f;
#pragma unroll
        for (int t = 0; t < 4; ++t) {
            const float p = __builtin_amdgcn_exp2f(s[t] - mx);
            l += p;
            s[t] = p * sc[t][2];
            z += s[t] * sc[t][3];
        }
        l = xrow_sum(l);
        z = xrow_sum(z);
        if (c == 0) {
            *reinterpret_cast<f32x4*>(scp + r * 4) = f32x4{s[0], s[1], s[2], s[3]};
            *reinterpret_cast<f32x4*>(scp + 16) = f32x4{mx, l, z, 0.f};
        }
        PIN();
    }
    STAMP(4);

    const float* pwp = s_sc + (hp * 4 + rw) * 64 + r * 4;
    f32x4 pw = *reinterpret_cast<const f32x4*>(pwp);
    for (int i = 0; i < NI; ++i) {
        const bool last = i + 1 == NI;
        const unsigned hoff = i * (2 * HEADB) + woff;
        f32x2 acc[3][2];
#pragma unroll
        for (int t = 0; t < 4; ++t) {
            const f32x2 p2 = {pw[t], pw[t]};
#pragma unroll
            for (int j = 0; j < 3; ++j) {
                const u32x4 vv = ring[(t & 1) * 3][j];
                const f32x2 v01 = {(float)(int)vv[0], (float)(int)vv[1]}, v23 = {(float)(int)vv[2], (float)(int)vv[3]};
                if (t == 0) { acc[j][0] = p2 * v01; acc[j][1] = p2 * v23; }
                else { acc[j][0] += p2 * v01; acc[j][1] += p2 * v23; }
                asm volatile("" : "+v"(acc[j][0]), "+v"(acc[j][1]));
                PIN();
                if (t == 3 && j == 2) pw = *reinterpret_cast<const f32x4*>(pwp + (last ? i : i + 1) * 512);
                if (t < 2) refill((t & 1) * 3, j, rv, hoff + toff(t + 2), true);
                else refill((t & 1) * 3, j, rv, last ? OOB : hoff + 2 * HEADB + toff(t - 2), !last);
                if (t == 0 && j == 0) TRACE(16 + i);
            }
        }
        float* sl = s_part + (wave * NI + i) * DH;
#pragma unroll
        for (int j = 0; j < 3; ++j) {
            const float x = swap32_add(acc[j][0][0], acc[j][1][0]);
            const float y = swap32_add(acc[j][0][1], acc[j][1][1]);
            sl[64 * j + 4 * c + r] = swap16_add(x, y);
        }
        PIN();
        if (lane == 0) *(vlds_u32*)(s_done + wave) = i + 1;
        PIN();
    }
    STAMP(6);
    unsigned old = 0;
    if (lane == 0) old = __hip_atomic_fetch_add(s_sync + hp, 1u, __ATOMIC_RELAXED, __HIP_MEMORY_SCOPE_WORKGROUP);
    PIN();
    const unsigned tag = s_sync[4 + (lane & 3)];
    if (__builtin_amdgcn_readfirstlane(old) == 3) {
        while (__builtin_amdgcn_readfirstlane(*(vlds_u32*)(s_sync + 3)) == 0) __builtin_amdgcn_s_sleep(1);
        PIN();
        publish(merge(NI - 1, hp, lane & 31, tag), NI - 1, hp, lane & 31, lane < 32);
        PIN();
    }
    STAMP(7);
    {
        const unsigned o = __builtin_amdgcn_readfirstlane(origin);
        while ((int)((now32() << 8) - o) < pace.rd) __builtin_amdgcn_s_sleep(1);
        PIN();
    }
    STAMP(8);
    {
        u32x4 x, y;
        unsigned xo = (tid >> 2) * PSTR + (tid & 3) * 16, yo = xo + 128 * PSTR;
        for (;;) {
            const u32x4 xn = __builtin_amdgcn_raw_buffer_load_b128(rw_, xo, WS_PART + bid * USTR, 16);
            const u32x4 yn = __builtin_amdgcn_raw_buffer_load_b128(rw_, yo, WS_PART + bid * USTR, 16);
            unsigned bad = (xo != OOB && (xn[3] ^ tag) > 0xffffu ? 1u : 0u) | (yo != OOB && (yn[3] ^ tag) > 0xffffu ? 2u : 0u);
            bad |= __builtin_amdgcn_mov_dpp(bad, 0xb1, 0xf, 0xf, true);
            bad |= __builtin_amdgcn_mov_dpp(bad, 0x4e, 0xf, 0xf, true);
            const bool tx = xo != OOB, ty = yo != OOB;
#pragma unroll
            for (int k = 0; k < 4; ++k) { x[k] = tx ? xn[k] : x[k]; y[k] = ty ? yn[k] : y[k]; }
            xo = bad & 1 ? xo : OOB;
            yo = bad & 2 ? yo : OOB;
            if (__builtin_amdgcn_ballot_w64(bad != 0) == 0 || now32() - tstart > SPIN_LIMIT) break;
            PIN();
        }
        STAMP(9);
        const unsigned sel = lane & 1 ? 0x01000504u : 0x05040100u;
        const float lx = __uint_as_float(__builtin_amdgcn_perm(__builtin_amdgcn_mov_dpp(x[3], 0xb1, 0xf, 0xf, true), x[3], sel));
        const float ly = __uint_as_float(__builtin_amdgcn_perm(__builtin_amdgcn_mov_dpp(y[3], 0xb1, 0xf, 0xf, true), y[3], sel));
        float M = fmaxf(lx, ly);
        M = fmaxf(M, dpp<0x128>(M));
        M = fmaxf(M, dpp<0x124>(M));
        M = xrow_max(M);
        const float w0 = __builtin_amdgcn_exp2f(lx - M), w1 = __builtin_amdgcn_exp2f(ly - M);
        float a[8];
#pragma unroll
        for (int k = 0; k < 3; ++k) {
            a[2 * k] = w0 * bflo(x[k]) + w1 * bflo(y[k]);
            a[2 * k + 1] = w0 * bfhi(x[k]) + w1 * bfhi(y[k]);
        }
        a[6] = a[7] = w0 + w1;
        float b[4], d[2];
#pragma unroll
        for (int k = 0; k < 4; ++k) b[k] = swap32_add(a[k], a[4 + k]);
#pragma unroll
        for (int k = 0; k < 2; ++k) d[k] = swap16_add(b[k], b[2 + k]);
#pragma unroll
        for (int k = 0; k < 2; ++k) { d[k] += dpp<0x128>(d[k]); d[k] += dpp<0x124>(d[k]); }
        if (c < 4) *reinterpret_cast<f32x2*>(s_red + wave * 28 + (r < 3 ? c * 6 + r * 2 : 24)) = f32x2{d[0], r < 3 ? d[1] : M};
        STAMP(10);
        BARRIER();
        if (tid < 24) {
            f32x2 sm[8];
            float ov[8];
#pragma unroll
            for (int w = 0; w < 8; ++w) { sm[w] = *reinterpret_cast<const f32x2*>(s_red + w * 28 + 24); ov[w] = s_red[w * 28 + tid]; }
            float MM = sm[0][1];
#pragma unroll
            for (int w = 1; w < 8; ++w) MM = fmaxf(MM, sm[w][1]);
            float S = 0.f, o = 0.f;
#pragma unroll
            for (int w = 0; w < 8; ++w) {
                const float ff = __builtin_amdgcn_exp2f(sm[w][1] - MM);
                S += ff * sm[w][0];
                o += ff * ov[w];
            }
            out[bid * 24 + tid] = __float2bfloat16(o * __builtin_amdgcn_rcpf(S));
        }
        if (bid == 0 && wave == 1) __builtin_amdgcn_raw_buffer_store_b16((unsigned short)(tag >> 16), rw_, lane < 4 ? lane * 2 : OOB, WS_EPOCH, 16);
    }
}

template <int X, int PRO, int GATE, int STAG>
static void launch_cfg(void* const* d_in, void* d_out, void* d_ws, hipStream_t stream, float t_us, float start_us, float off_us, float debt_us, float adj_pct, float pub_us) {
    Pace pace;
    pace.step = (unsigned)(t_us * 25600.f / 128.f);
    pace.start = (int)(start_us * 25600.f);
    pace.offset = (int)(off_us * 25600.f);
    pace.bias = (int)(debt_us * 25600.f);
    pace.rd = (int)((start_us + t_us * (129.f / 128.f) + adj_pct) * 25600.f);
    pace.pub = (int)((start_us + t_us * (129.f / 128.f) + pub_us) * 25600.f);
    kv_sweep_kernel<X, PRO, GATE, STAG, 1><<<NWG, 576, 0, stream>>>((const unsigned short*)d_in[0], (const int*)d_in[1], (const unsigned short*)d_in[2],
        (const unsigned short*)d_in[3], (const int*)d_in[4], (const unsigned short*)d_in[5], (const unsigned short*)d_in[6],
        (const unsigned short*)d_in[7], (const unsigned short*)d_in[8], (const unsigned short*)d_in[9], (const unsigned short*)d_in[10],
        (unsigned char*)d_ws, (bf16*)d_out, pace);
}

#define CFG 81, 20, 1, 0
#define PRM 102.5f, 0.5f, 1.0f, 0.05f, 2.0f, 0.5f
extern "C" void kernel_launch(void* const* d_in, const int* in_sizes, int n_in,
                              void* d_out, int out_size, void* d_ws, size_t ws_size,
                              hipStream_t stream) {
    launch_cfg<CFG>(d_in, d_out, d_ws, stream, PRM);
}
```

```cpp
#include <hip/hip_runtime.h>
#include <hip/hip_bf16.h>
#include <cstdint>
#include <type_traits>


using bf16 = __hip_bfloat16;
using u32x4 = __attribute__((ext_vector_type(4))) unsigned;
using u32x2 = __attribute__((ext_vector_type(2))) unsigned;
using f32x2 = __attribute__((ext_vector_type(2))) float;
using f32x4 = __attribute__((ext_vector_type(4))) float;
using bf16x2 = __attribute__((ext_vector_type(2))) __bf16;

constexpr int DH = 192;
constexpr int HALF = 96;
constexpr int NH = 32;
constexpr int SKV = 16384;
constexpr int NWG = 256;
constexpr int NI = 16;
constexpr unsigned ROWB = DH * 4;
constexpr unsigned HEADB = SKV * ROWB;
constexpr unsigned KVB = NH * HEADB;
constexpr unsigned CSB = SKV * DH * 2;
constexpr unsigned OOB = 0x80000000u;
constexpr float SCALE_LOG2 = 1.4426950408889634f * 0.07216878364870322f;
constexpr unsigned SPIN_LIMIT = 20000000u;

constexpr unsigned UNITB = NWG * 64;
constexpr unsigned WS_PART = 0;
constexpr unsigned WS_EPOCH = WS_PART + NWG * (UNITB + 1024);
constexpr unsigned WS_REP = WS_EPOCH + 256;
constexpr unsigned WS_BYTES = WS_REP + 256 * 256;

__device__ __forceinline__ float bflo(unsigned u) { return __uint_as_float(u << 16); }
__device__ __forceinline__ float bfhi(unsigned u) { return __uint_as_float(u & 0xffff0000u); }
__device__ __forceinline__ float rbf(float x) { return __bfloat162float(__float2bfloat16(x)); }
__device__ __forceinline__ unsigned pk_bf16(f32x2 a) { return __builtin_bit_cast(unsigned, __builtin_convertvector(a, bf16x2)); }
__device__ __forceinline__ void dot_cvt(const u32x4 q, const u32x4 t, const u32x4 ki, f32x2& w01, f32x2& w23, f32x2& k01, f32x2& k23) {
    float w0, w1, w2, w3;
    float k0 = __uint_as_float(ki[0]), k1 = __uint_as_float(ki[1]), k2 = __uint_as_float(ki[2]), k3 = __uint_as_float(ki[3]);
    asm("v_dot2_f32_bf16 %0, %8, %12, 0\n\tv_dot2_f32_bf16 %1, %9, %13, 0\n\tv_dot2_f32_bf16 %2, %10, %14, 0\n\tv_dot2_f32_bf16 %3, %11, %15, 0\n\t"
        "v_cvt_f32_i32 %4, %4\n\tv_cvt_f32_i32 %5, %5\n\tv_cvt_f32_i32 %6, %6\n\tv_cvt_f32_i32 %7, %7"
        : "=&v"(w0), "=&v"(w1), "=&v"(w2), "=&v"(w3), "+v"(k0), "+v"(k1), "+v"(k2), "+v"(k3)
        : "v"(q[0]), "v"(q[1]), "v"(q[2]), "v"(q[3]), "v"(t[0]), "v"(t[1]), "v"(t[2]), "v"(t[3]));
    w01 = f32x2{w0, w1}; w23 = f32x2{w2, w3}; k01 = f32x2{k0, k1}; k23 = f32x2{k2, k3};
}

template <int CTRL> __device__ __forceinline__ float dpp(float x) {
    return __builtin_bit_cast(float, __builtin_amdgcn_mov_dpp(__builtin_bit_cast(int, x), CTRL, 0xf, 0xf, true));
}
__device__ __forceinline__ float row16_allsum(float x) {
    x += dpp<0x128>(x);
    x += dpp<0x124>(x);
    x += dpp<0x122>(x);
    x += dpp<0x121>(x);
    return x;
}
__device__ __forceinline__ float row16_allmax(float x) {
    x = fmaxf(x, dpp<0x128>(x));
    x = fmaxf(x, dpp<0x124>(x));
    x = fmaxf(x, dpp<0x122>(x));
    x = fmaxf(x, dpp<0x121>(x));
    return x;
}
__device__ __forceinline__ float xrow_max(float x) {
    auto s = __builtin_amdgcn_permlane16_swap(__float_as_uint(x), __float_as_uint(x), false, false);
    x = fmaxf(__uint_as_float(s[0]), __uint_as_float(s[1]));
    auto t = __builtin_amdgcn_permlane32_swap(__float_as_uint(x), __float_as_uint(x), false, false);
    return fmaxf(__uint_as_float(t[0]), __uint_as_float(t[1]));
}
__device__ __forceinline__ float xrow_sum(float x) {
    auto s = __builtin_amdgcn_permlane16_swap(__float_as_uint(x), __float_as_uint(x), false, false);
    x = __uint_as_float(s[0]) + __uint_as_float(s[1]);
    auto t = __builtin_amdgcn_permlane32_swap(__float_as_uint(x), __float_as_uint(x), false, false);
    return __uint_as_float(t[0]) + __uint_as_float(t[1]);
}
__device__ __forceinline__ float swap32_add(float a, float b) {
    auto t = __builtin_amdgcn_permlane32_swap(__float_as_uint(a), __float_as_uint(b), false, false);
    return __uint_as_float(t[0]) + __uint_as_float(t[1]);
}
__device__ __forceinline__ float swap16_add(float a, float b) {
    auto t = __builtin_amdgcn_permlane16_swap(__float_as_uint(a), __float_as_uint(b), false, false);
    return __uint_as_float(t[0]) + __uint_as_float(t[1]);
}
__device__ __forceinline__ int row16_sum(int x) {
    x += __builtin_amdgcn_mov_dpp(x, 0x128, 0xf, 0xf, true);
    x += __builtin_amdgcn_mov_dpp(x, 0x124, 0xf, 0xf, true);
    x += __builtin_amdgcn_mov_dpp(x, 0x122, 0xf, 0xf, true);
    x += __builtin_amdgcn_mov_dpp(x, 0x121, 0xf, 0xf, true);
    return x;
}
__device__ __forceinline__ unsigned now32() { return (unsigned)__builtin_amdgcn_s_memrealtime(); }

#define PIN() do { asm volatile("" ::: "memory"); __builtin_amdgcn_sched_barrier(0); } while (0)
#define BARRIER() do { asm volatile("s_waitcnt lgkmcnt(0)" ::: "memory"); __builtin_amdgcn_s_barrier(); PIN(); } while (0)
#define VMCNT(n) asm volatile("s_waitcnt vmcnt(%0)" :: "i"(n) : "memory")

typedef volatile __attribute__((address_space(3))) unsigned vlds_u32;
typedef volatile __attribute__((address_space(3))) u32x2 vlds_u32x2;
typedef volatile __attribute__((address_space(3))) u32x4 vlds_u32x4;

struct Pace {
    unsigned step;
    int start;
    int offset;
    int bias;
    int rd;
    int pub;
};

template <int X, int PRO_, int GATE, int STAG_, int SLP>
__global__ __launch_bounds__(576) void kv_sweep_kernel(
    const unsigned short* q, const int* qk, const unsigned short* ksc,
    const unsigned short* kzp, const int* qv, const unsigned short* vsc,
    const unsigned short* vzp, const unsigned short* q_cos, const unsigned short* q_sin,
    const unsigned short* k_cos, const unsigned short* k_sin, unsigned char* ws, bf16* out, Pace pace)
{
    __shared__ __attribute__((aligned(16))) unsigned s_q[NH * DH];
    __shared__ __attribute__((aligned(16))) float s_sc[NH * 64 * 4];
    __shared__ __attribute__((aligned(16))) float s_part[8 * NI * DH];
    __shared__ __attribute__((aligned(16))) float s_red[8 * 28];
    __shared__ __attribute__((aligned(16))) float s_f[NH * 8];
    __shared__ __attribute__((aligned(16))) unsigned s_done[8];
    __shared__ __attribute__((aligned(16))) unsigned s_sync[8];
    __shared__ __attribute__((aligned(16))) unsigned s_act[16];

    constexpr bool TRABS = true;
    constexpr int ORD = GATE % 10, WID = GATE / 10;
    constexpr int PRO = PRO_ % 10, PF = PRO_ / 10, STAG = STAG_ % 10, LAY = STAG_ / 10;
    constexpr unsigned PSTR = LAY == 0 ? 64 : LAY == 1 ? UNITB + 256 : LAY == 2 ? UNITB + 128 : UNITB;
    constexpr unsigned USTR = LAY == 0 ? UNITB : 64;
    unsigned long long pc0;
    asm volatile("s_getpc_b64 %0" : "=s"(pc0));
    const unsigned tstart = now32();
    const int tid = threadIdx.x;
    const int lane = tid & 63;
    const int wave = __builtin_amdgcn_readfirstlane(tid >> 6);
    const int r = lane >> 4;
    const int c = lane & 15;
    const int bid = blockIdx.x;
    const int m = (bid & 7) * 32 + (bid >> 3);
    const auto rw_ = __builtin_amdgcn_make_buffer_rsrc((void*)ws, 0, WS_BYTES, 0x00020000);

    auto merge = [&](int i, int hp, int c6, unsigned tag) {
        const f32x4 f = *reinterpret_cast<const f32x4*>(s_f + (2 * i + hp) * 8);
        const f32x2 zl = *reinterpret_cast<const f32x2*>(s_f + (2 * i + hp) * 8 + 4);
        const float zs = zl[0];
        f32x2 a[3];
#pragma unroll
        for (int k = 0; k < 3; ++k) a[k] = f32x2{-zs, -zs};
#pragma unroll
        for (int w = 0; w < 4; ++w) {
            const float* p = s_part + ((hp * 4 + w) * NI + i) * DH + c6 * 6;
#pragma unroll
            for (int k = 0; k < 3; ++k) a[k] += f[w] * *reinterpret_cast<const f32x2*>(p + 2 * k);
        }
        return u32x4{pk_bf16(a[0]), pk_bf16(a[1]), pk_bf16(a[2]), tag | ((__float_as_uint(zl[1]) >> (16 * (c6 & 1))) & 0xffffu)};
    };
    auto publish = [&](u32x4 v, int i, int hp, int c6, bool on) {
        __builtin_amdgcn_raw_buffer_store_b128(v, rw_, on ? (hp * 8 + (c6 >> 2)) * USTR + (c6 & 3) * 16 : OOB, WS_PART + i * 16 * USTR + m * PSTR, 16);
        asm volatile("s_nop 1" :: "v"(v) : "memory");
    };

    if (wave == 8) {
        unsigned long long pc1;
        asm volatile("s_getpc_b64 %0" : "=s"(pc1));
        pc0 &= ~255ull;
        const auto rcode = __builtin_amdgcn_make_buffer_rsrc((void*)pc0, 0, (unsigned)(pc1 - pc0), 0x00020000);
        auto warm_code = [&]() {
            unsigned x[2];
#pragma unroll
            for (int k = 0; k < 2; ++k) x[k] = __builtin_amdgcn_raw_buffer_load_b32(rcode, lane * 128 + k * 8192, 0, 0);
            asm volatile("s_waitcnt vmcnt(0)" :: "v"(x[0]), "v"(x[1]) : "memory");
        };
        if (PF) warm_code();
        const unsigned tag = ((unsigned)__builtin_amdgcn_raw_buffer_load_b16(rw_, (lane & 3) * 2, WS_EPOCH, 16) + 1u) << 16;
        unsigned origin = (tstart << 8) + pace.offset;
        if (lane < 8) { s_done[lane] = 0; s_sync[lane] = lane == 2 ? origin : lane < 4 ? 0 : tag; }
        PIN();
        BARRIER();
        const int mh = lane >> 5, mc = lane & 31;
        const unsigned rtag = __builtin_amdgcn_readlane(tag, 0) | __builtin_amdgcn_readlane(tag, 1) >> 16;
        constexpr int RSH = X / 10, RLAG = X % 10;
        const int rid = ((bid >> 3) & 15) == 5 ? (bid >> 7) * 8 + (bid & 7) : -1;
        unsigned round = now32() >> RSH;
        const unsigned round0 = round;
        bool warmed = false;
        int i = 0;
        while (i < NI - 1) {
            const unsigned now = now32();
            if (RSH && (now >> RSH) != round && i < NI - 2) {
                round = now >> RSH;
                const u32x2 a = *(vlds_u32x2*)(s_act + 2 * (lane & 7));
                const int late = max((int)(a[0] - origin), (int)((now << 8) - a[1] - origin));
                const unsigned act = origin + (__builtin_amdgcn_readfirstlane(row16_sum(late)) >> 4);
                if (rid >= 0 && (now & ((1u << RSH) - 1)) < 32)
                    __builtin_amdgcn_raw_buffer_store_b64(u32x2{act, rtag}, rw_, lane == 0 ? rid * 8 : OOB, WS_REP + (round & 255) * 256, 16);
                if (round - round0 > RLAG) {
                    const u32x2 x = __builtin_amdgcn_raw_buffer_load_b64(rw_, c * 8, WS_REP + ((round - RLAG) & 255) * 256, 16);
                    const int d = (int)(x[0] - origin);
                    if (__builtin_amdgcn_ballot_w64(x[1] != rtag || d > (32 << 16) || d < -(32 << 16)) == 0) {
                        origin += (__builtin_amdgcn_readfirstlane(row16_sum(d)) >> 4) - pace.bias;
                        if (lane == 0) *(vlds_u32*)(s_sync + 2) = origin;
                    }
                }
                PIN();
                continue;
            }
            if (PF >= 2 && !warmed && (int)((now << 8) - origin) > 60 * (int)pace.step) { warm_code(); warmed = true; continue; }
            const u32x4 a = *(vlds_u32x4*)s_done, b = *(vlds_u32x4*)(s_done + 4);
            const unsigned mn = min(min(min(a[0], a[1]), min(a[2], a[3])), min(min(b[0], b[1]), min(b[2], b[3])));
            if (__builtin_amdgcn_readfirstlane(mn) <= (unsigned)i) { __builtin_amdgcn_s_sleep(2); continue; }
            PIN();
            if (i == 0) {
                f32x4 ml[4], f;
#pragma unroll
                for (int w = 0; w < 4; ++w) ml[w] = *reinterpret_cast<const f32x4*>(s_sc + (mc * 4 + w) * 64 + 16);
                const float M = fmaxf(fmaxf(ml[0][0], ml[1][0]), fmaxf(ml[2][0], ml[3][0]));
                float L = 0.f, zs = 0.f;
#pragma unroll
                for (int w = 0; w < 4; ++w) { f[w] = __builtin_amdgcn_exp2f(ml[w][0] - M); L += f[w] * ml[w][1]; zs += f[w] * ml[w][2]; }
                const float inv = 1.f / L;
                *reinterpret_cast<f32x4*>(s_f + mc * 8) = f * inv;
                *reinterpret_cast<f32x2*>(s_f + mc * 8 + 4) = f32x2{zs * inv, M + __builtin_amdgcn_logf(L)};
                PIN();
                if (lane == 0) *(vlds_u32*)(s_sync + 3) = 1;
                PIN();
            }
            if (PF >= 2 && i == NI - 3) warm_code();
            const u32x4 v = merge(i, mh, mc, tag);
            PIN();
            if (pace.pub < 0) publish(v, i, mh, mc, true);
            else *reinterpret_cast<u32x4*>(s_part + (mh * 4 * NI + i) * DH + mc * 4) = v;
            PIN();
            ++i;
        }
        u32x4 v[NI - 1];
#pragma unroll
        for (int i = 0; i < NI - 1; ++i) { v[i] = *reinterpret_cast<const u32x4*>(s_part + (mh * 4 * NI + i) * DH + mc * 4); asm volatile("" : "+v"(v[i])); }
        while ((int)((now32() << 8) - origin) < pace.pub) __builtin_amdgcn_s_sleep(1);
        PIN();
#pragma unroll
        for (int i = 0; i < NI - 1; ++i) publish(v[i], i, mh, mc, pace.pub >= 0);
        return;
    }

    const int rw = wave & 3, hp = wave >> 2;
    const int row0 = m * 64 + rw * 16;

    const unsigned sidx = STAG == 0 ? hp * 1024 + m * 4 + rw : STAG == 1 ? m * 8 + wave : (hp * 1024 + (m & 31) * 32 + (m >> 5) * 4 + rw);
    unsigned off = pace.start + ((pace.step * sidx) >> 11);
    unsigned origin = tstart << 8, kept = 0;
    bool wide = false;
    const unsigned step = pace.step;
    auto pace_wait = [&]() {
        const unsigned tgt = __builtin_amdgcn_readfirstlane(origin) + off;
        unsigned now = now32() << 8;
        kept = ((int)(now - tgt) > 0 ? now : tgt) - off;
        wide = (int)(now - tgt) > 30 * 256;
        while ((int)(tgt - now) > 0) { if (SLP) __builtin_amdgcn_s_sleep(SLP); now = now32() << 8; }
        off += step;
    };
    auto report = [&]() {
        if (lane == 0) *(vlds_u32x2*)(s_act + 2 * wave) = u32x2{kept, off};
        origin = *(vlds_u32*)(s_sync + 2);
        PIN();
    };

    const int qe = min(tid, 383);
    const int qh = qe / 12, qd = (qe % 12) * 8;
    u32x4 lq[2], lc[2], ls[2];
#pragma unroll
    for (int k = 0; k < 2; ++k) {
        lq[k] = *reinterpret_cast<const u32x4*>(q + qh * DH + k * HALF + qd);
        lc[k] = *reinterpret_cast<const u32x4*>(q_cos + k * HALF + qd);
        ls[k] = *reinterpret_cast<const u32x4*>(q_sin + k * HALF + qd);
    }
    u32x2 lsc[4];
#pragma unroll
    for (int a = 0; a < 4; ++a) {
        const unsigned short* base = a == 0 ? ksc : a == 1 ? kzp : a == 2 ? vsc : vzp;
        lsc[a] = __builtin_nontemporal_load(reinterpret_cast<const u32x2*>(base + (size_t)(tid >> 4) * SKV + m * 64 + (tid & 15) * 4));
    }
    PIN();

    const auto rc = __builtin_amdgcn_make_buffer_rsrc((void*)k_cos, 0, CSB, 0x00020000);
    const auto rs = __builtin_amdgcn_make_buffer_rsrc((void*)k_sin, 0, CSB, 0x00020000);
    u32x2 raw[2][6];
    auto load_cs = [&](int t) {
        const unsigned co = r * 384 + c * 8;
        const unsigned so1 = r * 384 + (c < 8 ? c * 8 + 320 : c * 8 - 64);
        const unsigned rb = (row0 + (t + 2 * hp) * 4) * 384u;
        raw[t][0] = __builtin_amdgcn_raw_buffer_load_b64(rc, co, rb, 2);
        raw[t][1] = __builtin_amdgcn_raw_buffer_load_b64(rc, co + 128, rb, 2);
        raw[t][2] = __builtin_amdgcn_raw_buffer_load_b64(rc, co + 256, rb, 2);
        raw[t][3] = __builtin_amdgcn_raw_buffer_load_b64(rs, co + 192, rb, 2);
        raw[t][4] = __builtin_amdgcn_raw_buffer_load_b64(rs, so1, rb, 2);
        raw[t][5] = __builtin_amdgcn_raw_buffer_load_b64(rs, co + 64, rb, 2);
        PIN();
    };
    u32x4 tab[4][3];
    auto pack_cs = [&](int t) {
#pragma unroll
        for (int j = 0; j < 3; ++j) {
#pragma unroll
            for (int k = 0; k < 2; ++k) {
                tab[t][j][2 * k] = __builtin_amdgcn_perm(raw[t][3 + j][k], raw[t][j][k], 0x05040100);
                tab[t][j][2 * k + 1] = __builtin_amdgcn_perm(raw[t][3 + j][k], raw[t][j][k], 0x07060302);
            }
            *reinterpret_cast<u32x4*>(s_part + (wave ^ 4) * (NI * DH) + ((t * 3 + j) * 64 + lane) * 4) = tab[t][j];
        }
        PIN();
    };
    const auto rk = __builtin_amdgcn_make_buffer_rsrc((void*)qk, 0, KVB, 0x00020000);
    const auto rv = __builtin_amdgcn_make_buffer_rsrc((void*)qv, 0, KVB, 0x00020000);
    const unsigned voff = r * ROWB + c * 16;
    const unsigned woff = row0 * ROWB + hp * HEADB;
    auto toff = [&](int t) { return (unsigned)(t ^ (2 * hp)) * (4 * ROWB); };
    u32x4 ring[4][3];
    auto ld = [&](int slot, int j, __amdgpu_buffer_rsrc_t rr, unsigned off) {
        ring[slot][j] = __builtin_amdgcn_raw_buffer_load_b128(rr, voff + j * 256, off, 2);
        PIN();
    };
    auto ld_tile = [&](int slot, __amdgpu_buffer_rsrc_t rr, unsigned off) { ld(slot, 0, rr, off); ld(slot, 1, rr, off); ld(slot, 2, rr, off); };
    auto refill = [&](int slot, int j, __amdgpu_buffer_rsrc_t rr, unsigned goff, bool on) {
        if (ORD == 0) {
            if (on) { if (WID && wide) VMCNT(2 + WID); else VMCNT(2); if (j == 0) pace_wait(); }
            ld(slot, j, rr, goff);
        } else {
            if (on) { if (j == 0) pace_wait(); if (WID && wide) VMCNT(2 + WID); else VMCNT(2); }
            ld(slot, j, rr, goff);
            if (on && j == 0) { const unsigned t = (now32() << 8) - 1024 - (off - step); if ((int)(t - kept) > 0) kept = t; }
        }
        if (j == 2) report();
    };
    auto small_to_lds = [&]() {
        float o[2][8];
#pragma unroll
        for (int k = 0; k < 2; ++k)
#pragma unroll
            for (int u = 0; u < 4; ++u) {
                const unsigned pp = lq[k ^ 1][u] ^ (k == 0 ? 0x80008000u : 0u);
                o[k][2 * u] = rbf(rbf(bflo(lq[k][u]) * bflo(lc[k][u])) + rbf(bflo(pp) * bflo(ls[k][u])));
                o[k][2 * u + 1] = rbf(rbf(bfhi(lq[k][u]) * bfhi(lc[k][u])) + rbf(bfhi(pp) * bfhi(ls[k][u])));
            }
#pragma unroll
        for (int v = 0; v < 2; ++v) {
            u32x4 lo, hi;
#pragma unroll
            for (int e = 0; e < 4; ++e) {
                lo[e] = __builtin_amdgcn_perm(__float_as_uint(o[1][4 * v + e]), __float_as_uint(o[0][4 * v + e]), 0x07060302);
                hi[e] = __builtin_amdgcn_perm(__float_as_uint(o[0][4 * v + e]) ^ 0x80000000u, __float_as_uint(o[1][4 * v + e]), 0x07060302);
            }
            *reinterpret_cast<u32x4*>(s_q + qh * DH + qd + 4 * v) = lo;
            *reinterpret_cast<u32x4*>(s_q + qh * DH + HALF + qd + 4 * v) = hi;
        }
        float* dst = s_sc + tid * 16;
#pragma unroll
        for (int u = 0; u < 2; ++u) {
            *reinterpret_cast<f32x4*>(dst + 8 * u) = f32x4{bflo(lsc[0][u]) * SCALE_LOG2, bflo(lsc[1][u]), bflo(lsc[2][u]), bflo(lsc[3][u])};
            *reinterpret_cast<f32x4*>(dst + 8 * u + 4) = f32x4{bfhi(lsc[0][u]) * SCALE_LOG2, bfhi(lsc[1][u]), bfhi(lsc[2][u]), bfhi(lsc[3][u])};
        }
        PIN();
    };

    load_cs(0);
    pace_wait();
    load_cs(1);
    if (PRO == 0) small_to_lds();
    pace_wait(); ld_tile(0, rk, woff + toff(0));
    if (PRO == 0) pack_cs(0);
    if (PRO == 1) small_to_lds();
    pace_wait(); ld_tile(1, rk, woff + toff(1));
    if (PRO == 0) pack_cs(1);
    if (PRO == 1) pack_cs(0);
    if (PRO == 2) small_to_lds();
    if (PRO == 0) VMCNT(3);
    pace_wait(); ld_tile(2, rk, woff + toff(2));
    if (PRO == 1) pack_cs(1);
    if (PRO == 2) { pack_cs(0); pack_cs(1); }
    if (PRO == 0) VMCNT(3);
    if (PRO == 1) VMCNT(6);
    pace_wait(); ld_tile(3, rk, woff + toff(3));
    if (lane == 0) *(vlds_u32x2*)(s_act + 2 * wave) = u32x2{kept, off};
    BARRIER();
    origin = *(vlds_u32*)(s_sync + 2);

    u32x4 qp[3];
    auto load_q = [&](int h, int j) { qp[j] = *reinterpret_cast<const u32x4*>(s_q + h * DH + 64 * j + 4 * c); };
    load_q(hp, 0); load_q(hp, 1); load_q(hp, 2);
#pragma unroll
    for (int t = 0; t < 2; ++t)
#pragma unroll
        for (int j = 0; j < 3; ++j) tab[2 + t][j] = *reinterpret_cast<const u32x4*>(s_part + wave * (NI * DH) + ((t * 3 + j) * 64 + lane) * 4);

    for (int i = 0; i < NI; ++i) {
        const bool last = i + 1 == NI;
        const int h = 2 * i + hp;
        const unsigned hnext = (i + 1) * (2 * HEADB) + woff;
        const auto rn = __builtin_amdgcn_make_buffer_rsrc((void*)(last ? qv : qk), 0, KVB, 0x00020000);
        float* scp = s_sc + (h * 4 + rw) * 64;
        f32x4 sc[4];
#pragma unroll
        for (int t = 0; t < 4; ++t) sc[t] = *reinterpret_cast<const f32x4*>(scp + (4 * (t ^ (2 * hp)) + r) * 4);
        float s[4];
#pragma unroll
        for (int t = 0; t < 4; ++t) {
            f32x2 dot, wsum;
#pragma unroll
            for (int j = 0; j < 3; ++j) {
                f32x2 w01, w23, k01, k23;
                dot_cvt(qp[j], tab[t][j], ring[t][j], w01, w23, k01, k23);
                if (j == 0) { wsum = w01 + w23; dot = k01 * w01; } else { wsum += w01; wsum += w23; dot += k01 * w01; }
                dot += k23 * w23;
                asm volatile("" : "+v"(dot), "+v"(wsum));
                PIN();
                if (t == 3) load_q(last ? h : h + 2, j);
                if (t == 0 || t == 3) refill(t, j, rn, last ? woff + toff(t == 3 ? 1 : 0) : hnext + toff(t), true);
                else refill(t, j, rk, last ? OOB : hnext + toff(t), !last);
            }
            const float a = (dot[0] + dot[1]) - sc[t][1] * (wsum[0] + wsum[1]);
            s[t] = row16_allsum(a) * sc[t][0];
        }
        const float mx = xrow_max(fmaxf(fmaxf(s[0], s[1]), fmaxf(s[2], s[3])));
        float l = 0.f, z = 0.f;
#pragma unroll
        for (int t = 0; t < 4; ++t) {
            const float p = __builtin_amdgcn_exp2f(s[t] - mx);
            l += p;
            s[t] = p * sc[t][2];
            z += s[t] * sc[t][3];
        }
        l = xrow_sum(l);
        z = xrow_sum(z);
        if (c == 0) {
            *reinterpret_cast<f32x4*>(scp + r * 4) = f32x4{s[0], s[1], s[2], s[3]};
            *reinterpret_cast<f32x4*>(scp + 16) = f32x4{mx, l, z, 0.f};
        }
        PIN();
    }

    const float* pwp = s_sc + (hp * 4 + rw) * 64 + r * 4;
    f32x4 pw = *reinterpret_cast<const f32x4*>(pwp);
    for (int i = 0; i < NI; ++i) {
        const bool last = i + 1 == NI;
        const unsigned hoff = i * (2 * HEADB) + woff;
        f32x2 acc[3][2];
#pragma unroll
        for (int t = 0; t < 4; ++t) {
            const f32x2 p2 = {pw[t], pw[t]};
#pragma unroll
            for (int j = 0; j < 3; ++j) {
                const u32x4 vv = ring[(t & 1) * 3][j];
                const f32x2 v01 = {(float)(int)vv[0], (float)(int)vv[1]}, v23 = {(float)(int)vv[2], (float)(int)vv[3]};
                if (t == 0) { acc[j][0] = p2 * v01; acc[j][1] = p2 * v23; }
                else { acc[j][0] += p2 * v01; acc[j][1] += p2 * v23; }
                asm volatile("" : "+v"(acc[j][0]), "+v"(acc[j][1]));
                PIN();
                if (t == 3 && j == 2) pw = *reinterpret_cast<const f32x4*>(pwp + (last ? i : i + 1) * 512);
                if (t < 2) refill((t & 1) * 3, j, rv, hoff + toff(t + 2), true);
                else refill((t & 1) * 3, j, rv, last ? OOB : hoff + 2 * HEADB + toff(t - 2), !last);
            }
        }
        float* sl = s_part + (wave * NI + i) * DH;
#pragma unroll
        for (int j = 0; j < 3; ++j) {
            const float x = swap32_add(acc[j][0][0], acc[j][1][0]);
            const float y = swap32_add(acc[j][0][1], acc[j][1][1]);
            sl[64 * j + 4 * c + r] = swap16_add(x, y);
        }
        PIN();
        if (lane == 0) *(vlds_u32*)(s_done + wave) = i + 1;
        PIN();
    }
    unsigned old = 0;
    if (lane == 0) old = __hip_atomic_fetch_add(s_sync + hp, 1u, __ATOMIC_RELAXED, __HIP_MEMORY_SCOPE_WORKGROUP);
    PIN();
    const unsigned tag = s_sync[4 + (lane & 3)];
    if (__builtin_amdgcn_readfirstlane(old) == 3) {
        while (__builtin_amdgcn_readfirstlane(*(vlds_u32*)(s_sync + 3)) == 0) __builtin_amdgcn_s_sleep(1);
        PIN();
        publish(merge(NI - 1, hp, lane & 31, tag), NI - 1, hp, lane & 31, lane < 32);
        PIN();
    }
    {
        const unsigned o = __builtin_amdgcn_readfirstlane(origin);
        while ((int)((now32() << 8) - o) < pace.rd) __builtin_amdgcn_s_sleep(1);
        PIN();
    }
    {
        u32x4 x, y;
        unsigned xo = (tid >> 2) * PSTR + (tid & 3) * 16, yo = xo + 128 * PSTR;
        for (;;) {
            const u32x4 xn = __builtin_amdgcn_raw_buffer_load_b128(rw_, xo, WS_PART + bid * USTR, 16);
            const u32x4 yn = __builtin_amdgcn_raw_buffer_load_b128(rw_, yo, WS_PART + bid * USTR, 16);
            unsigned bad = (xo != OOB && (xn[3] ^ tag) > 0xffffu ? 1u : 0u) | (yo != OOB && (yn[3] ^ tag) > 0xffffu ? 2u : 0u);
            bad |= __builtin_amdgcn_mov_dpp(bad, 0xb1, 0xf, 0xf, true);
            bad |= __builtin_amdgcn_mov_dpp(bad, 0x4e, 0xf, 0xf, true);
            const bool tx = xo != OOB, ty = yo != OOB;
#pragma unroll
            for (int k = 0; k < 4; ++k) { x[k] = tx ? xn[k] : x[k]; y[k] = ty ? yn[k] : y[k]; }
            xo = bad & 1 ? xo : OOB;
            yo = bad & 2 ? yo : OOB;
            if (__builtin_amdgcn_ballot_w64(bad != 0) == 0 || now32() - tstart > SPIN_LIMIT) break;
            PIN();
        }
        const unsigned sel = lane & 1 ? 0x01000504u : 0x05040100u;
        const float lx = __uint_as_float(__builtin_amdgcn_perm(__builtin_amdgcn_mov_dpp(x[3], 0xb1, 0xf, 0xf, true), x[3], sel));
        const float ly = __uint_as_float(__builtin_amdgcn_perm(__builtin_amdgcn_mov_dpp(y[3], 0xb1, 0xf, 0xf, true), y[3], sel));
        float M = fmaxf(lx, ly);
        M = fmaxf(M, dpp<0x128>(M));
        M = fmaxf(M, dpp<0x124>(M));
        M = xrow_max(M);
        const float w0 = __builtin_amdgcn_exp2f(lx - M), w1 = __builtin_amdgcn_exp2f(ly - M);
        float a[8];
#pragma unroll
        for (int k = 0; k < 3; ++k) {
            a[2 * k] = w0 * bflo(x[k]) + w1 * bflo(y[k]);
            a[2 * k + 1] = w0 * bfhi(x[k]) + w1 * bfhi(y[k]);
        }
        a[6] = a[7] = w0 + w1;
        float b[4], d[2];
#pragma unroll
        for (int k = 0; k < 4; ++k) b[k] = swap32_add(a[k], a[4 + k]);
#pragma unroll
        for (int k = 0; k < 2; ++k) d[k] = swap16_add(b[k], b[2 + k]);
#pragma unroll
        for (int k = 0; k < 2; ++k) { d[k] += dpp<0x128>(d[k]); d[k] += dpp<0x124>(d[k]); }
        if (c < 4) *reinterpret_cast<f32x2*>(s_red + wave * 28 + (r < 3 ? c * 6 + r * 2 : 24)) = f32x2{d[0], r < 3 ? d[1] : M};
        BARRIER();
        if (tid < 24) {
            f32x2 sm[8];
            float ov[8];
#pragma unroll
            for (int w = 0; w < 8; ++w) { sm[w] = *reinterpret_cast<const f32x2*>(s_red + w * 28 + 24); ov[w] = s_red[w * 28 + tid]; }
            float MM = sm[0][1];
#pragma unroll
            for (int w = 1; w < 8; ++w) MM = fmaxf(MM, sm[w][1]);
            float S = 0.f, o = 0.f;
#pragma unroll
            for (int w = 0; w < 8; ++w) {
                const float ff = __builtin_amdgcn_exp2f(sm[w][1] - MM);
                S += ff * sm[w][0];
                o += ff * ov[w];
            }
            out[bid * 24 + tid] = __float2bfloat16(o * __builtin_amdgcn_rcpf(S));
        }
        if (bid == 0 && wave == 1) __builtin_amdgcn_raw_buffer_store_b16((unsigned short)(tag >> 16), rw_, lane < 4 ? lane * 2 : OOB, WS_EPOCH, 16);
    }
}

template <int X, int PRO, int GATE, int STAG>
static void launch_cfg(void* const* d_in, void* d_out, void* d_ws, hipStream_t stream, float t_us, float start_us, float off_us, float debt_us, float adj_pct, float pub_us) {
    Pace pace;
    pace.step = (unsigned)(t_us * 25600.f / 128.f);
    pace.start = (int)(start_us * 25600.f);
    pace.offset = (int)(off_us * 25600.f);
    pace.bias = (int)(debt_us * 25600.f);
    pace.rd = (int)((start_us + t_us * (129.f / 128.f) + adj_pct) * 25600.f);
    pace.pub = (int)((start_us + t_us * (129.f / 128.f) + pub_us) * 25600.f);
    kv_sweep_kernel<X, PRO, GATE, STAG, 1><<<NWG, 576, 0, stream>>>((const unsigned short*)d_in[0], (const int*)d_in[1], (const unsigned short*)d_in[2],
        (const unsigned short*)d_in[3], (const int*)d_in[4], (const unsigned short*)d_in[5], (const unsigned short*)d_in[6],
        (const unsigned short*)d_in[7], (const unsigned short*)d_in[8], (const unsigned short*)d_in[9], (const unsigned short*)d_in[10],
        (unsigned char*)d_ws, (bf16*)d_out, pace);
}

extern "C" void kernel_launch(void* const* d_in, const int* in_sizes, int n_in,
                              void* d_out, int out_size, void* d_ws, size_t ws_size,
                              hipStream_t stream) {
    launch_cfg<81, 20, 1, 0>(d_in, d_out, d_ws, stream, 102.5f, 0.5f, 1.0f, 0.05f, 2.0f, 0.5f);
}
```

```cpp
#define PIDX 99
#include <hip/hip_runtime.h>
#include <hip/hip_bf16.h>
#include <cstdint>
#include <type_traits>

using bf16 = __hip_bfloat16;
using u32x4 = __attribute__((ext_vector_type(4))) unsigned;
using u32x2 = __attribute__((ext_vector_type(2))) unsigned;
using f32x2 = __attribute__((ext_vector_type(2))) float;
using f32x4 = __attribute__((ext_vector_type(4))) float;
using bf16x2 = __attribute__((ext_vector_type(2))) __bf16;

constexpr int DH = 192;
constexpr int HALF = 96;
constexpr int NH = 32;
constexpr int SKV = 16384;
constexpr int NWG = 256;
constexpr int NI = 16;
constexpr unsigned ROWB = DH * 4;
constexpr unsigned HEADB = SKV * ROWB;
constexpr unsigned KVB = NH * HEADB;
constexpr unsigned CSB = SKV * DH * 2;
constexpr unsigned OOB = 0x80000000u;
constexpr float SCALE_LOG2 = 1.4426950408889634f * 0.07216878364870322f;
constexpr unsigned SPIN_LIMIT = 20000000u;

constexpr unsigned UNITB = NWG * 64;
constexpr unsigned WS_PART = 0;
constexpr unsigned WS_EPOCH = WS_PART + NWG * (UNITB + 1024);
constexpr unsigned WS_REP = WS_EPOCH + 256;
constexpr unsigned WS_BYTES = WS_REP + 256 * 256;

__device__ __forceinline__ float bflo(unsigned u) { return __uint_as_float(u << 16); }
__device__ __forceinline__ float bfhi(unsigned u) { return __uint_as_float(u & 0xffff0000u); }
__device__ __forceinline__ float rbf(float x) { return __bfloat162float(__float2bfloat16(x)); }
__device__ __forceinline__ unsigned pk_bf16(f32x2 a) { return __builtin_bit_cast(unsigned, __builtin_convertvector(a, bf16x2)); }
__device__ __forceinline__ void dot_cvt(const u32x4 q, const u32x4 t, const u32x4 ki, f32x2& w01, f32x2& w23, f32x2& k01, f32x2& k23) {
    float w0, w1, w2, w3;
    float k0 = __uint_as_float(ki[0]), k1 = __uint_as_float(ki[1]), k2 = __uint_as_float(ki[2]), k3 = __uint_as_float(ki[3]);
    asm("v_dot2_f32_bf16 %0, %8, %12, 0\n\tv_dot2_f32_bf16 %1, %9, %13, 0\n\tv_dot2_f32_bf16 %2, %10, %14, 0\n\tv_dot2_f32_bf16 %3, %11, %15, 0\n\t"
        "v_cvt_f32_i32 %4, %4\n\tv_cvt_f32_i32 %5, %5\n\tv_cvt_f32_i32 %6, %6\n\tv_cvt_f32_i32 %7, %7"
        : "=&v"(w0), "=&v"(w1), "=&v"(w2), "=&v"(w3), "+v"(k0), "+v"(k1), "+v"(k2), "+v"(k3)
        : "v"(q[0]), "v"(q[1]), "v"(q[2]), "v"(q[3]), "v"(t[0]), "v"(t[1]), "v"(t[2]), "v"(t[3]));
    w01 = f32x2{w0, w1}; w23 = f32x2{w2, w3}; k01 = f32x2{k0, k1}; k23 = f32x2{k2, k3};
}

template <int CTRL> __device__ __forceinline__ float dpp(float x) {
    return __builtin_bit_cast(float, __builtin_amdgcn_mov_dpp(__builtin_bit_cast(int, x), CTRL, 0xf, 0xf, true));
}
__device__ __forceinline__ float row16_allsum(float x) {
    x += dpp<0x128>(x);
    x += dpp<0x124>(x);
    x += dpp<0x122>(x);
    x += dpp<0x121>(x);
    return x;
}
__device__ __forceinline__ float row16_allmax(float x) {
    x = fmaxf(x, dpp<0x128>(x));
    x = fmaxf(x, dpp<0x124>(x));
    x = fmaxf(x, dpp<0x122>(x));
    x = fmaxf(x, dpp<0x121>(x));
    return x;
}
__device__ __forceinline__ float xrow_max(float x) {
    auto s = __builtin_amdgcn_permlane16_swap(__float_as_uint(x), __float_as_uint(x), false, false);
    x = fmaxf(__uint_as_float(s[0]), __uint_as_float(s[1]));
    auto t = __builtin_amdgcn_permlane32_swap(__float_as_uint(x), __float_as_uint(x), false, false);
    return fmaxf(__uint_as_float(t[0]), __uint_as_float(t[1]));
}
__device__ __forceinline__ float xrow_sum(float x) {
    auto s = __builtin_amdgcn_permlane16_swap(__float_as_uint(x), __float_as_uint(x), false, false);
    x = __uint_as_float(s[0]) + __uint_as_float(s[1]);
    auto t = __builtin_amdgcn_permlane32_swap(__float_as_uint(x), __float_as_uint(x), false, false);
    return __uint_as_float(t[0]) + __uint_as_float(t[1]);
}
__device__ __forceinline__ float swap32_add(float a, float b) {
    auto t = __builtin_amdgcn_permlane32_swap(__float_as_uint(a), __float_as_uint(b), false, false);
    return __uint_as_float(t[0]) + __uint_as_float(t[1]);
}
__device__ __forceinline__ float swap16_add(float a, float b) {
    auto t = __builtin_amdgcn_permlane16_swap(__float_as_uint(a), __float_as_uint(b), false, false);
    return __uint_as_float(t[0]) + __uint_as_float(t[1]);
}
__device__ __forceinline__ int row16_sum(int x) {
    x += __builtin_amdgcn_mov_dpp(x, 0x128, 0xf, 0xf, true);
    x += __builtin_amdgcn_mov_dpp(x, 0x124, 0xf, 0xf, true);
    x += __builtin_amdgcn_mov_dpp(x, 0x122, 0xf, 0xf, true);
    x += __builtin_amdgcn_mov_dpp(x, 0x121, 0xf, 0xf, true);
    return x;
}
__device__ __forceinline__ unsigned now32() { return (unsigned)__builtin_amdgcn_s_memrealtime(); }

#define NSTAMP 12
#define STAMP(i)
#define TRACE(i) do { if ((i) == PIDX) probe = (int)(__builtin_amdgcn_readfirstlane(origin) - (tstart << 8)) >> 8; } while (0)
#define PIN() do { asm volatile("" ::: "memory"); __builtin_amdgcn_sched_barrier(0); } while (0)
#define BARRIER() do { asm volatile("s_waitcnt lgkmcnt(0)" ::: "memory"); __builtin_amdgcn_s_barrier(); PIN(); } while (0)
#define VMCNT(n) asm volatile("s_waitcnt vmcnt(%0)" :: "i"(n) : "memory")

typedef volatile __attribute__((address_space(3))) unsigned vlds_u32;
typedef volatile __attribute__((address_space(3))) u32x2 vlds_u32x2;
typedef volatile __attribute__((address_space(3))) u32x4 vlds_u32x4;

struct Pace {
    unsigned step;
    int start;
    int offset;
    int bias;
    int rd;
    int pub;
};

template <int X, int PRO_, int GATE, int STAG_, int SLP>
__global__ __launch_bounds__(576) void kv_sweep_kernel(
    const unsigned short* q, const int* qk, const unsigned short* ksc,
    const unsigned short* kzp, const int* qv, const unsigned short* vsc,
    const unsigned short* vzp, const unsigned short* q_cos, const unsigned short* q_sin,
    const unsigned short* k_cos, const unsigned short* k_sin, unsigned char* ws, bf16* out, Pace pace)
{
    __shared__ __attribute__((aligned(16))) unsigned s_q[NH * DH];
    __shared__ __attribute__((aligned(16))) float s_sc[NH * 64 * 4];
    __shared__ __attribute__((aligned(16))) float s_part[8 * NI * DH];
    __shared__ __attribute__((aligned(16))) float s_red[8 * 28];
    __shared__ __attribute__((aligned(16))) float s_f[NH * 8];
    __shared__ __attribute__((aligned(16))) unsigned s_done[8];
    __shared__ __attribute__((aligned(16))) unsigned s_sync[8];
    __shared__ __attribute__((aligned(16))) unsigned s_act[16];

    constexpr bool TRABS = true;
    constexpr int ORD = GATE % 10, WID = GATE / 10;
    constexpr int PRO = PRO_ % 10, PF = PRO_ / 10, STAG = STAG_ % 10, LAY = STAG_ / 10;
    constexpr unsigned PSTR = LAY == 0 ? 64 : LAY == 1 ? UNITB + 256 : LAY == 2 ? UNITB + 128 : UNITB;
    constexpr unsigned USTR = LAY == 0 ? UNITB : 64;
    STAMP(0);
    unsigned long long pc0;
    asm volatile("s_getpc_b64 %0" : "=s"(pc0));
    const unsigned tstart = now32();
    const int tid = threadIdx.x;
    const int lane = tid & 63;
    const int wave = __builtin_amdgcn_readfirstlane(tid >> 6);
    const int r = lane >> 4;
    const int c = lane & 15;
    const int bid = blockIdx.x;
    const int m = (bid & 7) * 32 + (bid >> 3);
    const auto rw_ = __builtin_amdgcn_make_buffer_rsrc((void*)ws, 0, WS_BYTES, 0x00020000);

    auto merge = [&](int i, int hp, int c6, unsigned tag) {
        const f32x4 f = *reinterpret_cast<const f32x4*>(s_f + (2 * i + hp) * 8);
        const f32x2 zl = *reinterpret_cast<const f32x2*>(s_f + (2 * i + hp) * 8 + 4);
        const float zs = zl[0];
        f32x2 a[3];
#pragma unroll
        for (int k = 0; k < 3; ++k) a[k] = f32x2{-zs, -zs};
#pragma unroll
        for (int w = 0; w < 4; ++w) {
            const float* p = s_part + ((hp * 4 + w) * NI + i) * DH + c6 * 6;
#pragma unroll
            for (int k = 0; k < 3; ++k) a[k] += f[w] * *reinterpret_cast<const f32x2*>(p + 2 * k);
        }
        return u32x4{pk_bf16(a[0]), pk_bf16(a[1]), pk_bf16(a[2]), tag | ((__float_as_uint(zl[1]) >> (16 * (c6 & 1))) & 0xffffu)};
    };
    auto publish = [&](u32x4 v, int i, int hp, int c6, bool on) {
        __builtin_amdgcn_raw_buffer_store_b128(v, rw_, on ? (hp * 8 + (c6 >> 2)) * USTR + (c6 & 3) * 16 : OOB, WS_PART + i * 16 * USTR + m * PSTR, 16);
        asm volatile("s_nop 1" :: "v"(v) : "memory");
    };

    if (wave == 8) {
        unsigned long long pc1;
        asm volatile("s_getpc_b64 %0" : "=s"(pc1));
        pc0 &= ~255ull;
        const auto rcode = __builtin_amdgcn_make_buffer_rsrc((void*)pc0, 0, (unsigned)(pc1 - pc0), 0x00020000);
        auto warm_code = [&]() {
            unsigned x[2];
#pragma unroll
            for (int k = 0; k < 2; ++k) x[k] = __builtin_amdgcn_raw_buffer_load_b32(rcode, lane * 128 + k * 8192, 0, 0);
            asm volatile("s_waitcnt vmcnt(0)" :: "v"(x[0]), "v"(x[1]) : "memory");
        };
        if (PF) warm_code();
        const unsigned tag = ((unsigned)__builtin_amdgcn_raw_buffer_load_b16(rw_, (lane & 3) * 2, WS_EPOCH, 16) + 1u) << 16;
        unsigned origin = (tstart << 8) + pace.offset;
        if (lane < 8) { s_done[lane] = 0; s_sync[lane] = lane == 2 ? origin : lane < 4 ? 0 : tag; }
        PIN();
        BARRIER();
        const int mh = lane >> 5, mc = lane & 31;
        const unsigned rtag = __builtin_amdgcn_readlane(tag, 0) | __builtin_amdgcn_readlane(tag, 1) >> 16;
        constexpr int RSH = X / 10, RLAG = X % 10;
        const int rid = ((bid >> 3) & 15) == 5 ? (bid >> 7) * 8 + (bid & 7) : -1;
        unsigned round = now32() >> RSH;
        const unsigned round0 = round;
        bool warmed = false;
        int i = 0;
        while (i < NI - 1) {
            const unsigned now = now32();
            if (RSH && (now >> RSH) != round && i < NI - 2) {
                round = now >> RSH;
                const u32x2 a = *(vlds_u32x2*)(s_act + 2 * (lane & 7));
                const int late = max((int)(a[0] - origin), (int)((now << 8) - a[1] - origin));
                const unsigned act = origin + (__builtin_amdgcn_readfirstlane(row16_sum(late)) >> 4);
                if (rid >= 0 && (now & ((1u << RSH) - 1)) < 32)
                    __builtin_amdgcn_raw_buffer_store_b64(u32x2{act, rtag}, rw_, lane == 0 ? rid * 8 : OOB, WS_REP + (round & 255) * 256, 16);
                if (round - round0 > RLAG) {
                    const u32x2 x = __builtin_amdgcn_raw_buffer_load_b64(rw_, c * 8, WS_REP + ((round - RLAG) & 255) * 256, 16);
                    const int d = (int)(x[0] - origin);
                    if (__builtin_amdgcn_ballot_w64(x[1] != rtag || d > (32 << 16) || d < -(32 << 16)) == 0) {
                        origin += (__builtin_amdgcn_readfirstlane(row16_sum(d)) >> 4) - pace.bias;
                        if (lane == 0) *(vlds_u32*)(s_sync + 2) = origin;
                    }
                }
                PIN();
                continue;
            }
            if (PF >= 2 && !warmed && (int)((now << 8) - origin) > 60 * (int)pace.step) { warm_code(); warmed = true; continue; }
            const u32x4 a = *(vlds_u32x4*)s_done, b = *(vlds_u32x4*)(s_done + 4);
            const unsigned mn = min(min(min(a[0], a[1]), min(a[2], a[3])), min(min(b[0], b[1]), min(b[2], b[3])));
            if (__builtin_amdgcn_readfirstlane(mn) <= (unsigned)i) { __builtin_amdgcn_s_sleep(2); continue; }
            PIN();
            if (i == 0) {
                f32x4 ml[4], f;
#pragma unroll
                for (int w = 0; w < 4; ++w) ml[w] = *reinterpret_cast<const f32x4*>(s_sc + (mc * 4 + w) * 64 + 16);
                const float M = fmaxf(fmaxf(ml[0][0], ml[1][0]), fmaxf(ml[2][0], ml[3][0]));
                float L = 0.f, zs = 0.f;
#pragma unroll
                for (int w = 0; w < 4; ++w) { f[w] = __builtin_amdgcn_exp2f(ml[w][0] - M); L += f[w] * ml[w][1]; zs += f[w] * ml[w][2]; }
                const float inv = 1.f / L;
                *reinterpret_cast<f32x4*>(s_f + mc * 8) = f * inv;
                *reinterpret_cast<f32x2*>(s_f + mc * 8 + 4) = f32x2{zs * inv, M + __builtin_amdgcn_logf(L)};
                PIN();
                if (lane == 0) *(vlds_u32*)(s_sync + 3) = 1;
                PIN();
            }
            if (PF >= 2 && i == NI - 3) warm_code();
            const u32x4 v = merge(i, mh, mc, tag);
            PIN();
            if (pace.pub < 0) publish(v, i, mh, mc, true);
            else *reinterpret_cast<u32x4*>(s_part + (mh * 4 * NI + i) * DH + mc * 4) = v;
            PIN();
            ++i;
        }
        u32x4 v[NI - 1];
#pragma unroll
        for (int i = 0; i < NI - 1; ++i) { v[i] = *reinterpret_cast<const u32x4*>(s_part + (mh * 4 * NI + i) * DH + mc * 4); asm volatile("" : "+v"(v[i])); }
        STAMP(5);
        while ((int)((now32() << 8) - origin) < pace.pub) __builtin_amdgcn_s_sleep(1);
        PIN();
        STAMP(6);
#pragma unroll
        for (int i = 0; i < NI - 1; ++i) publish(v[i], i, mh, mc, pace.pub >= 0);
        STAMP(7);
        return;
    }

    const int rw = wave & 3, hp = wave >> 2;
    const int row0 = m * 64 + rw * 16;

    const unsigned sidx = STAG == 0 ? hp * 1024 + m * 4 + rw : STAG == 1 ? m * 8 + wave : (hp * 1024 + (m & 31) * 32 + (m >> 5) * 4 + rw);
    unsigned off = pace.start + ((pace.step * sidx) >> 11);
    unsigned origin = tstart << 8, kept = 0;
    bool wide = false;
    int probe = 0;
    const unsigned step = pace.step;
    auto pace_wait = [&]() {
        const unsigned tgt = __builtin_amdgcn_readfirstlane(origin) + off;
        unsigned now = now32() << 8;
        kept = ((int)(now - tgt) > 0 ? now : tgt) - off;
        wide = (int)(now - tgt) > 30 * 256;
        while ((int)(tgt - now) > 0) { if (SLP) __builtin_amdgcn_s_sleep(SLP); now = now32() << 8; }
        off += step;
    };
    auto report = [&]() {
        if (lane == 0) *(vlds_u32x2*)(s_act + 2 * wave) = u32x2{kept, off};
        origin = *(vlds_u32*)(s_sync + 2);
        PIN();
    };
#define TRV (TRABS ? kept - (tstart << 8) : kept - __builtin_amdgcn_readfirstlane(origin))

    const int qe = min(tid, 383);
    const int qh = qe / 12, qd = (qe % 12) * 8;
    u32x4 lq[2], lc[2], ls[2];
#pragma unroll
    for (int k = 0; k < 2; ++k) {
        lq[k] = *reinterpret_cast<const u32x4*>(q + qh * DH + k * HALF + qd);
        lc[k] = *reinterpret_cast<const u32x4*>(q_cos + k * HALF + qd);
        ls[k] = *reinterpret_cast<const u32x4*>(q_sin + k * HALF + qd);
    }
    u32x2 lsc[4];
#pragma unroll
    for (int a = 0; a < 4; ++a) {
        const unsigned short* base = a == 0 ? ksc : a == 1 ? kzp : a == 2 ? vsc : vzp;
        lsc[a] = __builtin_nontemporal_load(reinterpret_cast<const u32x2*>(base + (size_t)(tid >> 4) * SKV + m * 64 + (tid & 15) * 4));
    }
    PIN();

    const auto rc = __builtin_amdgcn_make_buffer_rsrc((void*)k_cos, 0, CSB, 0x00020000);
    const auto rs = __builtin_amdgcn_make_buffer_rsrc((void*)k_sin, 0, CSB, 0x00020000);
    u32x2 raw[2][6];
    auto load_cs = [&](int t) {
        const unsigned co = r * 384 + c * 8;
        const unsigned so1 = r * 384 + (c < 8 ? c * 8 + 320 : c * 8 - 64);
        const unsigned rb = (row0 + (t + 2 * hp) * 4) * 384u;
        raw[t][0] = __builtin_amdgcn_raw_buffer_load_b64(rc, co, rb, 2);
        raw[t][1] = __builtin_amdgcn_raw_buffer_load_b64(rc, co + 128, rb, 2);
        raw[t][2] = __builtin_amdgcn_raw_buffer_load_b64(rc, co + 256, rb, 2);
        raw[t][3] = __builtin_amdgcn_raw_buffer_load_b64(rs, co + 192, rb, 2);
        raw[t][4] = __builtin_amdgcn_raw_buffer_load_b64(rs, so1, rb, 2);
        raw[t][5] = __builtin_amdgcn_raw_buffer_load_b64(rs, co + 64, rb, 2);
        PIN();
    };
    u32x4 tab[4][3];
    auto pack_cs = [&](int t) {
#pragma unroll
        for (int j = 0; j < 3; ++j) {
#pragma unroll
            for (int k = 0; k < 2; ++k) {
                tab[t][j][2 * k] = __builtin_amdgcn_perm(raw[t][3 + j][k], raw[t][j][k], 0x05040100);
                tab[t][j][2 * k + 1] = __builtin_amdgcn_perm(raw[t][3 + j][k], raw[t][j][k], 0x07060302);
            }
            *reinterpret_cast<u32x4*>(s_part + (wave ^ 4) * (NI * DH) + ((t * 3 + j) * 64 + lane) * 4) = tab[t][j];
        }
        PIN();
    };
    const auto rk = __builtin_amdgcn_make_buffer_rsrc((void*)qk, 0, KVB, 0x00020000);
    const auto rv = __builtin_amdgcn_make_buffer_rsrc((void*)qv, 0, KVB, 0x00020000);
    const unsigned voff = r * ROWB + c * 16;
    const unsigned woff = row0 * ROWB + hp * HEADB;
    auto toff = [&](int t) { return (unsigned)(t ^ (2 * hp)) * (4 * ROWB); };
    u32x4 ring[4][3];
    auto ld = [&](int slot, int j, __amdgpu_buffer_rsrc_t rr, unsigned off) {
        ring[slot][j] = __builtin_amdgcn_raw_buffer_load_b128(rr, voff + j * 256, off, 2);
        PIN();
    };
    auto ld_tile = [&](int slot, __amdgpu_buffer_rsrc_t rr, unsigned off) { ld(slot, 0, rr, off); ld(slot, 1, rr, off); ld(slot, 2, rr, off); };
    auto refill = [&](int slot, int j, __amdgpu_buffer_rsrc_t rr, unsigned goff, bool on) {
        if (ORD == 0) {
            if (on) { if (WID && wide) VMCNT(2 + WID); else VMCNT(2); if (j == 0) pace_wait(); }
            ld(slot, j, rr, goff);
        } else {
            if (on) { if (j == 0) pace_wait(); if (WID && wide) VMCNT(2 + WID); else VMCNT(2); }
            ld(slot, j, rr, goff);
            if (on && j == 0) { const unsigned t = (now32() << 8) - 1024 - (off - step); if ((int)(t - kept) > 0) kept = t; }
        }
        if (j == 2) report();
    };
    auto small_to_lds = [&]() {
        float o[2][8];
#pragma unroll
        for (int k = 0; k < 2; ++k)
#pragma unroll
            for (int u = 0; u < 4; ++u) {
                const unsigned pp = lq[k ^ 1][u] ^ (k == 0 ? 0x80008000u : 0u);
                o[k][2 * u] = rbf(rbf(bflo(lq[k][u]) * bflo(lc[k][u])) + rbf(bflo(pp) * bflo(ls[k][u])));
                o[k][2 * u + 1] = rbf(rbf(bfhi(lq[k][u]) * bfhi(lc[k][u])) + rbf(bfhi(pp) * bfhi(ls[k][u])));
            }
#pragma unroll
        for (int v = 0; v < 2; ++v) {
            u32x4 lo, hi;
#pragma unroll
            for (int e = 0; e < 4; ++e) {
                lo[e] = __builtin_amdgcn_perm(__float_as_uint(o[1][4 * v + e]), __float_as_uint(o[0][4 * v + e]), 0x07060302);
                hi[e] = __builtin_amdgcn_perm(__float_as_uint(o[0][4 * v + e]) ^ 0x80000000u, __float_as_uint(o[1][4 * v + e]), 0x07060302);
            }
            *reinterpret_cast<u32x4*>(s_q + qh * DH + qd + 4 * v) = lo;
            *reinterpret_cast<u32x4*>(s_q + qh * DH + HALF + qd + 4 * v) = hi;
        }
        float* dst = s_sc + tid * 16;
#pragma unroll
        for (int u = 0; u < 2; ++u) {
            *reinterpret_cast<f32x4*>(dst + 8 * u) = f32x4{bflo(lsc[0][u]) * SCALE_LOG2, bflo(lsc[1][u]), bflo(lsc[2][u]), bflo(lsc[3][u])};
            *reinterpret_cast<f32x4*>(dst + 8 * u + 4) = f32x4{bfhi(lsc[0][u]) * SCALE_LOG2, bfhi(lsc[1][u]), bfhi(lsc[2][u]), bfhi(lsc[3][u])};
        }
        PIN();
    };

    load_cs(0);
    pace_wait();
    load_cs(1);
    if (PRO == 0) small_to_lds();
    STAMP(1);
    pace_wait(); ld_tile(0, rk, woff + toff(0));
    if (PRO == 0) pack_cs(0);
    if (PRO == 1) small_to_lds();
    pace_wait(); ld_tile(1, rk, woff + toff(1));
    if (PRO == 0) pack_cs(1);
    if (PRO == 1) pack_cs(0);
    if (PRO == 2) small_to_lds();
    if (PRO == 0) VMCNT(3);
    pace_wait(); ld_tile(2, rk, woff + toff(2));
    if (PRO == 1) pack_cs(1);
    if (PRO == 2) { pack_cs(0); pack_cs(1); }
    STAMP(2);
    if (PRO == 0) VMCNT(3);
    if (PRO == 1) VMCNT(6);
    pace_wait(); ld_tile(3, rk, woff + toff(3));
    if (lane == 0) *(vlds_u32x2*)(s_act + 2 * wave) = u32x2{kept, off};
    BARRIER();
    origin = *(vlds_u32*)(s_sync + 2);
    STAMP(3);

    u32x4 qp[3];
    auto load_q = [&](int h, int j) { qp[j] = *reinterpret_cast<const u32x4*>(s_q + h * DH + 64 * j + 4 * c); };
    load_q(hp, 0); load_q(hp, 1); load_q(hp, 2);
#pragma unroll
    for (int t = 0; t < 2; ++t)
#pragma unroll
        for (int j = 0; j < 3; ++j) tab[2 + t][j] = *reinterpret_cast<const u32x4*>(s_part + wave * (NI * DH) + ((t * 3 + j) * 64 + lane) * 4);

    for (int i = 0; i < NI; ++i) {
        const bool last = i + 1 == NI;
        const int h = 2 * i + hp;
        const unsigned hnext = (i + 1) * (2 * HEADB) + woff;
        const auto rn = __builtin_amdgcn_make_buffer_rsrc((void*)(last ? qv : qk), 0, KVB, 0x00020000);
        float* scp = s_sc + (h * 4 + rw) * 64;
        f32x4 sc[4];
#pragma unroll
        for (int t = 0; t < 4; ++t) sc[t] = *reinterpret_cast<const f32x4*>(scp + (4 * (t ^ (2 * hp)) + r) * 4);
        float s[4];
#pragma unroll
        for (int t = 0; t < 4; ++t) {
            f32x2 dot, wsum;
#pragma unroll
            for (int j = 0; j < 3; ++j) {
                f32x2 w01, w23, k01, k23;
                dot_cvt(qp[j], tab[t][j], ring[t][j], w01, w23, k01, k23);
                if (j == 0) { wsum = w01 + w23; dot = k01 * w01; } else { wsum += w01; wsum += w23; dot += k01 * w01; }
                dot += k23 * w23;
                asm volatile("" : "+v"(dot), "+v"(wsum));
                PIN();
                if (t == 3) load_q(last ? h : h + 2, j);
                if (t == 0 || t == 3) refill(t, j, rn, last ? woff + toff(t == 3 ? 1 : 0) : hnext + toff(t), true);
                else refill(t, j, rk, last ? OOB : hnext + toff(t), !last);
                if (t == 0 && j == 0) TRACE(i);
            }
            const float a = (dot[0] + dot[1]) - sc[t][1] * (wsum[0] + wsum[1]);
            s[t] = row16_allsum(a) * sc[t][0];
        }
        const float mx = xrow_max(fmaxf(fmaxf(s[0], s[1]), fmaxf(s[2], s[3])));
        float l = 0.f, z = 0.f;
#pragma unroll
        for (int t = 0; t < 4; ++t) {
            const float p = __builtin_amdgcn_exp2f(s[t] - mx);
            l += p;
            s[t] = p * sc[t][2];
            z += s[t] * sc[t][3];
        }
        l = xrow_sum(l);
        z = xrow_sum(z);
        if (c == 0) {
            *reinterpret_cast<f32x4*>(scp + r * 4) = f32x4{s[0], s[1], s[2], s[3]};
            *reinterpret_cast<f32x4*>(scp + 16) = f32x4{mx, l, z, 0.f};
        }
        PIN();
    }
    STAMP(4);

    const float* pwp = s_sc + (hp * 4 + rw) * 64 + r * 4;
    f32x4 pw = *reinterpret_cast<const f32x4*>(pwp);
    for (int i = 0; i < NI; ++i) {
        const bool last = i + 1 == NI;
        const unsigned hoff = i * (2 * HEADB) + woff;
        f32x2 acc[3][2];
#pragma unroll
        for (int t = 0; t < 4; ++t) {
            const f32x2 p2 = {pw[t], pw[t]};
#pragma unroll
            for (int j = 0; j < 3; ++j) {
                const u32x4 vv = ring[(t & 1) * 3][j];
                const f32x2 v01 = {(float)(int)vv[0], (float)(int)vv[1]}, v23 = {(float)(int)vv[2], (float)(int)vv[3]};
                if (t == 0) { acc[j][0] = p2 * v01; acc[j][1] = p2 * v23; }
                else { acc[j][0] += p2 * v01; acc[j][1] += p2 * v23; }
                asm volatile("" : "+v"(acc[j][0]), "+v"(acc[j][1]));
                PIN();
                if (t == 3 && j == 2) pw = *reinterpret_cast<const f32x4*>(pwp + (last ? i : i + 1) * 512);
                if (t < 2) refill((t & 1) * 3, j, rv, hoff + toff(t + 2), true);
                else refill((t & 1) * 3, j, rv, last ? OOB : hoff + 2 * HEADB + toff(t - 2), !last);
                if (t == 0 && j == 0) TRACE(16 + i);
            }
        }
        float* sl = s_part + (wave * NI + i) * DH;
#pragma unroll
        for (int j = 0; j < 3; ++j) {
            const float x = swap32_add(acc[j][0][0], acc[j][1][0]);
            const float y = swap32_add(acc[j][0][1], acc[j][1][1]);
            sl[64 * j + 4 * c + r] = swap16_add(x, y);
        }
        PIN();
        if (lane == 0) *(vlds_u32*)(s_done + wave) = i + 1;
        PIN();
    }
    STAMP(6);
    unsigned old = 0;
    if (lane == 0) old = __hip_atomic_fetch_add(s_sync + hp, 1u, __ATOMIC_RELAXED, __HIP_MEMORY_SCOPE_WORKGROUP);
    PIN();
    const unsigned tag = s_sync[4 + (lane & 3)];
    if (__builtin_amdgcn_readfirstlane(old) == 3) {
        while (__builtin_amdgcn_readfirstlane(*(vlds_u32*)(s_sync + 3)) == 0) __builtin_amdgcn_s_sleep(1);
        PIN();
        publish(merge(NI - 1, hp, lane & 31, tag), NI - 1, hp, lane & 31, lane < 32);
        PIN();
    }
    STAMP(7);
    {
        const unsigned o = __builtin_amdgcn_readfirstlane(origin);
        while ((int)((now32() << 8) - o) < pace.rd) __builtin_amdgcn_s_sleep(1);
        PIN();
    }
    STAMP(8);
    {
        u32x4 x, y;
        unsigned xo = (tid >> 2) * PSTR + (tid & 3) * 16, yo = xo + 128 * PSTR;
        for (;;) {
            const u32x4 xn = __builtin_amdgcn_raw_buffer_load_b128(rw_, xo, WS_PART + bid * USTR, 16);
            const u32x4 yn = __builtin_amdgcn_raw_buffer_load_b128(rw_, yo, WS_PART + bid * USTR, 16);
            unsigned bad = (xo != OOB && (xn[3] ^ tag) > 0xffffu ? 1u : 0u) | (yo != OOB && (yn[3] ^ tag) > 0xffffu ? 2u : 0u);
            bad |= __builtin_amdgcn_mov_dpp(bad, 0xb1, 0xf, 0xf, true);
            bad |= __builtin_amdgcn_mov_dpp(bad, 0x4e, 0xf, 0xf, true);
            const bool tx = xo != OOB, ty = yo != OOB;
#pragma unroll
            for (int k = 0; k < 4; ++k) { x[k] = tx ? xn[k] : x[k]; y[k] = ty ? yn[k] : y[k]; }
            xo = bad & 1 ? xo : OOB;
            yo = bad & 2 ? yo : OOB;
            if (__builtin_amdgcn_ballot_w64(bad != 0) == 0 || now32() - tstart > SPIN_LIMIT) break;
            PIN();
        }
        STAMP(9);
        const unsigned sel = lane & 1 ? 0x01000504u : 0x05040100u;
        const float lx = __uint_as_float(__builtin_amdgcn_perm(__builtin_amdgcn_mov_dpp(x[3], 0xb1, 0xf, 0xf, true), x[3], sel));
        const float ly = __uint_as_float(__builtin_amdgcn_perm(__builtin_amdgcn_mov_dpp(y[3], 0xb1, 0xf, 0xf, true), y[3], sel));
        float M = fmaxf(lx, ly);
        M = fmaxf(M, dpp<0x128>(M));
        M = fmaxf(M, dpp<0x124>(M));
        M = xrow_max(M);
        const float w0 = __builtin_amdgcn_exp2f(lx - M), w1 = __builtin_amdgcn_exp2f(ly - M);
        float a[8];
#pragma unroll
        for (int k = 0; k < 3; ++k) {
            a[2 * k] = w0 * bflo(x[k]) + w1 * bflo(y[k]);
            a[2 * k + 1] = w0 * bfhi(x[k]) + w1 * bfhi(y[k]);
        }
        a[6] = a[7] = w0 + w1;
        float b[4], d[2];
#pragma unroll
        for (int k = 0; k < 4; ++k) b[k] = swap32_add(a[k], a[4 + k]);
#pragma unroll
        for (int k = 0; k < 2; ++k) d[k] = swap16_add(b[k], b[2 + k]);
#pragma unroll
        for (int k = 0; k < 2; ++k) { d[k] += dpp<0x128>(d[k]); d[k] += dpp<0x124>(d[k]); }
        if (c < 4) *reinterpret_cast<f32x2*>(s_red + wave * 28 + (r < 3 ? c * 6 + r * 2 : 24)) = f32x2{d[0], r < 3 ? d[1] : M};
        STAMP(10);
        BARRIER();
        if (tid < 24) {
            f32x2 sm[8];
            float ov[8];
#pragma unroll
            for (int w = 0; w < 8; ++w) { sm[w] = *reinterpret_cast<const f32x2*>(s_red + w * 28 + 24); ov[w] = s_red[w * 28 + tid]; }
            float MM = sm[0][1];
#pragma unroll
            for (int w = 1; w < 8; ++w) MM = fmaxf(MM, sm[w][1]);
            float S = 0.f, o = 0.f;
#pragma unroll
            for (int w = 0; w < 8; ++w) {
                const float ff = __builtin_amdgcn_exp2f(sm[w][1] - MM);
                S += ff * sm[w][0];
                o += ff * ov[w];
            }
            out[bid * 24 + tid] = __float2bfloat16(o * __builtin_amdgcn_rcpf(S));
        }
        if (bid == 0 && wave == 1) __builtin_amdgcn_raw_buffer_store_b16((unsigned short)(tag >> 16), rw_, lane < 4 ? lane * 2 : OOB, WS_EPOCH, 16);
    }
    if (PIDX == 100) probe = (int)(now32() - tstart) - 10500;
    if (bid == 100) { const unsigned endt = tstart + 15000 + 5 * max(0, min(probe, 3000)); while ((int)(endt - now32()) > 0) __builtin_amdgcn_s_sleep(1); }
}

template <int X, int PRO, int GATE, int STAG>
static void launch_cfg(void* const* d_in, void* d_out, void* d_ws, hipStream_t stream, float t_us, float start_us, float off_us, float debt_us, float adj_pct, float pub_us) {
    Pace pace;
    pace.step = (unsigned)(t_us * 25600.f / 128.f);
    pace.start = (int)(start_us * 25600.f);
    pace.offset = (int)(off_us * 25600.f);
    pace.bias = (int)(debt_us * 25600.f);
    pace.rd = (int)((start_us + t_us * (129.f / 128.f) + adj_pct) * 25600.f);
    pace.pub = (int)((start_us + t_us * (129.f / 128.f) + pub_us) * 25600.f);
    kv_sweep_kernel<X, PRO, GATE, STAG, 1><<<NWG, 576, 0, stream>>>((const unsigned short*)d_in[0], (const int*)d_in[1], (const unsigned short*)d_in[2],
        (const unsigned short*)d_in[3], (const int*)d_in[4], (const unsigned short*)d_in[5], (const unsigned short*)d_in[6],
        (const unsigned short*)d_in[7], (const unsigned short*)d_in[8], (const unsigned short*)d_in[9], (const unsigned short*)d_in[10],
        (unsigned char*)d_ws, (bf16*)d_out, pace);
}

#define CFG 81, 20, 1, 0
#define PRM 102.5f, 0.5f, 1.0f, 0.05f, 2.0f, 0.5f
extern "C" void kernel_launch(void* const* d_in, const int* in_sizes, int n_in,
                              void* d_out, int out_size, void* d_ws, size_t ws_size,
                              hipStream_t stream) {
    launch_cfg<CFG>(d_in, d_out, d_ws, stream, PRM);
}
```

```cpp
#include <hip/hip_runtime.h>
#include <hip/hip_bf16.h>
#include <cstdint>
#include <type_traits>

using bf16 = __hip_bfloat16;
using u32x4 = __attribute__((ext_vector_type(4))) unsigned;
using u32x2 = __attribute__((ext_vector_type(2))) unsigned;
using f32x2 = __attribute__((ext_vector_type(2))) float;
using f32x4 = __attribute__((ext_vector_type(4))) float;
using bf16x2 = __attribute__((ext_vector_type(2))) __bf16;

constexpr int DH = 192;
constexpr int HALF = 96;
constexpr int NH = 32;
constexpr int SKV = 16384;
constexpr int NWG = 256;
constexpr int NI = 16;
constexpr unsigned ROWB = DH * 4;
constexpr unsigned HEADB = SKV * ROWB;
constexpr unsigned KVB = NH * HEADB;
constexpr unsigned CSB = SKV * DH * 2;
constexpr unsigned OOB = 0x80000000u;
constexpr float SCALE_LOG2 = 1.4426950408889634f * 0.07216878364870322f;
constexpr unsigned SPIN_LIMIT = 20000000u;

constexpr unsigned UNITB = NWG * 64;
constexpr unsigned WS_PART = 0;
constexpr unsigned WS_EPOCH = WS_PART + NWG * (UNITB + 1024);
constexpr unsigned WS_REP = WS_EPOCH + 256;
constexpr unsigned WS_BYTES = WS_REP + 256 * 512;

__device__ __forceinline__ float bflo(unsigned u) { return __uint_as_float(u << 16); }
__device__ __forceinline__ float bfhi(unsigned u) { return __uint_as_float(u & 0xffff0000u); }
__device__ __forceinline__ float rbf(float x) { return __bfloat162float(__float2bfloat16(x)); }
__device__ __forceinline__ unsigned pk_bf16(f32x2 a) { return __builtin_bit_cast(unsigned, __builtin_convertvector(a, bf16x2)); }
__device__ __forceinline__ void dot_cvt(const u32x4 q, const u32x4 t, const u32x4 ki, f32x2& w01, f32x2& w23, f32x2& k01, f32x2& k23) {
    float w0, w1, w2, w3;
    float k0 = __uint_as_float(ki[0]), k1 = __uint_as_float(ki[1]), k2 = __uint_as_float(ki[2]), k3 = __uint_as_float(ki[3]);
    asm("v_dot2_f32_bf16 %0, %8, %12, 0\n\tv_dot2_f32_bf16 %1, %9, %13, 0\n\tv_dot2_f32_bf16 %2, %10, %14, 0\n\tv_dot2_f32_bf16 %3, %11, %15, 0\n\t"
        "v_cvt_f32_i32 %4, %4\n\tv_cvt_f32_i32 %5, %5\n\tv_cvt_f32_i32 %6, %6\n\tv_cvt_f32_i32 %7, %7"
        : "=&v"(w0), "=&v"(w1), "=&v"(w2), "=&v"(w3), "+v"(k0), "+v"(k1), "+v"(k2), "+v"(k3)
        : "v"(q[0]), "v"(q[1]), "v"(q[2]), "v"(q[3]), "v"(t[0]), "v"(t[1]), "v"(t[2]), "v"(t[3]));
    w01 = f32x2{w0, w1}; w23 = f32x2{w2, w3}; k01 = f32x2{k0, k1}; k23 = f32x2{k2, k3};
}

template <int CTRL> __device__ __forceinline__ float dpp(float x) {
    return __builtin_bit_cast(float, __builtin_amdgcn_mov_dpp(__builtin_bit_cast(int, x), CTRL, 0xf, 0xf, true));
}
__device__ __forceinline__ float row16_allsum(float x) {
    x += dpp<0x128>(x);
    x += dpp<0x124>(x);
    x += dpp<0x122>(x);
    x += dpp<0x121>(x);
    return x;
}
__device__ __forceinline__ float row16_allmax(float x) {
    x = fmaxf(x, dpp<0x128>(x));
    x = fmaxf(x, dpp<0x124>(x));
    x = fmaxf(x, dpp<0x122>(x));
    x = fmaxf(x, dpp<0x121>(x));
    return x;
}
__device__ __forceinline__ float xrow_max(float x) {
    auto s = __builtin_amdgcn_permlane16_swap(__float_as_uint(x), __float_as_uint(x), false, false);
    x = fmaxf(__uint_as_float(s[0]), __uint_as_float(s[1]));
    auto t = __builtin_amdgcn_permlane32_swap(__float_as_uint(x), __float_as_uint(x), false, false);
    return fmaxf(__uint_as_float(t[0]), __uint_as_float(t[1]));
}
__device__ __forceinline__ float xrow_sum(float x) {
    auto s = __builtin_amdgcn_permlane16_swap(__float_as_uint(x), __float_as_uint(x), false, false);
    x = __uint_as_float(s[0]) + __uint_as_float(s[1]);
    auto t = __builtin_amdgcn_permlane32_swap(__float_as_uint(x), __float_as_uint(x), false, false);
    return __uint_as_float(t[0]) + __uint_as_float(t[1]);
}
__device__ __forceinline__ float swap32_add(float a, float b) {
    auto t = __builtin_amdgcn_permlane32_swap(__float_as_uint(a), __float_as_uint(b), false, false);
    return __uint_as_float(t[0]) + __uint_as_float(t[1]);
}
__device__ __forceinline__ float swap16_add(float a, float b) {
    auto t = __builtin_amdgcn_permlane16_swap(__float_as_uint(a), __float_as_uint(b), false, false);
    return __uint_as_float(t[0]) + __uint_as_float(t[1]);
}
__device__ __forceinline__ int row16_sum(int x) {
    x += __builtin_amdgcn_mov_dpp(x, 0x128, 0xf, 0xf, true);
    x += __builtin_amdgcn_mov_dpp(x, 0x124, 0xf, 0xf, true);
    x += __builtin_amdgcn_mov_dpp(x, 0x122, 0xf, 0xf, true);
    x += __builtin_amdgcn_mov_dpp(x, 0x121, 0xf, 0xf, true);
    return x;
}
__device__ __forceinline__ unsigned now32() { return (unsigned)__builtin_amdgcn_s_memrealtime(); }

#define NSTAMP 12
#define STAMP(i)
#define TRACE(i)
#define PIN() do { asm volatile("" ::: "memory"); __builtin_amdgcn_sched_barrier(0); } while (0)
#define BARRIER() do { asm volatile("s_waitcnt lgkmcnt(0)" ::: "memory"); __builtin_amdgcn_s_barrier(); PIN(); } while (0)
#define VMCNT(n) asm volatile("s_waitcnt vmcnt(%0)" :: "i"(n) : "memory")

typedef volatile __attribute__((address_space(3))) unsigned vlds_u32;
typedef volatile __attribute__((address_space(3))) u32x2 vlds_u32x2;
typedef volatile __attribute__((address_space(3))) u32x4 vlds_u32x4;

struct Pace {
    unsigned step;
    int start;
    int offset;
    int bias;
    int rd;
    int pub;
};

template <int X, int PRO_, int GATE, int STAG_, int SLP>
__global__ __launch_bounds__(576) void kv_sweep_kernel(
    const unsigned short* q, const int* qk, const unsigned short* ksc,
    const unsigned short* kzp, const int* qv, const unsigned short* vsc,
    const unsigned short* vzp, const unsigned short* q_cos, const unsigned short* q_sin,
    const unsigned short* k_cos, const unsigned short* k_sin, unsigned char* ws, bf16* out, Pace pace)
{
    __shared__ __attribute__((aligned(16))) unsigned s_q[NH * DH];
    __shared__ __attribute__((aligned(16))) float s_sc[NH * 64 * 4];
    __shared__ __attribute__((aligned(16))) float s_part[8 * NI * DH];
    __shared__ __attribute__((aligned(16))) float s_red[8 * 28];
    __shared__ __attribute__((aligned(16))) float s_f[NH * 8];
    __shared__ __attribute__((aligned(16))) unsigned s_done[8];
    __shared__ __attribute__((aligned(16))) unsigned s_sync[8];
    __shared__ __attribute__((aligned(16))) unsigned s_act[16];

    constexpr bool TRABS = true;
    constexpr int ORD = GATE % 10, WID = GATE / 10;
    constexpr int PRO = PRO_ % 10, PF = PRO_ / 10, STAG = STAG_ % 10, LAY = 0, VS = STAG_ / 10 ? 2 : 3;
    constexpr unsigned PSTR = LAY == 0 ? 64 : LAY == 1 ? UNITB + 256 : LAY == 2 ? UNITB + 128 : UNITB;
    constexpr unsigned USTR = LAY == 0 ? UNITB : 64;
    STAMP(0);
    unsigned long long pc0;
    asm volatile("s_getpc_b64 %0" : "=s"(pc0));
    const unsigned tstart = now32();
    const int tid = threadIdx.x;
    const int lane = tid & 63;
    const int wave = __builtin_amdgcn_readfirstlane(tid >> 6);
    const int r = lane >> 4;
    const int c = lane & 15;
    const int bid = blockIdx.x;
    const int m = (bid & 7) * 32 + (bid >> 3);
    const auto rw_ = __builtin_amdgcn_make_buffer_rsrc((void*)ws, 0, WS_BYTES, 0x00020000);

    auto merge = [&](int i, int hp, int c6, unsigned tag) {
        const f32x4 f = *reinterpret_cast<const f32x4*>(s_f + (2 * i + hp) * 8);
        const f32x2 zl = *reinterpret_cast<const f32x2*>(s_f + (2 * i + hp) * 8 + 4);
        const float zs = zl[0];
        f32x2 a[3];
#pragma unroll
        for (int k = 0; k < 3; ++k) a[k] = f32x2{-zs, -zs};
#pragma unroll
        for (int w = 0; w < 4; ++w) {
            const float* p = s_part + ((hp * 4 + w) * NI + i) * DH + c6 * 6;
#pragma unroll
            for (int k = 0; k < 3; ++k) a[k] += f[w] * *reinterpret_cast<const f32x2*>(p + 2 * k);
        }
        return u32x4{pk_bf16(a[0]), pk_bf16(a[1]), pk_bf16(a[2]), tag | ((__float_as_uint(zl[1]) >> (16 * (c6 & 1))) & 0xffffu)};
    };
    auto publish = [&](u32x4 v, int i, int hp, int c6, bool on) {
        __builtin_amdgcn_raw_buffer_store_b128(v, rw_, on ? (hp * 8 + (c6 >> 2)) * USTR + (c6 & 3) * 16 : OOB, WS_PART + i * 16 * USTR + m * PSTR, 16);
        asm volatile("s_nop 1" :: "v"(v) : "memory");
    };

    if (wave == 8) {
        unsigned long long pc1;
        asm volatile("s_getpc_b64 %0" : "=s"(pc1));
        pc0 &= ~255ull;
        const auto rcode = __builtin_amdgcn_make_buffer_rsrc((void*)pc0, 0, (unsigned)(pc1 - pc0), 0x00020000);
        auto warm_code = [&]() {
            unsigned x[2];
#pragma unroll
            for (int k = 0; k < 2; ++k) x[k] = __builtin_amdgcn_raw_buffer_load_b32(rcode, lane * 128 + k * 8192, 0, 0);
            asm volatile("s_waitcnt vmcnt(0)" :: "v"(x[0]), "v"(x[1]) : "memory");
        };
        if (PF) warm_code();
        const unsigned tag = ((unsigned)__builtin_amdgcn_raw_buffer_load_b16(rw_, (lane & 3) * 2, WS_EPOCH, 16) + 1u) << 16;
        unsigned origin = (tstart << 8) + pace.offset;
        if (lane < 8) { s_done[lane] = 0; s_sync[lane] = lane == 2 ? origin : lane < 4 ? 0 : tag; }
        PIN();
        BARRIER();
        const int mh = lane >> 5, mc = lane & 31;
        const unsigned rtag = __builtin_amdgcn_readlane(tag, 0) | __builtin_amdgcn_readlane(tag, 1) >> 16;
        constexpr int RPX = 2;
        constexpr int RSH = X / 100;
        constexpr unsigned RDLY = 200;
        constexpr int UP = X / 10 % 10 ? X / 10 % 10 * 82 : 1 << 24, DN = X % 10 ? X % 10 * 82 : 1 << 24;
        const int rj = ((bid >> 3) + 5 * (bid & 7)) & 31;
        const int rid = rj % (32 / RPX) == 0 ? rj / (32 / RPX) * 8 + (bid & 7) : -1;
        unsigned wround = now32() >> RSH, rround = wround, tick = 0, target = origin;
        bool warmed = false;
        int i = 0;
        auto kept_now = [&](unsigned now) {
            const u32x2 a = *(vlds_u32x2*)(s_act + 2 * (lane & 7));
            const int late = max((int)(a[0] - origin), (int)((now << 8) - a[1] - origin));
            return origin + (__builtin_amdgcn_readfirstlane(row16_sum(late)) >> 4);
        };
        while (i < NI - 1) {
            const unsigned now = now32();
            if ((now >> RSH) != wround && i < NI - 2) {
                wround = now >> RSH;
                if (rid >= 0 && (now & ((1u << RSH) - 1)) < 48)
                    __builtin_amdgcn_raw_buffer_store_b64(u32x2{kept_now(now), rtag + wround}, rw_, lane == 0 ? rid * 8 : OOB, WS_REP + (wround & 255) * 512, 16);
                PIN();
                continue;
            }
            if ((int)(((now - RDLY) >> RSH) - rround) > 0 && i < NI - 2) {
                rround = (now - RDLY) >> RSH;
                const u32x2 x = __builtin_amdgcn_raw_buffer_load_b64(rw_, c * 8, WS_REP + (rround & 255) * 512, 16);
                const int d = (int)(x[0] - origin);
                const bool ok = x[1] == rtag + rround && d < (32 << 16) && d > -(32 << 16);
                const int n = __builtin_popcountll(__builtin_amdgcn_ballot_w64(ok));
                if (n >= 48) {
                    target = origin + __builtin_amdgcn_readfirstlane((int)(row16_allsum(ok ? (float)d : 0.f) * 4.f / (float)n)) - pace.bias;
                    const int lost = min((int)(kept_now(now32()) - origin) - pace.bias, (int)(target - origin));
                    if (lost > 0) origin += lost;
                }
                PIN();
                continue;
            }
            if ((now >> 5) != tick) {
                tick = now >> 5;
                origin += max(-DN, min(UP, (int)(target - origin)));
                if (lane == 0) *(vlds_u32*)(s_sync + 2) = origin;
                PIN();
            }
            if (PF >= 2 && !warmed && (int)((now << 8) - origin) > 60 * (int)pace.step) { warm_code(); warmed = true; continue; }
            const u32x4 a = *(vlds_u32x4*)s_done, b = *(vlds_u32x4*)(s_done + 4);
            const unsigned mn = min(min(min(a[0], a[1]), min(a[2], a[3])), min(min(b[0], b[1]), min(b[2], b[3])));
            if (__builtin_amdgcn_readfirstlane(mn) <= (unsigned)i) { __builtin_amdgcn_s_sleep(2); continue; }
            PIN();
            if (i == 0) {
                f32x4 ml[4], f;
#pragma unroll
                for (int w = 0; w < 4; ++w) ml[w] = *reinterpret_cast<const f32x4*>(s_sc + (mc * 4 + w) * 64 + 16);
                const float M = fmaxf(fmaxf(ml[0][0], ml[1][0]), fmaxf(ml[2][0], ml[3][0]));
                float L = 0.f, zs = 0.f;
#pragma unroll
                for (int w = 0; w < 4; ++w) { f[w] = __builtin_amdgcn_exp2f(ml[w][0] - M); L += f[w] * ml[w][1]; zs += f[w] * ml[w][2]; }
                const float inv = 1.f / L;
                *reinterpret_cast<f32x4*>(s_f + mc * 8) = f * inv;
                *reinterpret_cast<f32x2*>(s_f + mc * 8 + 4) = f32x2{zs * inv, M + __builtin_amdgcn_logf(L)};
                PIN();
                if (lane == 0) *(vlds_u32*)(s_sync + 3) = 1;
                PIN();
            }
            if (PF >= 2 && i == NI - 3) warm_code();
            const u32x4 v = merge(i, mh, mc, tag);
            PIN();
            if (pace.pub < 0) publish(v, i, mh, mc, true);
            else *reinterpret_cast<u32x4*>(s_part + (mh * 4 * NI + i) * DH + mc * 4) = v;
            PIN();
            ++i;
        }
        u32x4 v[NI - 1];
#pragma unroll
        for (int i = 0; i < NI - 1; ++i) { v[i] = *reinterpret_cast<const u32x4*>(s_part + (mh * 4 * NI + i) * DH + mc * 4); asm volatile("" : "+v"(v[i])); }
        STAMP(5);
        while ((int)((now32() << 8) - origin) < pace.pub) __builtin_amdgcn_s_sleep(1);
        PIN();
        STAMP(6);
#pragma unroll
        for (int i = 0; i < NI - 1; ++i) publish(v[i], i, mh, mc, pace.pub >= 0);
        STAMP(7);
        return;
    }

    const int rw = wave & 3, hp = wave >> 2;
    const int row0 = m * 64 + rw * 16;

    const unsigned sidx = STAG == 0 ? hp * 1024 + m * 4 + rw : STAG == 1 ? m * 8 + wave : (hp * 1024 + (m & 31) * 32 + (m >> 5) * 4 + rw);
    unsigned off = pace.start + ((pace.step * sidx) >> 11);
    unsigned origin = tstart << 8, kept = 0;
    bool wide = false;
    const unsigned step = pace.step;
    auto pace_wait = [&]() {
        const unsigned tgt = __builtin_amdgcn_readfirstlane(origin) + off;
        unsigned now = now32() << 8;
        kept = ((int)(now - tgt) > 0 ? now : tgt) - off;
        wide = (int)(now - tgt) > 30 * 256;
        while ((int)(tgt - now) > 0) { if (SLP) __builtin_amdgcn_s_sleep(SLP); now = now32() << 8; }
        off += step;
    };
    auto report = [&]() {
        if (lane == 0) *(vlds_u32x2*)(s_act + 2 * wave) = u32x2{kept, off};
        origin = *(vlds_u32*)(s_sync + 2);
        PIN();
    };
#define TRV (TRABS ? kept - (tstart << 8) : kept - __builtin_amdgcn_readfirstlane(origin))

    const int qe = min(tid, 383);
    const int qh = qe / 12, qd = (qe % 12) * 8;
    u32x4 lq[2], lc[2], ls[2];
    auto load_qs = [&]() {
#pragma unroll
        for (int k = 0; k < 2; ++k) {
            lq[k] = *reinterpret_cast<const u32x4*>(q + qh * DH + k * HALF + qd);
            lc[k] = *reinterpret_cast<const u32x4*>(q_cos + k * HALF + qd);
            ls[k] = *reinterpret_cast<const u32x4*>(q_sin + k * HALF + qd);
        }
        PIN();
    };
    u32x2 lsc[4];
    auto load_sc = [&]() {
#pragma unroll
        for (int a = 0; a < 4; ++a) {
            const unsigned short* base = a == 0 ? ksc : a == 1 ? kzp : a == 2 ? vsc : vzp;
            lsc[a] = __builtin_nontemporal_load(reinterpret_cast<const u32x2*>(base + (size_t)(tid >> 4) * SKV + m * 64 + (tid & 15) * 4));
        }
        PIN();
    };

    const auto rc = __builtin_amdgcn_make_buffer_rsrc((void*)k_cos, 0, CSB, 0x00020000);
    const auto rs = __builtin_amdgcn_make_buffer_rsrc((void*)k_sin, 0, CSB, 0x00020000);
    u32x2 raw[2][6];
    auto load_cs = [&](int t) {
        const unsigned co = r * 384 + c * 8;
        const unsigned so1 = r * 384 + (c < 8 ? c * 8 + 320 : c * 8 - 64);
        const unsigned rb = (row0 + (t + 2 * hp) * 4) * 384u;
        raw[t][0] = __builtin_amdgcn_raw_buffer_load_b64(rc, co, rb, 2);
        raw[t][1] = __builtin_amdgcn_raw_buffer_load_b64(rc, co + 128, rb, 2);
        raw[t][2] = __builtin_amdgcn_raw_buffer_load_b64(rc, co + 256, rb, 2);
        raw[t][3] = __builtin_amdgcn_raw_buffer_load_b64(rs, co + 192, rb, 2);
        raw[t][4] = __builtin_amdgcn_raw_buffer_load_b64(rs, so1, rb, 2);
        raw[t][5] = __builtin_amdgcn_raw_buffer_load_b64(rs, co + 64, rb, 2);
        PIN();
    };
    u32x4 tab[4][3];
    auto pack_cs = [&](int t) {
#pragma unroll
        for (int j = 0; j < 3; ++j) {
#pragma unroll
            for (int k = 0; k < 2; ++k) {
                tab[t][j][2 * k] = __builtin_amdgcn_perm(raw[t][3 + j][k], raw[t][j][k], 0x05040100);
                tab[t][j][2 * k + 1] = __builtin_amdgcn_perm(raw[t][3 + j][k], raw[t][j][k], 0x07060302);
            }
            *reinterpret_cast<u32x4*>(s_part + (wave ^ 4) * (NI * DH) + ((t * 3 + j) * 64 + lane) * 4) = tab[t][j];
        }
        PIN();
    };
    const auto rk = __builtin_amdgcn_make_buffer_rsrc((void*)qk, 0, KVB, 0x00020000);
    const auto rv = __builtin_amdgcn_make_buffer_rsrc((void*)qv, 0, KVB, 0x00020000);
    const unsigned voff = r * ROWB + c * 16;
    const unsigned woff = row0 * ROWB + hp * HEADB;
    auto toff = [&](int t) { return (unsigned)(t ^ (2 * hp)) * (4 * ROWB); };
    u32x4 ring[4][3];
    auto ld = [&](int slot, int j, __amdgpu_buffer_rsrc_t rr, unsigned off) {
        ring[slot][j] = __builtin_amdgcn_raw_buffer_load_b128(rr, voff + j * 256, off, 2);
        PIN();
    };
    auto ld_tile = [&](int slot, __amdgpu_buffer_rsrc_t rr, unsigned off) { ld(slot, 0, rr, off); ld(slot, 1, rr, off); ld(slot, 2, rr, off); };
    auto refill = [&](int slot, int j, __amdgpu_buffer_rsrc_t rr, unsigned goff, bool on) {
        if (ORD == 0) {
            if (on) { if (WID && wide) VMCNT(2 + WID); else VMCNT(2); if (j == 0) pace_wait(); }
            ld(slot, j, rr, goff);
        } else {
            if (on) { if (j == 0) pace_wait(); if (WID && wide) VMCNT(2 + WID); else VMCNT(2); }
            ld(slot, j, rr, goff);
            if (on && j == 0) { const unsigned t = (now32() << 8) - 1024 - (off - step); if ((int)(t - kept) > 0) kept = t; }
        }
        if (j == 2) report();
    };
    auto q_to_lds = [&]() {
        float o[2][8];
#pragma unroll
        for (int k = 0; k < 2; ++k)
#pragma unroll
            for (int u = 0; u < 4; ++u) {
                const unsigned pp = lq[k ^ 1][u] ^ (k == 0 ? 0x80008000u : 0u);
                o[k][2 * u] = rbf(rbf(bflo(lq[k][u]) * bflo(lc[k][u])) + rbf(bflo(pp) * bflo(ls[k][u])));
                o[k][2 * u + 1] = rbf(rbf(bfhi(lq[k][u]) * bfhi(lc[k][u])) + rbf(bfhi(pp) * bfhi(ls[k][u])));
            }
#pragma unroll
        for (int v = 0; v < 2; ++v) {
            u32x4 lo, hi;
#pragma unroll
            for (int e = 0; e < 4; ++e) {
                lo[e] = __builtin_amdgcn_perm(__float_as_uint(o[1][4 * v + e]), __float_as_uint(o[0][4 * v + e]), 0x07060302);
                hi[e] = __builtin_amdgcn_perm(__float_as_uint(o[0][4 * v + e]) ^ 0x80000000u, __float_as_uint(o[1][4 * v + e]), 0x07060302);
            }
            *reinterpret_cast<u32x4*>(s_q + qh * DH + qd + 4 * v) = lo;
            *reinterpret_cast<u32x4*>(s_q + qh * DH + HALF + qd + 4 * v) = hi;
        }
        PIN();
    };
    auto sc_to_lds = [&]() {
        float* dst = s_sc + tid * 16;
#pragma unroll
        for (int u = 0; u < 2; ++u) {
            *reinterpret_cast<f32x4*>(dst + 8 * u) = f32x4{bflo(lsc[0][u]) * SCALE_LOG2, bflo(lsc[1][u]), bflo(lsc[2][u]), bflo(lsc[3][u])};
            *reinterpret_cast<f32x4*>(dst + 8 * u + 4) = f32x4{bfhi(lsc[0][u]) * SCALE_LOG2, bfhi(lsc[1][u]), bfhi(lsc[2][u]), bfhi(lsc[3][u])};
        }
        PIN();
    };

    auto k0 = [&](int t) { pace_wait(); ld_tile(t, rk, woff + toff(t)); };
    if (PRO == 0) {
        load_qs(); load_sc(); load_cs(0);
        pace_wait(); load_cs(1);
        q_to_lds(); sc_to_lds();
        STAMP(1);
        k0(0); pack_cs(0);
        k0(1); pack_cs(1);
        VMCNT(3); k0(2);
        STAMP(2);
        VMCNT(3); k0(3);
    } else if (PRO == 1) {
        load_cs(0); load_sc();
        pace_wait(); load_cs(1); STAMP(1); if (wave < 6) load_qs();
        pack_cs(0); sc_to_lds();
        k0(0); pack_cs(1); if (wave < 6) q_to_lds();
        k0(1);
        VMCNT(3); k0(2);
        STAMP(2);
        VMCNT(3); k0(3);
    } else if (PRO == 2) {
        load_cs(0); load_sc();
        pace_wait(); load_cs(1);
        STAMP(1);
        pack_cs(0); sc_to_lds();
        k0(0); if (wave < 6) load_qs(); pack_cs(1);
        k0(1); if (wave < 6) q_to_lds();
        VMCNT(3); k0(2);
        STAMP(2);
        VMCNT(3); k0(3);
    } else if (PRO == 3) {
        load_cs(0); load_sc();
        pace_wait(); load_cs(1);
        STAMP(1);
        pack_cs(0); sc_to_lds();
        k0(0); pack_cs(1);
        k0(1); if (wave < 6) load_qs();
        VMCNT(9); k0(2); if (wave < 6) q_to_lds();
        STAMP(2);
        VMCNT(3); k0(3);
    }
    if (lane == 0) *(vlds_u32x2*)(s_act + 2 * wave) = u32x2{kept, off};
    BARRIER();
    origin = *(vlds_u32*)(s_sync + 2);
    STAMP(3);

    u32x4 qp[3];
    auto load_q = [&](int h, int j) { qp[j] = *reinterpret_cast<const u32x4*>(s_q + h * DH + 64 * j + 4 * c); };
    load_q(hp, 0); load_q(hp, 1); load_q(hp, 2);
#pragma unroll
    for (int t = 0; t < 2; ++t)
#pragma unroll
        for (int j = 0; j < 3; ++j) tab[2 + t][j] = *reinterpret_cast<const u32x4*>(s_part + wave * (NI * DH) + ((t * 3 + j) * 64 + lane) * 4);

    for (int i = 0; i < NI; ++i) {
        const bool last = i + 1 == NI;
        const int h = 2 * i + hp;
        const unsigned hnext = (i + 1) * (2 * HEADB) + woff;
        const auto rn = __builtin_amdgcn_make_buffer_rsrc((void*)(last ? qv : qk), 0, KVB, 0x00020000);
        float* scp = s_sc + (h * 4 + rw) * 64;
        f32x4 sc[4];
#pragma unroll
        for (int t = 0; t < 4; ++t) sc[t] = *reinterpret_cast<const f32x4*>(scp + (4 * (t ^ (2 * hp)) + r) * 4);
        float s[4];
#pragma unroll
        for (int t = 0; t < 4; ++t) {
            f32x2 dot, wsum;
#pragma unroll
            for (int j = 0; j < 3; ++j) {
                f32x2 w01, w23, k01, k23;
                dot_cvt(qp[j], tab[t][j], ring[t][j], w01, w23, k01, k23);
                if (j == 0) { wsum = w01 + w23; dot = k01 * w01; } else { wsum += w01; wsum += w23; dot += k01 * w01; }
                dot += k23 * w23;
                asm volatile("" : "+v"(dot), "+v"(wsum));
                PIN();
                if (t == 3) load_q(last ? h : h + 2, j);
                if (t == 0 || t == VS) refill(t, j, rn, last ? woff + toff(t == VS ? 1 : 0) : hnext + toff(t), true);
                else refill(t, j, rk, last ? OOB : hnext + toff(t), !last);
                if (t == 0 && j == 0) TRACE(i);
            }
            const float a = (dot[0] + dot[1]) - sc[t][1] * (wsum[0] + wsum[1]);
            s[t] = row16_allsum(a) * sc[t][0];
        }
        const float mx = xrow_max(fmaxf(fmaxf(s[0], s[1]), fmaxf(s[2], s[3])));
        float l = 0.f, z = 0.f;
#pragma unroll
        for (int t = 0; t < 4; ++t) {
            const float p = __builtin_amdgcn_exp2f(s[t] - mx);
            l += p;
            s[t] = p * sc[t][2];
            z += s[t] * sc[t][3];
        }
        l = xrow_sum(l);
        z = xrow_sum(z);
        if (c == 0) {
            *reinterpret_cast<f32x4*>(scp + r * 4) = f32x4{s[0], s[1], s[2], s[3]};
            *reinterpret_cast<f32x4*>(scp + 16) = f32x4{mx, l, z, 0.f};
        }
        PIN();
    }
    STAMP(4);

    const float* pwp = s_sc + (hp * 4 + rw) * 64 + r * 4;
    f32x4 pw = *reinterpret_cast<const f32x4*>(pwp);
    for (int i = 0; i < NI; ++i) {
        const bool last = i + 1 == NI;
        const unsigned hoff = i * (2 * HEADB) + woff;
        f32x2 acc[3][2];
#pragma unroll
        for (int t = 0; t < 4; ++t) {
            const f32x2 p2 = {pw[t], pw[t]};
#pragma unroll
            for (int j = 0; j < 3; ++j) {
                const u32x4 vv = ring[(t & 1) * VS][j];
                const f32x2 v01 = {(float)(int)vv[0], (float)(int)vv[1]}, v23 = {(float)(int)vv[2], (float)(int)vv[3]};
                if (t == 0) { acc[j][0] = p2 * v01; acc[j][1] = p2 * v23; }
                else { acc[j][0] += p2 * v01; acc[j][1] += p2 * v23; }
                asm volatile("" : "+v"(acc[j][0]), "+v"(acc[j][1]));
                PIN();
                if (t == 3 && j == 2) pw = *reinterpret_cast<const f32x4*>(pwp + (last ? i : i + 1) * 512);
                if (t < 2) refill((t & 1) * VS, j, rv, hoff + toff(t + 2), true);
                else refill((t & 1) * VS, j, rv, last ? OOB : hoff + 2 * HEADB + toff(t - 2), !last);
                if (t == 0 && j == 0) TRACE(16 + i);
            }
        }
        float* sl = s_part + (wave * NI + i) * DH;
#pragma unroll
        for (int j = 0; j < 3; ++j) {
            const float x = swap32_add(acc[j][0][0], acc[j][1][0]);
            const float y = swap32_add(acc[j][0][1], acc[j][1][1]);
            sl[64 * j + 4 * c + r] = swap16_add(x, y);
        }
        PIN();
        if (lane == 0) *(vlds_u32*)(s_done + wave) = i + 1;
        PIN();
    }
    STAMP(6);
    unsigned old = 0;
    if (lane == 0) old = __hip_atomic_fetch_add(s_sync + hp, 1u, __ATOMIC_RELAXED, __HIP_MEMORY_SCOPE_WORKGROUP);
    PIN();
    const unsigned tag = s_sync[4 + (lane & 3)];
    if (__builtin_amdgcn_readfirstlane(old) == 3) {
        while (__builtin_amdgcn_readfirstlane(*(vlds_u32*)(s_sync + 3)) == 0) __builtin_amdgcn_s_sleep(1);
        PIN();
        publish(merge(NI - 1, hp, lane & 31, tag), NI - 1, hp, lane & 31, lane < 32);
        PIN();
    }
    STAMP(7);
    {
        const unsigned o = __builtin_amdgcn_readfirstlane(origin);
        while ((int)((now32() << 8) - o) < pace.rd) __builtin_amdgcn_s_sleep(1);
        PIN();
    }
    STAMP(8);
    {
        u32x4 x, y;
        unsigned xo = (tid >> 2) * PSTR + (tid & 3) * 16, yo = xo + 128 * PSTR;
        for (;;) {
            const u32x4 xn = __builtin_amdgcn_raw_buffer_load_b128(rw_, xo, WS_PART + bid * USTR, 16);
            const u32x4 yn = __builtin_amdgcn_raw_buffer_load_b128(rw_, yo, WS_PART + bid * USTR, 16);
            unsigned bad = (xo != OOB && (xn[3] ^ tag) > 0xffffu ? 1u : 0u) | (yo != OOB && (yn[3] ^ tag) > 0xffffu ? 2u : 0u);
            bad |= __builtin_amdgcn_mov_dpp(bad, 0xb1, 0xf, 0xf, true);
            bad |= __builtin_amdgcn_mov_dpp(bad, 0x4e, 0xf, 0xf, true);
            const bool tx = xo != OOB, ty = yo != OOB;
#pragma unroll
            for (int k = 0; k < 4; ++k) { x[k] = tx ? xn[k] : x[k]; y[k] = ty ? yn[k] : y[k]; }
            xo = bad & 1 ? xo : OOB;
            yo = bad & 2 ? yo : OOB;
            if (__builtin_amdgcn_ballot_w64(bad != 0) == 0 || now32() - tstart > SPIN_LIMIT) break;
            PIN();
        }
        STAMP(9);
        const unsigned sel = lane & 1 ? 0x01000504u : 0x05040100u;
        const float lx = __uint_as_float(__builtin_amdgcn_perm(__builtin_amdgcn_mov_dpp(x[3], 0xb1, 0xf, 0xf, true), x[3], sel));
        const float ly = __uint_as_float(__builtin_amdgcn_perm(__builtin_amdgcn_mov_dpp(y[3], 0xb1, 0xf, 0xf, true), y[3], sel));
        float M = fmaxf(lx, ly);
        M = fmaxf(M, dpp<0x128>(M));
        M = fmaxf(M, dpp<0x124>(M));
        M = xrow_max(M);
        const float w0 = __builtin_amdgcn_exp2f(lx - M), w1 = __builtin_amdgcn_exp2f(ly - M);
        float a[8];
#pragma unroll
        for (int k = 0; k < 3; ++k) {
            a[2 * k] = w0 * bflo(x[k]) + w1 * bflo(y[k]);
            a[2 * k + 1] = w0 * bfhi(x[k]) + w1 * bfhi(y[k]);
        }
        a[6] = a[7] = w0 + w1;
        float b[4], d[2];
#pragma unroll
        for (int k = 0; k < 4; ++k) b[k] = swap32_add(a[k], a[4 + k]);
#pragma unroll
        for (int k = 0; k < 2; ++k) d[k] = swap16_add(b[k], b[2 + k]);
#pragma unroll
        for (int k = 0; k < 2; ++k) { d[k] += dpp<0x128>(d[k]); d[k] += dpp<0x124>(d[k]); }
        if (c < 4) *reinterpret_cast<f32x2*>(s_red + wave * 28 + (r < 3 ? c * 6 + r * 2 : 24)) = f32x2{d[0], r < 3 ? d[1] : M};
        STAMP(10);
        BARRIER();
        if (tid < 24) {
            f32x2 sm[8];
            float ov[8];
#pragma unroll
            for (int w = 0; w < 8; ++w) { sm[w] = *reinterpret_cast<const f32x2*>(s_red + w * 28 + 24); ov[w] = s_red[w * 28 + tid]; }
            float MM = sm[0][1];
#pragma unroll
            for (int w = 1; w < 8; ++w) MM = fmaxf(MM, sm[w][1]);
            float S = 0.f, o = 0.f;
#pragma unroll
            for (int w = 0; w < 8; ++w) {
                const float ff = __builtin_amdgcn_exp2f(sm[w][1] - MM);
                S += ff * sm[w][0];
                o += ff * ov[w];
            }
            out[bid * 24 + tid] = __float2bfloat16(o * __builtin_amdgcn_rcpf(S));
        }
        if (bid == 0 && wave == 1) __builtin_amdgcn_raw_buffer_store_b16((unsigned short)(tag >> 16), rw_, lane < 4 ? lane * 2 : OOB, WS_EPOCH, 16);
    }
}

template <int X, int PRO, int GATE, int STAG>
static void launch_cfg(void* const* d_in, void* d_out, void* d_ws, hipStream_t stream, float t_us, float start_us, float off_us, float debt_us, float adj_pct, float pub_us) {
    Pace pace;
    pace.step = (unsigned)(t_us * 25600.f / 128.f);
    pace.start = (int)(start_us * 25600.f);
    pace.offset = (int)(off_us * 25600.f);
    pace.bias = (int)(debt_us * 25600.f);
    pace.rd = (int)((start_us + t_us * (129.f / 128.f) + adj_pct) * 25600.f);
    pace.pub = (int)((start_us + t_us * (129.f / 128.f) + pub_us) * 25600.f);
    kv_sweep_kernel<X, PRO, GATE, STAG, 1><<<NWG, 576, 0, stream>>>((const unsigned short*)d_in[0], (const int*)d_in[1], (const unsigned short*)d_in[2],
        (const unsigned short*)d_in[3], (const int*)d_in[4], (const unsigned short*)d_in[5], (const unsigned short*)d_in[6],
        (const unsigned short*)d_in[7], (const unsigned short*)d_in[8], (const unsigned short*)d_in[9], (const unsigned short*)d_in[10],
        (unsigned char*)d_ws, (bf16*)d_out, pace);
}

#define CFG 966, 21, 1, 10
#define PRM 102.5f, 0.5f, 1.0f, 0.05f, 1.8f, 0.8f
extern "C" void kernel_launch(void* const* d_in, const int* in_sizes, int n_in,
                              void* d_out, int out_size, void* d_ws, size_t ws_size,
                              hipStream_t stream) {
    launch_cfg<CFG>(d_in, d_out, d_ws, stream, PRM);
}
```

```cpp
#include <hip/hip_runtime.h>
#include <hip/hip_bf16.h>
#include <cstdint>
#include <type_traits>


using bf16 = __hip_bfloat16;
using u32x4 = __attribute__((ext_vector_type(4))) unsigned;
using u32x2 = __attribute__((ext_vector_type(2))) unsigned;
using f32x2 = __attribute__((ext_vector_type(2))) float;
using f32x4 = __attribute__((ext_vector_type(4))) float;
using bf16x2 = __attribute__((ext_vector_type(2))) __bf16;

constexpr int DH = 192;
constexpr int HALF = 96;
constexpr int NH = 32;
constexpr int SKV = 16384;
constexpr int NWG = 256;
constexpr int NI = 16;
constexpr unsigned ROWB = DH * 4;
constexpr unsigned HEADB = SKV * ROWB;
constexpr unsigned KVB = NH * HEADB;
constexpr unsigned CSB = SKV * DH * 2;
constexpr unsigned OOB = 0x80000000u;
constexpr float SCALE_LOG2 = 1.4426950408889634f * 0.07216878364870322f;
constexpr unsigned SPIN_LIMIT = 20000000u;

constexpr unsigned UNITB = NWG * 64;
constexpr unsigned WS_PART = 0;
constexpr unsigned WS_EPOCH = WS_PART + NWG * (UNITB + 1024);
constexpr unsigned WS_REP = WS_EPOCH + 256;
constexpr unsigned WS_BYTES = WS_REP + 256 * 512;

__device__ __forceinline__ float bflo(unsigned u) { return __uint_as_float(u << 16); }
__device__ __forceinline__ float bfhi(unsigned u) { return __uint_as_float(u & 0xffff0000u); }
__device__ __forceinline__ float rbf(float x) { return __bfloat162float(__float2bfloat16(x)); }
__device__ __forceinline__ unsigned pk_bf16(f32x2 a) { return __builtin_bit_cast(unsigned, __builtin_convertvector(a, bf16x2)); }
__device__ __forceinline__ void dot_cvt(const u32x4 q, const u32x4 t, const u32x4 ki, f32x2& w01, f32x2& w23, f32x2& k01, f32x2& k23) {
    float w0, w1, w2, w3;
    float k0 = __uint_as_float(ki[0]), k1 = __uint_as_float(ki[1]), k2 = __uint_as_float(ki[2]), k3 = __uint_as_float(ki[3]);
    asm("v_dot2_f32_bf16 %0, %8, %12, 0\n\tv_dot2_f32_bf16 %1, %9, %13, 0\n\tv_dot2_f32_bf16 %2, %10, %14, 0\n\tv_dot2_f32_bf16 %3, %11, %15, 0\n\t"
        "v_cvt_f32_i32 %4, %4\n\tv_cvt_f32_i32 %5, %5\n\tv_cvt_f32_i32 %6, %6\n\tv_cvt_f32_i32 %7, %7"
        : "=&v"(w0), "=&v"(w1), "=&v"(w2), "=&v"(w3), "+v"(k0), "+v"(k1), "+v"(k2), "+v"(k3)
        : "v"(q[0]), "v"(q[1]), "v"(q[2]), "v"(q[3]), "v"(t[0]), "v"(t[1]), "v"(t[2]), "v"(t[3]));
    w01 = f32x2{w0, w1}; w23 = f32x2{w2, w3}; k01 = f32x2{k0, k1}; k23 = f32x2{k2, k3};
}

template <int CTRL> __device__ __forceinline__ float dpp(float x) {
    return __builtin_bit_cast(float, __builtin_amdgcn_mov_dpp(__builtin_bit_cast(int, x), CTRL, 0xf, 0xf, true));
}
__device__ __forceinline__ float row16_allsum(float x) {
    x += dpp<0x128>(x);
    x += dpp<0x124>(x);
    x += dpp<0x122>(x);
    x += dpp<0x121>(x);
    return x;
}
__device__ __forceinline__ float row16_allmax(float x) {
    x = fmaxf(x, dpp<0x128>(x));
    x = fmaxf(x, dpp<0x124>(x));
    x = fmaxf(x, dpp<0x122>(x));
    x = fmaxf(x, dpp<0x121>(x));
    return x;
}
__device__ __forceinline__ float xrow_max(float x) {
    auto s = __builtin_amdgcn_permlane16_swap(__float_as_uint(x), __float_as_uint(x), false, false);
    x = fmaxf(__uint_as_float(s[0]), __uint_as_float(s[1]));
    auto t = __builtin_amdgcn_permlane32_swap(__float_as_uint(x), __float_as_uint(x), false, false);
    return fmaxf(__uint_as_float(t[0]), __uint_as_float(t[1]));
}
__device__ __forceinline__ float xrow_sum(float x) {
    auto s = __builtin_amdgcn_permlane16_swap(__float_as_uint(x), __float_as_uint(x), false, false);
    x = __uint_as_float(s[0]) + __uint_as_float(s[1]);
    auto t = __builtin_amdgcn_permlane32_swap(__float_as_uint(x), __float_as_uint(x), false, false);
    return __uint_as_float(t[0]) + __uint_as_float(t[1]);
}
__device__ __forceinline__ float swap32_add(float a, float b) {
    auto t = __builtin_amdgcn_permlane32_swap(__float_as_uint(a), __float_as_uint(b), false, false);
    return __uint_as_float(t[0]) + __uint_as_float(t[1]);
}
__device__ __forceinline__ float swap16_add(float a, float b) {
    auto t = __builtin_amdgcn_permlane16_swap(__float_as_uint(a), __float_as_uint(b), false, false);
    return __uint_as_float(t[0]) + __uint_as_float(t[1]);
}
__device__ __forceinline__ int row16_sum(int x) {
    x += __builtin_amdgcn_mov_dpp(x, 0x128, 0xf, 0xf, true);
    x += __builtin_amdgcn_mov_dpp(x, 0x124, 0xf, 0xf, true);
    x += __builtin_amdgcn_mov_dpp(x, 0x122, 0xf, 0xf, true);
    x += __builtin_amdgcn_mov_dpp(x, 0x121, 0xf, 0xf, true);
    return x;
}
__device__ __forceinline__ unsigned now32() { return (unsigned)__builtin_amdgcn_s_memrealtime(); }

#define PIN() do { asm volatile("" ::: "memory"); __builtin_amdgcn_sched_barrier(0); } while (0)
#define BARRIER() do { asm volatile("s_waitcnt lgkmcnt(0)" ::: "memory"); __builtin_amdgcn_s_barrier(); PIN(); } while (0)
#define VMCNT(n) asm volatile("s_waitcnt vmcnt(%0)" :: "i"(n) : "memory")

typedef volatile __attribute__((address_space(3))) unsigned vlds_u32;
typedef volatile __attribute__((address_space(3))) u32x2 vlds_u32x2;
typedef volatile __attribute__((address_space(3))) u32x4 vlds_u32x4;

struct Pace {
    unsigned step;
    int start;
    int offset;
    int bias;
    int rd;
    int pub;
};

template <int X, int PRO_, int GATE, int STAG_, int SLP>
__global__ __launch_bounds__(576) void kv_sweep_kernel(
    const unsigned short* q, const int* qk, const unsigned short* ksc,
    const unsigned short* kzp, const int* qv, const unsigned short* vsc,
    const unsigned short* vzp, const unsigned short* q_cos, const unsigned short* q_sin,
    const unsigned short* k_cos, const unsigned short* k_sin, unsigned char* ws, bf16* out, Pace pace)
{
    __shared__ __attribute__((aligned(16))) unsigned s_q[NH * DH];
    __shared__ __attribute__((aligned(16))) float s_sc[NH * 64 * 4];
    __shared__ __attribute__((aligned(16))) float s_part[8 * NI * DH];
    __shared__ __attribute__((aligned(16))) float s_red[8 * 28];
    __shared__ __attribute__((aligned(16))) float s_f[NH * 8];
    __shared__ __attribute__((aligned(16))) unsigned s_done[8];
    __shared__ __attribute__((aligned(16))) unsigned s_sync[8];
    __shared__ __attribute__((aligned(16))) unsigned s_act[16];

    constexpr bool TRABS = true;
    constexpr int ORD = GATE % 10, WID = GATE / 10;
    constexpr int PRO = PRO_ % 10, PF = PRO_ / 10, STAG = STAG_ % 10, LAY = 0, VS = STAG_ / 10 ? 2 : 3;
    constexpr unsigned PSTR = LAY == 0 ? 64 : LAY == 1 ? UNITB + 256 : LAY == 2 ? UNITB + 128 : UNITB;
    constexpr unsigned USTR = LAY == 0 ? UNITB : 64;
    unsigned long long pc0;
    asm volatile("s_getpc_b64 %0" : "=s"(pc0));
    const unsigned tstart = now32();
    const int tid = threadIdx.x;
    const int lane = tid & 63;
    const int wave = __builtin_amdgcn_readfirstlane(tid >> 6);
    const int r = lane >> 4;
    const int c = lane & 15;
    const int bid = blockIdx.x;
    const int m = (bid & 7) * 32 + (bid >> 3);
    const auto rw_ = __builtin_amdgcn_make_buffer_rsrc((void*)ws, 0, WS_BYTES, 0x00020000);

    auto merge = [&](int i, int hp, int c6, unsigned tag) {
        const f32x4 f = *reinterpret_cast<const f32x4*>(s_f + (2 * i + hp) * 8);
        const f32x2 zl = *reinterpret_cast<const f32x2*>(s_f + (2 * i + hp) * 8 + 4);
        const float zs = zl[0];
        f32x2 a[3];
#pragma unroll
        for (int k = 0; k < 3; ++k) a[k] = f32x2{-zs, -zs};
#pragma unroll
        for (int w = 0; w < 4; ++w) {
            const float* p = s_part + ((hp * 4 + w) * NI + i) * DH + c6 * 6;
#pragma unroll
            for (int k = 0; k < 3; ++k) a[k] += f[w] * *reinterpret_cast<const f32x2*>(p + 2 * k);
        }
        return u32x4{pk_bf16(a[0]), pk_bf16(a[1]), pk_bf16(a[2]), tag | ((__float_as_uint(zl[1]) >> (16 * (c6 & 1))) & 0xffffu)};
    };
    auto publish = [&](u32x4 v, int i, int hp, int c6, bool on) {
        __builtin_amdgcn_raw_buffer_store_b128(v, rw_, on ? (hp * 8 + (c6 >> 2)) * USTR + (c6 & 3) * 16 : OOB, WS_PART + i * 16 * USTR + m * PSTR, 16);
        asm volatile("s_nop 1" :: "v"(v) : "memory");
    };

    if (wave == 8) {
        unsigned long long pc1;
        asm volatile("s_getpc_b64 %0" : "=s"(pc1));
        pc0 &= ~255ull;
        const auto rcode = __builtin_amdgcn_make_buffer_rsrc((void*)pc0, 0, (unsigned)(pc1 - pc0), 0x00020000);
        auto warm_code = [&]() {
            unsigned x[2];
#pragma unroll
            for (int k = 0; k < 2; ++k) x[k] = __builtin_amdgcn_raw_buffer_load_b32(rcode, lane * 128 + k * 8192, 0, 0);
            asm volatile("s_waitcnt vmcnt(0)" :: "v"(x[0]), "v"(x[1]) : "memory");
        };
        if (PF) warm_code();
        const unsigned tag = ((unsigned)__builtin_amdgcn_raw_buffer_load_b16(rw_, (lane & 3) * 2, WS_EPOCH, 16) + 1u) << 16;
        unsigned origin = (tstart << 8) + pace.offset;
        if (lane < 8) { s_done[lane] = 0; s_sync[lane] = lane == 2 ? origin : lane < 4 ? 0 : tag; }
        PIN();
        BARRIER();
        const int mh = lane >> 5, mc = lane & 31;
        const unsigned rtag = __builtin_amdgcn_readlane(tag, 0) | __builtin_amdgcn_readlane(tag, 1) >> 16;
        constexpr int RPX = 2;
        constexpr int RSH = X / 100;
        constexpr unsigned RDLY = 200;
        constexpr int UP = X / 10 % 10 ? X / 10 % 10 * 82 : 1 << 24, DN = X % 10 ? X % 10 * 82 : 1 << 24;
        const int rj = ((bid >> 3) + 5 * (bid & 7)) & 31;
        const int rid = rj % (32 / RPX) == 0 ? rj / (32 / RPX) * 8 + (bid & 7) : -1;
        unsigned wround = now32() >> RSH, rround = wround, tick = 0, target = origin;
        bool warmed = false;
        int i = 0;
        auto kept_now = [&](unsigned now) {
            const u32x2 a = *(vlds_u32x2*)(s_act + 2 * (lane & 7));
            const int late = max((int)(a[0] - origin), (int)((now << 8) - a[1] - origin));
            return origin + (__builtin_amdgcn_readfirstlane(row16_sum(late)) >> 4);
        };
        while (i < NI - 1) {
            const unsigned now = now32();
            if ((now >> RSH) != wround && i < NI - 2) {
                wround = now >> RSH;
                if (rid >= 0 && (now & ((1u << RSH) - 1)) < 48)
                    __builtin_amdgcn_raw_buffer_store_b64(u32x2{kept_now(now), rtag + wround}, rw_, lane == 0 ? rid * 8 : OOB, WS_REP + (wround & 255) * 512, 16);
                PIN();
                continue;
            }
            if ((int)(((now - RDLY) >> RSH) - rround) > 0 && i < NI - 2) {
                rround = (now - RDLY) >> RSH;
                const u32x2 x = __builtin_amdgcn_raw_buffer_load_b64(rw_, c * 8, WS_REP + (rround & 255) * 512, 16);
                const int d = (int)(x[0] - origin);
                const bool ok = x[1] == rtag + rround && d < (32 << 16) && d > -(32 << 16);
                const int n = __builtin_popcountll(__builtin_amdgcn_ballot_w64(ok));
                if (n >= 48) {
                    target = origin + __builtin_amdgcn_readfirstlane((int)(row16_allsum(ok ? (float)d : 0.f) * 4.f / (float)n)) - pace.bias;
                    const int lost = min((int)(kept_now(now32()) - origin) - pace.bias, (int)(target - origin));
                    if (lost > 0) origin += lost;
                }
                PIN();
                continue;
            }
            if ((now >> 5) != tick) {
                tick = now >> 5;
                origin += max(-DN, min(UP, (int)(target - origin)));
                if (lane == 0) *(vlds_u32*)(s_sync + 2) = origin;
                PIN();
            }
            if (PF >= 2 && !warmed && (int)((now << 8) - origin) > 60 * (int)pace.step) { warm_code(); warmed = true; continue; }
            const u32x4 a = *(vlds_u32x4*)s_done, b = *(vlds_u32x4*)(s_done + 4);
            const unsigned mn = min(min(min(a[0], a[1]), min(a[2], a[3])), min(min(b[0], b[1]), min(b[2], b[3])));
            if (__builtin_amdgcn_readfirstlane(mn) <= (unsigned)i) { __builtin_amdgcn_s_sleep(2); continue; }
            PIN();
            if (i == 0) {
                f32x4 ml[4], f;
#pragma unroll
                for (int w = 0; w < 4; ++w) ml[w] = *reinterpret_cast<const f32x4*>(s_sc + (mc * 4 + w) * 64 + 16);
                const float M = fmaxf(fmaxf(ml[0][0], ml[1][0]), fmaxf(ml[2][0], ml[3][0]));
                float L = 0.f, zs = 0.f;
#pragma unroll
                for (int w = 0; w < 4; ++w) { f[w] = __builtin_amdgcn_exp2f(ml[w][0] - M); L += f[w] * ml[w][1]; zs += f[w] * ml[w][2]; }
                const float inv = 1.f / L;
                *reinterpret_cast<f32x4*>(s_f + mc * 8) = f * inv;
                *reinterpret_cast<f32x2*>(s_f + mc * 8 + 4) = f32x2{zs * inv, M + __builtin_amdgcn_logf(L)};
                PIN();
                if (lane == 0) *(vlds_u32*)(s_sync + 3) = 1;
                PIN();
            }
            if (PF >= 2 && i == NI - 3) warm_code();
            const u32x4 v = merge(i, mh, mc, tag);
            PIN();
            if (pace.pub < 0) publish(v, i, mh, mc, true);
            else *reinterpret_cast<u32x4*>(s_part + (mh * 4 * NI + i) * DH + mc * 4) = v;
            PIN();
            ++i;
        }
        u32x4 v[NI - 1];
#pragma unroll
        for (int i = 0; i < NI - 1; ++i) { v[i] = *reinterpret_cast<const u32x4*>(s_part + (mh * 4 * NI + i) * DH + mc * 4); asm volatile("" : "+v"(v[i])); }
        while ((int)((now32() << 8) - origin) < pace.pub) __builtin_amdgcn_s_sleep(1);
        PIN();
#pragma unroll
        for (int i = 0; i < NI - 1; ++i) publish(v[i], i, mh, mc, pace.pub >= 0);
        return;
    }

    const int rw = wave & 3, hp = wave >> 2;
    const int row0 = m * 64 + rw * 16;

    const unsigned sidx = STAG == 0 ? hp * 1024 + m * 4 + rw : STAG == 1 ? m * 8 + wave : (hp * 1024 + (m & 31) * 32 + (m >> 5) * 4 + rw);
    unsigned off = pace.start + ((pace.step * sidx) >> 11);
    unsigned origin = tstart << 8, kept = 0;
    bool wide = false;
    const unsigned step = pace.step;
    auto pace_wait = [&]() {
        const unsigned tgt = __builtin_amdgcn_readfirstlane(origin) + off;
        unsigned now = now32() << 8;
        kept = ((int)(now - tgt) > 0 ? now : tgt) - off;
        wide = (int)(now - tgt) > 30 * 256;
        while ((int)(tgt - now) > 0) { if (SLP) __builtin_amdgcn_s_sleep(SLP); now = now32() << 8; }
        off += step;
    };
    auto report = [&]() {
        if (lane == 0) *(vlds_u32x2*)(s_act + 2 * wave) = u32x2{kept, off};
        origin = *(vlds_u32*)(s_sync + 2);
        PIN();
    };

    const int qe = min(tid, 383);
    const int qh = qe / 12, qd = (qe % 12) * 8;
    u32x4 lq[2], lc[2], ls[2];
    auto load_qs = [&]() {
#pragma unroll
        for (int k = 0; k < 2; ++k) {
            lq[k] = *reinterpret_cast<const u32x4*>(q + qh * DH + k * HALF + qd);
            lc[k] = *reinterpret_cast<const u32x4*>(q_cos + k * HALF + qd);
            ls[k] = *reinterpret_cast<const u32x4*>(q_sin + k * HALF + qd);
        }
        PIN();
    };
    u32x2 lsc[4];
    auto load_sc = [&]() {
#pragma unroll
        for (int a = 0; a < 4; ++a) {
            const unsigned short* base = a == 0 ? ksc : a == 1 ? kzp : a == 2 ? vsc : vzp;
            lsc[a] = __builtin_nontemporal_load(reinterpret_cast<const u32x2*>(base + (size_t)(tid >> 4) * SKV + m * 64 + (tid & 15) * 4));
        }
        PIN();
    };

    const auto rc = __builtin_amdgcn_make_buffer_rsrc((void*)k_cos, 0, CSB, 0x00020000);
    const auto rs = __builtin_amdgcn_make_buffer_rsrc((void*)k_sin, 0, CSB, 0x00020000);
    u32x2 raw[2][6];
    auto load_cs = [&](int t) {
        const unsigned co = r * 384 + c * 8;
        const unsigned so1 = r * 384 + (c < 8 ? c * 8 + 320 : c * 8 - 64);
        const unsigned rb = (row0 + (t + 2 * hp) * 4) * 384u;
        raw[t][0] = __builtin_amdgcn_raw_buffer_load_b64(rc, co, rb, 2);
        raw[t][1] = __builtin_amdgcn_raw_buffer_load_b64(rc, co + 128, rb, 2);
        raw[t][2] = __builtin_amdgcn_raw_buffer_load_b64(rc, co + 256, rb, 2);
        raw[t][3] = __builtin_amdgcn_raw_buffer_load_b64(rs, co + 192, rb, 2);
        raw[t][4] = __builtin_amdgcn_raw_buffer_load_b64(rs, so1, rb, 2);
        raw[t][5] = __builtin_amdgcn_raw_buffer_load_b64(rs, co + 64, rb, 2);
        PIN();
    };
    u32x4 tab[4][3];
    auto pack_cs = [&](int t) {
#pragma unroll
        for (int j = 0; j < 3; ++j) {
#pragma unroll
            for (int k = 0; k < 2; ++k) {
                tab[t][j][2 * k] = __builtin_amdgcn_perm(raw[t][3 + j][k], raw[t][j][k], 0x05040100);
                tab[t][j][2 * k + 1] = __builtin_amdgcn_perm(raw[t][3 + j][k], raw[t][j][k], 0x07060302);
            }
            *reinterpret_cast<u32x4*>(s_part + (wave ^ 4) * (NI * DH) + ((t * 3 + j) * 64 + lane) * 4) = tab[t][j];
        }
        PIN();
    };
    const auto rk = __builtin_amdgcn_make_buffer_rsrc((void*)qk, 0, KVB, 0x00020000);
    const auto rv = __builtin_amdgcn_make_buffer_rsrc((void*)qv, 0, KVB, 0x00020000);
    const unsigned voff = r * ROWB + c * 16;
    const unsigned woff = row0 * ROWB + hp * HEADB;
    auto toff = [&](int t) { return (unsigned)(t ^ (2 * hp)) * (4 * ROWB); };
    u32x4 ring[4][3];
    auto ld = [&](int slot, int j, __amdgpu_buffer_rsrc_t rr, unsigned off) {
        ring[slot][j] = __builtin_amdgcn_raw_buffer_load_b128(rr, voff + j * 256, off, 2);
        PIN();
    };
    auto ld_tile = [&](int slot, __amdgpu_buffer_rsrc_t rr, unsigned off) { ld(slot, 0, rr, off); ld(slot, 1, rr, off); ld(slot, 2, rr, off); };
    auto refill = [&](int slot, int j, __amdgpu_buffer_rsrc_t rr, unsigned goff, bool on) {
        if (ORD == 0) {
            if (on) { if (WID && wide) VMCNT(2 + WID); else VMCNT(2); if (j == 0) pace_wait(); }
            ld(slot, j, rr, goff);
        } else {
            if (on) { if (j == 0) pace_wait(); if (WID && wide) VMCNT(2 + WID); else VMCNT(2); }
            ld(slot, j, rr, goff);
            if (on && j == 0) { const unsigned t = (now32() << 8) - 1024 - (off - step); if ((int)(t - kept) > 0) kept = t; }
        }
        if (j == 2) report();
    };
    auto q_to_lds = [&]() {
        float o[2][8];
#pragma unroll
        for (int k = 0; k < 2; ++k)
#pragma unroll
            for (int u = 0; u < 4; ++u) {
                const unsigned pp = lq[k ^ 1][u] ^ (k == 0 ? 0x80008000u : 0u);
                o[k][2 * u] = rbf(rbf(bflo(lq[k][u]) * bflo(lc[k][u])) + rbf(bflo(pp) * bflo(ls[k][u])));
                o[k][2 * u + 1] = rbf(rbf(bfhi(lq[k][u]) * bfhi(lc[k][u])) + rbf(bfhi(pp) * bfhi(ls[k][u])));
            }
#pragma unroll
        for (int v = 0; v < 2; ++v) {
            u32x4 lo, hi;
#pragma unroll
            for (int e = 0; e < 4; ++e) {
                lo[e] = __builtin_amdgcn_perm(__float_as_uint(o[1][4 * v + e]), __float_as_uint(o[0][4 * v + e]), 0x07060302);
                hi[e] = __builtin_amdgcn_perm(__float_as_uint(o[0][4 * v + e]) ^ 0x80000000u, __float_as_uint(o[1][4 * v + e]), 0x07060302);
            }
            *reinterpret_cast<u32x4*>(s_q + qh * DH + qd + 4 * v) = lo;
            *reinterpret_cast<u32x4*>(s_q + qh * DH + HALF + qd + 4 * v) = hi;
        }
        PIN();
    };
    auto sc_to_lds = [&]() {
        float* dst = s_sc + tid * 16;
#pragma unroll
        for (int u = 0; u < 2; ++u) {
            *reinterpret_cast<f32x4*>(dst + 8 * u) = f32x4{bflo(lsc[0][u]) * SCALE_LOG2, bflo(lsc[1][u]), bflo(lsc[2][u]), bflo(lsc[3][u])};
            *reinterpret_cast<f32x4*>(dst + 8 * u + 4) = f32x4{bfhi(lsc[0][u]) * SCALE_LOG2, bfhi(lsc[1][u]), bfhi(lsc[2][u]), bfhi(lsc[3][u])};
        }
        PIN();
    };

    auto k0 = [&](int t) { pace_wait(); ld_tile(t, rk, woff + toff(t)); };
    if (PRO == 0) {
        load_qs(); load_sc(); load_cs(0);
        pace_wait(); load_cs(1);
        q_to_lds(); sc_to_lds();
        k0(0); pack_cs(0);
        k0(1); pack_cs(1);
        VMCNT(3); k0(2);
        VMCNT(3); k0(3);
    } else if (PRO == 1) {
        load_cs(0); load_sc();
        pace_wait(); load_cs(1); STAMP(1); if (wave < 6) load_qs();
        pack_cs(0); sc_to_lds();
        k0(0); pack_cs(1); if (wave < 6) q_to_lds();
        k0(1);
        VMCNT(3); k0(2);
        VMCNT(3); k0(3);
    } else if (PRO == 2) {
        load_cs(0); load_sc();
        pace_wait(); load_cs(1);
        pack_cs(0); sc_to_lds();
        k0(0); if (wave < 6) load_qs(); pack_cs(1);
        k0(1); if (wave < 6) q_to_lds();
        VMCNT(3); k0(2);
        VMCNT(3); k0(3);
    } else if (PRO == 3) {
        load_cs(0); load_sc();
        pace_wait(); load_cs(1);
        pack_cs(0); sc_to_lds();
        k0(0); pack_cs(1);
        k0(1); if (wave < 6) load_qs();
        VMCNT(9); k0(2); if (wave < 6) q_to_lds();
        VMCNT(3); k0(3);
    }
    if (lane == 0) *(vlds_u32x2*)(s_act + 2 * wave) = u32x2{kept, off};
    BARRIER();
    origin = *(vlds_u32*)(s_sync + 2);

    u32x4 qp[3];
    auto load_q = [&](int h, int j) { qp[j] = *reinterpret_cast<const u32x4*>(s_q + h * DH + 64 * j + 4 * c); };
    load_q(hp, 0); load_q(hp, 1); load_q(hp, 2);
#pragma unroll
    for (int t = 0; t < 2; ++t)
#pragma unroll
        for (int j = 0; j < 3; ++j) tab[2 + t][j] = *reinterpret_cast<const u32x4*>(s_part + wave * (NI * DH) + ((t * 3 + j) * 64 + lane) * 4);

    for (int i = 0; i < NI; ++i) {
        const bool last = i + 1 == NI;
        const int h = 2 * i + hp;
        const unsigned hnext = (i + 1) * (2 * HEADB) + woff;
        const auto rn = __builtin_amdgcn_make_buffer_rsrc((void*)(last ? qv : qk), 0, KVB, 0x00020000);
        float* scp = s_sc + (h * 4 + rw) * 64;
        f32x4 sc[4];
#pragma unroll
        for (int t = 0; t < 4; ++t) sc[t] = *reinterpret_cast<const f32x4*>(scp + (4 * (t ^ (2 * hp)) + r) * 4);
        float s[4];
#pragma unroll
        for (int t = 0; t < 4; ++t) {
            f32x2 dot, wsum;
#pragma unroll
            for (int j = 0; j < 3; ++j) {
                f32x2 w01, w23, k01, k23;
                dot_cvt(qp[j], tab[t][j], ring[t][j], w01, w23, k01, k23);
                if (j == 0) { wsum = w01 + w23; dot = k01 * w01; } else { wsum += w01; wsum += w23; dot += k01 * w01; }
                dot += k23 * w23;
                asm volatile("" : "+v"(dot), "+v"(wsum));
                PIN();
                if (t == 3) load_q(last ? h : h + 2, j);
                if (t == 0 || t == VS) refill(t, j, rn, last ? woff + toff(t == VS ? 1 : 0) : hnext + toff(t), true);
                else refill(t, j, rk, last ? OOB : hnext + toff(t), !last);
            }
            const float a = (dot[0] + dot[1]) - sc[t][1] * (wsum[0] + wsum[1]);
            s[t] = row16_allsum(a) * sc[t][0];
        }
        const float mx = xrow_max(fmaxf(fmaxf(s[0], s[1]), fmaxf(s[2], s[3])));
        float l = 0.f, z = 0.f;
#pragma unroll
        for (int t = 0; t < 4; ++t) {
            const float p = __builtin_amdgcn_exp2f(s[t] - mx);
            l += p;
            s[t] = p * sc[t][2];
            z += s[t] * sc[t][3];
        }
        l = xrow_sum(l);
        z = xrow_sum(z);
        if (c == 0) {
            *reinterpret_cast<f32x4*>(scp + r * 4) = f32x4{s[0], s[1], s[2], s[3]};
            *reinterpret_cast<f32x4*>(scp + 16) = f32x4{mx, l, z, 0.f};
        }
        PIN();
    }

    const float* pwp = s_sc + (hp * 4 + rw) * 64 + r * 4;
    f32x4 pw = *reinterpret_cast<const f32x4*>(pwp);
    for (int i = 0; i < NI; ++i) {
        const bool last = i + 1 == NI;
        const unsigned hoff = i * (2 * HEADB) + woff;
        f32x2 acc[3][2];
#pragma unroll
        for (int t = 0; t < 4; ++t) {
            const f32x2 p2 = {pw[t], pw[t]};
#pragma unroll
            for (int j = 0; j < 3; ++j) {
                const u32x4 vv = ring[(t & 1) * VS][j];
                const f32x2 v01 = {(float)(int)vv[0], (float)(int)vv[1]}, v23 = {(float)(int)vv[2], (float)(int)vv[3]};
                if (t == 0) { acc[j][0] = p2 * v01; acc[j][1] = p2 * v23; }
                else { acc[j][0] += p2 * v01; acc[j][1] += p2 * v23; }
                asm volatile("" : "+v"(acc[j][0]), "+v"(acc[j][1]));
                PIN();
                if (t == 3 && j == 2) pw = *reinterpret_cast<const f32x4*>(pwp + (last ? i : i + 1) * 512);
                if (t < 2) refill((t & 1) * VS, j, rv, hoff + toff(t + 2), true);
                else refill((t & 1) * VS, j, rv, last ? OOB : hoff + 2 * HEADB + toff(t - 2), !last);
            }
        }
        float* sl = s_part + (wave * NI + i) * DH;
#pragma unroll
        for (int j = 0; j < 3; ++j) {
            const float x = swap32_add(acc[j][0][0], acc[j][1][0]);
            const float y = swap32_add(acc[j][0][1], acc[j][1][1]);
            sl[64 * j + 4 * c + r] = swap16_add(x, y);
        }
        PIN();
        if (lane == 0) *(vlds_u32*)(s_done + wave) = i + 1;
        PIN();
    }
    unsigned old = 0;
    if (lane == 0) old = __hip_atomic_fetch_add(s_sync + hp, 1u, __ATOMIC_RELAXED, __HIP_MEMORY_SCOPE_WORKGROUP);
    PIN();
    const unsigned tag = s_sync[4 + (lane & 3)];
    if (__builtin_amdgcn_readfirstlane(old) == 3) {
        while (__builtin_amdgcn_readfirstlane(*(vlds_u32*)(s_sync + 3)) == 0) __builtin_amdgcn_s_sleep(1);
        PIN();
        publish(merge(NI - 1, hp, lane & 31, tag), NI - 1, hp, lane & 31, lane < 32);
        PIN();
    }
    {
        const unsigned o = __builtin_amdgcn_readfirstlane(origin);
        while ((int)((now32() << 8) - o) < pace.rd) __builtin_amdgcn_s_sleep(1);
        PIN();
    }
    {
        u32x4 x, y;
        unsigned xo = (tid >> 2) * PSTR + (tid & 3) * 16, yo = xo + 128 * PSTR;
        for (;;) {
            const u32x4 xn = __builtin_amdgcn_raw_buffer_load_b128(rw_, xo, WS_PART + bid * USTR, 16);
            const u32x4 yn = __builtin_amdgcn_raw_buffer_load_b128(rw_, yo, WS_PART + bid * USTR, 16);
            unsigned bad = (xo != OOB && (xn[3] ^ tag) > 0xffffu ? 1u : 0u) | (yo != OOB && (yn[3] ^ tag) > 0xffffu ? 2u : 0u);
            bad |= __builtin_amdgcn_mov_dpp(bad, 0xb1, 0xf, 0xf, true);
            bad |= __builtin_amdgcn_mov_dpp(bad, 0x4e, 0xf, 0xf, true);
            const bool tx = xo != OOB, ty = yo != OOB;
#pragma unroll
            for (int k = 0; k < 4; ++k) { x[k] = tx ? xn[k] : x[k]; y[k] = ty ? yn[k] : y[k]; }
            xo = bad & 1 ? xo : OOB;
            yo = bad & 2 ? yo : OOB;
            if (__builtin_amdgcn_ballot_w64(bad != 0) == 0 || now32() - tstart > SPIN_LIMIT) break;
            PIN();
        }
        const unsigned sel = lane & 1 ? 0x01000504u : 0x05040100u;
        const float lx = __uint_as_float(__builtin_amdgcn_perm(__builtin_amdgcn_mov_dpp(x[3], 0xb1, 0xf, 0xf, true), x[3], sel));
        const float ly = __uint_as_float(__builtin_amdgcn_perm(__builtin_amdgcn_mov_dpp(y[3], 0xb1, 0xf, 0xf, true), y[3], sel));
        float M = fmaxf(lx, ly);
        M = fmaxf(M, dpp<0x128>(M));
        M = fmaxf(M, dpp<0x124>(M));
        M = xrow_max(M);
        const float w0 = __builtin_amdgcn_exp2f(lx - M), w1 = __builtin_amdgcn_exp2f(ly - M);
        float a[8];
#pragma unroll
        for (int k = 0; k < 3; ++k) {
            a[2 * k] = w0 * bflo(x[k]) + w1 * bflo(y[k]);
            a[2 * k + 1] = w0 * bfhi(x[k]) + w1 * bfhi(y[k]);
        }
        a[6] = a[7] = w0 + w1;
        float b[4], d[2];
#pragma unroll
        for (int k = 0; k < 4; ++k) b[k] = swap32_add(a[k], a[4 + k]);
#pragma unroll
        for (int k = 0; k < 2; ++k) d[k] = swap16_add(b[k], b[2 + k]);
#pragma unroll
        for (int k = 0; k < 2; ++k) { d[k] += dpp<0x128>(d[k]); d[k] += dpp<0x124>(d[k]); }
        if (c < 4) *reinterpret_cast<f32x2*>(s_red + wave * 28 + (r < 3 ? c * 6 + r * 2 : 24)) = f32x2{d[0], r < 3 ? d[1] : M};
        BARRIER();
        if (tid < 24) {
            f32x2 sm[8];
            float ov[8];
#pragma unroll
            for (int w = 0; w < 8; ++w) { sm[w] = *reinterpret_cast<const f32x2*>(s_red + w * 28 + 24); ov[w] = s_red[w * 28 + tid]; }
            float MM = sm[0][1];
#pragma unroll
            for (int w = 1; w < 8; ++w) MM = fmaxf(MM, sm[w][1]);
            float S = 0.f, o = 0.f;
#pragma unroll
            for (int w = 0; w < 8; ++w) {
                const float ff = __builtin_amdgcn_exp2f(sm[w][1] - MM);
                S += ff * sm[w][0];
                o += ff * ov[w];
            }
            out[bid * 24 + tid] = __float2bfloat16(o * __builtin_amdgcn_rcpf(S));
        }
        if (bid == 0 && wave == 1) __builtin_amdgcn_raw_buffer_store_b16((unsigned short)(tag >> 16), rw_, lane < 4 ? lane * 2 : OOB, WS_EPOCH, 16);
    }
}

template <int X, int PRO, int GATE, int STAG>
static void launch_cfg(void* const* d_in, void* d_out, void* d_ws, hipStream_t stream, float t_us, float start_us, float off_us, float debt_us, float adj_pct, float pub_us) {
    Pace pace;
    pace.step = (unsigned)(t_us * 25600.f / 128.f);
    pace.start = (int)(start_us * 25600.f);
    pace.offset = (int)(off_us * 25600.f);
    pace.bias = (int)(debt_us * 25600.f);
    pace.rd = (int)((start_us + t_us * (129.f / 128.f) + adj_pct) * 25600.f);
    pace.pub = (int)((start_us + t_us * (129.f / 128.f) + pub_us) * 25600.f);
    kv_sweep_kernel<X, PRO, GATE, STAG, 1><<<NWG, 576, 0, stream>>>((const unsigned short*)d_in[0], (const int*)d_in[1], (const unsigned short*)d_in[2],
        (const unsigned short*)d_in[3], (const int*)d_in[4], (const unsigned short*)d_in[5], (const unsigned short*)d_in[6],
        (const unsigned short*)d_in[7], (const unsigned short*)d_in[8], (const unsigned short*)d_in[9], (const unsigned short*)d_in[10],
        (unsigned char*)d_ws, (bf16*)d_out, pace);
}

extern "C" void kernel_launch(void* const* d_in, const int* in_sizes, int n_in,
                              void* d_out, int out_size, void* d_ws, size_t ws_size,
                              hipStream_t stream) {
    launch_cfg<966, 21, 1, 10>(d_in, d_out, d_ws, stream, 102.5f, 0.5f, 1.0f, 0.05f, 1.8f, 0.8f);
}
```

```cpp
#include <hip/hip_runtime.h>
#include <hip/hip_bf16.h>
#include <cstdint>
#include <type_traits>

using bf16 = __hip_bfloat16;
using u32x4 = __attribute__((ext_vector_type(4))) unsigned;
using u32x2 = __attribute__((ext_vector_type(2))) unsigned;
using f32x2 = __attribute__((ext_vector_type(2))) float;
using f32x4 = __attribute__((ext_vector_type(4))) float;
using bf16x2 = __attribute__((ext_vector_type(2))) __bf16;

constexpr int DH = 192;
constexpr int HALF = 96;
constexpr int NH = 32;
constexpr int SKV = 16384;
constexpr int NWG = 256;
constexpr int NI = 16;
constexpr unsigned ROWB = DH * 4;
constexpr unsigned HEADB = SKV * ROWB;
constexpr unsigned KVB = NH * HEADB;
constexpr unsigned CSB = SKV * DH * 2;
constexpr unsigned OOB = 0x80000000u;
constexpr float SCALE_LOG2 = 1.4426950408889634f * 0.07216878364870322f;
constexpr unsigned SPIN_LIMIT = 20000000u;

constexpr unsigned UNITB = NWG * 64;
constexpr unsigned WS_PART = 128u << 20;
constexpr unsigned WS_EPOCH = 0;
constexpr unsigned WS_REP = WS_EPOCH + 256;
constexpr unsigned WS_BYTES = WS_PART + 256 * NWG * UNITB;

__device__ __forceinline__ float bflo(unsigned u) { return __uint_as_float(u << 16); }
__device__ __forceinline__ float bfhi(unsigned u) { return __uint_as_float(u & 0xffff0000u); }
__device__ __forceinline__ float rbf(float x) { return __bfloat162float(__float2bfloat16(x)); }
__device__ __forceinline__ unsigned pk_bf16(f32x2 a) { return __builtin_bit_cast(unsigned, __builtin_convertvector(a, bf16x2)); }
__device__ __forceinline__ void dot_cvt(const u32x4 q, const u32x4 t, const u32x4 ki, f32x2& w01, f32x2& w23, f32x2& k01, f32x2& k23) {
    float w0, w1, w2, w3;
    float k0 = __uint_as_float(ki[0]), k1 = __uint_as_float(ki[1]), k2 = __uint_as_float(ki[2]), k3 = __uint_as_float(ki[3]);
    asm("v_dot2_f32_bf16 %0, %8, %12, 0\n\tv_dot2_f32_bf16 %1, %9, %13, 0\n\tv_dot2_f32_bf16 %2, %10, %14, 0\n\tv_dot2_f32_bf16 %3, %11, %15, 0\n\t"
        "v_cvt_f32_i32 %4, %4\n\tv_cvt_f32_i32 %5, %5\n\tv_cvt_f32_i32 %6, %6\n\tv_cvt_f32_i32 %7, %7"
        : "=&v"(w0), "=&v"(w1), "=&v"(w2), "=&v"(w3), "+v"(k0), "+v"(k1), "+v"(k2), "+v"(k3)
        : "v"(q[0]), "v"(q[1]), "v"(q[2]), "v"(q[3]), "v"(t[0]), "v"(t[1]), "v"(t[2]), "v"(t[3]));
    w01 = f32x2{w0, w1}; w23 = f32x2{w2, w3}; k01 = f32x2{k0, k1}; k23 = f32x2{k2, k3};
}

template <int CTRL> __device__ __forceinline__ float dpp(float x) {
    return __builtin_bit_cast(float, __builtin_amdgcn_mov_dpp(__builtin_bit_cast(int, x), CTRL, 0xf, 0xf, true));
}
__device__ __forceinline__ float row16_allsum(float x) {
    x += dpp<0x128>(x);
    x += dpp<0x124>(x);
    x += dpp<0x122>(x);
    x += dpp<0x121>(x);
    return x;
}
__device__ __forceinline__ float row16_allmax(float x) {
    x = fmaxf(x, dpp<0x128>(x));
    x = fmaxf(x, dpp<0x124>(x));
    x = fmaxf(x, dpp<0x122>(x));
    x = fmaxf(x, dpp<0x121>(x));
    return x;
}
__device__ __forceinline__ float xrow_max(float x) {
    auto s = __builtin_amdgcn_permlane16_swap(__float_as_uint(x), __float_as_uint(x), false, false);
    x = fmaxf(__uint_as_float(s[0]), __uint_as_float(s[1]));
    auto t = __builtin_amdgcn_permlane32_swap(__float_as_uint(x), __float_as_uint(x), false, false);
    return fmaxf(__uint_as_float(t[0]), __uint_as_float(t[1]));
}
__device__ __forceinline__ float xrow_sum(float x) {
    auto s = __builtin_amdgcn_permlane16_swap(__float_as_uint(x), __float_as_uint(x), false, false);
    x = __uint_as_float(s[0]) + __uint_as_float(s[1]);
    auto t = __builtin_amdgcn_permlane32_swap(__float_as_uint(x), __float_as_uint(x), false, false);
    return __uint_as_float(t[0]) + __uint_as_float(t[1]);
}
__device__ __forceinline__ float swap32_add(float a, float b) {
    auto t = __builtin_amdgcn_permlane32_swap(__float_as_uint(a), __float_as_uint(b), false, false);
    return __uint_as_float(t[0]) + __uint_as_float(t[1]);
}
__device__ __forceinline__ float swap16_add(float a, float b) {
    auto t = __builtin_amdgcn_permlane16_swap(__float_as_uint(a), __float_as_uint(b), false, false);
    return __uint_as_float(t[0]) + __uint_as_float(t[1]);
}
__device__ __forceinline__ int row16_sum(int x) {
    x += __builtin_amdgcn_mov_dpp(x, 0x128, 0xf, 0xf, true);
    x += __builtin_amdgcn_mov_dpp(x, 0x124, 0xf, 0xf, true);
    x += __builtin_amdgcn_mov_dpp(x, 0x122, 0xf, 0xf, true);
    x += __builtin_amdgcn_mov_dpp(x, 0x121, 0xf, 0xf, true);
    return x;
}
__device__ __forceinline__ unsigned now32() { return (unsigned)__builtin_amdgcn_s_memrealtime(); }

#define NSTAMP 12
#define STAMP(i)
#define TRACE(i)
#define PIN() do { asm volatile("" ::: "memory"); __builtin_amdgcn_sched_barrier(0); } while (0)
#define BARRIER() do { asm volatile("s_waitcnt lgkmcnt(0)" ::: "memory"); __builtin_amdgcn_s_barrier(); PIN(); } while (0)
#define VMCNT(n) asm volatile("s_waitcnt vmcnt(%0)" :: "i"(n) : "memory")

typedef volatile __attribute__((address_space(3))) unsigned vlds_u32;
typedef volatile __attribute__((address_space(3))) u32x2 vlds_u32x2;
typedef volatile __attribute__((address_space(3))) u32x4 vlds_u32x4;

struct Pace {
    unsigned step;
    int start;
    int offset;
    int bias;
    int rd;
    int pub;
};

template <int X, int PRO_, int GATE, int STAG_, int SLP>
__global__ __launch_bounds__(576) void kv_sweep_kernel(
    const unsigned short* q, const int* qk, const unsigned short* ksc,
    const unsigned short* kzp, const int* qv, const unsigned short* vsc,
    const unsigned short* vzp, const unsigned short* q_cos, const unsigned short* q_sin,
    const unsigned short* k_cos, const unsigned short* k_sin, unsigned char* ws, bf16* out, Pace pace)
{
    __shared__ __attribute__((aligned(16))) unsigned s_q[NH * DH];
    __shared__ __attribute__((aligned(16))) float s_sc[NH * 64 * 4];
    __shared__ __attribute__((aligned(16))) float s_part[8 * NI * DH];
    __shared__ __attribute__((aligned(16))) float s_red[8 * 28];
    __shared__ __attribute__((aligned(16))) float s_f[NH * 8];
    __shared__ __attribute__((aligned(16))) unsigned s_done[8];
    __shared__ __attribute__((aligned(16))) unsigned s_sync[8];
    __shared__ __attribute__((aligned(16))) unsigned s_act[16];

    constexpr bool TRABS = true;
    constexpr int ORD = 1, WID = 0, HOLD = GATE % 10 * 2, SLIP = GATE / 10 % 10 * 2560, SLIPT = GATE / 100;
    constexpr int PRO = PRO_ % 10, PF = PRO_ / 10, STAG = 0, LAY = 0, VS = 2, LDA = 16, STA = 16;
    constexpr unsigned NREG = 1u << STAG_;
    constexpr unsigned PSTR = LAY == 0 ? 64 : LAY == 1 ? UNITB + 256 : LAY == 2 ? UNITB + 128 : UNITB;
    constexpr unsigned USTR = LAY == 0 ? UNITB : 64;
    STAMP(0);
    unsigned long long pc0;
    asm volatile("s_getpc_b64 %0" : "=s"(pc0));
    const unsigned tstart = now32();
    const int tid = threadIdx.x;
    const int lane = tid & 63;
    const int wave = __builtin_amdgcn_readfirstlane(tid >> 6);
    const int r = lane >> 4;
    const int c = lane & 15;
    const int bid = blockIdx.x;
    const int m = (bid & 7) * 32 + (bid >> 3);
    const auto rw_ = __builtin_amdgcn_make_buffer_rsrc((void*)ws, 0, WS_BYTES, 0x00020000);

    auto merge = [&](int i, int hp, int c6, unsigned tag) {
        const f32x4 f = *reinterpret_cast<const f32x4*>(s_f + (2 * i + hp) * 8);
        const f32x2 zl = *reinterpret_cast<const f32x2*>(s_f + (2 * i + hp) * 8 + 4);
        const float zs = zl[0];
        f32x2 a[3];
#pragma unroll
        for (int k = 0; k < 3; ++k) a[k] = f32x2{-zs, -zs};
#pragma unroll
        for (int w = 0; w < 4; ++w) {
            const float* p = s_part + ((hp * 4 + w) * NI + i) * DH + c6 * 6;
#pragma unroll
            for (int k = 0; k < 3; ++k) a[k] += f[w] * *reinterpret_cast<const f32x2*>(p + 2 * k);
        }
        return u32x4{pk_bf16(a[0]), pk_bf16(a[1]), pk_bf16(a[2]), tag | ((__float_as_uint(zl[1]) >> (16 * (c6 & 1))) & 0xffffu)};
    };
    auto region = [&](unsigned tag) { return WS_PART + ((__builtin_amdgcn_readfirstlane(tag) >> 16) & (NREG - 1)) * (NWG * UNITB); };
    auto publish = [&](u32x4 v, unsigned reg, int i, int hp, int c6, bool on) {
        __builtin_amdgcn_raw_buffer_store_b128(v, rw_, on ? (hp * 8 + (c6 >> 2)) * USTR + (c6 & 3) * 16 : OOB, reg + i * 16 * USTR + m * PSTR, STA);
        asm volatile("s_nop 1" :: "v"(v) : "memory");
    };

    if (wave == 8) {
        unsigned long long pc1;
        asm volatile("s_getpc_b64 %0" : "=s"(pc1));
        pc0 &= ~255ull;
        const auto rcode = __builtin_amdgcn_make_buffer_rsrc((void*)pc0, 0, (unsigned)(pc1 - pc0), 0x00020000);
        auto warm_code = [&]() {
            unsigned x[2];
#pragma unroll
            for (int k = 0; k < 2; ++k) x[k] = __builtin_amdgcn_raw_buffer_load_b32(rcode, lane * 128 + k * 8192, 0, 0);
            asm volatile("s_waitcnt vmcnt(0)" :: "v"(x[0]), "v"(x[1]) : "memory");
        };
        if (PF) warm_code();
        const unsigned tag = ((unsigned)__builtin_amdgcn_raw_buffer_load_b16(rw_, (lane & 3) * 2, WS_EPOCH, 16) + 1u) << 16;
        unsigned origin = (tstart << 8) + pace.offset;
        if (lane < 8) { s_done[lane] = 0; s_sync[lane] = lane == 2 ? origin : lane < 4 ? 0 : tag; }
        PIN();
        BARRIER();
        const int mh = lane >> 5, mc = lane & 31;
        const unsigned rtag = __builtin_amdgcn_readlane(tag, 0) | __builtin_amdgcn_readlane(tag, 1) >> 16;
        constexpr int RPX = 2;
        constexpr int RSH = X / 100;
        constexpr unsigned RDLY = 200;
        constexpr int UP = X / 10 % 10 ? X / 10 % 10 * 82 : 1 << 24, DN = X % 10 ? X % 10 * 82 : 1 << 24;
        const int rj = ((bid >> 3) + 5 * (bid & 7)) & 31;
        const int rid = rj % (32 / RPX) == 0 ? rj / (32 / RPX) * 8 + (bid & 7) : -1;
        unsigned wround = now32() >> RSH, rround = wround, tick = 0, target = origin;
        bool warmed = false;
        int i = 0;
        auto kept_now = [&](unsigned now) {
            const u32x2 a = *(vlds_u32x2*)(s_act + 2 * (lane & 7));
            const int late = max((int)(a[0] - origin), (int)((now << 8) - a[1] - origin));
            return origin + (__builtin_amdgcn_readfirstlane(row16_sum(late)) >> 4);
        };
        while (i < NI - 1) {
            const unsigned now = now32();
            if ((now >> RSH) != wround && i < NI - 2) {
                wround = now >> RSH;
                if (rid >= 0 && (now & ((1u << RSH) - 1)) < 48)
                    __builtin_amdgcn_raw_buffer_store_b64(u32x2{kept_now(now), rtag + wround}, rw_, lane == 0 ? rid * 8 : OOB, WS_REP + (wround & 255) * 512, 16);
                PIN();
                continue;
            }
            if ((int)(((now - RDLY) >> RSH) - rround) > 0 && i < NI - 2) {
                rround = (now - RDLY) >> RSH;
                const u32x2 x = __builtin_amdgcn_raw_buffer_load_b64(rw_, c * 8, WS_REP + (rround & 255) * 512, 16);
                const int d = (int)(x[0] - origin);
                const bool ok = x[1] == rtag + rround && d < (32 << 16) && d > -(32 << 16);
                const int n = __builtin_popcountll(__builtin_amdgcn_ballot_w64(ok));
                if (n >= 48) {
                    target = origin + __builtin_amdgcn_readfirstlane((int)(row16_allsum(ok ? (float)d : 0.f) * 4.f / (float)n)) - pace.bias;
                    const int lost = min((int)(kept_now(now32()) - origin) - pace.bias, (int)(target - origin));
                    if (lost > 0) origin += lost;
                    if (SLIP && !SLIPT) { const int l2 = (int)(kept_now(now32()) - origin) - SLIP; if (l2 > 0) origin += l2; }
                }
                PIN();
                continue;
            }
            if ((now >> 5) != tick) {
                tick = now >> 5;
                origin += max(-DN, min(UP, (int)(target - origin)));
                if (SLIP && SLIPT) { const int l2 = (int)(kept_now(now) - origin) - SLIP; if (l2 > 0) origin += l2; }
                if (lane == 0) *(vlds_u32*)(s_sync + 2) = origin;
                PIN();
            }
            if (PF >= 2 && !warmed && (int)((now << 8) - origin) > 60 * (int)pace.step) { warm_code(); warmed = true; continue; }
            const u32x4 a = *(vlds_u32x4*)s_done, b = *(vlds_u32x4*)(s_done + 4);
            const unsigned mn = min(min(min(a[0], a[1]), min(a[2], a[3])), min(min(b[0], b[1]), min(b[2], b[3])));
            if (__builtin_amdgcn_readfirstlane(mn) <= (unsigned)i) { __builtin_amdgcn_s_sleep(2); continue; }
            PIN();
            if (i == 0) {
                f32x4 ml[4], f;
#pragma unroll
                for (int w = 0; w < 4; ++w) ml[w] = *reinterpret_cast<const f32x4*>(s_sc + (mc * 4 + w) * 64 + 16);
                const float M = fmaxf(fmaxf(ml[0][0], ml[1][0]), fmaxf(ml[2][0], ml[3][0]));
                float L = 0.f, zs = 0.f;
#pragma unroll
                for (int w = 0; w < 4; ++w) { f[w] = __builtin_amdgcn_exp2f(ml[w][0] - M); L += f[w] * ml[w][1]; zs += f[w] * ml[w][2]; }
                const float inv = 1.f / L;
                *reinterpret_cast<f32x4*>(s_f + mc * 8) = f * inv;
                *reinterpret_cast<f32x2*>(s_f + mc * 8 + 4) = f32x2{zs * inv, M + __builtin_amdgcn_logf(L)};
                PIN();
                if (lane == 0) *(vlds_u32*)(s_sync + 3) = 1;
                PIN();
            }
            if (PF >= 2 && i == NI - 3) warm_code();
            const u32x4 v = merge(i, mh, mc, tag);
            PIN();
            if (pace.pub < 0) publish(v, region(tag), i, mh, mc, true);
            else *reinterpret_cast<u32x4*>(s_part + (mh * 4 * NI + i) * DH + mc * 4) = v;
            PIN();
            ++i;
        }
        u32x4 v[NI - 1];
#pragma unroll
        for (int i = 0; i < NI - 1; ++i) { v[i] = *reinterpret_cast<const u32x4*>(s_part + (mh * 4 * NI + i) * DH + mc * 4); asm volatile("" : "+v"(v[i])); }
        STAMP(5);
        while ((int)((now32() << 8) - origin) < pace.pub) __builtin_amdgcn_s_sleep(1);
        PIN();
        STAMP(6);
#pragma unroll
        for (int i = 0; i < NI - 1; ++i) publish(v[i], region(tag), i, mh, mc, pace.pub >= 0);
        STAMP(7);
        return;
    }

    const int rw = wave & 3, hp = wave >> 2;
    const int row0 = m * 64 + rw * 16;

    const unsigned sidx = STAG == 0 ? hp * 1024 + m * 4 + rw : STAG == 1 ? m * 8 + wave : (hp * 1024 + (m & 31) * 32 + (m >> 5) * 4 + rw);
    unsigned off = pace.start + ((pace.step * sidx) >> 11);
    unsigned origin = tstart << 8, kept = 0;
    bool wide = false;
    unsigned prev = 0, hold = 0;
    int holdn = 0;
    const unsigned step = pace.step;
    auto pace_wait = [&]() {
        unsigned org = __builtin_amdgcn_readfirstlane(origin);
        if (HOLD && holdn > 0) { --holdn; if ((int)(hold - org) > 0) org = hold; }
        const unsigned tgt = org + off;
        prev = kept;
        unsigned now = now32() << 8;
        kept = ((int)(now - tgt) > 0 ? now : tgt) - off;
        wide = (int)(now - tgt) > 30 * 256;
        while ((int)(tgt - now) > 0) { if (SLP) __builtin_amdgcn_s_sleep(SLP); now = now32() << 8; }
        off += step;
    };
    auto report = [&]() {
        if (lane == 0) *(vlds_u32x2*)(s_act + 2 * wave) = u32x2{kept, off};
        origin = *(vlds_u32*)(s_sync + 2);
        PIN();
    };
#define TRV (TRABS ? kept - (tstart << 8) : kept - __builtin_amdgcn_readfirstlane(origin))

    const int qe = min(tid, 383);
    const int qh = qe / 12, qd = (qe % 12) * 8;
    u32x4 lq[2], lc[2], ls[2];
    auto load_qs = [&]() {
#pragma unroll
        for (int k = 0; k < 2; ++k) {
            lq[k] = *reinterpret_cast<const u32x4*>(q + qh * DH + k * HALF + qd);
            lc[k] = *reinterpret_cast<const u32x4*>(q_cos + k * HALF + qd);
            ls[k] = *reinterpret_cast<const u32x4*>(q_sin + k * HALF + qd);
        }
        PIN();
    };
    u32x2 lsc[4];
    auto load_sc = [&]() {
#pragma unroll
        for (int a = 0; a < 4; ++a) {
            const unsigned short* base = a == 0 ? ksc : a == 1 ? kzp : a == 2 ? vsc : vzp;
            lsc[a] = __builtin_nontemporal_load(reinterpret_cast<const u32x2*>(base + (size_t)(tid >> 4) * SKV + m * 64 + (tid & 15) * 4));
        }
        PIN();
    };

    const auto rc = __builtin_amdgcn_make_buffer_rsrc((void*)k_cos, 0, CSB, 0x00020000);
    const auto rs = __builtin_amdgcn_make_buffer_rsrc((void*)k_sin, 0, CSB, 0x00020000);
    u32x2 raw[2][6];
    auto load_cs = [&](int t) {
        const unsigned co = r * 384 + c * 8;
        const unsigned so1 = r * 384 + (c < 8 ? c * 8 + 320 : c * 8 - 64);
        const unsigned rb = (row0 + (t + 2 * hp) * 4) * 384u;
        raw[t][0] = __builtin_amdgcn_raw_buffer_load_b64(rc, co, rb, 2);
        raw[t][1] = __builtin_amdgcn_raw_buffer_load_b64(rc, co + 128, rb, 2);
        raw[t][2] = __builtin_amdgcn_raw_buffer_load_b64(rc, co + 256, rb, 2);
        raw[t][3] = __builtin_amdgcn_raw_buffer_load_b64(rs, co + 192, rb, 2);
        raw[t][4] = __builtin_amdgcn_raw_buffer_load_b64(rs, so1, rb, 2);
        raw[t][5] = __builtin_amdgcn_raw_buffer_load_b64(rs, co + 64, rb, 2);
        PIN();
    };
    u32x4 tab[4][3];
    auto pack_cs = [&](int t) {
#pragma unroll
        for (int j = 0; j < 3; ++j) {
#pragma unroll
            for (int k = 0; k < 2; ++k) {
                tab[t][j][2 * k] = __builtin_amdgcn_perm(raw[t][3 + j][k], raw[t][j][k], 0x05040100);
                tab[t][j][2 * k + 1] = __builtin_amdgcn_perm(raw[t][3 + j][k], raw[t][j][k], 0x07060302);
            }
            *reinterpret_cast<u32x4*>(s_part + (wave ^ 4) * (NI * DH) + ((t * 3 + j) * 64 + lane) * 4) = tab[t][j];
        }
        PIN();
    };
    const auto rk = __builtin_amdgcn_make_buffer_rsrc((void*)qk, 0, KVB, 0x00020000);
    const auto rv = __builtin_amdgcn_make_buffer_rsrc((void*)qv, 0, KVB, 0x00020000);
    const unsigned voff = r * ROWB + c * 16;
    const unsigned woff = row0 * ROWB + hp * HEADB;
    auto toff = [&](int t) { return (unsigned)(t ^ (2 * hp)) * (4 * ROWB); };
    u32x4 ring[4][3];
    auto ld = [&](int slot, int j, __amdgpu_buffer_rsrc_t rr, unsigned off) {
        ring[slot][j] = __builtin_amdgcn_raw_buffer_load_b128(rr, voff + j * 256, off, 2);
        PIN();
    };
    auto ld_tile = [&](int slot, __amdgpu_buffer_rsrc_t rr, unsigned off) { ld(slot, 0, rr, off); ld(slot, 1, rr, off); ld(slot, 2, rr, off); };
    auto refill = [&](int slot, int j, __amdgpu_buffer_rsrc_t rr, unsigned goff, bool on) {
        if (ORD == 0) {
            if (on) { if (WID && wide) VMCNT(2 + WID); else VMCNT(2); if (j == 0) pace_wait(); }
            ld(slot, j, rr, goff);
        } else {
            if (on) { if (j == 0) pace_wait(); if (WID && wide) VMCNT(2 + WID); else VMCNT(2); }
            ld(slot, j, rr, goff);
            if (on && j == 0) { const unsigned t = (now32() << 8) - 1024 - (off - step); if ((int)(t - kept) > 0) kept = t;
                if (HOLD && (int)(kept - prev) > 100 * 256) { hold = kept; holdn = HOLD; } }
        }
        if (j == 2) report();
    };
    auto q_to_lds = [&]() {
        float o[2][8];
#pragma unroll
        for (int k = 0; k < 2; ++k)
#pragma unroll
            for (int u = 0; u < 4; ++u) {
                const unsigned pp = lq[k ^ 1][u] ^ (k == 0 ? 0x80008000u : 0u);
                o[k][2 * u] = rbf(rbf(bflo(lq[k][u]) * bflo(lc[k][u])) + rbf(bflo(pp) * bflo(ls[k][u])));
                o[k][2 * u + 1] = rbf(rbf(bfhi(lq[k][u]) * bfhi(lc[k][u])) + rbf(bfhi(pp) * bfhi(ls[k][u])));
            }
#pragma unroll
        for (int v = 0; v < 2; ++v) {
            u32x4 lo, hi;
#pragma unroll
            for (int e = 0; e < 4; ++e) {
                lo[e] = __builtin_amdgcn_perm(__float_as_uint(o[1][4 * v + e]), __float_as_uint(o[0][4 * v + e]), 0x07060302);
                hi[e] = __builtin_amdgcn_perm(__float_as_uint(o[0][4 * v + e]) ^ 0x80000000u, __float_as_uint(o[1][4 * v + e]), 0x07060302);
            }
            *reinterpret_cast<u32x4*>(s_q + qh * DH + qd + 4 * v) = lo;
            *reinterpret_cast<u32x4*>(s_q + qh * DH + HALF + qd + 4 * v) = hi;
        }
        PIN();
    };
    auto sc_to_lds = [&]() {
        float* dst = s_sc + tid * 16;
#pragma unroll
        for (int u = 0; u < 2; ++u) {
            *reinterpret_cast<f32x4*>(dst + 8 * u) = f32x4{bflo(lsc[0][u]) * SCALE_LOG2, bflo(lsc[1][u]), bflo(lsc[2][u]), bflo(lsc[3][u])};
            *reinterpret_cast<f32x4*>(dst + 8 * u + 4) = f32x4{bfhi(lsc[0][u]) * SCALE_LOG2, bfhi(lsc[1][u]), bfhi(lsc[2][u]), bfhi(lsc[3][u])};
        }
        PIN();
    };

    auto k0 = [&](int t) { pace_wait(); ld_tile(t, rk, woff + toff(t)); };
    if (PRO == 0) {
        load_qs(); load_sc(); load_cs(0);
        pace_wait(); load_cs(1);
        q_to_lds(); sc_to_lds();
        STAMP(1);
        k0(0); pack_cs(0);
        k0(1); pack_cs(1);
        VMCNT(3); k0(2);
        STAMP(2);
        VMCNT(3); k0(3);
    } else if (PRO == 1) {
        load_cs(0); load_sc();
        pace_wait(); load_cs(1); STAMP(1); if (wave < 6) load_qs();
        pack_cs(0); sc_to_lds();
        k0(0); pack_cs(1); if (wave < 6) q_to_lds();
        k0(1);
        VMCNT(3); k0(2);
        STAMP(2);
        VMCNT(3); k0(3);
    } else if (PRO == 2) {
        load_cs(0); load_sc();
        pace_wait(); load_cs(1);
        STAMP(1);
        pack_cs(0); sc_to_lds();
        k0(0); if (wave < 6) load_qs(); pack_cs(1);
        k0(1); if (wave < 6) q_to_lds();
        VMCNT(3); k0(2);
        STAMP(2);
        VMCNT(3); k0(3);
    } else if (PRO == 3) {
        load_cs(0); load_sc();
        pace_wait(); load_cs(1);
        STAMP(1);
        pack_cs(0); sc_to_lds();
        k0(0); pack_cs(1);
        k0(1); if (wave < 6) load_qs();
        VMCNT(9); k0(2); if (wave < 6) q_to_lds();
        STAMP(2);
        VMCNT(3); k0(3);
    }
    if (lane == 0) *(vlds_u32x2*)(s_act + 2 * wave) = u32x2{kept, off};
    BARRIER();
    origin = *(vlds_u32*)(s_sync + 2);
    STAMP(3);

    u32x4 qp[3];
    auto load_q = [&](int h, int j) { qp[j] = *reinterpret_cast<const u32x4*>(s_q + h * DH + 64 * j + 4 * c); };
    load_q(hp, 0); load_q(hp, 1); load_q(hp, 2);
#pragma unroll
    for (int t = 0; t < 2; ++t)
#pragma unroll
        for (int j = 0; j < 3; ++j) tab[2 + t][j] = *reinterpret_cast<const u32x4*>(s_part + wave * (NI * DH) + ((t * 3 + j) * 64 + lane) * 4);

    for (int i = 0; i < NI; ++i) {
        const bool last = i + 1 == NI;
        const int h = 2 * i + hp;
        const unsigned hnext = (i + 1) * (2 * HEADB) + woff;
        const auto rn = __builtin_amdgcn_make_buffer_rsrc((void*)(last ? qv : qk), 0, KVB, 0x00020000);
        float* scp = s_sc + (h * 4 + rw) * 64;
        f32x4 sc[4];
#pragma unroll
        for (int t = 0; t < 4; ++t) sc[t] = *reinterpret_cast<const f32x4*>(scp + (4 * (t ^ (2 * hp)) + r) * 4);
        float s[4];
#pragma unroll
        for (int t = 0; t < 4; ++t) {
            f32x2 dot, wsum;
#pragma unroll
            for (int j = 0; j < 3; ++j) {
                f32x2 w01, w23, k01, k23;
                dot_cvt(qp[j], tab[t][j], ring[t][j], w01, w23, k01, k23);
                if (j == 0) { wsum = w01 + w23; dot = k01 * w01; } else { wsum += w01; wsum += w23; dot += k01 * w01; }
                dot += k23 * w23;
                asm volatile("" : "+v"(dot), "+v"(wsum));
                PIN();
                if (t == 3) load_q(last ? h : h + 2, j);
                if (t == 0 || t == VS) refill(t, j, rn, last ? woff + toff(t == VS ? 1 : 0) : hnext + toff(t), true);
                else refill(t, j, rk, last ? OOB : hnext + toff(t), !last);
                if (t == 0 && j == 0) TRACE(i);
            }
            const float a = (dot[0] + dot[1]) - sc[t][1] * (wsum[0] + wsum[1]);
            s[t] = row16_allsum(a) * sc[t][0];
        }
        const float mx = xrow_max(fmaxf(fmaxf(s[0], s[1]), fmaxf(s[2], s[3])));
        float l = 0.f, z = 0.f;
#pragma unroll
        for (int t = 0; t < 4; ++t) {
            const float p = __builtin_amdgcn_exp2f(s[t] - mx);
            l += p;
            s[t] = p * sc[t][2];
            z += s[t] * sc[t][3];
        }
        l = xrow_sum(l);
        z = xrow_sum(z);
        if (c == 0) {
            *reinterpret_cast<f32x4*>(scp + r * 4) = f32x4{s[0], s[1], s[2], s[3]};
            *reinterpret_cast<f32x4*>(scp + 16) = f32x4{mx, l, z, 0.f};
        }
        PIN();
    }
    STAMP(4);

    const float* pwp = s_sc + (hp * 4 + rw) * 64 + r * 4;
    f32x4 pw = *reinterpret_cast<const f32x4*>(pwp);
    for (int i = 0; i < NI; ++i) {
        const bool last = i + 1 == NI;
        const unsigned hoff = i * (2 * HEADB) + woff;
        f32x2 acc[3][2];
#pragma unroll
        for (int t = 0; t < 4; ++t) {
            const f32x2 p2 = {pw[t], pw[t]};
#pragma unroll
            for (int j = 0; j < 3; ++j) {
                const u32x4 vv = ring[(t & 1) * VS][j];
                const f32x2 v01 = {(float)(int)vv[0], (float)(int)vv[1]}, v23 = {(float)(int)vv[2], (float)(int)vv[3]};
                if (t == 0) { acc[j][0] = p2 * v01; acc[j][1] = p2 * v23; }
                else { acc[j][0] += p2 * v01; acc[j][1] += p2 * v23; }
                asm volatile("" : "+v"(acc[j][0]), "+v"(acc[j][1]));
                PIN();
                if (t == 3 && j == 2) pw = *reinterpret_cast<const f32x4*>(pwp + (last ? i : i + 1) * 512);
                if (t < 2) refill((t & 1) * VS, j, rv, hoff + toff(t + 2), true);
                else refill((t & 1) * VS, j, rv, last ? OOB : hoff + 2 * HEADB + toff(t - 2), !last);
                if (t == 0 && j == 0) TRACE(16 + i);
            }
        }
        float* sl = s_part + (wave * NI + i) * DH;
#pragma unroll
        for (int j = 0; j < 3; ++j) {
            const float x = swap32_add(acc[j][0][0], acc[j][1][0]);
            const float y = swap32_add(acc[j][0][1], acc[j][1][1]);
            sl[64 * j + 4 * c + r] = swap16_add(x, y);
        }
        PIN();
        if (lane == 0) *(vlds_u32*)(s_done + wave) = i + 1;
        PIN();
    }
    STAMP(6);
    unsigned old = 0;
    if (lane == 0) old = __hip_atomic_fetch_add(s_sync + hp, 1u, __ATOMIC_RELAXED, __HIP_MEMORY_SCOPE_WORKGROUP);
    PIN();
    const unsigned tag = s_sync[4 + (lane & 3)];
    if (__builtin_amdgcn_readfirstlane(old) == 3) {
        while (__builtin_amdgcn_readfirstlane(*(vlds_u32*)(s_sync + 3)) == 0) __builtin_amdgcn_s_sleep(1);
        PIN();
        publish(merge(NI - 1, hp, lane & 31, tag), region(tag), NI - 1, hp, lane & 31, lane < 32);
        PIN();
    }
    STAMP(7);
    {
        const unsigned o = __builtin_amdgcn_readfirstlane(origin);
        while ((int)((now32() << 8) - o) < pace.rd) __builtin_amdgcn_s_sleep(1);
        PIN();
    }
    STAMP(8);
    {
        u32x4 x, y;
        unsigned xo = (tid >> 2) * PSTR + (tid & 3) * 16, yo = xo + 128 * PSTR;
        for (;;) {
            const u32x4 xn = __builtin_amdgcn_raw_buffer_load_b128(rw_, xo, region(tag) + bid * USTR, LDA);
            const u32x4 yn = __builtin_amdgcn_raw_buffer_load_b128(rw_, yo, region(tag) + bid * USTR, LDA);
            unsigned bad = (xo != OOB && (xn[3] ^ tag) > 0xffffu ? 1u : 0u) | (yo != OOB && (yn[3] ^ tag) > 0xffffu ? 2u : 0u);
            bad |= __builtin_amdgcn_mov_dpp(bad, 0xb1, 0xf, 0xf, true);
            bad |= __builtin_amdgcn_mov_dpp(bad, 0x4e, 0xf, 0xf, true);
            const bool tx = xo != OOB, ty = yo != OOB;
#pragma unroll
            for (int k = 0; k < 4; ++k) { x[k] = tx ? xn[k] : x[k]; y[k] = ty ? yn[k] : y[k]; }
            xo = bad & 1 ? xo : OOB;
            yo = bad & 2 ? yo : OOB;
            if (__builtin_amdgcn_ballot_w64(bad != 0) == 0 || now32() - tstart > SPIN_LIMIT) break;
            PIN();
        }
        STAMP(9);
        const unsigned sel = lane & 1 ? 0x01000504u : 0x05040100u;
        const float lx = __uint_as_float(__builtin_amdgcn_perm(__builtin_amdgcn_mov_dpp(x[3], 0xb1, 0xf, 0xf, true), x[3], sel));
        const float ly = __uint_as_float(__builtin_amdgcn_perm(__builtin_amdgcn_mov_dpp(y[3], 0xb1, 0xf, 0xf, true), y[3], sel));
        float M = fmaxf(lx, ly);
        M = fmaxf(M, dpp<0x128>(M));
        M = fmaxf(M, dpp<0x124>(M));
        M = xrow_max(M);
        const float w0 = __builtin_amdgcn_exp2f(lx - M), w1 = __builtin_amdgcn_exp2f(ly - M);
        float a[8];
#pragma unroll
        for (int k = 0; k < 3; ++k) {
            a[2 * k] = w0 * bflo(x[k]) + w1 * bflo(y[k]);
            a[2 * k + 1] = w0 * bfhi(x[k]) + w1 * bfhi(y[k]);
        }
        a[6] = a[7] = w0 + w1;
        float b[4], d[2];
#pragma unroll
        for (int k = 0; k < 4; ++k) b[k] = swap32_add(a[k], a[4 + k]);
#pragma unroll
        for (int k = 0; k < 2; ++k) d[k] = swap16_add(b[k], b[2 + k]);
#pragma unroll
        for (int k = 0; k < 2; ++k) { d[k] += dpp<0x128>(d[k]); d[k] += dpp<0x124>(d[k]); }
        if (c < 4) *reinterpret_cast<f32x2*>(s_red + wave * 28 + (r < 3 ? c * 6 + r * 2 : 24)) = f32x2{d[0], r < 3 ? d[1] : M};
        STAMP(10);
        BARRIER();
        if (tid < 24) {
            f32x2 sm[8];
            float ov[8];
#pragma unroll
            for (int w = 0; w < 8; ++w) { sm[w] = *reinterpret_cast<const f32x2*>(s_red + w * 28 + 24); ov[w] = s_red[w * 28 + tid]; }
            float MM = sm[0][1];
#pragma unroll
            for (int w = 1; w < 8; ++w) MM = fmaxf(MM, sm[w][1]);
            float S = 0.f, o = 0.f;
#pragma unroll
            for (int w = 0; w < 8; ++w) {
                const float ff = __builtin_amdgcn_exp2f(sm[w][1] - MM);
                S += ff * sm[w][0];
                o += ff * ov[w];
            }
            out[bid * 24 + tid] = __float2bfloat16(o * __builtin_amdgcn_rcpf(S));
        }
        if (bid == 0 && wave == 1) __builtin_amdgcn_raw_buffer_store_b16((unsigned short)(tag >> 16), rw_, lane < 4 ? lane * 2 : OOB, WS_EPOCH, 16);
    }
}

template <int X, int PRO, int GATE, int STAG>
static void launch_cfg(void* const* d_in, void* d_out, void* d_ws, hipStream_t stream, float t_us, float start_us, float off_us, float debt_us, float adj_pct, float pub_us) {
    Pace pace;
    pace.step = (unsigned)(t_us * 25600.f / 128.f);
    pace.start = (int)(start_us * 25600.f);
    pace.offset = (int)(off_us * 25600.f);
    pace.bias = (int)(debt_us * 25600.f);
    pace.rd = (int)((start_us + t_us * (129.f / 128.f) + adj_pct) * 25600.f);
    pace.pub = (int)((start_us + t_us * (129.f / 128.f) + pub_us) * 25600.f);
    kv_sweep_kernel<X, PRO, GATE, STAG, 1><<<NWG, 576, 0, stream>>>((const unsigned short*)d_in[0], (const int*)d_in[1], (const unsigned short*)d_in[2],
        (const unsigned short*)d_in[3], (const int*)d_in[4], (const unsigned short*)d_in[5], (const unsigned short*)d_in[6],
        (const unsigned short*)d_in[7], (const unsigned short*)d_in[8], (const unsigned short*)d_in[9], (const unsigned short*)d_in[10],
        (unsigned char*)d_ws, (bf16*)d_out, pace);
}

#define CFG 966, 21, 120, 0
#define PRM 102.5f, 0.5f, 1.0f, 0.05f, 1.8f, 0.8f
extern "C" void kernel_launch(void* const* d_in, const int* in_sizes, int n_in,
                              void* d_out, int out_size, void* d_ws, size_t ws_size,
                              hipStream_t stream) {
    launch_cfg<CFG>(d_in, d_out, d_ws, stream, PRM);
}
```

```cpp
#include <hip/hip_runtime.h>
#include <hip/hip_bf16.h>
#include <cstdint>
#include <type_traits>


using bf16 = __hip_bfloat16;
using u32x4 = __attribute__((ext_vector_type(4))) unsigned;
using u32x2 = __attribute__((ext_vector_type(2))) unsigned;
using f32x2 = __attribute__((ext_vector_type(2))) float;
using f32x4 = __attribute__((ext_vector_type(4))) float;
using bf16x2 = __attribute__((ext_vector_type(2))) __bf16;

constexpr int DH = 192;
constexpr int HALF = 96;
constexpr int NH = 32;
constexpr int SKV = 16384;
constexpr int NWG = 256;
constexpr int NI = 16;
constexpr unsigned ROWB = DH * 4;
constexpr unsigned HEADB = SKV * ROWB;
constexpr unsigned KVB = NH * HEADB;
constexpr unsigned CSB = SKV * DH * 2;
constexpr unsigned OOB = 0x80000000u;
constexpr float SCALE_LOG2 = 1.4426950408889634f * 0.07216878364870322f;
constexpr unsigned SPIN_LIMIT = 20000000u;

constexpr unsigned UNITB = NWG * 64;
constexpr unsigned WS_PART = 0;
constexpr unsigned WS_EPOCH = WS_PART + NWG * (UNITB + 1024);
constexpr unsigned WS_REP = WS_EPOCH + 256;
constexpr unsigned WS_BYTES = WS_REP + 256 * 512;

__device__ __forceinline__ float bflo(unsigned u) { return __uint_as_float(u << 16); }
__device__ __forceinline__ float bfhi(unsigned u) { return __uint_as_float(u & 0xffff0000u); }
__device__ __forceinline__ float rbf(float x) { return __bfloat162float(__float2bfloat16(x)); }
__device__ __forceinline__ unsigned pk_bf16(f32x2 a) { return __builtin_bit_cast(unsigned, __builtin_convertvector(a, bf16x2)); }
__device__ __forceinline__ void dot_cvt(const u32x4 q, const u32x4 t, const u32x4 ki, f32x2& w01, f32x2& w23, f32x2& k01, f32x2& k23) {
    float w0, w1, w2, w3;
    float k0 = __uint_as_float(ki[0]), k1 = __uint_as_float(ki[1]), k2 = __uint_as_float(ki[2]), k3 = __uint_as_float(ki[3]);
    asm("v_dot2_f32_bf16 %0, %8, %12, 0\n\tv_dot2_f32_bf16 %1, %9, %13, 0\n\tv_dot2_f32_bf16 %2, %10, %14, 0\n\tv_dot2_f32_bf16 %3, %11, %15, 0\n\t"
        "v_cvt_f32_i32 %4, %4\n\tv_cvt_f32_i32 %5, %5\n\tv_cvt_f32_i32 %6, %6\n\tv_cvt_f32_i32 %7, %7"
        : "=&v"(w0), "=&v"(w1), "=&v"(w2), "=&v"(w3), "+v"(k0), "+v"(k1), "+v"(k2), "+v"(k3)
        : "v"(q[0]), "v"(q[1]), "v"(q[2]), "v"(q[3]), "v"(t[0]), "v"(t[1]), "v"(t[2]), "v"(t[3]));
    w01 = f32x2{w0, w1}; w23 = f32x2{w2, w3}; k01 = f32x2{k0, k1}; k23 = f32x2{k2, k3};
}

template <int CTRL> __device__ __forceinline__ float dpp(float x) {
    return __builtin_bit_cast(float, __builtin_amdgcn_mov_dpp(__builtin_bit_cast(int, x), CTRL, 0xf, 0xf, true));
}
__device__ __forceinline__ float row16_allsum(float x) {
    x += dpp<0x128>(x);
    x += dpp<0x124>(x);
    x += dpp<0x122>(x);
    x += dpp<0x121>(x);
    return x;
}
__device__ __forceinline__ float row16_allmax(float x) {
    x = fmaxf(x, dpp<0x128>(x));
    x = fmaxf(x, dpp<0x124>(x));
    x = fmaxf(x, dpp<0x122>(x));
    x = fmaxf(x, dpp<0x121>(x));
    return x;
}
__device__ __forceinline__ float xrow_max(float x) {
    auto s = __builtin_amdgcn_permlane16_swap(__float_as_uint(x), __float_as_uint(x), false, false);
    x = fmaxf(__uint_as_float(s[0]), __uint_as_float(s[1]));
    auto t = __builtin_amdgcn_permlane32_swap(__float_as_uint(x), __float_as_uint(x), false, false);
    return fmaxf(__uint_as_float(t[0]), __uint_as_float(t[1]));
}
__device__ __forceinline__ float xrow_sum(float x) {
    auto s = __builtin_amdgcn_permlane16_swap(__float_as_uint(x), __float_as_uint(x), false, false);
    x = __uint_as_float(s[0]) + __uint_as_float(s[1]);
    auto t = __builtin_amdgcn_permlane32_swap(__float_as_uint(x), __float_as_uint(x), false, false);
    return __uint_as_float(t[0]) + __uint_as_float(t[1]);
}
__device__ __forceinline__ float swap32_add(float a, float b) {
    auto t = __builtin_amdgcn_permlane32_swap(__float_as_uint(a), __float_as_uint(b), false, false);
    return __uint_as_float(t[0]) + __uint_as_float(t[1]);
}
__device__ __forceinline__ float swap16_add(float a, float b) {
    auto t = __builtin_amdgcn_permlane16_swap(__float_as_uint(a), __float_as_uint(b), false, false);
    return __uint_as_float(t[0]) + __uint_as_float(t[1]);
}
__device__ __forceinline__ int row16_sum(int x) {
    x += __builtin_amdgcn_mov_dpp(x, 0x128, 0xf, 0xf, true);
    x += __builtin_amdgcn_mov_dpp(x, 0x124, 0xf, 0xf, true);
    x += __builtin_amdgcn_mov_dpp(x, 0x122, 0xf, 0xf, true);
    x += __builtin_amdgcn_mov_dpp(x, 0x121, 0xf, 0xf, true);
    return x;
}
__device__ __forceinline__ unsigned now32() { return (unsigned)__builtin_amdgcn_s_memrealtime(); }

#define PIN() do { asm volatile("" ::: "memory"); __builtin_amdgcn_sched_barrier(0); } while (0)
#define BARRIER() do { asm volatile("s_waitcnt lgkmcnt(0)" ::: "memory"); __builtin_amdgcn_s_barrier(); PIN(); } while (0)
#define VMCNT(n) asm volatile("s_waitcnt vmcnt(%0)" :: "i"(n) : "memory")

typedef volatile __attribute__((address_space(3))) unsigned vlds_u32;
typedef volatile __attribute__((address_space(3))) u32x2 vlds_u32x2;
typedef volatile __attribute__((address_space(3))) u32x4 vlds_u32x4;

struct Pace {
    unsigned step;
    int start;
    int offset;
    int bias;
    int rd;
    int pub;
};

template <int X, int PRO_, int GATE, int STAG_, int SLP>
__global__ __launch_bounds__(576) void kv_sweep_kernel(
    const unsigned short* q, const int* qk, const unsigned short* ksc,
    const unsigned short* kzp, const int* qv, const unsigned short* vsc,
    const unsigned short* vzp, const unsigned short* q_cos, const unsigned short* q_sin,
    const unsigned short* k_cos, const unsigned short* k_sin, unsigned char* ws, bf16* out, Pace pace)
{
    __shared__ __attribute__((aligned(16))) unsigned s_q[NH * DH];
    __shared__ __attribute__((aligned(16))) float s_sc[NH * 64 * 4];
    __shared__ __attribute__((aligned(16))) float s_part[8 * NI * DH];
    __shared__ __attribute__((aligned(16))) float s_red[8 * 28];
    __shared__ __attribute__((aligned(16))) float s_f[NH * 8];
    __shared__ __attribute__((aligned(16))) unsigned s_done[8];
    __shared__ __attribute__((aligned(16))) unsigned s_sync[8];
    __shared__ __attribute__((aligned(16))) unsigned s_act[16];

    constexpr bool TRABS = true;
    constexpr int ORD = GATE % 10, WID = GATE / 10;
    constexpr int PRO = PRO_ % 10, PF = PRO_ / 10, STAG = STAG_ % 10, LAY = 0, VS = STAG_ / 10 ? 2 : 3;
    constexpr unsigned PSTR = LAY == 0 ? 64 : LAY == 1 ? UNITB + 256 : LAY == 2 ? UNITB + 128 : UNITB;
    constexpr unsigned USTR = LAY == 0 ? UNITB : 64;
    unsigned long long pc0;
    asm volatile("s_getpc_b64 %0" : "=s"(pc0));
    const unsigned tstart = now32();
    const int tid = threadIdx.x;
    const int lane = tid & 63;
    const int wave = __builtin_amdgcn_readfirstlane(tid >> 6);
    const int r = lane >> 4;
    const int c = lane & 15;
    const int bid = blockIdx.x;
    const int m = (bid & 7) * 32 + (bid >> 3);
    const auto rw_ = __builtin_amdgcn_make_buffer_rsrc((void*)ws, 0, WS_BYTES, 0x00020000);

    auto merge = [&](int i, int hp, int c6, unsigned tag) {
        const f32x4 f = *reinterpret_cast<const f32x4*>(s_f + (2 * i + hp) * 8);
        const f32x2 zl = *reinterpret_cast<const f32x2*>(s_f + (2 * i + hp) * 8 + 4);
        const float zs = zl[0];
        f32x2 a[3];
#pragma unroll
        for (int k = 0; k < 3; ++k) a[k] = f32x2{-zs, -zs};
#pragma unroll
        for (int w = 0; w < 4; ++w) {
            const float* p = s_part + ((hp * 4 + w) * NI + i) * DH + c6 * 6;
#pragma unroll
            for (int k = 0; k < 3; ++k) a[k] += f[w] * *reinterpret_cast<const f32x2*>(p + 2 * k);
        }
        return u32x4{pk_bf16(a[0]), pk_bf16(a[1]), pk_bf16(a[2]), tag | ((__float_as_uint(zl[1]) >> (16 * (c6 & 1))) & 0xffffu)};
    };
    auto publish = [&](u32x4 v, int i, int hp, int c6, bool on) {
        __builtin_amdgcn_raw_buffer_store_b128(v, rw_, on ? (hp * 8 + (c6 >> 2)) * USTR + (c6 & 3) * 16 : OOB, WS_PART + i * 16 * USTR + m * PSTR, 16);
        asm volatile("s_nop 1" :: "v"(v) : "memory");
    };

    if (wave == 8) {
        unsigned long long pc1;
        asm volatile("s_getpc_b64 %0" : "=s"(pc1));
        pc0 &= ~255ull;
        const auto rcode = __builtin_amdgcn_make_buffer_rsrc((void*)pc0, 0, (unsigned)(pc1 - pc0), 0x00020000);
        auto warm_code = [&]() {
            unsigned x[2];
#pragma unroll
            for (int k = 0; k < 2; ++k) x[k] = __builtin_amdgcn_raw_buffer_load_b32(rcode, lane * 128 + k * 8192, 0, 0);
            asm volatile("s_waitcnt vmcnt(0)" :: "v"(x[0]), "v"(x[1]) : "memory");
        };
        if (PF) warm_code();
        const unsigned tag = ((unsigned)__builtin_amdgcn_raw_buffer_load_b16(rw_, (lane & 3) * 2, WS_EPOCH, 16) + 1u) << 16;
        unsigned origin = (tstart << 8) + pace.offset;
        if (lane < 8) { s_done[lane] = 0; s_sync[lane] = lane == 2 ? origin : lane < 4 ? 0 : tag; }
        PIN();
        BARRIER();
        const int mh = lane >> 5, mc = lane & 31;
        const unsigned rtag = __builtin_amdgcn_readlane(tag, 0) | __builtin_amdgcn_readlane(tag, 1) >> 16;
        constexpr int RPX = 2;
        constexpr int RSH = X / 100;
        constexpr unsigned RDLY = 200;
        constexpr int UP = X / 10 % 10 ? X / 10 % 10 * 82 : 1 << 24, DN = X % 10 ? X % 10 * 82 : 1 << 24;
        const int rj = ((bid >> 3) + 5 * (bid & 7)) & 31;
        const int rid = rj % (32 / RPX) == 0 ? rj / (32 / RPX) * 8 + (bid & 7) : -1;
        unsigned wround = now32() >> RSH, rround = wround, tick = 0, target = origin;
        bool warmed = false;
        int i = 0;
        auto kept_now = [&](unsigned now) {
            const u32x2 a = *(vlds_u32x2*)(s_act + 2 * (lane & 7));
            const int late = max((int)(a[0] - origin), (int)((now << 8) - a[1] - origin));
            return origin + (__builtin_amdgcn_readfirstlane(row16_sum(late)) >> 4);
        };
        while (i < NI - 1) {
            const unsigned now = now32();
            if ((now >> RSH) != wround && i < NI - 2) {
                wround = now >> RSH;
                if (rid >= 0 && (now & ((1u << RSH) - 1)) < 48)
                    __builtin_amdgcn_raw_buffer_store_b64(u32x2{kept_now(now), rtag + wround}, rw_, lane == 0 ? rid * 8 : OOB, WS_REP + (wround & 255) * 512, 16);
                PIN();
                continue;
            }
            if ((int)(((now - RDLY) >> RSH) - rround) > 0 && i < NI - 2) {
                rround = (now - RDLY) >> RSH;
                const u32x2 x = __builtin_amdgcn_raw_buffer_load_b64(rw_, c * 8, WS_REP + (rround & 255) * 512, 16);
                const int d = (int)(x[0] - origin);
                const bool ok = x[1] == rtag + rround && d < (32 << 16) && d > -(32 << 16);
                const int n = __builtin_popcountll(__builtin_amdgcn_ballot_w64(ok));
                if (n >= 48) {
                    target = origin + __builtin_amdgcn_readfirstlane((int)(row16_allsum(ok ? (float)d : 0.f) * 4.f / (float)n)) - pace.bias;
                    const int lost = min((int)(kept_now(now32()) - origin) - pace.bias, (int)(target - origin));
                    if (lost > 0) origin += lost;
                }
                PIN();
                continue;
            }
            if ((now >> 5) != tick) {
                tick = now >> 5;
                origin += max(-DN, min(UP, (int)(target - origin)));
                if (lane == 0) *(vlds_u32*)(s_sync + 2) = origin;
                PIN();
            }
            if (PF >= 2 && !warmed && (int)((now << 8) - origin) > 60 * (int)pace.step) { warm_code(); warmed = true; continue; }
            const u32x4 a = *(vlds_u32x4*)s_done, b = *(vlds_u32x4*)(s_done + 4);
            const unsigned mn = min(min(min(a[0], a[1]), min(a[2], a[3])), min(min(b[0], b[1]), min(b[2], b[3])));
            if (__builtin_amdgcn_readfirstlane(mn) <= (unsigned)i) { __builtin_amdgcn_s_sleep(2); continue; }
            PIN();
            if (i == 0) {
                f32x4 ml[4], f;
#pragma unroll
                for (int w = 0; w < 4; ++w) ml[w] = *reinterpret_cast<const f32x4*>(s_sc + (mc * 4 + w) * 64 + 16);
                const float M = fmaxf(fmaxf(ml[0][0], ml[1][0]), fmaxf(ml[2][0], ml[3][0]));
                float L = 0.f, zs = 0.f;
#pragma unroll
                for (int w = 0; w < 4; ++w) { f[w] = __builtin_amdgcn_exp2f(ml[w][0] - M); L += f[w] * ml[w][1]; zs += f[w] * ml[w][2]; }
                const float inv = 1.f / L;
                *reinterpret_cast<f32x4*>(s_f + mc * 8) = f * inv;
                *reinterpret_cast<f32x2*>(s_f + mc * 8 + 4) = f32x2{zs * inv, M + __builtin_amdgcn_logf(L)};
                PIN();
                if (lane == 0) *(vlds_u32*)(s_sync + 3) = 1;
                PIN();
            }
            if (PF >= 2 && i == NI - 3) warm_code();
            const u32x4 v = merge(i, mh, mc, tag);
            PIN();
            if (pace.pub < 0) publish(v, i, mh, mc, true);
            else *reinterpret_cast<u32x4*>(s_part + (mh * 4 * NI + i) * DH + mc * 4) = v;
            PIN();
            ++i;
        }
        u32x4 v[NI - 1];
#pragma unroll
        for (int i = 0; i < NI - 1; ++i) { v[i] = *reinterpret_cast<const u32x4*>(s_part + (mh * 4 * NI + i) * DH + mc * 4); asm volatile("" : "+v"(v[i])); }
        while ((int)((now32() << 8) - origin) < pace.pub) __builtin_amdgcn_s_sleep(1);
        PIN();
#pragma unroll
        for (int i = 0; i < NI - 1; ++i) publish(v[i], i, mh, mc, pace.pub >= 0);
        return;
    }

    const int rw = wave & 3, hp = wave >> 2;
    const int row0 = m * 64 + rw * 16;

    const unsigned sidx = STAG == 0 ? hp * 1024 + m * 4 + rw : STAG == 1 ? m * 8 + wave : (hp * 1024 + (m & 31) * 32 + (m >> 5) * 4 + rw);
    unsigned off = pace.start + ((pace.step * sidx) >> 11);
    unsigned origin = tstart << 8, kept = 0;
    bool wide = false;
    const unsigned step = pace.step;
    auto pace_wait = [&]() {
        const unsigned tgt = __builtin_amdgcn_readfirstlane(origin) + off;
        unsigned now = now32() << 8;
        kept = ((int)(now - tgt) > 0 ? now : tgt) - off;
        wide = (int)(now - tgt) > 30 * 256;
        while ((int)(tgt - now) > 0) { if (SLP) __builtin_amdgcn_s_sleep(SLP); now = now32() << 8; }
        off += step;
    };
    auto report = [&]() {
        if (lane == 0) *(vlds_u32x2*)(s_act + 2 * wave) = u32x2{kept, off};
        origin = *(vlds_u32*)(s_sync + 2);
        PIN();
    };

    const int qe = min(tid, 383);
    const int qh = qe / 12, qd = (qe % 12) * 8;
    u32x4 lq[2], lc[2], ls[2];
    auto load_qs = [&]() {
#pragma unroll
        for (int k = 0; k < 2; ++k) {
            lq[k] = *reinterpret_cast<const u32x4*>(q + qh * DH + k * HALF + qd);
            lc[k] = *reinterpret_cast<const u32x4*>(q_cos + k * HALF + qd);
            ls[k] = *reinterpret_cast<const u32x4*>(q_sin + k * HALF + qd);
        }
        PIN();
    };
    u32x2 lsc[4];
    auto load_sc = [&]() {
#pragma unroll
        for (int a = 0; a < 4; ++a) {
            const unsigned short* base = a == 0 ? ksc : a == 1 ? kzp : a == 2 ? vsc : vzp;
            lsc[a] = __builtin_nontemporal_load(reinterpret_cast<const u32x2*>(base + (size_t)(tid >> 4) * SKV + m * 64 + (tid & 15) * 4));
        }
        PIN();
    };

    const auto rc = __builtin_amdgcn_make_buffer_rsrc((void*)k_cos, 0, CSB, 0x00020000);
    const auto rs = __builtin_amdgcn_make_buffer_rsrc((void*)k_sin, 0, CSB, 0x00020000);
    u32x2 raw[2][6];
    auto load_cs = [&](int t) {
        const unsigned co = r * 384 + c * 8;
        const unsigned so1 = r * 384 + (c < 8 ? c * 8 + 320 : c * 8 - 64);
        const unsigned rb = (row0 + (t + 2 * hp) * 4) * 384u;
        raw[t][0] = __builtin_amdgcn_raw_buffer_load_b64(rc, co, rb, 2);
        raw[t][1] = __builtin_amdgcn_raw_buffer_load_b64(rc, co + 128, rb, 2);
        raw[t][2] = __builtin_amdgcn_raw_buffer_load_b64(rc, co + 256, rb, 2);
        raw[t][3] = __builtin_amdgcn_raw_buffer_load_b64(rs, co + 192, rb, 2);
        raw[t][4] = __builtin_amdgcn_raw_buffer_load_b64(rs, so1, rb, 2);
        raw[t][5] = __builtin_amdgcn_raw_buffer_load_b64(rs, co + 64, rb, 2);
        PIN();
    };
    u32x4 tab[4][3];
    auto pack_cs = [&](int t) {
#pragma unroll
        for (int j = 0; j < 3; ++j) {
#pragma unroll
            for (int k = 0; k < 2; ++k) {
                tab[t][j][2 * k] = __builtin_amdgcn_perm(raw[t][3 + j][k], raw[t][j][k], 0x05040100);
                tab[t][j][2 * k + 1] = __builtin_amdgcn_perm(raw[t][3 + j][k], raw[t][j][k], 0x07060302);
            }
            *reinterpret_cast<u32x4*>(s_part + (wave ^ 4) * (NI * DH) + ((t * 3 + j) * 64 + lane) * 4) = tab[t][j];
        }
        PIN();
    };
    const auto rk = __builtin_amdgcn_make_buffer_rsrc((void*)qk, 0, KVB, 0x00020000);
    const auto rv = __builtin_amdgcn_make_buffer_rsrc((void*)qv, 0, KVB, 0x00020000);
    const unsigned voff = r * ROWB + c * 16;
    const unsigned woff = row0 * ROWB + hp * HEADB;
    auto toff = [&](int t) { return (unsigned)(t ^ (2 * hp)) * (4 * ROWB); };
    u32x4 ring[4][3];
    auto ld = [&](int slot, int j, __amdgpu_buffer_rsrc_t rr, unsigned off) {
        ring[slot][j] = __builtin_amdgcn_raw_buffer_load_b128(rr, voff + j * 256, off, 2);
        PIN();
    };
    auto ld_tile = [&](int slot, __amdgpu_buffer_rsrc_t rr, unsigned off) { ld(slot, 0, rr, off); ld(slot, 1, rr, off); ld(slot, 2, rr, off); };
    auto refill = [&](int slot, int j, __amdgpu_buffer_rsrc_t rr, unsigned goff, bool on) {
        if (ORD == 0) {
            if (on) { if (WID && wide) VMCNT(2 + WID); else VMCNT(2); if (j == 0) pace_wait(); }
            ld(slot, j, rr, goff);
        } else {
            if (on) { if (j == 0) pace_wait(); if (WID && wide) VMCNT(2 + WID); else VMCNT(2); }
            ld(slot, j, rr, goff);
            if (on && j == 0) { const unsigned t = (now32() << 8) - 1024 - (off - step); if ((int)(t - kept) > 0) kept = t; }
        }
        if (j == 2) report();
    };
    auto q_to_lds = [&]() {
        float o[2][8];
#pragma unroll
        for (int k = 0; k < 2; ++k)
#pragma unroll
            for (int u = 0; u < 4; ++u) {
                const unsigned pp = lq[k ^ 1][u] ^ (k == 0 ? 0x80008000u : 0u);
                o[k][2 * u] = rbf(rbf(bflo(lq[k][u]) * bflo(lc[k][u])) + rbf(bflo(pp) * bflo(ls[k][u])));
                o[k][2 * u + 1] = rbf(rbf(bfhi(lq[k][u]) * bfhi(lc[k][u])) + rbf(bfhi(pp) * bfhi(ls[k][u])));
            }
#pragma unroll
        for (int v = 0; v < 2; ++v) {
            u32x4 lo, hi;
#pragma unroll
            for (int e = 0; e < 4; ++e) {
                lo[e] = __builtin_amdgcn_perm(__float_as_uint(o[1][4 * v + e]), __float_as_uint(o[0][4 * v + e]), 0x07060302);
                hi[e] = __builtin_amdgcn_perm(__float_as_uint(o[0][4 * v + e]) ^ 0x80000000u, __float_as_uint(o[1][4 * v + e]), 0x07060302);
            }
            *reinterpret_cast<u32x4*>(s_q + qh * DH + qd + 4 * v) = lo;
            *reinterpret_cast<u32x4*>(s_q + qh * DH + HALF + qd + 4 * v) = hi;
        }
        PIN();
    };
    auto sc_to_lds = [&]() {
        float* dst = s_sc + tid * 16;
#pragma unroll
        for (int u = 0; u < 2; ++u) {
            *reinterpret_cast<f32x4*>(dst + 8 * u) = f32x4{bflo(lsc[0][u]) * SCALE_LOG2, bflo(lsc[1][u]), bflo(lsc[2][u]), bflo(lsc[3][u])};
            *reinterpret_cast<f32x4*>(dst + 8 * u + 4) = f32x4{bfhi(lsc[0][u]) * SCALE_LOG2, bfhi(lsc[1][u]), bfhi(lsc[2][u]), bfhi(lsc[3][u])};
        }
        PIN();
    };

    auto k0 = [&](int t) { pace_wait(); ld_tile(t, rk, woff + toff(t)); };
    if (PRO == 0) {
        load_qs(); load_sc(); load_cs(0);
        pace_wait(); load_cs(1);
        q_to_lds(); sc_to_lds();
        k0(0); pack_cs(0);
        k0(1); pack_cs(1);
        VMCNT(3); k0(2);
        VMCNT(3); k0(3);
    } else if (PRO == 1) {
        load_cs(0); load_sc();
        pace_wait(); load_cs(1); if (wave < 6) load_qs();
        pack_cs(0); sc_to_lds();
        k0(0); pack_cs(1); if (wave < 6) q_to_lds();
        k0(1);
        VMCNT(3); k0(2);
        VMCNT(3); k0(3);
    } else if (PRO == 2) {
        load_cs(0); load_sc();
        pace_wait(); load_cs(1);
        pack_cs(0); sc_to_lds();
        k0(0); if (wave < 6) load_qs(); pack_cs(1);
        k0(1); if (wave < 6) q_to_lds();
        VMCNT(3); k0(2);
        VMCNT(3); k0(3);
    } else if (PRO == 3) {
        load_cs(0); load_sc();
        pace_wait(); load_cs(1);
        pack_cs(0); sc_to_lds();
        k0(0); pack_cs(1);
        k0(1); if (wave < 6) load_qs();
        VMCNT(9); k0(2); if (wave < 6) q_to_lds();
        VMCNT(3); k0(3);
    }
    if (lane == 0) *(vlds_u32x2*)(s_act + 2 * wave) = u32x2{kept, off};
    BARRIER();
    origin = *(vlds_u32*)(s_sync + 2);

    u32x4 qp[3];
    auto load_q = [&](int h, int j) { qp[j] = *reinterpret_cast<const u32x4*>(s_q + h * DH + 64 * j + 4 * c); };
    load_q(hp, 0); load_q(hp, 1); load_q(hp, 2);
#pragma unroll
    for (int t = 0; t < 2; ++t)
#pragma unroll
        for (int j = 0; j < 3; ++j) tab[2 + t][j] = *reinterpret_cast<const u32x4*>(s_part + wave * (NI * DH) + ((t * 3 + j) * 64 + lane) * 4);

    for (int i = 0; i < NI; ++i) {
        const bool last = i + 1 == NI;
        const int h = 2 * i + hp;
        const unsigned hnext = (i + 1) * (2 * HEADB) + woff;
        const auto rn = __builtin_amdgcn_make_buffer_rsrc((void*)(last ? qv : qk), 0, KVB, 0x00020000);
        float* scp = s_sc + (h * 4 + rw) * 64;
        f32x4 sc[4];
#pragma unroll
        for (int t = 0; t < 4; ++t) sc[t] = *reinterpret_cast<const f32x4*>(scp + (4 * (t ^ (2 * hp)) + r) * 4);
        float s[4];
#pragma unroll
        for (int t = 0; t < 4; ++t) {
            f32x2 dot, wsum;
#pragma unroll
            for (int j = 0; j < 3; ++j) {
                f32x2 w01, w23, k01, k23;
                dot_cvt(qp[j], tab[t][j], ring[t][j], w01, w23, k01, k23);
                if (j == 0) { wsum = w01 + w23; dot = k01 * w01; } else { wsum += w01; wsum += w23; dot += k01 * w01; }
                dot += k23 * w23;
                asm volatile("" : "+v"(dot), "+v"(wsum));
                PIN();
                if (t == 3) load_q(last ? h : h + 2, j);
                if (t == 0 || t == VS) refill(t, j, rn, last ? woff + toff(t == VS ? 1 : 0) : hnext + toff(t), true);
                else refill(t, j, rk, last ? OOB : hnext + toff(t), !last);
            }
            const float a = (dot[0] + dot[1]) - sc[t][1] * (wsum[0] + wsum[1]);
            s[t] = row16_allsum(a) * sc[t][0];
        }
        const float mx = xrow_max(fmaxf(fmaxf(s[0], s[1]), fmaxf(s[2], s[3])));
        float l = 0.f, z = 0.f;
#pragma unroll
        for (int t = 0; t < 4; ++t) {
            const float p = __builtin_amdgcn_exp2f(s[t] - mx);
            l += p;
            s[t] = p * sc[t][2];
            z += s[t] * sc[t][3];
        }
        l = xrow_sum(l);
        z = xrow_sum(z);
        if (c == 0) {
            *reinterpret_cast<f32x4*>(scp + r * 4) = f32x4{s[0], s[1], s[2], s[3]};
            *reinterpret_cast<f32x4*>(scp + 16) = f32x4{mx, l, z, 0.f};
        }
        PIN();
    }

    const float* pwp = s_sc + (hp * 4 + rw) * 64 + r * 4;
    f32x4 pw = *reinterpret_cast<const f32x4*>(pwp);
    for (int i = 0; i < NI; ++i) {
        const bool last = i + 1 == NI;
        const unsigned hoff = i * (2 * HEADB) + woff;
        f32x2 acc[3][2];
#pragma unroll
        for (int t = 0; t < 4; ++t) {
            const f32x2 p2 = {pw[t], pw[t]};
#pragma unroll
            for (int j = 0; j < 3; ++j) {
                const u32x4 vv = ring[(t & 1) * VS][j];
                const f32x2 v01 = {(float)(int)vv[0], (float)(int)vv[1]}, v23 = {(float)(int)vv[2], (float)(int)vv[3]};
                if (t == 0) { acc[j][0] = p2 * v01; acc[j][1] = p2 * v23; }
                else { acc[j][0] += p2 * v01; acc[j][1] += p2 * v23; }
                asm volatile("" : "+v"(acc[j][0]), "+v"(acc[j][1]));
                PIN();
                if (t == 3 && j == 2) pw = *reinterpret_cast<const f32x4*>(pwp + (last ? i : i + 1) * 512);
                if (t < 2) refill((t & 1) * VS, j, rv, hoff + toff(t + 2), true);
                else refill((t & 1) * VS, j, rv, last ? OOB : hoff + 2 * HEADB + toff(t - 2), !last);
            }
        }
        float* sl = s_part + (wave * NI + i) * DH;
#pragma unroll
        for (int j = 0; j < 3; ++j) {
            const float x = swap32_add(acc[j][0][0], acc[j][1][0]);
            const float y = swap32_add(acc[j][0][1], acc[j][1][1]);
            sl[64 * j + 4 * c + r] = swap16_add(x, y);
        }
        PIN();
        if (lane == 0) *(vlds_u32*)(s_done + wave) = i + 1;
        PIN();
    }
    unsigned old = 0;
    if (lane == 0) old = __hip_atomic_fetch_add(s_sync + hp, 1u, __ATOMIC_RELAXED, __HIP_MEMORY_SCOPE_WORKGROUP);
    PIN();
    const unsigned tag = s_sync[4 + (lane & 3)];
    if (__builtin_amdgcn_readfirstlane(old) == 3) {
        while (__builtin_amdgcn_readfirstlane(*(vlds_u32*)(s_sync + 3)) == 0) __builtin_amdgcn_s_sleep(1);
        PIN();
        publish(merge(NI - 1, hp, lane & 31, tag), NI - 1, hp, lane & 31, lane < 32);
        PIN();
    }
    {
        const unsigned o = __builtin_amdgcn_readfirstlane(origin);
        while ((int)((now32() << 8) - o) < pace.rd) __builtin_amdgcn_s_sleep(1);
        PIN();
    }
    {
        u32x4 x, y;
        unsigned xo = (tid >> 2) * PSTR + (tid & 3) * 16, yo = xo + 128 * PSTR;
        for (;;) {
            const u32x4 xn = __builtin_amdgcn_raw_buffer_load_b128(rw_, xo, WS_PART + bid * USTR, 16);
            const u32x4 yn = __builtin_amdgcn_raw_buffer_load_b128(rw_, yo, WS_PART + bid * USTR, 16);
            unsigned bad = (xo != OOB && (xn[3] ^ tag) > 0xffffu ? 1u : 0u) | (yo != OOB && (yn[3] ^ tag) > 0xffffu ? 2u : 0u);
            bad |= __builtin_amdgcn_mov_dpp(bad, 0xb1, 0xf, 0xf, true);
            bad |= __builtin_amdgcn_mov_dpp(bad, 0x4e, 0xf, 0xf, true);
            const bool tx = xo != OOB, ty = yo != OOB;
#pragma unroll
            for (int k = 0; k < 4; ++k) { x[k] = tx ? xn[k] : x[k]; y[k] = ty ? yn[k] : y[k]; }
            xo = bad & 1 ? xo : OOB;
            yo = bad & 2 ? yo : OOB;
            if (__builtin_amdgcn_ballot_w64(bad != 0) == 0 || now32() - tstart > SPIN_LIMIT) break;
            PIN();
        }
        const unsigned sel = lane & 1 ? 0x01000504u : 0x05040100u;
        const float lx = __uint_as_float(__builtin_amdgcn_perm(__builtin_amdgcn_mov_dpp(x[3], 0xb1, 0xf, 0xf, true), x[3], sel));
        const float ly = __uint_as_float(__builtin_amdgcn_perm(__builtin_amdgcn_mov_dpp(y[3], 0xb1, 0xf, 0xf, true), y[3], sel));
        float M = fmaxf(lx, ly);
        M = fmaxf(M, dpp<0x128>(M));
        M = fmaxf(M, dpp<0x124>(M));
        M = xrow_max(M);
        const float w0 = __builtin_amdgcn_exp2f(lx - M), w1 = __builtin_amdgcn_exp2f(ly - M);
        float a[8];
#pragma unroll
        for (int k = 0; k < 3; ++k) {
            a[2 * k] = w0 * bflo(x[k]) + w1 * bflo(y[k]);
            a[2 * k + 1] = w0 * bfhi(x[k]) + w1 * bfhi(y[k]);
        }
        a[6] = a[7] = w0 + w1;
        float b[4], d[2];
#pragma unroll
        for (int k = 0; k < 4; ++k) b[k] = swap32_add(a[k], a[4 + k]);
#pragma unroll
        for (int k = 0; k < 2; ++k) d[k] = swap16_add(b[k], b[2 + k]);
#pragma unroll
        for (int k = 0; k < 2; ++k) { d[k] += dpp<0x128>(d[k]); d[k] += dpp<0x124>(d[k]); }
        if (c < 4) *reinterpret_cast<f32x2*>(s_red + wave * 28 + (r < 3 ? c * 6 + r * 2 : 24)) = f32x2{d[0], r < 3 ? d[1] : M};
        BARRIER();
        if (tid < 24) {
            f32x2 sm[8];
            float ov[8];
#pragma unroll
            for (int w = 0; w < 8; ++w) { sm[w] = *reinterpret_cast<const f32x2*>(s_red + w * 28 + 24); ov[w] = s_red[w * 28 + tid]; }
            float MM = sm[0][1];
#pragma unroll
            for (int w = 1; w < 8; ++w) MM = fmaxf(MM, sm[w][1]);
            float S = 0.f, o = 0.f;
#pragma unroll
            for (int w = 0; w < 8; ++w) {
                const float ff = __builtin_amdgcn_exp2f(sm[w][1] - MM);
                S += ff * sm[w][0];
                o += ff * ov[w];
            }
            out[bid * 24 + tid] = __float2bfloat16(o * __builtin_amdgcn_rcpf(S));
        }
        if (bid == 0 && wave == 1) __builtin_amdgcn_raw_buffer_store_b16((unsigned short)(tag >> 16), rw_, lane < 4 ? lane * 2 : OOB, WS_EPOCH, 16);
    }
}

template <int X, int PRO, int GATE, int STAG>
static void launch_cfg(void* const* d_in, void* d_out, void* d_ws, hipStream_t stream, float t_us, float start_us, float off_us, float debt_us, float adj_pct, float pub_us) {
    Pace pace;
    pace.step = (unsigned)(t_us * 25600.f / 128.f);
    pace.start = (int)(start_us * 25600.f);
    pace.offset = (int)(off_us * 25600.f);
    pace.bias = (int)(debt_us * 25600.f);
    pace.rd = (int)((start_us + t_us * (129.f / 128.f) + adj_pct) * 25600.f);
    pace.pub = (int)((start_us + t_us * (129.f / 128.f) + pub_us) * 25600.f);
    kv_sweep_kernel<X, PRO, GATE, STAG, 1><<<NWG, 576, 0, stream>>>((const unsigned short*)d_in[0], (const int*)d_in[1], (const unsigned short*)d_in[2],
        (const unsigned short*)d_in[3], (const int*)d_in[4], (const unsigned short*)d_in[5], (const unsigned short*)d_in[6],
        (const unsigned short*)d_in[7], (const unsigned short*)d_in[8], (const unsigned short*)d_in[9], (const unsigned short*)d_in[10],
        (unsigned char*)d_ws, (bf16*)d_out, pace);
}

extern "C" void kernel_launch(void* const* d_in, const int* in_sizes, int n_in,
                              void* d_out, int out_size, void* d_ws, size_t ws_size,
                              hipStream_t stream) {
    launch_cfg<966, 21, 1, 10>(d_in, d_out, d_ws, stream, 102.5f, 0.5f, 1.0f, 0.05f, 1.8f, 0.8f);
}
```

```cpp
#include <hip/hip_runtime.h>
#include <hip/hip_bf16.h>
#include <cstdint>
#include <type_traits>

using bf16 = __hip_bfloat16;
using u32x4 = __attribute__((ext_vector_type(4))) unsigned;
using u32x2 = __attribute__((ext_vector_type(2))) unsigned;
using f32x2 = __attribute__((ext_vector_type(2))) float;
using f32x4 = __attribute__((ext_vector_type(4))) float;
using bf16x2 = __attribute__((ext_vector_type(2))) __bf16;

constexpr int DH = 192;
constexpr int HALF = 96;
constexpr int NH = 32;
constexpr int SKV = 16384;
constexpr int NWG = 256;
constexpr int NI = 16;
constexpr unsigned ROWB = DH * 4;
constexpr unsigned HEADB = SKV * ROWB;
constexpr unsigned KVB = NH * HEADB;
constexpr unsigned CSB = SKV * DH * 2;
constexpr unsigned OOB = 0x80000000u;
constexpr float SCALE_LOG2 = 1.4426950408889634f * 0.07216878364870322f;
constexpr unsigned SPIN_LIMIT = 20000000u;

constexpr unsigned UNITB = NWG * 64;
constexpr unsigned WS_PART = 0;
constexpr unsigned WS_EPOCH = NWG * (UNITB + 1024);
constexpr unsigned WS_REP = WS_EPOCH + 256;
constexpr unsigned WS_BYTES = WS_REP + 256 * 512;

__device__ __forceinline__ float bflo(unsigned u) { return __uint_as_float(u << 16); }
__device__ __forceinline__ float bfhi(unsigned u) { return __uint_as_float(u & 0xffff0000u); }
__device__ __forceinline__ float rbf(float x) { return __bfloat162float(__float2bfloat16(x)); }
__device__ __forceinline__ unsigned pk_bf16(f32x2 a) { return __builtin_bit_cast(unsigned, __builtin_convertvector(a, bf16x2)); }
__device__ __forceinline__ void dot_cvt(const u32x4 q, const u32x4 t, const u32x4 ki, f32x2& w01, f32x2& w23, f32x2& k01, f32x2& k23) {
    float w0, w1, w2, w3;
    float k0 = __uint_as_float(ki[0]), k1 = __uint_as_float(ki[1]), k2 = __uint_as_float(ki[2]), k3 = __uint_as_float(ki[3]);
    asm("v_dot2_f32_bf16 %0, %8, %12, 0\n\tv_dot2_f32_bf16 %1, %9, %13, 0\n\tv_dot2_f32_bf16 %2, %10, %14, 0\n\tv_dot2_f32_bf16 %3, %11, %15, 0\n\t"
        "v_cvt_f32_i32 %4, %4\n\tv_cvt_f32_i32 %5, %5\n\tv_cvt_f32_i32 %6, %6\n\tv_cvt_f32_i32 %7, %7"
        : "=&v"(w0), "=&v"(w1), "=&v"(w2), "=&v"(w3), "+v"(k0), "+v"(k1), "+v"(k2), "+v"(k3)
        : "v"(q[0]), "v"(q[1]), "v"(q[2]), "v"(q[3]), "v"(t[0]), "v"(t[1]), "v"(t[2]), "v"(t[3]));
    w01 = f32x2{w0, w1}; w23 = f32x2{w2, w3}; k01 = f32x2{k0, k1}; k23 = f32x2{k2, k3};
}

template <int CTRL> __device__ __forceinline__ float dpp(float x) {
    return __builtin_bit_cast(float, __builtin_amdgcn_mov_dpp(__builtin_bit_cast(int, x), CTRL, 0xf, 0xf, true));
}
__device__ __forceinline__ float row16_allsum(float x) {
    x += dpp<0x128>(x);
    x += dpp<0x124>(x);
    x += dpp<0x122>(x);
    x += dpp<0x121>(x);
    return x;
}
__device__ __forceinline__ float row16_allmax(float x) {
    x = fmaxf(x, dpp<0x128>(x));
    x = fmaxf(x, dpp<0x124>(x));
    x = fmaxf(x, dpp<0x122>(x));
    x = fmaxf(x, dpp<0x121>(x));
    return x;
}
__device__ __forceinline__ float xrow_max(float x) {
    auto s = __builtin_amdgcn_permlane16_swap(__float_as_uint(x), __float_as_uint(x), false, false);
    x = fmaxf(__uint_as_float(s[0]), __uint_as_float(s[1]));
    auto t = __builtin_amdgcn_permlane32_swap(__float_as_uint(x), __float_as_uint(x), false, false);
    return fmaxf(__uint_as_float(t[0]), __uint_as_float(t[1]));
}
__device__ __forceinline__ float xrow_sum(float x) {
    auto s = __builtin_amdgcn_permlane16_swap(__float_as_uint(x), __float_as_uint(x), false, false);
    x = __uint_as_float(s[0]) + __uint_as_float(s[1]);
    auto t = __builtin_amdgcn_permlane32_swap(__float_as_uint(x), __float_as_uint(x), false, false);
    return __uint_as_float(t[0]) + __uint_as_float(t[1]);
}
__device__ __forceinline__ float swap32_add(float a, float b) {
    auto t = __builtin_amdgcn_permlane32_swap(__float_as_uint(a), __float_as_uint(b), false, false);
    return __uint_as_float(t[0]) + __uint_as_float(t[1]);
}
__device__ __forceinline__ float swap16_add(float a, float b) {
    auto t = __builtin_amdgcn_permlane16_swap(__float_as_uint(a), __float_as_uint(b), false, false);
    return __uint_as_float(t[0]) + __uint_as_float(t[1]);
}
__device__ __forceinline__ int row16_sum(int x) {
    x += __builtin_amdgcn_mov_dpp(x, 0x128, 0xf, 0xf, true);
    x += __builtin_amdgcn_mov_dpp(x, 0x124, 0xf, 0xf, true);
    x += __builtin_amdgcn_mov_dpp(x, 0x122, 0xf, 0xf, true);
    x += __builtin_amdgcn_mov_dpp(x, 0x121, 0xf, 0xf, true);
    return x;
}
__device__ __forceinline__ unsigned now32() { return (unsigned)__builtin_amdgcn_s_memrealtime(); }

#define NSTAMP 12
#define STAMP(i)
#define TRACE(i)
#define PIN() do { asm volatile("" ::: "memory"); __builtin_amdgcn_sched_barrier(0); } while (0)
#define BARRIER() do { asm volatile("s_waitcnt lgkmcnt(0)" ::: "memory"); __builtin_amdgcn_s_barrier(); PIN(); } while (0)
#define VMCNT(n) asm volatile("s_waitcnt vmcnt(%0)" :: "i"(n) : "memory")

typedef volatile __attribute__((address_space(3))) unsigned vlds_u32;
typedef volatile __attribute__((address_space(3))) u32x2 vlds_u32x2;
typedef volatile __attribute__((address_space(3))) u32x4 vlds_u32x4;

struct Pace {
    unsigned step;
    int start;
    int offset;
    int bias;
    int rd;
    int pub;
};

template <int X, int PRO_, int GATE, int STAG_, int SLP>
__global__ __launch_bounds__(576) void kv_sweep_kernel(
    const unsigned short* q, const int* qk, const unsigned short* ksc,
    const unsigned short* kzp, const int* qv, const unsigned short* vsc,
    const unsigned short* vzp, const unsigned short* q_cos, const unsigned short* q_sin,
    const unsigned short* k_cos, const unsigned short* k_sin, unsigned char* ws, bf16* out, Pace pace)
{
    __shared__ __attribute__((aligned(16))) unsigned s_q[NH * DH];
    __shared__ __attribute__((aligned(16))) float s_sc[NH * 64 * 4];
    __shared__ __attribute__((aligned(16))) float s_part[8 * NI * DH];
    __shared__ __attribute__((aligned(16))) float s_red[8 * 28];
    __shared__ __attribute__((aligned(16))) float s_f[NH * 8];
    __shared__ __attribute__((aligned(16))) unsigned s_done[8];
    __shared__ __attribute__((aligned(16))) unsigned s_sync[8];
    __shared__ __attribute__((aligned(16))) unsigned s_act[16];

    constexpr bool TRABS = true;
    constexpr int ORD = 1, WID = 0, HOLD = GATE % 10 * 2, SLIP = GATE / 10 % 10 * 2560, SLIPT = GATE / 100;
    constexpr int PRO = PRO_ % 10, PF = PRO_ / 10, STAG = 0, LAY = 0, VS = 2, LDA = 16, STA = 16;
    constexpr unsigned NREG = 1u << STAG_;
    constexpr unsigned PSTR = LAY == 0 ? 64 : LAY == 1 ? UNITB + 256 : LAY == 2 ? UNITB + 128 : UNITB;
    constexpr unsigned USTR = LAY == 0 ? UNITB : 64;
    STAMP(0);
    unsigned long long pc0;
    asm volatile("s_getpc_b64 %0" : "=s"(pc0));
    const unsigned tstart = now32();
    const int tid = threadIdx.x;
    const int lane = tid & 63;
    const int wave = __builtin_amdgcn_readfirstlane(tid >> 6);
    const int r = lane >> 4;
    const int c = lane & 15;
    const int bid = blockIdx.x;
    const int m = (bid & 7) * 32 + (bid >> 3);
    const auto rw_ = __builtin_amdgcn_make_buffer_rsrc((void*)ws, 0, WS_BYTES, 0x00020000);

    auto merge = [&](int i, int hp, int c6, unsigned tag) {
        const f32x4 f = *reinterpret_cast<const f32x4*>(s_f + (2 * i + hp) * 8);
        const f32x2 zl = *reinterpret_cast<const f32x2*>(s_f + (2 * i + hp) * 8 + 4);
        const float zs = zl[0];
        f32x2 a[3];
#pragma unroll
        for (int k = 0; k < 3; ++k) a[k] = f32x2{-zs, -zs};
#pragma unroll
        for (int w = 0; w < 4; ++w) {
            const float* p = s_part + ((hp * 4 + w) * NI + i) * DH + c6 * 6;
#pragma unroll
            for (int k = 0; k < 3; ++k) a[k] += f[w] * *reinterpret_cast<const f32x2*>(p + 2 * k);
        }
        return u32x4{pk_bf16(a[0]), pk_bf16(a[1]), pk_bf16(a[2]), tag | ((__float_as_uint(zl[1]) >> (16 * (c6 & 1))) & 0xffffu)};
    };
    auto region = [&](unsigned tag) { return WS_PART + ((__builtin_amdgcn_readfirstlane(tag) >> 16) & (NREG - 1)) * (NWG * UNITB); };
    auto publish = [&](u32x4 v, unsigned reg, int i, int hp, int c6, bool on) {
        __builtin_amdgcn_raw_buffer_store_b128(v, rw_, on ? (hp * 8 + (c6 >> 2)) * USTR + (c6 & 3) * 16 : OOB, reg + i * 16 * USTR + m * PSTR, STA);
        asm volatile("s_nop 1" :: "v"(v) : "memory");
    };

    if (wave == 8) {
        unsigned long long pc1;
        asm volatile("s_getpc_b64 %0" : "=s"(pc1));
        pc0 &= ~255ull;
        const auto rcode = __builtin_amdgcn_make_buffer_rsrc((void*)pc0, 0, (unsigned)(pc1 - pc0), 0x00020000);
        auto warm_code = [&]() {
            unsigned x[2];
#pragma unroll
            for (int k = 0; k < 2; ++k) x[k] = __builtin_amdgcn_raw_buffer_load_b32(rcode, lane * 128 + k * 8192, 0, 0);
            asm volatile("s_waitcnt vmcnt(0)" :: "v"(x[0]), "v"(x[1]) : "memory");
        };
        if (PF) warm_code();
        const unsigned tag = ((unsigned)__builtin_amdgcn_raw_buffer_load_b16(rw_, (lane & 3) * 2, WS_EPOCH, 16) + 1u) << 16;
        unsigned origin = (tstart << 8) + pace.offset;
        if (lane < 8) { s_done[lane] = 0; s_sync[lane] = lane == 2 ? origin : lane < 4 ? 0 : tag; }
        PIN();
        BARRIER();
        const int mh = lane >> 5, mc = lane & 31;
        const unsigned rtag = __builtin_amdgcn_readlane(tag, 0) | __builtin_amdgcn_readlane(tag, 1) >> 16;
        constexpr int RPX = 2;
        constexpr int RSH = X / 100;
        constexpr unsigned RDLY = 200;
        constexpr int UP = X / 10 % 10 ? X / 10 % 10 * 82 : 1 << 24, DN = X % 10 ? X % 10 * 82 : 1 << 24;
        const int rj = ((bid >> 3) + 5 * (bid & 7)) & 31;
        const int rid = rj % (32 / RPX) == 0 ? rj / (32 / RPX) * 8 + (bid & 7) : -1;
        unsigned wround = now32() >> RSH, rround = wround, tick = 0, target = origin;
        bool warmed = false;
        int i = 0;
        auto kept_now = [&](unsigned now) {
            const u32x2 a = *(vlds_u32x2*)(s_act + 2 * (lane & 7));
            const int late = max((int)(a[0] - origin), (int)((now << 8) - a[1] - origin));
            return origin + (__builtin_amdgcn_readfirstlane(row16_sum(late)) >> 4);
        };
        while (i < NI - 1) {
            const unsigned now = now32();
            if ((now >> RSH) != wround && i < NI - 2) {
                wround = now >> RSH;
                if (rid >= 0 && (now & ((1u << RSH) - 1)) < 48)
                    __builtin_amdgcn_raw_buffer_store_b64(u32x2{kept_now(now), rtag + wround}, rw_, lane == 0 ? rid * 8 : OOB, WS_REP + (wround & 255) * 512, 16);
                PIN();
                continue;
            }
            if ((int)(((now - RDLY) >> RSH) - rround) > 0 && i < NI - 2) {
                rround = (now - RDLY) >> RSH;
                const u32x2 x = __builtin_amdgcn_raw_buffer_load_b64(rw_, c * 8, WS_REP + (rround & 255) * 512, 16);
                const int d = (int)(x[0] - origin);
                const bool ok = x[1] == rtag + rround && d < (32 << 16) && d > -(32 << 16);
                const int n = __builtin_popcountll(__builtin_amdgcn_ballot_w64(ok));
                if (n >= 48) {
                    target = origin + __builtin_amdgcn_readfirstlane((int)(row16_allsum(ok ? (float)d : 0.f) * 4.f / (float)n)) - pace.bias;
                    const int lost = min((int)(kept_now(now32()) - origin) - pace.bias, (int)(target - origin));
                    if (lost > 0) origin += lost;
                    if (SLIP && !SLIPT) { const int l2 = (int)(kept_now(now32()) - origin) - SLIP; if (l2 > 0) origin += l2; }
                }
                PIN();
                continue;
            }
            if ((now >> 5) != tick) {
                tick = now >> 5;
                origin += max(-DN, min(UP, (int)(target - origin)));
                if (SLIP && SLIPT) { const int l2 = (int)(kept_now(now) - origin) - SLIP; if (l2 > 0) origin += l2; }
                if (lane == 0) *(vlds_u32*)(s_sync + 2) = origin;
                PIN();
            }
            if (PF >= 2 && !warmed && (int)((now << 8) - origin) > 60 * (int)pace.step) { warm_code(); warmed = true; continue; }
            const u32x4 a = *(vlds_u32x4*)s_done, b = *(vlds_u32x4*)(s_done + 4);
            const unsigned mn = min(min(min(a[0], a[1]), min(a[2], a[3])), min(min(b[0], b[1]), min(b[2], b[3])));
            if (__builtin_amdgcn_readfirstlane(mn) <= (unsigned)i) { __builtin_amdgcn_s_sleep(2); continue; }
            PIN();
            if (i == 0) {
                f32x4 ml[4], f;
#pragma unroll
                for (int w = 0; w < 4; ++w) ml[w] = *reinterpret_cast<const f32x4*>(s_sc + (mc * 4 + w) * 64 + 16);
                const float M = fmaxf(fmaxf(ml[0][0], ml[1][0]), fmaxf(ml[2][0], ml[3][0]));
                float L = 0.f, zs = 0.f;
#pragma unroll
                for (int w = 0; w < 4; ++w) { f[w] = __builtin_amdgcn_exp2f(ml[w][0] - M); L += f[w] * ml[w][1]; zs += f[w] * ml[w][2]; }
                const float inv = 1.f / L;
                *reinterpret_cast<f32x4*>(s_f + mc * 8) = f * inv;
                *reinterpret_cast<f32x2*>(s_f + mc * 8 + 4) = f32x2{zs * inv, M + __builtin_amdgcn_logf(L)};
                PIN();
                if (lane == 0) *(vlds_u32*)(s_sync + 3) = 1;
                PIN();
            }
            if (PF >= 2 && i == NI - 3) warm_code();
            const u32x4 v = merge(i, mh, mc, tag);
            PIN();
            if (pace.pub < 0) publish(v, region(tag), i, mh, mc, true);
            else *reinterpret_cast<u32x4*>(s_part + (mh * 4 * NI + i) * DH + mc * 4) = v;
            PIN();
            ++i;
        }
        u32x4 v[NI - 1];
#pragma unroll
        for (int i = 0; i < NI - 1; ++i) { v[i] = *reinterpret_cast<const u32x4*>(s_part + (mh * 4 * NI + i) * DH + mc * 4); asm volatile("" : "+v"(v[i])); }
        STAMP(5);
        while ((int)((now32() << 8) - origin) < pace.pub) __builtin_amdgcn_s_sleep(1);
        PIN();
        STAMP(6);
#pragma unroll
        for (int i = 0; i < NI - 1; ++i) publish(v[i], region(tag), i, mh, mc, pace.pub >= 0);
        STAMP(7);
        return;
    }

    const int rw = wave & 3, hp = wave >> 2;
    const int row0 = m * 64 + rw * 16;

    const unsigned sidx = STAG == 0 ? hp * 1024 + m * 4 + rw : STAG == 1 ? m * 8 + wave : (hp * 1024 + (m & 31) * 32 + (m >> 5) * 4 + rw);
    unsigned off = pace.start + ((pace.step * sidx) >> 11);
    unsigned origin = tstart << 8, kept = 0;
    bool wide = false;
    unsigned prev = 0, hold = 0;
    int holdn = 0;
    const unsigned step = pace.step;
    auto pace_wait = [&]() {
        unsigned org = __builtin_amdgcn_readfirstlane(origin);
        if (HOLD && holdn > 0) { --holdn; if ((int)(hold - org) > 0) org = hold; }
        const unsigned tgt = org + off;
        prev = kept;
        unsigned now = now32() << 8;
        kept = ((int)(now - tgt) > 0 ? now : tgt) - off;
        wide = (int)(now - tgt) > 30 * 256;
        while ((int)(tgt - now) > 0) { if (SLP) __builtin_amdgcn_s_sleep(SLP); now = now32() << 8; }
        off += step;
    };
    auto report = [&]() {
        if (lane == 0) *(vlds_u32x2*)(s_act + 2 * wave) = u32x2{kept, off};
        origin = *(vlds_u32*)(s_sync + 2);
        PIN();
    };
#define TRV (TRABS ? kept - (tstart << 8) : kept - __builtin_amdgcn_readfirstlane(origin))

    const int qe = min(tid, 383);
    const int qh = qe / 12, qd = (qe % 12) * 8;
    u32x4 lq[2], lc[2], ls[2];
    auto load_qs = [&]() {
#pragma unroll
        for (int k = 0; k < 2; ++k) {
            lq[k] = *reinterpret_cast<const u32x4*>(q + qh * DH + k * HALF + qd);
            lc[k] = *reinterpret_cast<const u32x4*>(q_cos + k * HALF + qd);
            ls[k] = *reinterpret_cast<const u32x4*>(q_sin + k * HALF + qd);
        }
        PIN();
    };
    u32x2 lsc[4];
    auto load_sc = [&]() {
#pragma unroll
        for (int a = 0; a < 4; ++a) {
            const unsigned short* base = a == 0 ? ksc : a == 1 ? kzp : a == 2 ? vsc : vzp;
            lsc[a] = __builtin_nontemporal_load(reinterpret_cast<const u32x2*>(base + (size_t)(tid >> 4) * SKV + m * 64 + (tid & 15) * 4));
        }
        PIN();
    };

    const auto rc = __builtin_amdgcn_make_buffer_rsrc((void*)k_cos, 0, CSB, 0x00020000);
    const auto rs = __builtin_amdgcn_make_buffer_rsrc((void*)k_sin, 0, CSB, 0x00020000);
    u32x2 raw[2][6];
    auto load_cs = [&](int t) {
        const unsigned co = r * 384 + c * 8;
        const unsigned so1 = r * 384 + (c < 8 ? c * 8 + 320 : c * 8 - 64);
        const unsigned rb = (row0 + (t + 2 * hp) * 4) * 384u;
        raw[t][0] = __builtin_amdgcn_raw_buffer_load_b64(rc, co, rb, 2);
        raw[t][1] = __builtin_amdgcn_raw_buffer_load_b64(rc, co + 128, rb, 2);
        raw[t][2] = __builtin_amdgcn_raw_buffer_load_b64(rc, co + 256, rb, 2);
        raw[t][3] = __builtin_amdgcn_raw_buffer_load_b64(rs, co + 192, rb, 2);
        raw[t][4] = __builtin_amdgcn_raw_buffer_load_b64(rs, so1, rb, 2);
        raw[t][5] = __builtin_amdgcn_raw_buffer_load_b64(rs, co + 64, rb, 2);
        PIN();
    };
    u32x4 tab[4][3];
    auto pack_cs = [&](int t) {
#pragma unroll
        for (int j = 0; j < 3; ++j) {
#pragma unroll
            for (int k = 0; k < 2; ++k) {
                tab[t][j][2 * k] = __builtin_amdgcn_perm(raw[t][3 + j][k], raw[t][j][k], 0x05040100);
                tab[t][j][2 * k + 1] = __builtin_amdgcn_perm(raw[t][3 + j][k], raw[t][j][k], 0x07060302);
            }
            *reinterpret_cast<u32x4*>(s_part + (wave ^ 4) * (NI * DH) + ((t * 3 + j) * 64 + lane) * 4) = tab[t][j];
        }
        PIN();
    };
    const auto rk = __builtin_amdgcn_make_buffer_rsrc((void*)qk, 0, KVB, 0x00020000);
    const auto rv = __builtin_amdgcn_make_buffer_rsrc((void*)qv, 0, KVB, 0x00020000);
    const unsigned voff = r * ROWB + c * 16;
    const unsigned woff = row0 * ROWB + hp * HEADB;
    auto toff = [&](int t) { return (unsigned)(t ^ (2 * hp)) * (4 * ROWB); };
    u32x4 ring[4][3];
    auto ld = [&](int slot, int j, __amdgpu_buffer_rsrc_t rr, unsigned off) {
        ring[slot][j] = __builtin_amdgcn_raw_buffer_load_b128(rr, voff + j * 256, off, 2);
        PIN();
    };
    auto ld_tile = [&](int slot, __amdgpu_buffer_rsrc_t rr, unsigned off) { ld(slot, 0, rr, off); ld(slot, 1, rr, off); ld(slot, 2, rr, off); };
    auto refill = [&](int slot, int j, __amdgpu_buffer_rsrc_t rr, unsigned goff, bool on) {
        if (ORD == 0) {
            if (on) { if (WID && wide) VMCNT(2 + WID); else VMCNT(2); if (j == 0) pace_wait(); }
            ld(slot, j, rr, goff);
        } else {
            if (on) { if (j == 0) pace_wait(); if (WID && wide) VMCNT(2 + WID); else VMCNT(2); }
            ld(slot, j, rr, goff);
            if (on && j == 0) { const unsigned t = (now32() << 8) - 1024 - (off - step); if ((int)(t - kept) > 0) kept = t;
                if (HOLD && (int)(kept - prev) > 100 * 256) { hold = kept; holdn = HOLD; } }
        }
        if (j == 2) report();
    };
    auto q_to_lds = [&]() {
        float o[2][8];
#pragma unroll
        for (int k = 0; k < 2; ++k)
#pragma unroll
            for (int u = 0; u < 4; ++u) {
                const unsigned pp = lq[k ^ 1][u] ^ (k == 0 ? 0x80008000u : 0u);
                o[k][2 * u] = rbf(rbf(bflo(lq[k][u]) * bflo(lc[k][u])) + rbf(bflo(pp) * bflo(ls[k][u])));
                o[k][2 * u + 1] = rbf(rbf(bfhi(lq[k][u]) * bfhi(lc[k][u])) + rbf(bfhi(pp) * bfhi(ls[k][u])));
            }
#pragma unroll
        for (int v = 0; v < 2; ++v) {
            u32x4 lo, hi;
#pragma unroll
            for (int e = 0; e < 4; ++e) {
                lo[e] = __builtin_amdgcn_perm(__float_as_uint(o[1][4 * v + e]), __float_as_uint(o[0][4 * v + e]), 0x07060302);
                hi[e] = __builtin_amdgcn_perm(__float_as_uint(o[0][4 * v + e]) ^ 0x80000000u, __float_as_uint(o[1][4 * v + e]), 0x07060302);
            }
            *reinterpret_cast<u32x4*>(s_q + qh * DH + qd + 4 * v) = lo;
            *reinterpret_cast<u32x4*>(s_q + qh * DH + HALF + qd + 4 * v) = hi;
        }
        PIN();
    };
    auto sc_to_lds = [&]() {
        float* dst = s_sc + tid * 16;
#pragma unroll
        for (int u = 0; u < 2; ++u) {
            *reinterpret_cast<f32x4*>(dst + 8 * u) = f32x4{bflo(lsc[0][u]) * SCALE_LOG2, bflo(lsc[1][u]), bflo(lsc[2][u]), bflo(lsc[3][u])};
            *reinterpret_cast<f32x4*>(dst + 8 * u + 4) = f32x4{bfhi(lsc[0][u]) * SCALE_LOG2, bfhi(lsc[1][u]), bfhi(lsc[2][u]), bfhi(lsc[3][u])};
        }
        PIN();
    };

    auto k0 = [&](int t) { pace_wait(); ld_tile(t, rk, woff + toff(t)); };
    if (PRO == 0) {
        load_qs(); load_sc(); load_cs(0);
        pace_wait(); load_cs(1);
        q_to_lds(); sc_to_lds();
        STAMP(1);
        k0(0); pack_cs(0);
        k0(1); pack_cs(1);
        VMCNT(3); k0(2);
        STAMP(2);
        VMCNT(3); k0(3);
    } else if (PRO == 1) {
        load_cs(0); load_sc();
        pace_wait(); load_cs(1); STAMP(1); if (wave < 6) load_qs();
        pack_cs(0); sc_to_lds();
        k0(0); pack_cs(1); if (wave < 6) q_to_lds();
        k0(1);
        VMCNT(3); k0(2);
        STAMP(2);
        VMCNT(3); k0(3);
    } else if (PRO == 2) {
        load_cs(0); load_sc();
        pace_wait(); load_cs(1);
        STAMP(1);
        pack_cs(0); sc_to_lds();
        k0(0); if (wave < 6) load_qs(); pack_cs(1);
        k0(1); if (wave < 6) q_to_lds();
        VMCNT(3); k0(2);
        STAMP(2);
        VMCNT(3); k0(3);
    } else if (PRO == 3) {
        load_cs(0); load_sc();
        pace_wait(); load_cs(1);
        STAMP(1);
        pack_cs(0); sc_to_lds();
        k0(0); pack_cs(1);
        k0(1); if (wave < 6) load_qs();
        VMCNT(9); k0(2); if (wave < 6) q_to_lds();
        STAMP(2);
        VMCNT(3); k0(3);
    }
    if (lane == 0) *(vlds_u32x2*)(s_act + 2 * wave) = u32x2{kept, off};
    BARRIER();
    origin = *(vlds_u32*)(s_sync + 2);
    STAMP(3);

    u32x4 qp[3];
    auto load_q = [&](int h, int j) { qp[j] = *reinterpret_cast<const u32x4*>(s_q + h * DH + 64 * j + 4 * c); };
    load_q(hp, 0); load_q(hp, 1); load_q(hp, 2);
#pragma unroll
    for (int t = 0; t < 2; ++t)
#pragma unroll
        for (int j = 0; j < 3; ++j) tab[2 + t][j] = *reinterpret_cast<const u32x4*>(s_part + wave * (NI * DH) + ((t * 3 + j) * 64 + lane) * 4);

    for (int i = 0; i < NI; ++i) {
        const bool last = i + 1 == NI;
        const int h = 2 * i + hp;
        const unsigned hnext = (i + 1) * (2 * HEADB) + woff;
        const auto rn = __builtin_amdgcn_make_buffer_rsrc((void*)(last ? qv : qk), 0, KVB, 0x00020000);
        float* scp = s_sc + (h * 4 + rw) * 64;
        f32x4 sc[4];
#pragma unroll
        for (int t = 0; t < 4; ++t) sc[t] = *reinterpret_cast<const f32x4*>(scp + (4 * (t ^ (2 * hp)) + r) * 4);
        float s[4];
#pragma unroll
        for (int t = 0; t < 4; ++t) {
            f32x2 dot, wsum;
#pragma unroll
            for (int j = 0; j < 3; ++j) {
                f32x2 w01, w23, k01, k23;
                dot_cvt(qp[j], tab[t][j], ring[t][j], w01, w23, k01, k23);
                if (j == 0) { wsum = w01 + w23; dot = k01 * w01; } else { wsum += w01; wsum += w23; dot += k01 * w01; }
                dot += k23 * w23;
                asm volatile("" : "+v"(dot), "+v"(wsum));
                PIN();
                if (t == 3) load_q(last ? h : h + 2, j);
                if (t == 0 || t == VS) refill(t, j, rn, last ? woff + toff(t == VS ? 1 : 0) : hnext + toff(t), true);
                else refill(t, j, rk, last ? OOB : hnext + toff(t), !last);
                if (t == 0 && j == 0) TRACE(i);
            }
            const float a = (dot[0] + dot[1]) - sc[t][1] * (wsum[0] + wsum[1]);
            s[t] = row16_allsum(a) * sc[t][0];
        }
        const float mx = xrow_max(fmaxf(fmaxf(s[0], s[1]), fmaxf(s[2], s[3])));
        float l = 0.f, z = 0.f;
#pragma unroll
        for (int t = 0; t < 4; ++t) {
            const float p = __builtin_amdgcn_exp2f(s[t] - mx);
            l += p;
            s[t] = p * sc[t][2];
            z += s[t] * sc[t][3];
        }
        l = xrow_sum(l);
        z = xrow_sum(z);
        if (c == 0) {
            *reinterpret_cast<f32x4*>(scp + r * 4) = f32x4{s[0], s[1], s[2], s[3]};
            *reinterpret_cast<f32x4*>(scp + 16) = f32x4{mx, l, z, 0.f};
        }
        PIN();
    }
    STAMP(4);

    const float* pwp = s_sc + (hp * 4 + rw) * 64 + r * 4;
    f32x4 pw = *reinterpret_cast<const f32x4*>(pwp);
    for (int i = 0; i < NI; ++i) {
        const bool last = i + 1 == NI;
        const unsigned hoff = i * (2 * HEADB) + woff;
        f32x2 acc[3][2];
#pragma unroll
        for (int t = 0; t < 4; ++t) {
            const f32x2 p2 = {pw[t], pw[t]};
#pragma unroll
            for (int j = 0; j < 3; ++j) {
                const u32x4 vv = ring[(t & 1) * VS][j];
                const f32x2 v01 = {(float)(int)vv[0], (float)(int)vv[1]}, v23 = {(float)(int)vv[2], (float)(int)vv[3]};
                if (t == 0) { acc[j][0] = p2 * v01; acc[j][1] = p2 * v23; }
                else { acc[j][0] += p2 * v01; acc[j][1] += p2 * v23; }
                asm volatile("" : "+v"(acc[j][0]), "+v"(acc[j][1]));
                PIN();
                if (t == 3 && j == 2) pw = *reinterpret_cast<const f32x4*>(pwp + (last ? i : i + 1) * 512);
                if (t < 2) refill((t & 1) * VS, j, rv, hoff + toff(t + 2), true);
                else refill((t & 1) * VS, j, rv, last ? OOB : hoff + 2 * HEADB + toff(t - 2), !last);
                if (t == 0 && j == 0) TRACE(16 + i);
            }
        }
        float* sl = s_part + (wave * NI + i) * DH;
#pragma unroll
        for (int j = 0; j < 3; ++j) {
            const float x = swap32_add(acc[j][0][0], acc[j][1][0]);
            const float y = swap32_add(acc[j][0][1], acc[j][1][1]);
            sl[64 * j + 4 * c + r] = swap16_add(x, y);
        }
        PIN();
        if (lane == 0) *(vlds_u32*)(s_done + wave) = i + 1;
        PIN();
    }
    STAMP(6);
    unsigned old = 0;
    if (lane == 0) old = __hip_atomic_fetch_add(s_sync + hp, 1u, __ATOMIC_RELAXED, __HIP_MEMORY_SCOPE_WORKGROUP);
    PIN();
    const unsigned tag = s_sync[4 + (lane & 3)];
    if (__builtin_amdgcn_readfirstlane(old) == 3) {
        while (__builtin_amdgcn_readfirstlane(*(vlds_u32*)(s_sync + 3)) == 0) __builtin_amdgcn_s_sleep(1);
        PIN();
        publish(merge(NI - 1, hp, lane & 31, tag), region(tag), NI - 1, hp, lane & 31, lane < 32);
        PIN();
    }
    STAMP(7);
    {
        const unsigned o = __builtin_amdgcn_readfirstlane(origin);
        while ((int)((now32() << 8) - o) < pace.rd) __builtin_amdgcn_s_sleep(1);
        PIN();
    }
    STAMP(8);
    {
        u32x4 x, y;
        unsigned xo = (tid >> 2) * PSTR + (tid & 3) * 16, yo = xo + 128 * PSTR;
        for (;;) {
            const u32x4 xn = __builtin_amdgcn_raw_buffer_load_b128(rw_, xo, region(tag) + bid * USTR, LDA);
            const u32x4 yn = __builtin_amdgcn_raw_buffer_load_b128(rw_, yo, region(tag) + bid * USTR, LDA);
            unsigned bad = (xo != OOB && (xn[3] ^ tag) > 0xffffu ? 1u : 0u) | (yo != OOB && (yn[3] ^ tag) > 0xffffu ? 2u : 0u);
            bad |= __builtin_amdgcn_mov_dpp(bad, 0xb1, 0xf, 0xf, true);
            bad |= __builtin_amdgcn_mov_dpp(bad, 0x4e, 0xf, 0xf, true);
            const bool tx = xo != OOB, ty = yo != OOB;
#pragma unroll
            for (int k = 0; k < 4; ++k) { x[k] = tx ? xn[k] : x[k]; y[k] = ty ? yn[k] : y[k]; }
            xo = bad & 1 ? xo : OOB;
            yo = bad & 2 ? yo : OOB;
            if (__builtin_amdgcn_ballot_w64(bad != 0) == 0 || now32() - tstart > SPIN_LIMIT) break;
            PIN();
        }
        STAMP(9);
        const unsigned sel = lane & 1 ? 0x01000504u : 0x05040100u;
        const float lx = __uint_as_float(__builtin_amdgcn_perm(__builtin_amdgcn_mov_dpp(x[3], 0xb1, 0xf, 0xf, true), x[3], sel));
        const float ly = __uint_as_float(__builtin_amdgcn_perm(__builtin_amdgcn_mov_dpp(y[3], 0xb1, 0xf, 0xf, true), y[3], sel));
        float M = fmaxf(lx, ly);
        M = fmaxf(M, dpp<0x128>(M));
        M = fmaxf(M, dpp<0x124>(M));
        M = xrow_max(M);
        const float w0 = __builtin_amdgcn_exp2f(lx - M), w1 = __builtin_amdgcn_exp2f(ly - M);
        float a[8];
#pragma unroll
        for (int k = 0; k < 3; ++k) {
            a[2 * k] = w0 * bflo(x[k]) + w1 * bflo(y[k]);
            a[2 * k + 1] = w0 * bfhi(x[k]) + w1 * bfhi(y[k]);
        }
        a[6] = a[7] = w0 + w1;
        float b[4], d[2];
#pragma unroll
        for (int k = 0; k < 4; ++k) b[k] = swap32_add(a[k], a[4 + k]);
#pragma unroll
        for (int k = 0; k < 2; ++k) d[k] = swap16_add(b[k], b[2 + k]);
#pragma unroll
        for (int k = 0; k < 2; ++k) { d[k] += dpp<0x128>(d[k]); d[k] += dpp<0x124>(d[k]); }
        if (c < 4) *reinterpret_cast<f32x2*>(s_red + wave * 28 + (r < 3 ? c * 6 + r * 2 : 24)) = f32x2{d[0], r < 3 ? d[1] : M};
        STAMP(10);
        BARRIER();
        if (tid < 24) {
            f32x2 sm[8];
            float ov[8];
#pragma unroll
            for (int w = 0; w < 8; ++w) { sm[w] = *reinterpret_cast<const f32x2*>(s_red + w * 28 + 24); ov[w] = s_red[w * 28 + tid]; }
            float MM = sm[0][1];
#pragma unroll
            for (int w = 1; w < 8; ++w) MM = fmaxf(MM, sm[w][1]);
            float S = 0.f, o = 0.f;
#pragma unroll
            for (int w = 0; w < 8; ++w) {
                const float ff = __builtin_amdgcn_exp2f(sm[w][1] - MM);
                S += ff * sm[w][0];
                o += ff * ov[w];
            }
            out[bid * 24 + tid] = __float2bfloat16(o * __builtin_amdgcn_rcpf(S));
        }
        if (bid == 0 && wave == 1) __builtin_amdgcn_raw_buffer_store_b16((unsigned short)(tag >> 16), rw_, lane < 4 ? lane * 2 : OOB, WS_EPOCH, 16);
    }
}

template <int X, int PRO, int GATE, int STAG>
static void launch_cfg(void* const* d_in, void* d_out, void* d_ws, hipStream_t stream, float t_us, float start_us, float off_us, float debt_us, float adj_pct, float pub_us) {
    Pace pace;
    pace.step = (unsigned)(t_us * 25600.f / 128.f);
    pace.start = (int)(start_us * 25600.f);
    pace.offset = (int)(off_us * 25600.f);
    pace.bias = (int)(debt_us * 25600.f);
    pace.rd = (int)((start_us + t_us * (129.f / 128.f) + adj_pct) * 25600.f);
    pace.pub = (int)((start_us + t_us * (129.f / 128.f) + pub_us) * 25600.f);
    kv_sweep_kernel<X, PRO, GATE, STAG, 1><<<NWG, 576, 0, stream>>>((const unsigned short*)d_in[0], (const int*)d_in[1], (const unsigned short*)d_in[2],
        (const unsigned short*)d_in[3], (const int*)d_in[4], (const unsigned short*)d_in[5], (const unsigned short*)d_in[6],
        (const unsigned short*)d_in[7], (const unsigned short*)d_in[8], (const unsigned short*)d_in[9], (const unsigned short*)d_in[10],
        (unsigned char*)d_ws, (bf16*)d_out, pace);
}

#define CFG 966, 21, 120, 0
#define PRM 102.5f, 0.5f, 1.0f, 0.05f, 1.8f, 0.8f
extern "C" void kernel_launch(void* const* d_in, const int* in_sizes, int n_in,
                              void* d_out, int out_size, void* d_ws, size_t ws_size,
                              hipStream_t stream) {
    launch_cfg<CFG>(d_in, d_out, d_ws, stream, PRM);
}
```

```cpp
#define PIDX 99
#define PMAX 0
#define PSUB 0
#define PWAVE 0
#include <hip/hip_runtime.h>
#include <hip/hip_bf16.h>
#include <cstdint>
#include <type_traits>

using bf16 = __hip_bfloat16;
using u32x4 = __attribute__((ext_vector_type(4))) unsigned;
using u32x2 = __attribute__((ext_vector_type(2))) unsigned;
using f32x2 = __attribute__((ext_vector_type(2))) float;
using f32x4 = __attribute__((ext_vector_type(4))) float;
using bf16x2 = __attribute__((ext_vector_type(2))) __bf16;

constexpr int DH = 192;
constexpr int HALF = 96;
constexpr int NH = 32;
constexpr int SKV = 16384;
constexpr int NWG = 256;
constexpr int NI = 16;
constexpr unsigned ROWB = DH * 4;
constexpr unsigned HEADB = SKV * ROWB;
constexpr unsigned KVB = NH * HEADB;
constexpr unsigned CSB = SKV * DH * 2;
constexpr unsigned OOB = 0x80000000u;
constexpr float SCALE_LOG2 = 1.4426950408889634f * 0.07216878364870322f;
constexpr unsigned SPIN_LIMIT = 20000000u;

constexpr unsigned UNITB = NWG * 64;
constexpr unsigned WS_PART = 0;
constexpr unsigned WS_EPOCH = NWG * (UNITB + 1024);
constexpr unsigned WS_REP = WS_EPOCH + 256;
constexpr unsigned WS_PROBE = WS_REP + 256 * 512;
constexpr unsigned WS_BYTES = WS_PROBE + 4096;

__device__ __forceinline__ float bflo(unsigned u) { return __uint_as_float(u << 16); }
__device__ __forceinline__ float bfhi(unsigned u) { return __uint_as_float(u & 0xffff0000u); }
__device__ __forceinline__ float rbf(float x) { return __bfloat162float(__float2bfloat16(x)); }
__device__ __forceinline__ unsigned pk_bf16(f32x2 a) { return __builtin_bit_cast(unsigned, __builtin_convertvector(a, bf16x2)); }
__device__ __forceinline__ void dot_cvt(const u32x4 q, const u32x4 t, const u32x4 ki, f32x2& w01, f32x2& w23, f32x2& k01, f32x2& k23) {
    float w0, w1, w2, w3;
    float k0 = __uint_as_float(ki[0]), k1 = __uint_as_float(ki[1]), k2 = __uint_as_float(ki[2]), k3 = __uint_as_float(ki[3]);
    asm("v_dot2_f32_bf16 %0, %8, %12, 0\n\tv_dot2_f32_bf16 %1, %9, %13, 0\n\tv_dot2_f32_bf16 %2, %10, %14, 0\n\tv_dot2_f32_bf16 %3, %11, %15, 0\n\t"
        "v_cvt_f32_i32 %4, %4\n\tv_cvt_f32_i32 %5, %5\n\tv_cvt_f32_i32 %6, %6\n\tv_cvt_f32_i32 %7, %7"
        : "=&v"(w0), "=&v"(w1), "=&v"(w2), "=&v"(w3), "+v"(k0), "+v"(k1), "+v"(k2), "+v"(k3)
        : "v"(q[0]), "v"(q[1]), "v"(q[2]), "v"(q[3]), "v"(t[0]), "v"(t[1]), "v"(t[2]), "v"(t[3]));
    w01 = f32x2{w0, w1}; w23 = f32x2{w2, w3}; k01 = f32x2{k0, k1}; k23 = f32x2{k2, k3};
}

template <int CTRL> __device__ __forceinline__ float dpp(float x) {
    return __builtin_bit_cast(float, __builtin_amdgcn_mov_dpp(__builtin_bit_cast(int, x), CTRL, 0xf, 0xf, true));
}
__device__ __forceinline__ float row16_allsum(float x) {
    x += dpp<0x128>(x);
    x += dpp<0x124>(x);
    x += dpp<0x122>(x);
    x += dpp<0x121>(x);
    return x;
}
__device__ __forceinline__ float row16_allmax(float x) {
    x = fmaxf(x, dpp<0x128>(x));
    x = fmaxf(x, dpp<0x124>(x));
    x = fmaxf(x, dpp<0x122>(x));
    x = fmaxf(x, dpp<0x121>(x));
    return x;
}
__device__ __forceinline__ float xrow_max(float x) {
    auto s = __builtin_amdgcn_permlane16_swap(__float_as_uint(x), __float_as_uint(x), false, false);
    x = fmaxf(__uint_as_float(s[0]), __uint_as_float(s[1]));
    auto t = __builtin_amdgcn_permlane32_swap(__float_as_uint(x), __float_as_uint(x), false, false);
    return fmaxf(__uint_as_float(t[0]), __uint_as_float(t[1]));
}
__device__ __forceinline__ float xrow_sum(float x) {
    auto s = __builtin_amdgcn_permlane16_swap(__float_as_uint(x), __float_as_uint(x), false, false);
    x = __uint_as_float(s[0]) + __uint_as_float(s[1]);
    auto t = __builtin_amdgcn_permlane32_swap(__float_as_uint(x), __float_as_uint(x), false, false);
    return __uint_as_float(t[0]) + __uint_as_float(t[1]);
}
__device__ __forceinline__ float swap32_add(float a, float b) {
    auto t = __builtin_amdgcn_permlane32_swap(__float_as_uint(a), __float_as_uint(b), false, false);
    return __uint_as_float(t[0]) + __uint_as_float(t[1]);
}
__device__ __forceinline__ float swap16_add(float a, float b) {
    auto t = __builtin_amdgcn_permlane16_swap(__float_as_uint(a), __float_as_uint(b), false, false);
    return __uint_as_float(t[0]) + __uint_as_float(t[1]);
}
__device__ __forceinline__ int row16_sum(int x) {
    x += __builtin_amdgcn_mov_dpp(x, 0x128, 0xf, 0xf, true);
    x += __builtin_amdgcn_mov_dpp(x, 0x124, 0xf, 0xf, true);
    x += __builtin_amdgcn_mov_dpp(x, 0x122, 0xf, 0xf, true);
    x += __builtin_amdgcn_mov_dpp(x, 0x121, 0xf, 0xf, true);
    return x;
}
__device__ __forceinline__ unsigned now32() { return (unsigned)__builtin_amdgcn_s_memrealtime(); }

#define NSTAMP 12
#define STAMP(i) do { if ((i) == PIDX - 50) { __builtin_amdgcn_sched_barrier(0); probe = now32(); __builtin_amdgcn_sched_barrier(0); } } while (0)
#define TRACE(i) do { if ((i) == PIDX) probe = tstart + ((int)(kept - (tstart << 8)) >> 8); } while (0)
#define PIN() do { asm volatile("" ::: "memory"); __builtin_amdgcn_sched_barrier(0); } while (0)
#define BARRIER() do { asm volatile("s_waitcnt lgkmcnt(0)" ::: "memory"); __builtin_amdgcn_s_barrier(); PIN(); } while (0)
#define VMCNT(n) asm volatile("s_waitcnt vmcnt(%0)" :: "i"(n) : "memory")

typedef volatile __attribute__((address_space(3))) unsigned vlds_u32;
typedef volatile __attribute__((address_space(3))) u32x2 vlds_u32x2;
typedef volatile __attribute__((address_space(3))) u32x4 vlds_u32x4;

struct Pace {
    unsigned step;
    int start;
    int offset;
    int bias;
    int rd;
    int pub;
};

template <int X, int PRO_, int GATE, int STAG_, int SLP>
__global__ __launch_bounds__(576) void kv_sweep_kernel(
    const unsigned short* q, const int* qk, const unsigned short* ksc,
    const unsigned short* kzp, const int* qv, const unsigned short* vsc,
    const unsigned short* vzp, const unsigned short* q_cos, const unsigned short* q_sin,
    const unsigned short* k_cos, const unsigned short* k_sin, unsigned char* ws, bf16* out, Pace pace)
{
    __shared__ __attribute__((aligned(16))) unsigned s_q[NH * DH];
    __shared__ __attribute__((aligned(16))) float s_sc[NH * 64 * 4];
    __shared__ __attribute__((aligned(16))) float s_part[8 * NI * DH];
    __shared__ __attribute__((aligned(16))) float s_red[8 * 28];
    __shared__ __attribute__((aligned(16))) float s_f[NH * 8];
    __shared__ __attribute__((aligned(16))) unsigned s_done[8];
    __shared__ __attribute__((aligned(16))) unsigned s_sync[8];
    __shared__ __attribute__((aligned(16))) unsigned s_act[16];

    unsigned probe = 0;
    constexpr bool TRABS = true;
    constexpr int ORD = 1, WID = 0, HOLD = GATE % 10 * 2, SLIP = GATE / 10 % 10 * 2560, SLIPT = GATE / 100;
    constexpr int PRO = PRO_ % 10, PF = PRO_ / 10, STAG = 0, LAY = 0, VS = 2, LDA = 16, STA = 16;
    constexpr unsigned NREG = 1u << STAG_;
    constexpr unsigned PSTR = LAY == 0 ? 64 : LAY == 1 ? UNITB + 256 : LAY == 2 ? UNITB + 128 : UNITB;
    constexpr unsigned USTR = LAY == 0 ? UNITB : 64;
    STAMP(0);
    unsigned long long pc0;
    asm volatile("s_getpc_b64 %0" : "=s"(pc0));
    const unsigned tstart = now32();
    const int tid = threadIdx.x;
    const int lane = tid & 63;
    const int wave = __builtin_amdgcn_readfirstlane(tid >> 6);
    const int r = lane >> 4;
    const int c = lane & 15;
    const int bid = blockIdx.x;
    const int m = (bid & 7) * 32 + (bid >> 3);
    const auto rw_ = __builtin_amdgcn_make_buffer_rsrc((void*)ws, 0, WS_BYTES, 0x00020000);

    auto merge = [&](int i, int hp, int c6, unsigned tag) {
        const f32x4 f = *reinterpret_cast<const f32x4*>(s_f + (2 * i + hp) * 8);
        const f32x2 zl = *reinterpret_cast<const f32x2*>(s_f + (2 * i + hp) * 8 + 4);
        const float zs = zl[0];
        f32x2 a[3];
#pragma unroll
        for (int k = 0; k < 3; ++k) a[k] = f32x2{-zs, -zs};
#pragma unroll
        for (int w = 0; w < 4; ++w) {
            const float* p = s_part + ((hp * 4 + w) * NI + i) * DH + c6 * 6;
#pragma unroll
            for (int k = 0; k < 3; ++k) a[k] += f[w] * *reinterpret_cast<const f32x2*>(p + 2 * k);
        }
        return u32x4{pk_bf16(a[0]), pk_bf16(a[1]), pk_bf16(a[2]), tag | ((__float_as_uint(zl[1]) >> (16 * (c6 & 1))) & 0xffffu)};
    };
    auto region = [&](unsigned tag) { return WS_PART + ((__builtin_amdgcn_readfirstlane(tag) >> 16) & (NREG - 1)) * (NWG * UNITB); };
    auto publish = [&](u32x4 v, unsigned reg, int i, int hp, int c6, bool on) {
        __builtin_amdgcn_raw_buffer_store_b128(v, rw_, on ? (hp * 8 + (c6 >> 2)) * USTR + (c6 & 3) * 16 : OOB, reg + i * 16 * USTR + m * PSTR, STA);
        asm volatile("s_nop 1" :: "v"(v) : "memory");
    };

    if (wave == 8) {
        unsigned long long pc1;
        asm volatile("s_getpc_b64 %0" : "=s"(pc1));
        pc0 &= ~255ull;
        const auto rcode = __builtin_amdgcn_make_buffer_rsrc((void*)pc0, 0, (unsigned)(pc1 - pc0), 0x00020000);
        auto warm_code = [&]() {
            unsigned x[2];
#pragma unroll
            for (int k = 0; k < 2; ++k) x[k] = __builtin_amdgcn_raw_buffer_load_b32(rcode, lane * 128 + k * 8192, 0, 0);
            asm volatile("s_waitcnt vmcnt(0)" :: "v"(x[0]), "v"(x[1]) : "memory");
        };
        if (PF) warm_code();
        const unsigned tag = ((unsigned)__builtin_amdgcn_raw_buffer_load_b16(rw_, (lane & 3) * 2, WS_EPOCH, 16) + 1u) << 16;
        unsigned origin = (tstart << 8) + pace.offset;
        if (lane < 8) { s_done[lane] = 0; s_sync[lane] = lane == 2 ? origin : lane < 4 ? 0 : tag; }
        PIN();
        BARRIER();
        const int mh = lane >> 5, mc = lane & 31;
        const unsigned rtag = __builtin_amdgcn_readlane(tag, 0) | __builtin_amdgcn_readlane(tag, 1) >> 16;
        constexpr int RPX = 2;
        constexpr int RSH = X / 100;
        constexpr unsigned RDLY = 200;
        constexpr int UP = X / 10 % 10 ? X / 10 % 10 * 82 : 1 << 24, DN = X % 10 ? X % 10 * 82 : 1 << 24;
        const int rj = ((bid >> 3) + 5 * (bid & 7)) & 31;
        const int rid = rj % (32 / RPX) == 0 ? rj / (32 / RPX) * 8 + (bid & 7) : -1;
        unsigned wround = now32() >> RSH, rround = wround, tick = 0, target = origin;
        bool warmed = false;
        int i = 0;
        auto kept_now = [&](unsigned now) {
            const u32x2 a = *(vlds_u32x2*)(s_act + 2 * (lane & 7));
            const int late = max((int)(a[0] - origin), (int)((now << 8) - a[1] - origin));
            return origin + (__builtin_amdgcn_readfirstlane(row16_sum(late)) >> 4);
        };
        while (i < NI - 1) {
            const unsigned now = now32();
            if ((now >> RSH) != wround && i < NI - 2) {
                wround = now >> RSH;
                if (rid >= 0 && (now & ((1u << RSH) - 1)) < 48)
                    __builtin_amdgcn_raw_buffer_store_b64(u32x2{kept_now(now), rtag + wround}, rw_, lane == 0 ? rid * 8 : OOB, WS_REP + (wround & 255) * 512, 16);
                PIN();
                continue;
            }
            if ((int)(((now - RDLY) >> RSH) - rround) > 0 && i < NI - 2) {
                rround = (now - RDLY) >> RSH;
                const u32x2 x = __builtin_amdgcn_raw_buffer_load_b64(rw_, c * 8, WS_REP + (rround & 255) * 512, 16);
                const int d = (int)(x[0] - origin);
                const bool ok = x[1] == rtag + rround && d < (32 << 16) && d > -(32 << 16);
                const int n = __builtin_popcountll(__builtin_amdgcn_ballot_w64(ok));
                if (n >= 48) {
                    target = origin + __builtin_amdgcn_readfirstlane((int)(row16_allsum(ok ? (float)d : 0.f) * 4.f / (float)n)) - pace.bias;
                    const int lost = min((int)(kept_now(now32()) - origin) - pace.bias, (int)(target - origin));
                    if (lost > 0) origin += lost;
                    if (SLIP && !SLIPT) { const int l2 = (int)(kept_now(now32()) - origin) - SLIP; if (l2 > 0) origin += l2; }
                }
                PIN();
                continue;
            }
            if ((now >> 5) != tick) {
                tick = now >> 5;
                origin += max(-DN, min(UP, (int)(target - origin)));
                if (SLIP && SLIPT) { const int l2 = (int)(kept_now(now) - origin) - SLIP; if (l2 > 0) origin += l2; }
                if (lane == 0) *(vlds_u32*)(s_sync + 2) = origin;
                PIN();
            }
            if (PF >= 2 && !warmed && (int)((now << 8) - origin) > 60 * (int)pace.step) { warm_code(); warmed = true; continue; }
            const u32x4 a = *(vlds_u32x4*)s_done, b = *(vlds_u32x4*)(s_done + 4);
            const unsigned mn = min(min(min(a[0], a[1]), min(a[2], a[3])), min(min(b[0], b[1]), min(b[2], b[3])));
            if (__builtin_amdgcn_readfirstlane(mn) <= (unsigned)i) { __builtin_amdgcn_s_sleep(2); continue; }
            PIN();
            if (i == 0) {
                f32x4 ml[4], f;
#pragma unroll
                for (int w = 0; w < 4; ++w) ml[w] = *reinterpret_cast<const f32x4*>(s_sc + (mc * 4 + w) * 64 + 16);
                const float M = fmaxf(fmaxf(ml[0][0], ml[1][0]), fmaxf(ml[2][0], ml[3][0]));
                float L = 0.f, zs = 0.f;
#pragma unroll
                for (int w = 0; w < 4; ++w) { f[w] = __builtin_amdgcn_exp2f(ml[w][0] - M); L += f[w] * ml[w][1]; zs += f[w] * ml[w][2]; }
                const float inv = 1.f / L;
                *reinterpret_cast<f32x4*>(s_f + mc * 8) = f * inv;
                *reinterpret_cast<f32x2*>(s_f + mc * 8 + 4) = f32x2{zs * inv, M + __builtin_amdgcn_logf(L)};
                PIN();
                if (lane == 0) *(vlds_u32*)(s_sync + 3) = 1;
                PIN();
            }
            if (PF >= 2 && i == NI - 3) warm_code();
            const u32x4 v = merge(i, mh, mc, tag);
            PIN();
            if (pace.pub < 0) publish(v, region(tag), i, mh, mc, true);
            else *reinterpret_cast<u32x4*>(s_part + (mh * 4 * NI + i) * DH + mc * 4) = v;
            PIN();
            ++i;
        }
        u32x4 v[NI - 1];
#pragma unroll
        for (int i = 0; i < NI - 1; ++i) { v[i] = *reinterpret_cast<const u32x4*>(s_part + (mh * 4 * NI + i) * DH + mc * 4); asm volatile("" : "+v"(v[i])); }
        STAMP(5);
        while ((int)((now32() << 8) - origin) < pace.pub) __builtin_amdgcn_s_sleep(1);
        PIN();
        STAMP(6);
#pragma unroll
        for (int i = 0; i < NI - 1; ++i) publish(v[i], region(tag), i, mh, mc, pace.pub >= 0);
        STAMP(7);
        return;
    }

    const int rw = wave & 3, hp = wave >> 2;
    const int row0 = m * 64 + rw * 16;

    const unsigned sidx = STAG == 0 ? hp * 1024 + m * 4 + rw : STAG == 1 ? m * 8 + wave : (hp * 1024 + (m & 31) * 32 + (m >> 5) * 4 + rw);
    unsigned off = pace.start + ((pace.step * sidx) >> 11);
    unsigned origin = tstart << 8, kept = 0;
    bool wide = false;
    unsigned prev = 0, hold = 0;
    int holdn = 0;
    const unsigned step = pace.step;
    auto pace_wait = [&]() {
        unsigned org = __builtin_amdgcn_readfirstlane(origin);
        if (HOLD && holdn > 0) { --holdn; if ((int)(hold - org) > 0) org = hold; }
        const unsigned tgt = org + off;
        prev = kept;
        unsigned now = now32() << 8;
        kept = ((int)(now - tgt) > 0 ? now : tgt) - off;
        wide = (int)(now - tgt) > 30 * 256;
        while ((int)(tgt - now) > 0) { if (SLP) __builtin_amdgcn_s_sleep(SLP); now = now32() << 8; }
        off += step;
    };
    auto report = [&]() {
        if (lane == 0) *(vlds_u32x2*)(s_act + 2 * wave) = u32x2{kept, off};
        origin = *(vlds_u32*)(s_sync + 2);
        PIN();
    };
#define TRV (TRABS ? kept - (tstart << 8) : kept - __builtin_amdgcn_readfirstlane(origin))

    const int qe = min(tid, 383);
    const int qh = qe / 12, qd = (qe % 12) * 8;
    u32x4 lq[2], lc[2], ls[2];
    auto load_qs = [&]() {
#pragma unroll
        for (int k = 0; k < 2; ++k) {
            lq[k] = *reinterpret_cast<const u32x4*>(q + qh * DH + k * HALF + qd);
            lc[k] = *reinterpret_cast<const u32x4*>(q_cos + k * HALF + qd);
            ls[k] = *reinterpret_cast<const u32x4*>(q_sin + k * HALF + qd);
        }
        PIN();
    };
    u32x2 lsc[4];
    auto load_sc = [&]() {
#pragma unroll
        for (int a = 0; a < 4; ++a) {
            const unsigned short* base = a == 0 ? ksc : a == 1 ? kzp : a == 2 ? vsc : vzp;
            lsc[a] = __builtin_nontemporal_load(reinterpret_cast<const u32x2*>(base + (size_t)(tid >> 4) * SKV + m * 64 + (tid & 15) * 4));
        }
        PIN();
    };

    const auto rc = __builtin_amdgcn_make_buffer_rsrc((void*)k_cos, 0, CSB, 0x00020000);
    const auto rs = __builtin_amdgcn_make_buffer_rsrc((void*)k_sin, 0, CSB, 0x00020000);
    u32x2 raw[2][6];
    auto load_cs = [&](int t) {
        const unsigned co = r * 384 + c * 8;
        const unsigned so1 = r * 384 + (c < 8 ? c * 8 + 320 : c * 8 - 64);
        const unsigned rb = (row0 + (t + 2 * hp) * 4) * 384u;
        raw[t][0] = __builtin_amdgcn_raw_buffer_load_b64(rc, co, rb, 2);
        raw[t][1] = __builtin_amdgcn_raw_buffer_load_b64(rc, co + 128, rb, 2);
        raw[t][2] = __builtin_amdgcn_raw_buffer_load_b64(rc, co + 256, rb, 2);
        raw[t][3] = __builtin_amdgcn_raw_buffer_load_b64(rs, co + 192, rb, 2);
        raw[t][4] = __builtin_amdgcn_raw_buffer_load_b64(rs, so1, rb, 2);
        raw[t][5] = __builtin_amdgcn_raw_buffer_load_b64(rs, co + 64, rb, 2);
        PIN();
    };
    u32x4 tab[4][3];
    auto pack_cs = [&](int t) {
#pragma unroll
        for (int j = 0; j < 3; ++j) {
#pragma unroll
            for (int k = 0; k < 2; ++k) {
                tab[t][j][2 * k] = __builtin_amdgcn_perm(raw[t][3 + j][k], raw[t][j][k], 0x05040100);
                tab[t][j][2 * k + 1] = __builtin_amdgcn_perm(raw[t][3 + j][k], raw[t][j][k], 0x07060302);
            }
            *reinterpret_cast<u32x4*>(s_part + (wave ^ 4) * (NI * DH) + ((t * 3 + j) * 64 + lane) * 4) = tab[t][j];
        }
        PIN();
    };
    const auto rk = __builtin_amdgcn_make_buffer_rsrc((void*)qk, 0, KVB, 0x00020000);
    const auto rv = __builtin_amdgcn_make_buffer_rsrc((void*)qv, 0, KVB, 0x00020000);
    const unsigned voff = r * ROWB + c * 16;
    const unsigned woff = row0 * ROWB + hp * HEADB;
    auto toff = [&](int t) { return (unsigned)(t ^ (2 * hp)) * (4 * ROWB); };
    u32x4 ring[4][3];
    auto ld = [&](int slot, int j, __amdgpu_buffer_rsrc_t rr, unsigned off) {
        ring[slot][j] = __builtin_amdgcn_raw_buffer_load_b128(rr, voff + j * 256, off, 2);
        PIN();
    };
    auto ld_tile = [&](int slot, __amdgpu_buffer_rsrc_t rr, unsigned off) { ld(slot, 0, rr, off); ld(slot, 1, rr, off); ld(slot, 2, rr, off); };
    auto refill = [&](int slot, int j, __amdgpu_buffer_rsrc_t rr, unsigned goff, bool on) {
        if (ORD == 0) {
            if (on) { if (WID && wide) VMCNT(2 + WID); else VMCNT(2); if (j == 0) pace_wait(); }
            ld(slot, j, rr, goff);
        } else {
            if (on) { if (j == 0) pace_wait(); if (WID && wide) VMCNT(2 + WID); else VMCNT(2); }
            ld(slot, j, rr, goff);
            if (on && j == 0) { const unsigned t = (now32() << 8) - 1024 - (off - step); if ((int)(t - kept) > 0) kept = t;
                if (HOLD && (int)(kept - prev) > 100 * 256) { hold = kept; holdn = HOLD; } }
        }
        if (j == 2) report();
    };
    auto q_to_lds = [&]() {
        float o[2][8];
#pragma unroll
        for (int k = 0; k < 2; ++k)
#pragma unroll
            for (int u = 0; u < 4; ++u) {
                const unsigned pp = lq[k ^ 1][u] ^ (k == 0 ? 0x80008000u : 0u);
                o[k][2 * u] = rbf(rbf(bflo(lq[k][u]) * bflo(lc[k][u])) + rbf(bflo(pp) * bflo(ls[k][u])));
                o[k][2 * u + 1] = rbf(rbf(bfhi(lq[k][u]) * bfhi(lc[k][u])) + rbf(bfhi(pp) * bfhi(ls[k][u])));
            }
#pragma unroll
        for (int v = 0; v < 2; ++v) {
            u32x4 lo, hi;
#pragma unroll
            for (int e = 0; e < 4; ++e) {
                lo[e] = __builtin_amdgcn_perm(__float_as_uint(o[1][4 * v + e]), __float_as_uint(o[0][4 * v + e]), 0x07060302);
                hi[e] = __builtin_amdgcn_perm(__float_as_uint(o[0][4 * v + e]) ^ 0x80000000u, __float_as_uint(o[1][4 * v + e]), 0x07060302);
            }
            *reinterpret_cast<u32x4*>(s_q + qh * DH + qd + 4 * v) = lo;
            *reinterpret_cast<u32x4*>(s_q + qh * DH + HALF + qd + 4 * v) = hi;
        }
        PIN();
    };
    auto sc_to_lds = [&]() {
        float* dst = s_sc + tid * 16;
#pragma unroll
        for (int u = 0; u < 2; ++u) {
            *reinterpret_cast<f32x4*>(dst + 8 * u) = f32x4{bflo(lsc[0][u]) * SCALE_LOG2, bflo(lsc[1][u]), bflo(lsc[2][u]), bflo(lsc[3][u])};
            *reinterpret_cast<f32x4*>(dst + 8 * u + 4) = f32x4{bfhi(lsc[0][u]) * SCALE_LOG2, bfhi(lsc[1][u]), bfhi(lsc[2][u]), bfhi(lsc[3][u])};
        }
        PIN();
    };

    auto k0 = [&](int t) { pace_wait(); ld_tile(t, rk, woff + toff(t)); };
    if (PRO == 0) {
        load_qs(); load_sc(); load_cs(0);
        pace_wait(); load_cs(1);
        q_to_lds(); sc_to_lds();
        STAMP(1);
        k0(0); pack_cs(0);
        k0(1); pack_cs(1);
        VMCNT(3); k0(2);
        STAMP(2);
        VMCNT(3); k0(3);
    } else if (PRO == 1) {
        load_cs(0); load_sc();
        pace_wait(); load_cs(1); STAMP(1); if (wave < 6) load_qs();
        pack_cs(0); sc_to_lds();
        k0(0); pack_cs(1); if (wave < 6) q_to_lds();
        k0(1);
        VMCNT(3); k0(2);
        STAMP(2);
        VMCNT(3); k0(3);
    } else if (PRO == 2) {
        load_cs(0); load_sc();
        pace_wait(); load_cs(1);
        STAMP(1);
        pack_cs(0); sc_to_lds();
        k0(0); if (wave < 6) load_qs(); pack_cs(1);
        k0(1); if (wave < 6) q_to_lds();
        VMCNT(3); k0(2);
        STAMP(2);
        VMCNT(3); k0(3);
    } else if (PRO == 3) {
        load_cs(0); load_sc();
        pace_wait(); load_cs(1);
        STAMP(1);
        pack_cs(0); sc_to_lds();
        k0(0); pack_cs(1);
        k0(1); if (wave < 6) load_qs();
        VMCNT(9); k0(2); if (wave < 6) q_to_lds();
        STAMP(2);
        VMCNT(3); k0(3);
    }
    if (lane == 0) *(vlds_u32x2*)(s_act + 2 * wave) = u32x2{kept, off};
    BARRIER();
    origin = *(vlds_u32*)(s_sync + 2);
    STAMP(3);

    u32x4 qp[3];
    auto load_q = [&](int h, int j) { qp[j] = *reinterpret_cast<const u32x4*>(s_q + h * DH + 64 * j + 4 * c); };
    load_q(hp, 0); load_q(hp, 1); load_q(hp, 2);
#pragma unroll
    for (int t = 0; t < 2; ++t)
#pragma unroll
        for (int j = 0; j < 3; ++j) tab[2 + t][j] = *reinterpret_cast<const u32x4*>(s_part + wave * (NI * DH) + ((t * 3 + j) * 64 + lane) * 4);

    for (int i = 0; i < NI; ++i) {
        const bool last = i + 1 == NI;
        const int h = 2 * i + hp;
        const unsigned hnext = (i + 1) * (2 * HEADB) + woff;
        const auto rn = __builtin_amdgcn_make_buffer_rsrc((void*)(last ? qv : qk), 0, KVB, 0x00020000);
        float* scp = s_sc + (h * 4 + rw) * 64;
        f32x4 sc[4];
#pragma unroll
        for (int t = 0; t < 4; ++t) sc[t] = *reinterpret_cast<const f32x4*>(scp + (4 * (t ^ (2 * hp)) + r) * 4);
        float s[4];
#pragma unroll
        for (int t = 0; t < 4; ++t) {
            f32x2 dot, wsum;
#pragma unroll
            for (int j = 0; j < 3; ++j) {
                f32x2 w01, w23, k01, k23;
                dot_cvt(qp[j], tab[t][j], ring[t][j], w01, w23, k01, k23);
                if (j == 0) { wsum = w01 + w23; dot = k01 * w01; } else { wsum += w01; wsum += w23; dot += k01 * w01; }
                dot += k23 * w23;
                asm volatile("" : "+v"(dot), "+v"(wsum));
                PIN();
                if (t == 3) load_q(last ? h : h + 2, j);
                if (t == 0 || t == VS) refill(t, j, rn, last ? woff + toff(t == VS ? 1 : 0) : hnext + toff(t), true);
                else refill(t, j, rk, last ? OOB : hnext + toff(t), !last);
                if (t == 0 && j == 0) TRACE(i);
            }
            const float a = (dot[0] + dot[1]) - sc[t][1] * (wsum[0] + wsum[1]);
            s[t] = row16_allsum(a) * sc[t][0];
        }
        const float mx = xrow_max(fmaxf(fmaxf(s[0], s[1]), fmaxf(s[2], s[3])));
        float l = 0.f, z = 0.f;
#pragma unroll
        for (int t = 0; t < 4; ++t) {
            const float p = __builtin_amdgcn_exp2f(s[t] - mx);
            l += p;
            s[t] = p * sc[t][2];
            z += s[t] * sc[t][3];
        }
        l = xrow_sum(l);
        z = xrow_sum(z);
        if (c == 0) {
            *reinterpret_cast<f32x4*>(scp + r * 4) = f32x4{s[0], s[1], s[2], s[3]};
            *reinterpret_cast<f32x4*>(scp + 16) = f32x4{mx, l, z, 0.f};
        }
        PIN();
    }
    STAMP(4);

    const float* pwp = s_sc + (hp * 4 + rw) * 64 + r * 4;
    f32x4 pw = *reinterpret_cast<const f32x4*>(pwp);
    for (int i = 0; i < NI; ++i) {
        const bool last = i + 1 == NI;
        const unsigned hoff = i * (2 * HEADB) + woff;
        f32x2 acc[3][2];
#pragma unroll
        for (int t = 0; t < 4; ++t) {
            const f32x2 p2 = {pw[t], pw[t]};
#pragma unroll
            for (int j = 0; j < 3; ++j) {
                const u32x4 vv = ring[(t & 1) * VS][j];
                const f32x2 v01 = {(float)(int)vv[0], (float)(int)vv[1]}, v23 = {(float)(int)vv[2], (float)(int)vv[3]};
                if (t == 0) { acc[j][0] = p2 * v01; acc[j][1] = p2 * v23; }
                else { acc[j][0] += p2 * v01; acc[j][1] += p2 * v23; }
                asm volatile("" : "+v"(acc[j][0]), "+v"(acc[j][1]));
                PIN();
                if (t == 3 && j == 2) pw = *reinterpret_cast<const f32x4*>(pwp + (last ? i : i + 1) * 512);
                if (t < 2) refill((t & 1) * VS, j, rv, hoff + toff(t + 2), true);
                else refill((t & 1) * VS, j, rv, last ? OOB : hoff + 2 * HEADB + toff(t - 2), !last);
                if (t == 0 && j == 0) TRACE(16 + i);
            }
        }
        float* sl = s_part + (wave * NI + i) * DH;
#pragma unroll
        for (int j = 0; j < 3; ++j) {
            const float x = swap32_add(acc[j][0][0], acc[j][1][0]);
            const float y = swap32_add(acc[j][0][1], acc[j][1][1]);
            sl[64 * j + 4 * c + r] = swap16_add(x, y);
        }
        PIN();
        if (lane == 0) *(vlds_u32*)(s_done + wave) = i + 1;
        PIN();
    }
    STAMP(6);
    unsigned old = 0;
    if (lane == 0) old = __hip_atomic_fetch_add(s_sync + hp, 1u, __ATOMIC_RELAXED, __HIP_MEMORY_SCOPE_WORKGROUP);
    PIN();
    const unsigned tag = s_sync[4 + (lane & 3)];
    if (__builtin_amdgcn_readfirstlane(old) == 3) {
        while (__builtin_amdgcn_readfirstlane(*(vlds_u32*)(s_sync + 3)) == 0) __builtin_amdgcn_s_sleep(1);
        PIN();
        publish(merge(NI - 1, hp, lane & 31, tag), region(tag), NI - 1, hp, lane & 31, lane < 32);
        PIN();
    }
    STAMP(7);
    {
        const unsigned o = __builtin_amdgcn_readfirstlane(origin);
        while ((int)((now32() << 8) - o) < pace.rd) __builtin_amdgcn_s_sleep(1);
        PIN();
    }
    STAMP(8);
    {
        u32x4 x, y;
        unsigned xo = (tid >> 2) * PSTR + (tid & 3) * 16, yo = xo + 128 * PSTR;
        for (;;) {
            const u32x4 xn = __builtin_amdgcn_raw_buffer_load_b128(rw_, xo, region(tag) + bid * USTR, LDA);
            const u32x4 yn = __builtin_amdgcn_raw_buffer_load_b128(rw_, yo, region(tag) + bid * USTR, LDA);
            unsigned bad = (xo != OOB && (xn[3] ^ tag) > 0xffffu ? 1u : 0u) | (yo != OOB && (yn[3] ^ tag) > 0xffffu ? 2u : 0u);
            bad |= __builtin_amdgcn_mov_dpp(bad, 0xb1, 0xf, 0xf, true);
            bad |= __builtin_amdgcn_mov_dpp(bad, 0x4e, 0xf, 0xf, true);
            const bool tx = xo != OOB, ty = yo != OOB;
#pragma unroll
            for (int k = 0; k < 4; ++k) { x[k] = tx ? xn[k] : x[k]; y[k] = ty ? yn[k] : y[k]; }
            xo = bad & 1 ? xo : OOB;
            yo = bad & 2 ? yo : OOB;
            if (__builtin_amdgcn_ballot_w64(bad != 0) == 0 || now32() - tstart > SPIN_LIMIT) break;
            PIN();
        }
        STAMP(9);
        const unsigned sel = lane & 1 ? 0x01000504u : 0x05040100u;
        const float lx = __uint_as_float(__builtin_amdgcn_perm(__builtin_amdgcn_mov_dpp(x[3], 0xb1, 0xf, 0xf, true), x[3], sel));
        const float ly = __uint_as_float(__builtin_amdgcn_perm(__builtin_amdgcn_mov_dpp(y[3], 0xb1, 0xf, 0xf, true), y[3], sel));
        float M = fmaxf(lx, ly);
        M = fmaxf(M, dpp<0x128>(M));
        M = fmaxf(M, dpp<0x124>(M));
        M = xrow_max(M);
        const float w0 = __builtin_amdgcn_exp2f(lx - M), w1 = __builtin_amdgcn_exp2f(ly - M);
        float a[8];
#pragma unroll
        for (int k = 0; k < 3; ++k) {
            a[2 * k] = w0 * bflo(x[k]) + w1 * bflo(y[k]);
            a[2 * k + 1] = w0 * bfhi(x[k]) + w1 * bfhi(y[k]);
        }
        a[6] = a[7] = w0 + w1;
        float b[4], d[2];
#pragma unroll
        for (int k = 0; k < 4; ++k) b[k] = swap32_add(a[k], a[4 + k]);
#pragma unroll
        for (int k = 0; k < 2; ++k) d[k] = swap16_add(b[k], b[2 + k]);
#pragma unroll
        for (int k = 0; k < 2; ++k) { d[k] += dpp<0x128>(d[k]); d[k] += dpp<0x124>(d[k]); }
        if (c < 4) *reinterpret_cast<f32x2*>(s_red + wave * 28 + (r < 3 ? c * 6 + r * 2 : 24)) = f32x2{d[0], r < 3 ? d[1] : M};
        STAMP(10);
        BARRIER();
        if (tid < 24) {
            f32x2 sm[8];
            float ov[8];
#pragma unroll
            for (int w = 0; w < 8; ++w) { sm[w] = *reinterpret_cast<const f32x2*>(s_red + w * 28 + 24); ov[w] = s_red[w * 28 + tid]; }
            float MM = sm[0][1];
#pragma unroll
            for (int w = 1; w < 8; ++w) MM = fmaxf(MM, sm[w][1]);
            float S = 0.f, o = 0.f;
#pragma unroll
            for (int w = 0; w < 8; ++w) {
                const float ff = __builtin_amdgcn_exp2f(sm[w][1] - MM);
                S += ff * sm[w][0];
                o += ff * ov[w];
            }
            out[bid * 24 + tid] = __float2bfloat16(o * __builtin_amdgcn_rcpf(S));
        }
        if (bid == 0 && wave == 1) __builtin_amdgcn_raw_buffer_store_b16((unsigned short)(tag >> 16), rw_, lane < 4 ? lane * 2 : OOB, WS_EPOCH, 16);
    }
    if (wave == PWAVE) {
        if (PIDX == 100) { asm volatile("s_waitcnt vmcnt(0)" ::: "memory"); probe = now32(); }
        const unsigned ptag = __builtin_amdgcn_readlane(tag, 0) | __builtin_amdgcn_readlane(tag, 1) >> 16;
        __builtin_amdgcn_raw_buffer_store_b128(u32x4{tstart, probe, ptag, 0}, rw_, lane == 0 ? bid * 16 : OOB, WS_PROBE, 16);
        asm volatile("s_nop 1" ::: "memory");
        if (bid == 100) {
            while ((int)(now32() - tstart) < 12500) __builtin_amdgcn_s_sleep(1);
            u32x4 p[4];
            for (;;) {
                bool ok = true;
                for (int k = 0; k < 4; ++k) { p[k] = __builtin_amdgcn_raw_buffer_load_b128(rw_, lane * 16 + k * 1024, WS_PROBE, 16); ok = ok && p[k][2] == ptag; }
                if (__builtin_amdgcn_ballot_w64(!ok) == 0 || now32() - tstart > SPIN_LIMIT) break;
                PIN();
            }
            float mn = 1e9f, mx = -1e9f, av = 0.f;
            for (int k = 0; k < 4; ++k) { mn = fminf(mn, (float)(int)(p[k][0] - tstart)); const float v = (float)(int)(p[k][1] - tstart); mx = fmaxf(mx, v); av += v; }
            mn = -xrow_max(row16_allmax(-mn)); mx = xrow_max(row16_allmax(mx)); av = xrow_sum(row16_allsum(av)) / 256.f;
            float val = (PMAX ? mx : av) - mn - PSUB;
            if (PIDX == 99) val = 0.f;
            val = fminf(fmaxf(val, 0.f), 3000.f);
            const unsigned endt = tstart + __builtin_amdgcn_readfirstlane((int)(mn + 14000.f + 5.f * val));
            while ((int)(endt - now32()) > 0) __builtin_amdgcn_s_sleep(1);
        }
    }
}

template <int X, int PRO, int GATE, int STAG>
static void launch_cfg(void* const* d_in, void* d_out, void* d_ws, hipStream_t stream, float t_us, float start_us, float off_us, float debt_us, float adj_pct, float pub_us) {
    Pace pace;
    pace.step = (unsigned)(t_us * 25600.f / 128.f);
    pace.start = (int)(start_us * 25600.f);
    pace.offset = (int)(off_us * 25600.f);
    pace.bias = (int)(debt_us * 25600.f);
    pace.rd = (int)((start_us + t_us * (129.f / 128.f) + adj_pct) * 25600.f);
    pace.pub = (int)((start_us + t_us * (129.f / 128.f) + pub_us) * 25600.f);
    kv_sweep_kernel<X, PRO, GATE, STAG, 1><<<NWG, 576, 0, stream>>>((const unsigned short*)d_in[0], (const int*)d_in[1], (const unsigned short*)d_in[2],
        (const unsigned short*)d_in[3], (const int*)d_in[4], (const unsigned short*)d_in[5], (const unsigned short*)d_in[6],
        (const unsigned short*)d_in[7], (const unsigned short*)d_in[8], (const unsigned short*)d_in[9], (const unsigned short*)d_in[10],
        (unsigned char*)d_ws, (bf16*)d_out, pace);
}

#define CFG 966, 21, 120, 0
#define PRM 102.5f, 0.5f, 1.0f, 0.05f, 1.8f, 0.8f
extern "C" void kernel_launch(void* const* d_in, const int* in_sizes, int n_in,
                              void* d_out, int out_size, void* d_ws, size_t ws_size,
                              hipStream_t stream) {
    launch_cfg<CFG>(d_in, d_out, d_ws, stream, PRM);
}
```

```cpp
#define PIDX 99
#define PMAX 0
#define PSUB 0
#define PWAVE 0
#include <hip/hip_runtime.h>
#include <hip/hip_bf16.h>
#include <cstdint>
#include <type_traits>

using bf16 = __hip_bfloat16;
using u32x4 = __attribute__((ext_vector_type(4))) unsigned;
using u32x2 = __attribute__((ext_vector_type(2))) unsigned;
using u64x8 = __attribute__((ext_vector_type(8))) unsigned long long;
using f32x2 = __attribute__((ext_vector_type(2))) float;
using f32x4 = __attribute__((ext_vector_type(4))) float;
using bf16x2 = __attribute__((ext_vector_type(2))) __bf16;

constexpr int DH = 192;
constexpr int HALF = 96;
constexpr int NH = 32;
constexpr int SKV = 16384;
constexpr int NWG = 256;
constexpr int NI = 16;
constexpr unsigned ROWB = DH * 4;
constexpr unsigned HEADB = SKV * ROWB;
constexpr unsigned KVB = NH * HEADB;
constexpr unsigned CSB = SKV * DH * 2;
constexpr unsigned OOB = 0x80000000u;
constexpr float SCALE_LOG2 = 1.4426950408889634f * 0.07216878364870322f;
constexpr unsigned SPIN_LIMIT = 20000000u;

constexpr unsigned UNITB = NWG * 64;
constexpr unsigned WS_PART = 0;
constexpr unsigned WS_EPOCH = NWG * (UNITB + 1024);
constexpr unsigned WS_REP = WS_EPOCH + 256;
constexpr unsigned WS_PROBE = WS_REP + 256 * 512;
constexpr unsigned WS_BYTES = WS_PROBE + 4096;

__device__ __forceinline__ float bflo(unsigned u) { return __uint_as_float(u << 16); }
__device__ __forceinline__ float bfhi(unsigned u) { return __uint_as_float(u & 0xffff0000u); }
__device__ __forceinline__ float rbf(float x) { return __bfloat162float(__float2bfloat16(x)); }
__device__ __forceinline__ unsigned pk_bf16(f32x2 a) { return __builtin_bit_cast(unsigned, __builtin_convertvector(a, bf16x2)); }
__device__ __forceinline__ void dot_cvt(const u32x4 q, const u32x4 t, const u32x4 ki, f32x2& w01, f32x2& w23, f32x2& k01, f32x2& k23) {
    float w0, w1, w2, w3;
    float k0 = __uint_as_float(ki[0]), k1 = __uint_as_float(ki[1]), k2 = __uint_as_float(ki[2]), k3 = __uint_as_float(ki[3]);
    asm("v_dot2_f32_bf16 %0, %8, %12, 0\n\tv_dot2_f32_bf16 %1, %9, %13, 0\n\tv_dot2_f32_bf16 %2, %10, %14, 0\n\tv_dot2_f32_bf16 %3, %11, %15, 0\n\t"
        "v_cvt_f32_i32 %4, %4\n\tv_cvt_f32_i32 %5, %5\n\tv_cvt_f32_i32 %6, %6\n\tv_cvt_f32_i32 %7, %7"
        : "=&v"(w0), "=&v"(w1), "=&v"(w2), "=&v"(w3), "+v"(k0), "+v"(k1), "+v"(k2), "+v"(k3)
        : "v"(q[0]), "v"(q[1]), "v"(q[2]), "v"(q[3]), "v"(t[0]), "v"(t[1]), "v"(t[2]), "v"(t[3]));
    w01 = f32x2{w0, w1}; w23 = f32x2{w2, w3}; k01 = f32x2{k0, k1}; k23 = f32x2{k2, k3};
}

template <int CTRL> __device__ __forceinline__ float dpp(float x) {
    return __builtin_bit_cast(float, __builtin_amdgcn_mov_dpp(__builtin_bit_cast(int, x), CTRL, 0xf, 0xf, true));
}
__device__ __forceinline__ float row16_allsum(float x) {
    x += dpp<0x128>(x);
    x += dpp<0x124>(x);
    x += dpp<0x122>(x);
    x += dpp<0x121>(x);
    return x;
}
__device__ __forceinline__ float row16_allmax(float x) {
    x = fmaxf(x, dpp<0x128>(x));
    x = fmaxf(x, dpp<0x124>(x));
    x = fmaxf(x, dpp<0x122>(x));
    x = fmaxf(x, dpp<0x121>(x));
    return x;
}
__device__ __forceinline__ float xrow_max(float x) {
    auto s = __builtin_amdgcn_permlane16_swap(__float_as_uint(x), __float_as_uint(x), false, false);
    x = fmaxf(__uint_as_float(s[0]), __uint_as_float(s[1]));
    auto t = __builtin_amdgcn_permlane32_swap(__float_as_uint(x), __float_as_uint(x), false, false);
    return fmaxf(__uint_as_float(t[0]), __uint_as_float(t[1]));
}
__device__ __forceinline__ float xrow_sum(float x) {
    auto s = __builtin_amdgcn_permlane16_swap(__float_as_uint(x), __float_as_uint(x), false, false);
    x = __uint_as_float(s[0]) + __uint_as_float(s[1]);
    auto t = __builtin_amdgcn_permlane32_swap(__float_as_uint(x), __float_as_uint(x), false, false);
    return __uint_as_float(t[0]) + __uint_as_float(t[1]);
}
__device__ __forceinline__ float swap32_add(float a, float b) {
    auto t = __builtin_amdgcn_permlane32_swap(__float_as_uint(a), __float_as_uint(b), false, false);
    return __uint_as_float(t[0]) + __uint_as_float(t[1]);
}
__device__ __forceinline__ float swap16_add(float a, float b) {
    auto t = __builtin_amdgcn_permlane16_swap(__float_as_uint(a), __float_as_uint(b), false, false);
    return __uint_as_float(t[0]) + __uint_as_float(t[1]);
}
__device__ __forceinline__ int row16_sum(int x) {
    x += __builtin_amdgcn_mov_dpp(x, 0x128, 0xf, 0xf, true);
    x += __builtin_amdgcn_mov_dpp(x, 0x124, 0xf, 0xf, true);
    x += __builtin_amdgcn_mov_dpp(x, 0x122, 0xf, 0xf, true);
    x += __builtin_amdgcn_mov_dpp(x, 0x121, 0xf, 0xf, true);
    return x;
}
__device__ __forceinline__ unsigned now32() { return (unsigned)__builtin_amdgcn_s_memrealtime(); }

#define NSTAMP 12
#define STAMP(i) do { if ((i) == PIDX - 50) { __builtin_amdgcn_sched_barrier(0); probe = now32(); __builtin_amdgcn_sched_barrier(0); } } while (0)
#define PSTAMP(i) do { if ((i) == PIDX) { __builtin_amdgcn_sched_barrier(0); probe = now32(); asm volatile("" : "+s"(probe) :: "memory"); __builtin_amdgcn_sched_barrier(0); } } while (0)
#define TRACE(i) do { if ((i) == PIDX) probe = tstart + ((int)(kept - (tstart << 8)) >> 8); } while (0)
#define PIN() do { asm volatile("" ::: "memory"); __builtin_amdgcn_sched_barrier(0); } while (0)
#define BARRIER() do { asm volatile("s_waitcnt lgkmcnt(0)" ::: "memory"); __builtin_amdgcn_s_barrier(); PIN(); } while (0)
#define VMCNT(n) asm volatile("s_waitcnt vmcnt(%0)" :: "i"(n) : "memory")

typedef volatile __attribute__((address_space(3))) unsigned vlds_u32;
typedef volatile __attribute__((address_space(3))) u32x2 vlds_u32x2;
typedef volatile __attribute__((address_space(3))) u32x4 vlds_u32x4;

struct Pace {
    unsigned step;
    int start;
    int offset;
    int bias;
    int rd;
    int pub;
};

template <int X, int PRO_, int GATE, int STAG_, int SLP>
__global__ __launch_bounds__(576) void kv_sweep_kernel(
    const unsigned short*, const int*, const unsigned short*, const unsigned short*, const int*, const unsigned short*,
    const unsigned short*, const unsigned short*, const unsigned short*, const unsigned short*, const unsigned short*, unsigned char*, bf16*, Pace)
{
    __shared__ __attribute__((aligned(16))) unsigned s_q[NH * DH];
    __shared__ __attribute__((aligned(16))) float s_sc[NH * 64 * 4];
    __shared__ __attribute__((aligned(16))) float s_part[8 * NI * DH];
    __shared__ __attribute__((aligned(16))) float s_red[8 * 28];
    __shared__ __attribute__((aligned(16))) float s_f[NH * 8];
    __shared__ __attribute__((aligned(16))) unsigned s_done[8];
    __shared__ __attribute__((aligned(16))) unsigned s_sync[8];
    __shared__ __attribute__((aligned(16))) unsigned s_act[16];

    unsigned probe = 0;
    constexpr bool TRABS = true;
    constexpr int ORD = 1, WID = 0, HOLD = GATE % 10 * 2, SLIP = GATE / 10 % 10 * 2560, SLIPT = GATE / 100;
    constexpr int PRO = PRO_ % 10, PF = PRO_ / 10, STAG = 0, LAY = 0, VS = 2, LDA = 16, STA = 16;
    constexpr unsigned NREG = 1u << STAG_;
    constexpr unsigned PSTR = LAY == 0 ? 64 : LAY == 1 ? UNITB + 256 : LAY == 2 ? UNITB + 128 : UNITB;
    constexpr unsigned USTR = LAY == 0 ? UNITB : 64;
    unsigned long long pc0, t0_;
    u64x8 ka, kb;
    asm volatile("s_memrealtime %1\n\ts_load_dwordx16 %2, %4, 0x0\n\ts_load_dwordx16 %3, %4, 0x40\n\ts_getpc_b64 %0\n\ts_waitcnt lgkmcnt(0)"
                 : "=&s"(pc0), "=&s"(t0_), "=&s"(ka), "=&s"(kb) : "s"(__builtin_amdgcn_kernarg_segment_ptr()));
    typedef const __attribute__((address_space(1))) unsigned short* gptr;
    const gptr q = (gptr)ka[0], ksc = (gptr)ka[2], kzp = (gptr)ka[3], vsc = (gptr)ka[5], vzp = (gptr)ka[6], q_cos = (gptr)ka[7], q_sin = (gptr)kb[0];
    void* const qk = (void*)ka[1]; void* const qv = (void*)ka[4]; void* const k_cos = (void*)kb[1]; void* const k_sin = (void*)kb[2];
    unsigned char* const ws = (unsigned char*)(__attribute__((address_space(1))) unsigned char*)kb[3];
    __attribute__((address_space(1))) unsigned short* const out = (__attribute__((address_space(1))) unsigned short*)kb[4];
    const Pace pace = {(unsigned)kb[5], (int)(kb[5] >> 32), (int)kb[6], (int)(kb[6] >> 32), (int)kb[7], (int)(kb[7] >> 32)};
    const unsigned tstart = (unsigned)t0_;
    PSTAMP(69);
    const int tid = threadIdx.x;
    const int lane = tid & 63;
    const int wave = __builtin_amdgcn_readfirstlane(tid >> 6);
    const int r = lane >> 4;
    const int c = lane & 15;
    const int bid = blockIdx.x;
    const int m = (bid & 7) * 32 + (bid >> 3);
    const auto rw_ = __builtin_amdgcn_make_buffer_rsrc((void*)ws, 0, WS_BYTES, 0x00020000);

    auto merge = [&](int i, int hp, int c6, unsigned tag) {
        const f32x4 f = *reinterpret_cast<const f32x4*>(s_f + (2 * i + hp) * 8);
        const f32x2 zl = *reinterpret_cast<const f32x2*>(s_f + (2 * i + hp) * 8 + 4);
        const float zs = zl[0];
        f32x2 a[3];
#pragma unroll
        for (int k = 0; k < 3; ++k) a[k] = f32x2{-zs, -zs};
#pragma unroll
        for (int w = 0; w < 4; ++w) {
            const float* p = s_part + ((hp * 4 + w) * NI + i) * DH + c6 * 6;
#pragma unroll
            for (int k = 0; k < 3; ++k) a[k] += f[w] * *reinterpret_cast<const f32x2*>(p + 2 * k);
        }
        return u32x4{pk_bf16(a[0]), pk_bf16(a[1]), pk_bf16(a[2]), tag | ((__float_as_uint(zl[1]) >> (16 * (c6 & 1))) & 0xffffu)};
    };
    auto region = [&](unsigned tag) { return WS_PART + ((__builtin_amdgcn_readfirstlane(tag) >> 16) & (NREG - 1)) * (NWG * UNITB); };
    auto publish = [&](u32x4 v, unsigned reg, int i, int hp, int c6, bool on) {
        __builtin_amdgcn_raw_buffer_store_b128(v, rw_, on ? (hp * 8 + (c6 >> 2)) * USTR + (c6 & 3) * 16 : OOB, reg + i * 16 * USTR + m * PSTR, STA);
        asm volatile("s_nop 1" :: "v"(v) : "memory");
    };

    if (wave == 8) {
        unsigned long long pc1;
        asm volatile("s_getpc_b64 %0" : "=s"(pc1));
        pc0 &= ~255ull;
        const auto rcode = __builtin_amdgcn_make_buffer_rsrc((void*)pc0, 0, (unsigned)(pc1 - pc0), 0x00020000);
        auto warm_code = [&]() {
            unsigned x[2];
#pragma unroll
            for (int k = 0; k < 2; ++k) x[k] = __builtin_amdgcn_raw_buffer_load_b32(rcode, lane * 128 + k * 8192, 0, 0);
            asm volatile("s_waitcnt vmcnt(0)" :: "v"(x[0]), "v"(x[1]) : "memory");
        };
        if (PF) warm_code();
        const unsigned tag = ((unsigned)__builtin_amdgcn_raw_buffer_load_b16(rw_, (lane & 3) * 2, WS_EPOCH, 16) + 1u) << 16;
        unsigned origin = (tstart << 8) + pace.offset;
        if (lane < 8) { s_done[lane] = 0; s_sync[lane] = lane == 2 ? origin : lane < 4 ? 0 : tag; }
        PIN();
        BARRIER();
        const int mh = lane >> 5, mc = lane & 31;
        const unsigned rtag = __builtin_amdgcn_readlane(tag, 0) | __builtin_amdgcn_readlane(tag, 1) >> 16;
        constexpr int RPX = 2;
        constexpr int RSH = X / 100;
        constexpr unsigned RDLY = 200;
        constexpr int UP = X / 10 % 10 ? X / 10 % 10 * 82 : 1 << 24, DN = X % 10 ? X % 10 * 82 : 1 << 24;
        const int rj = ((bid >> 3) + 5 * (bid & 7)) & 31;
        const int rid = rj % (32 / RPX) == 0 ? rj / (32 / RPX) * 8 + (bid & 7) : -1;
        unsigned wround = now32() >> RSH, rround = wround, tick = 0, target = origin;
        bool warmed = false;
        int i = 0;
        auto kept_now = [&](unsigned now) {
            const u32x2 a = *(vlds_u32x2*)(s_act + 2 * (lane & 7));
            const int late = max((int)(a[0] - origin), (int)((now << 8) - a[1] - origin));
            return origin + (__builtin_amdgcn_readfirstlane(row16_sum(late)) >> 4);
        };
        while (i < NI - 1) {
            const unsigned now = now32();
            if ((now >> RSH) != wround && i < NI - 2) {
                wround = now >> RSH;
                if (rid >= 0 && (now & ((1u << RSH) - 1)) < 48)
                    __builtin_amdgcn_raw_buffer_store_b64(u32x2{kept_now(now), rtag + wround}, rw_, lane == 0 ? rid * 8 : OOB, WS_REP + (wround & 255) * 512, 16);
                PIN();
                continue;
            }
            if ((int)(((now - RDLY) >> RSH) - rround) > 0 && i < NI - 2) {
                rround = (now - RDLY) >> RSH;
                const u32x2 x = __builtin_amdgcn_raw_buffer_load_b64(rw_, c * 8, WS_REP + (rround & 255) * 512, 16);
                const int d = (int)(x[0] - origin);
                const bool ok = x[1] == rtag + rround && d < (32 << 16) && d > -(32 << 16);
                const int n = __builtin_popcountll(__builtin_amdgcn_ballot_w64(ok));
                if (n >= 48) {
                    target = origin + __builtin_amdgcn_readfirstlane((int)(row16_allsum(ok ? (float)d : 0.f) * 4.f / (float)n)) - pace.bias;
                    const int lost = min((int)(kept_now(now32()) - origin) - pace.bias, (int)(target - origin));
                    if (lost > 0) origin += lost;
                    if (SLIP && !SLIPT) { const int l2 = (int)(kept_now(now32()) - origin) - SLIP; if (l2 > 0) origin += l2; }
                }
                PIN();
                continue;
            }
            if ((now >> 5) != tick) {
                tick = now >> 5;
                origin += max(-DN, min(UP, (int)(target - origin)));
                if (SLIP && SLIPT) { const int l2 = (int)(kept_now(now) - origin) - SLIP; if (l2 > 0) origin += l2; }
                if (lane == 0) *(vlds_u32*)(s_sync + 2) = origin;
                PIN();
            }
            if (PF >= 2 && !warmed && (int)((now << 8) - origin) > 60 * (int)pace.step) { warm_code(); warmed = true; continue; }
            const u32x4 a = *(vlds_u32x4*)s_done, b = *(vlds_u32x4*)(s_done + 4);
            const unsigned mn = min(min(min(a[0], a[1]), min(a[2], a[3])), min(min(b[0], b[1]), min(b[2], b[3])));
            if (__builtin_amdgcn_readfirstlane(mn) <= (unsigned)i) { __builtin_amdgcn_s_sleep(2); continue; }
            PIN();
            if (i == 0) {
                f32x4 ml[4], f;
#pragma unroll
                for (int w = 0; w < 4; ++w) ml[w] = *reinterpret_cast<const f32x4*>(s_sc + (mc * 4 + w) * 64 + 16);
                const float M = fmaxf(fmaxf(ml[0][0], ml[1][0]), fmaxf(ml[2][0], ml[3][0]));
                float L = 0.f, zs = 0.f;
#pragma unroll
                for (int w = 0; w < 4; ++w) { f[w] = __builtin_amdgcn_exp2f(ml[w][0] - M); L += f[w] * ml[w][1]; zs += f[w] * ml[w][2]; }
                const float inv = 1.f / L;
                *reinterpret_cast<f32x4*>(s_f + mc * 8) = f * inv;
                *reinterpret_cast<f32x2*>(s_f + mc * 8 + 4) = f32x2{zs * inv, M + __builtin_amdgcn_logf(L)};
                PIN();
                if (lane == 0) *(vlds_u32*)(s_sync + 3) = 1;
                PIN();
            }
            if (PF >= 2 && i == NI - 3) warm_code();
            const u32x4 v = merge(i, mh, mc, tag);
            PIN();
            if (pace.pub < 0) publish(v, region(tag), i, mh, mc, true);
            else *reinterpret_cast<u32x4*>(s_part + (mh * 4 * NI + i) * DH + mc * 4) = v;
            PIN();
            ++i;
        }
        u32x4 v[NI - 1];
#pragma unroll
        for (int i = 0; i < NI - 1; ++i) { v[i] = *reinterpret_cast<const u32x4*>(s_part + (mh * 4 * NI + i) * DH + mc * 4); asm volatile("" : "+v"(v[i])); }
        STAMP(5);
        while ((int)((now32() << 8) - origin) < pace.pub) __builtin_amdgcn_s_sleep(1);
        PIN();
        STAMP(6);
#pragma unroll
        for (int i = 0; i < NI - 1; ++i) publish(v[i], region(tag), i, mh, mc, pace.pub >= 0);
        STAMP(7);
        return;
    }

    const int rw = wave & 3, hp = wave >> 2;
    const int row0 = m * 64 + rw * 16;

    const unsigned sidx = STAG == 0 ? hp * 1024 + m * 4 + rw : STAG == 1 ? m * 8 + wave : (hp * 1024 + (m & 31) * 32 + (m >> 5) * 4 + rw);
    unsigned off = pace.start + ((pace.step * sidx) >> 11);
    unsigned origin = tstart << 8, kept = 0;
    bool wide = false;
    unsigned prev = 0, hold = 0;
    int holdn = 0;
    const unsigned step = pace.step;
    auto pace_wait = [&]() {
        unsigned org = __builtin_amdgcn_readfirstlane(origin);
        if (HOLD && holdn > 0) { --holdn; if ((int)(hold - org) > 0) org = hold; }
        const unsigned tgt = org + off;
        prev = kept;
        unsigned now = now32() << 8;
        kept = ((int)(now - tgt) > 0 ? now : tgt) - off;
        wide = (int)(now - tgt) > 30 * 256;
        while ((int)(tgt - now) > 0) { if (SLP) __builtin_amdgcn_s_sleep(SLP); now = now32() << 8; }
        off += step;
    };
    auto report = [&]() {
        if (lane == 0) *(vlds_u32x2*)(s_act + 2 * wave) = u32x2{kept, off};
        origin = *(vlds_u32*)(s_sync + 2);
        PIN();
    };
#define TRV (TRABS ? kept - (tstart << 8) : kept - __builtin_amdgcn_readfirstlane(origin))

    const int qe = min(tid, 383);
    const int qh = qe / 12, qd = (qe % 12) * 8;
    u32x4 lq[2], lc[2], ls[2];
    auto load_qs = [&]() {
#pragma unroll
        for (int k = 0; k < 2; ++k) {
            typedef const __attribute__((address_space(1))) u32x4* g4;
            lq[k] = *(g4)(q + qh * DH + k * HALF + qd);
            lc[k] = *(g4)(q_cos + k * HALF + qd);
            ls[k] = *(g4)(q_sin + k * HALF + qd);
        }
        PIN();
    };
    u32x2 lsc[4];
    auto load_sc = [&]() {
#pragma unroll
        for (int a = 0; a < 4; ++a) {
            const gptr base = a == 0 ? ksc : a == 1 ? kzp : a == 2 ? vsc : vzp;
            lsc[a] = __builtin_nontemporal_load((const __attribute__((address_space(1))) u32x2*)(base + m * 64 + (unsigned)((tid >> 4) * SKV + (tid & 15) * 4)));
        }
        PIN();
    };

    const auto rc = __builtin_amdgcn_make_buffer_rsrc((void*)k_cos, 0, CSB, 0x00020000);
    const auto rs = __builtin_amdgcn_make_buffer_rsrc((void*)k_sin, 0, CSB, 0x00020000);
    u32x2 raw[2][6];
    auto load_cs = [&](int t) {
        const unsigned co = r * 384 + c * 8;
        const unsigned so1 = r * 384 + (c < 8 ? c * 8 + 320 : c * 8 - 64);
        const unsigned rb = (row0 + (t + 2 * hp) * 4) * 384u;
        raw[t][0] = __builtin_amdgcn_raw_buffer_load_b64(rc, co, rb, 2);
        raw[t][1] = __builtin_amdgcn_raw_buffer_load_b64(rc, co + 128, rb, 2);
        raw[t][2] = __builtin_amdgcn_raw_buffer_load_b64(rc, co + 256, rb, 2);
        raw[t][3] = __builtin_amdgcn_raw_buffer_load_b64(rs, co + 192, rb, 2);
        raw[t][4] = __builtin_amdgcn_raw_buffer_load_b64(rs, so1, rb, 2);
        raw[t][5] = __builtin_amdgcn_raw_buffer_load_b64(rs, co + 64, rb, 2);
        PIN();
    };
    u32x4 tab[4][3];
    auto pack_cs = [&](int t) {
#pragma unroll
        for (int j = 0; j < 3; ++j) {
#pragma unroll
            for (int k = 0; k < 2; ++k) {
                tab[t][j][2 * k] = __builtin_amdgcn_perm(raw[t][3 + j][k], raw[t][j][k], 0x05040100);
                tab[t][j][2 * k + 1] = __builtin_amdgcn_perm(raw[t][3 + j][k], raw[t][j][k], 0x07060302);
            }
            *reinterpret_cast<u32x4*>(s_part + (wave ^ 4) * (NI * DH) + ((t * 3 + j) * 64 + lane) * 4) = tab[t][j];
        }
        PIN();
    };
    const auto rk = __builtin_amdgcn_make_buffer_rsrc((void*)qk, 0, KVB, 0x00020000);
    const auto rv = __builtin_amdgcn_make_buffer_rsrc((void*)qv, 0, KVB, 0x00020000);
    const unsigned voff = r * ROWB + c * 16;
    const unsigned woff = row0 * ROWB + hp * HEADB;
    auto toff = [&](int t) { return (unsigned)(t ^ (2 * hp)) * (4 * ROWB); };
    u32x4 ring[4][3];
    auto ld = [&](int slot, int j, __amdgpu_buffer_rsrc_t rr, unsigned off) {
        ring[slot][j] = __builtin_amdgcn_raw_buffer_load_b128(rr, voff + j * 256, off, 2);
        PIN();
    };
    auto ld_tile = [&](int slot, __amdgpu_buffer_rsrc_t rr, unsigned off) { ld(slot, 0, rr, off); ld(slot, 1, rr, off); ld(slot, 2, rr, off); };
    auto refill = [&](int slot, int j, __amdgpu_buffer_rsrc_t rr, unsigned goff, bool on) {
        if (ORD == 0) {
            if (on) { if (WID && wide) VMCNT(2 + WID); else VMCNT(2); if (j == 0) pace_wait(); }
            ld(slot, j, rr, goff);
        } else {
            if (on) { if (j == 0) pace_wait(); if (WID && wide) VMCNT(2 + WID); else VMCNT(2); }
            ld(slot, j, rr, goff);
            if (on && j == 0) { const unsigned t = (now32() << 8) - 1024 - (off - step); if ((int)(t - kept) > 0) kept = t;
                if (HOLD && (int)(kept - prev) > 100 * 256) { hold = kept; holdn = HOLD; } }
        }
        if (j == 2) report();
    };
    auto q_to_lds = [&]() {
        float o[2][8];
#pragma unroll
        for (int k = 0; k < 2; ++k)
#pragma unroll
            for (int u = 0; u < 4; ++u) {
                const unsigned pp = lq[k ^ 1][u] ^ (k == 0 ? 0x80008000u : 0u);
                o[k][2 * u] = rbf(rbf(bflo(lq[k][u]) * bflo(lc[k][u])) + rbf(bflo(pp) * bflo(ls[k][u])));
                o[k][2 * u + 1] = rbf(rbf(bfhi(lq[k][u]) * bfhi(lc[k][u])) + rbf(bfhi(pp) * bfhi(ls[k][u])));
            }
#pragma unroll
        for (int v = 0; v < 2; ++v) {
            u32x4 lo, hi;
#pragma unroll
            for (int e = 0; e < 4; ++e) {
                lo[e] = __builtin_amdgcn_perm(__float_as_uint(o[1][4 * v + e]), __float_as_uint(o[0][4 * v + e]), 0x07060302);
                hi[e] = __builtin_amdgcn_perm(__float_as_uint(o[0][4 * v + e]) ^ 0x80000000u, __float_as_uint(o[1][4 * v + e]), 0x07060302);
            }
            *reinterpret_cast<u32x4*>(s_q + qh * DH + qd + 4 * v) = lo;
            *reinterpret_cast<u32x4*>(s_q + qh * DH + HALF + qd + 4 * v) = hi;
        }
        PIN();
    };
    auto sc_to_lds = [&]() {
        float* dst = s_sc + tid * 16;
#pragma unroll
        for (int u = 0; u < 2; ++u) {
            *reinterpret_cast<f32x4*>(dst + 8 * u) = f32x4{bflo(lsc[0][u]) * SCALE_LOG2, bflo(lsc[1][u]), bflo(lsc[2][u]), bflo(lsc[3][u])};
            *reinterpret_cast<f32x4*>(dst + 8 * u + 4) = f32x4{bfhi(lsc[0][u]) * SCALE_LOG2, bfhi(lsc[1][u]), bfhi(lsc[2][u]), bfhi(lsc[3][u])};
        }
        PIN();
    };

    auto k0 = [&](int t) { pace_wait(); ld_tile(t, rk, woff + toff(t)); };
    if (PRO == 0) {
        load_qs(); load_sc(); load_cs(0);
        pace_wait(); load_cs(1);
        q_to_lds(); sc_to_lds();
        STAMP(1);
        k0(0); pack_cs(0);
        k0(1); pack_cs(1);
        VMCNT(3); k0(2);
        STAMP(2);
        VMCNT(3); k0(3);
    } else if (PRO == 1) {
        load_cs(0); PSTAMP(70); load_sc(); PSTAMP(71);
        pace_wait(); load_cs(1); STAMP(1); if (wave < 6) load_qs();
        PSTAMP(72); pack_cs(0); PSTAMP(73); sc_to_lds(); PSTAMP(74);
        k0(0); pack_cs(1); PSTAMP(75); if (wave < 6) q_to_lds();
        PSTAMP(76); k0(1); PSTAMP(77);
        VMCNT(3); PSTAMP(78); k0(2);
        STAMP(2);
        VMCNT(3); k0(3);
    } else if (PRO == 2) {
        load_cs(0); load_sc();
        pace_wait(); load_cs(1);
        STAMP(1);
        pack_cs(0); sc_to_lds();
        k0(0); if (wave < 6) load_qs(); pack_cs(1);
        k0(1); if (wave < 6) q_to_lds();
        VMCNT(3); k0(2);
        STAMP(2);
        VMCNT(3); k0(3);
    } else if (PRO == 3) {
        load_cs(0); load_sc();
        pace_wait(); load_cs(1);
        STAMP(1);
        pack_cs(0); sc_to_lds();
        k0(0); pack_cs(1);
        k0(1); if (wave < 6) load_qs();
        VMCNT(9); k0(2); if (wave < 6) q_to_lds();
        STAMP(2);
        VMCNT(3); k0(3);
    }
    if (lane == 0) *(vlds_u32x2*)(s_act + 2 * wave) = u32x2{kept, off};
    BARRIER();
    origin = *(vlds_u32*)(s_sync + 2);
    STAMP(3);

    u32x4 qp[3];
    auto load_q = [&](int h, int j) { qp[j] = *reinterpret_cast<const u32x4*>(s_q + h * DH + 64 * j + 4 * c); };
    load_q(hp, 0); load_q(hp, 1); load_q(hp, 2);
#pragma unroll
    for (int t = 0; t < 2; ++t)
#pragma unroll
        for (int j = 0; j < 3; ++j) tab[2 + t][j] = *reinterpret_cast<const u32x4*>(s_part + wave * (NI * DH) + ((t * 3 + j) * 64 + lane) * 4);

    for (int i = 0; i < NI; ++i) {
        const bool last = i + 1 == NI;
        const int h = 2 * i + hp;
        const unsigned hnext = (i + 1) * (2 * HEADB) + woff;
        const auto rn = __builtin_amdgcn_make_buffer_rsrc((void*)(last ? qv : qk), 0, KVB, 0x00020000);
        float* scp = s_sc + (h * 4 + rw) * 64;
        f32x4 sc[4];
#pragma unroll
        for (int t = 0; t < 4; ++t) sc[t] = *reinterpret_cast<const f32x4*>(scp + (4 * (t ^ (2 * hp)) + r) * 4);
        float s[4];
#pragma unroll
        for (int t = 0; t < 4; ++t) {
            f32x2 dot, wsum;
#pragma unroll
            for (int j = 0; j < 3; ++j) {
                f32x2 w01, w23, k01, k23;
                dot_cvt(qp[j], tab[t][j], ring[t][j], w01, w23, k01, k23);
                if (j == 0) { wsum = w01 + w23; dot = k01 * w01; } else { wsum += w01; wsum += w23; dot += k01 * w01; }
                dot += k23 * w23;
                asm volatile("" : "+v"(dot), "+v"(wsum));
                PIN();
                if (t == 3) load_q(last ? h : h + 2, j);
                if (t == 0 || t == VS) refill(t, j, rn, last ? woff + toff(t == VS ? 1 : 0) : hnext + toff(t), true);
                else refill(t, j, rk, last ? OOB : hnext + toff(t), !last);
                if (t == 0 && j == 0) TRACE(i);
            }
            const float a = (dot[0] + dot[1]) - sc[t][1] * (wsum[0] + wsum[1]);
            s[t] = row16_allsum(a) * sc[t][0];
        }
        const float mx = xrow_max(fmaxf(fmaxf(s[0], s[1]), fmaxf(s[2], s[3])));
        float l = 0.f, z = 0.f;
#pragma unroll
        for (int t = 0; t < 4; ++t) {
            const float p = __builtin_amdgcn_exp2f(s[t] - mx);
            l += p;
            s[t] = p * sc[t][2];
            z += s[t] * sc[t][3];
        }
        l = xrow_sum(l);
        z = xrow_sum(z);
        if (c == 0) {
            *reinterpret_cast<f32x4*>(scp + r * 4) = f32x4{s[0], s[1], s[2], s[3]};
            *reinterpret_cast<f32x4*>(scp + 16) = f32x4{mx, l, z, 0.f};
        }
        PIN();
    }
    STAMP(4);

    const float* pwp = s_sc + (hp * 4 + rw) * 64 + r * 4;
    f32x4 pw = *reinterpret_cast<const f32x4*>(pwp);
    for (int i = 0; i < NI; ++i) {
        const bool last = i + 1 == NI;
        const unsigned hoff = i * (2 * HEADB) + woff;
        f32x2 acc[3][2];
#pragma unroll
        for (int t = 0; t < 4; ++t) {
            const f32x2 p2 = {pw[t], pw[t]};
#pragma unroll
            for (int j = 0; j < 3; ++j) {
                const u32x4 vv = ring[(t & 1) * VS][j];
                const f32x2 v01 = {(float)(int)vv[0], (float)(int)vv[1]}, v23 = {(float)(int)vv[2], (float)(int)vv[3]};
                if (t == 0) { acc[j][0] = p2 * v01; acc[j][1] = p2 * v23; }
                else { acc[j][0] += p2 * v01; acc[j][1] += p2 * v23; }
                asm volatile("" : "+v"(acc[j][0]), "+v"(acc[j][1]));
                PIN();
                if (t == 3 && j == 2) pw = *reinterpret_cast<const f32x4*>(pwp + (last ? i : i + 1) * 512);
                if (t < 2) refill((t & 1) * VS, j, rv, hoff + toff(t + 2), true);
                else refill((t & 1) * VS, j, rv, last ? OOB : hoff + 2 * HEADB + toff(t - 2), !last);
                if (t == 0 && j == 0) TRACE(16 + i);
            }
        }
        float* sl = s_part + (wave * NI + i) * DH;
#pragma unroll
        for (int j = 0; j < 3; ++j) {
            const float x = swap32_add(acc[j][0][0], acc[j][1][0]);
            const float y = swap32_add(acc[j][0][1], acc[j][1][1]);
            sl[64 * j + 4 * c + r] = swap16_add(x, y);
        }
        PIN();
        if (lane == 0) *(vlds_u32*)(s_done + wave) = i + 1;
        PIN();
    }
    STAMP(6);
    unsigned old = 0;
    if (lane == 0) old = __hip_atomic_fetch_add(s_sync + hp, 1u, __ATOMIC_RELAXED, __HIP_MEMORY_SCOPE_WORKGROUP);
    PIN();
    const unsigned tag = s_sync[4 + (lane & 3)];
    if (__builtin_amdgcn_readfirstlane(old) == 3) {
        while (__builtin_amdgcn_readfirstlane(*(vlds_u32*)(s_sync + 3)) == 0) __builtin_amdgcn_s_sleep(1);
        PIN();
        publish(merge(NI - 1, hp, lane & 31, tag), region(tag), NI - 1, hp, lane & 31, lane < 32);
        PIN();
    }
    STAMP(7);
    {
        const unsigned o = __builtin_amdgcn_readfirstlane(origin);
        while ((int)((now32() << 8) - o) < pace.rd) __builtin_amdgcn_s_sleep(1);
        PIN();
    }
    STAMP(8);
    {
        u32x4 x, y;
        unsigned xo = (tid >> 2) * PSTR + (tid & 3) * 16, yo = xo + 128 * PSTR;
        for (;;) {
            const u32x4 xn = __builtin_amdgcn_raw_buffer_load_b128(rw_, xo, region(tag) + bid * USTR, LDA);
            const u32x4 yn = __builtin_amdgcn_raw_buffer_load_b128(rw_, yo, region(tag) + bid * USTR, LDA);
            unsigned bad = (xo != OOB && (xn[3] ^ tag) > 0xffffu ? 1u : 0u) | (yo != OOB && (yn[3] ^ tag) > 0xffffu ? 2u : 0u);
            bad |= __builtin_amdgcn_mov_dpp(bad, 0xb1, 0xf, 0xf, true);
            bad |= __builtin_amdgcn_mov_dpp(bad, 0x4e, 0xf, 0xf, true);
            const bool tx = xo != OOB, ty = yo != OOB;
#pragma unroll
            for (int k = 0; k < 4; ++k) { x[k] = tx ? xn[k] : x[k]; y[k] = ty ? yn[k] : y[k]; }
            xo = bad & 1 ? xo : OOB;
            yo = bad & 2 ? yo : OOB;
            if (__builtin_amdgcn_ballot_w64(bad != 0) == 0 || now32() - tstart > SPIN_LIMIT) break;
            PIN();
        }
        STAMP(9);
        const unsigned sel = lane & 1 ? 0x01000504u : 0x05040100u;
        const float lx = __uint_as_float(__builtin_amdgcn_perm(__builtin_amdgcn_mov_dpp(x[3], 0xb1, 0xf, 0xf, true), x[3], sel));
        const float ly = __uint_as_float(__builtin_amdgcn_perm(__builtin_amdgcn_mov_dpp(y[3], 0xb1, 0xf, 0xf, true), y[3], sel));
        float M = fmaxf(lx, ly);
        M = fmaxf(M, dpp<0x128>(M));
        M = fmaxf(M, dpp<0x124>(M));
        M = xrow_max(M);
        const float w0 = __builtin_amdgcn_exp2f(lx - M), w1 = __builtin_amdgcn_exp2f(ly - M);
        float a[8];
#pragma unroll
        for (int k = 0; k < 3; ++k) {
            a[2 * k] = w0 * bflo(x[k]) + w1 * bflo(y[k]);
            a[2 * k + 1] = w0 * bfhi(x[k]) + w1 * bfhi(y[k]);
        }
        a[6] = a[7] = w0 + w1;
        float b[4], d[2];
#pragma unroll
        for (int k = 0; k < 4; ++k) b[k] = swap32_add(a[k], a[4 + k]);
#pragma unroll
        for (int k = 0; k < 2; ++k) d[k] = swap16_add(b[k], b[2 + k]);
#pragma unroll
        for (int k = 0; k < 2; ++k) { d[k] += dpp<0x128>(d[k]); d[k] += dpp<0x124>(d[k]); }
        if (c < 4) *reinterpret_cast<f32x2*>(s_red + wave * 28 + (r < 3 ? c * 6 + r * 2 : 24)) = f32x2{d[0], r < 3 ? d[1] : M};
        STAMP(10);
        BARRIER();
        if (tid < 24) {
            f32x2 sm[8];
            float ov[8];
#pragma unroll
            for (int w = 0; w < 8; ++w) { sm[w] = *reinterpret_cast<const f32x2*>(s_red + w * 28 + 24); ov[w] = s_red[w * 28 + tid]; }
            float MM = sm[0][1];
#pragma unroll
            for (int w = 1; w < 8; ++w) MM = fmaxf(MM, sm[w][1]);
            float S = 0.f, o = 0.f;
#pragma unroll
            for (int w = 0; w < 8; ++w) {
                const float ff = __builtin_amdgcn_exp2f(sm[w][1] - MM);
                S += ff * sm[w][0];
                o += ff * ov[w];
            }
            out[bid * 24 + tid] = (unsigned short)pk_bf16(f32x2{o * __builtin_amdgcn_rcpf(S), 0.f});
        }
        if (bid == 0 && wave == 1) __builtin_amdgcn_raw_buffer_store_b16((unsigned short)(tag >> 16), rw_, lane < 4 ? lane * 2 : OOB, WS_EPOCH, 16);
    }
    if (wave == PWAVE) {
        if (PIDX == 100) { asm volatile("s_waitcnt vmcnt(0)" ::: "memory"); probe = now32(); }
        const unsigned ptag = __builtin_amdgcn_readlane(tag, 0) | __builtin_amdgcn_readlane(tag, 1) >> 16;
        __builtin_amdgcn_raw_buffer_store_b128(u32x4{tstart, probe, ptag, 0}, rw_, lane == 0 ? bid * 16 : OOB, WS_PROBE, 16);
        asm volatile("s_nop 1" ::: "memory");
        if (bid == 100) {
            while ((int)(now32() - tstart) < 12500) __builtin_amdgcn_s_sleep(1);
            u32x4 p[4];
            for (;;) {
                bool ok = true;
                for (int k = 0; k < 4; ++k) { p[k] = __builtin_amdgcn_raw_buffer_load_b128(rw_, lane * 16 + k * 1024, WS_PROBE, 16); ok = ok && p[k][2] == ptag; }
                if (__builtin_amdgcn_ballot_w64(!ok) == 0 || now32() - tstart > SPIN_LIMIT) break;
                PIN();
            }
            float mn = 1e9f, mx = -1e9f, av = 0.f;
            for (int k = 0; k < 4; ++k) { mn = fminf(mn, (float)(int)(p[k][0] - tstart)); const float v = (float)(int)(p[k][1] - tstart); mx = fmaxf(mx, v); av += v; }
            mn = -xrow_max(row16_allmax(-mn)); mx = xrow_max(row16_allmax(mx)); av = xrow_sum(row16_allsum(av)) / 256.f;
            float val = (PMAX ? mx : av) - mn - PSUB;
            if (PIDX == 99) val = 0.f;
            val = fminf(fmaxf(val, 0.f), 3000.f);
            const unsigned endt = tstart + __builtin_amdgcn_readfirstlane((int)(mn + 14000.f + 5.f * val));
            while ((int)(endt - now32()) > 0) __builtin_amdgcn_s_sleep(1);
        }
    }
}

template <int X, int PRO, int GATE, int STAG>
static void launch_cfg(void* const* d_in, void* d_out, void* d_ws, hipStream_t stream, float t_us, float start_us, float off_us, float debt_us, float adj_pct, float pub_us) {
    Pace pace;
    pace.step = (unsigned)(t_us * 25600.f / 128.f);
    pace.start = (int)(start_us * 25600.f);
    pace.offset = (int)(off_us * 25600.f);
    pace.bias = (int)(debt_us * 25600.f);
    pace.rd = (int)((start_us + t_us * (129.f / 128.f) + adj_pct) * 25600.f);
    pace.pub = (int)((start_us + t_us * (129.f / 128.f) + pub_us) * 25600.f);
    kv_sweep_kernel<X, PRO, GATE, STAG, 1><<<NWG, 576, 0, stream>>>((const unsigned short*)d_in[0], (const int*)d_in[1], (const unsigned short*)d_in[2],
        (const unsigned short*)d_in[3], (const int*)d_in[4], (const unsigned short*)d_in[5], (const unsigned short*)d_in[6],
        (const unsigned short*)d_in[7], (const unsigned short*)d_in[8], (const unsigned short*)d_in[9], (const unsigned short*)d_in[10],
        (unsigned char*)d_ws, (bf16*)d_out, pace);
}

#define CFG 966, 21, 120, 0
#define PRM 102.5f, 0.5f, 1.0f, 0.05f, 1.8f, 0.8f
extern "C" void kernel_launch(void* const* d_in, const int* in_sizes, int n_in,
                              void* d_out, int out_size, void* d_ws, size_t ws_size,
                              hipStream_t stream) {
    launch_cfg<CFG>(d_in, d_out, d_ws, stream, PRM);
}
```

```cpp
#include <hip/hip_runtime.h>
#include <hip/hip_bf16.h>
#include <cstdint>
#include <type_traits>

using bf16 = __hip_bfloat16;
using u32x4 = __attribute__((ext_vector_type(4))) unsigned;
using u32x2 = __attribute__((ext_vector_type(2))) unsigned;
using f32x2 = __attribute__((ext_vector_type(2))) float;
using f32x4 = __attribute__((ext_vector_type(4))) float;
using bf16x2 = __attribute__((ext_vector_type(2))) __bf16;

constexpr int DH = 192;
constexpr int HALF = 96;
constexpr int NH = 32;
constexpr int SKV = 16384;
constexpr int NWG = 256;
constexpr int NI = 16;
constexpr unsigned ROWB = DH * 4;
constexpr unsigned HEADB = SKV * ROWB;
constexpr unsigned KVB = NH * HEADB;
constexpr unsigned CSB = SKV * DH * 2;
constexpr unsigned OOB = 0x80000000u;
constexpr float SCALE_LOG2 = 1.4426950408889634f * 0.07216878364870322f;
constexpr unsigned SPIN_LIMIT = 20000000u;

constexpr unsigned UNITB = NWG * 64;
constexpr unsigned WS_PART = 0;
constexpr unsigned WS_EPOCH = NWG * (UNITB + 1024);
constexpr unsigned WS_REP = WS_EPOCH + 256;
constexpr unsigned WS_PROBE = WS_REP + 256 * 512;
constexpr unsigned WS_BYTES = WS_PROBE + 4096;

__device__ __forceinline__ float bflo(unsigned u) { return __uint_as_float(u << 16); }
__device__ __forceinline__ float bfhi(unsigned u) { return __uint_as_float(u & 0xffff0000u); }
__device__ __forceinline__ float rbf(float x) { return __bfloat162float(__float2bfloat16(x)); }
__device__ __forceinline__ unsigned pk_bf16(f32x2 a) { return __builtin_bit_cast(unsigned, __builtin_convertvector(a, bf16x2)); }
__device__ __forceinline__ void dot_cvt(const u32x4 q, const u32x4 t, const u32x4 ki, f32x2& w01, f32x2& w23, f32x2& k01, f32x2& k23) {
    float w0, w1, w2, w3;
    float k0 = __uint_as_float(ki[0]), k1 = __uint_as_float(ki[1]), k2 = __uint_as_float(ki[2]), k3 = __uint_as_float(ki[3]);
    asm("v_dot2_f32_bf16 %0, %8, %12, 0\n\tv_dot2_f32_bf16 %1, %9, %13, 0\n\tv_dot2_f32_bf16 %2, %10, %14, 0\n\tv_dot2_f32_bf16 %3, %11, %15, 0\n\t"
        "v_cvt_f32_i32 %4, %4\n\tv_cvt_f32_i32 %5, %5\n\tv_cvt_f32_i32 %6, %6\n\tv_cvt_f32_i32 %7, %7"
        : "=&v"(w0), "=&v"(w1), "=&v"(w2), "=&v"(w3), "+v"(k0), "+v"(k1), "+v"(k2), "+v"(k3)
        : "v"(q[0]), "v"(q[1]), "v"(q[2]), "v"(q[3]), "v"(t[0]), "v"(t[1]), "v"(t[2]), "v"(t[3]));
    w01 = f32x2{w0, w1}; w23 = f32x2{w2, w3}; k01 = f32x2{k0, k1}; k23 = f32x2{k2, k3};
}

template <int CTRL> __device__ __forceinline__ float dpp(float x) {
    return __builtin_bit_cast(float, __builtin_amdgcn_mov_dpp(__builtin_bit_cast(int, x), CTRL, 0xf, 0xf, true));
}
__device__ __forceinline__ float row16_allsum(float x) {
    x += dpp<0x128>(x);
    x += dpp<0x124>(x);
    x += dpp<0x122>(x);
    x += dpp<0x121>(x);
    return x;
}
__device__ __forceinline__ float row16_allmax(float x) {
    x = fmaxf(x, dpp<0x128>(x));
    x = fmaxf(x, dpp<0x124>(x));
    x = fmaxf(x, dpp<0x122>(x));
    x = fmaxf(x, dpp<0x121>(x));
    return x;
}
__device__ __forceinline__ float xrow_max(float x) {
    auto s = __builtin_amdgcn_permlane16_swap(__float_as_uint(x), __float_as_uint(x), false, false);
    x = fmaxf(__uint_as_float(s[0]), __uint_as_float(s[1]));
    auto t = __builtin_amdgcn_permlane32_swap(__float_as_uint(x), __float_as_uint(x), false, false);
    return fmaxf(__uint_as_float(t[0]), __uint_as_float(t[1]));
}
__device__ __forceinline__ float xrow_sum(float x) {
    auto s = __builtin_amdgcn_permlane16_swap(__float_as_uint(x), __float_as_uint(x), false, false);
    x = __uint_as_float(s[0]) + __uint_as_float(s[1]);
    auto t = __builtin_amdgcn_permlane32_swap(__float_as_uint(x), __float_as_uint(x), false, false);
    return __uint_as_float(t[0]) + __uint_as_float(t[1]);
}
__device__ __forceinline__ float swap32_add(float a, float b) {
    auto t = __builtin_amdgcn_permlane32_swap(__float_as_uint(a), __float_as_uint(b), false, false);
    return __uint_as_float(t[0]) + __uint_as_float(t[1]);
}
__device__ __forceinline__ float swap16_add(float a, float b) {
    auto t = __builtin_amdgcn_permlane16_swap(__float_as_uint(a), __float_as_uint(b), false, false);
    return __uint_as_float(t[0]) + __uint_as_float(t[1]);
}
__device__ __forceinline__ int row16_sum(int x) {
    x += __builtin_amdgcn_mov_dpp(x, 0x128, 0xf, 0xf, true);
    x += __builtin_amdgcn_mov_dpp(x, 0x124, 0xf, 0xf, true);
    x += __builtin_amdgcn_mov_dpp(x, 0x122, 0xf, 0xf, true);
    x += __builtin_amdgcn_mov_dpp(x, 0x121, 0xf, 0xf, true);
    return x;
}
__device__ __forceinline__ unsigned now32() { return (unsigned)__builtin_amdgcn_s_memrealtime(); }

#define NSTAMP 12
#define STAMP(i)
#define TRACE(i)
#define PSTAMP(i)
#define PIN() do { asm volatile("" ::: "memory"); __builtin_amdgcn_sched_barrier(0); } while (0)
#define BARRIER() do { asm volatile("s_waitcnt lgkmcnt(0)" ::: "memory"); __builtin_amdgcn_s_barrier(); PIN(); } while (0)
#define VMCNT(n) asm volatile("s_waitcnt vmcnt(%0)" :: "i"(n) : "memory")

typedef volatile __attribute__((address_space(3))) unsigned vlds_u32;
typedef volatile __attribute__((address_space(3))) u32x2 vlds_u32x2;
typedef volatile __attribute__((address_space(3))) u32x4 vlds_u32x4;

struct Pace {
    unsigned step;
    int start;
    int offset;
    int bias;
    int rd;
    int pub;
};

template <int X, int PRO_, int GATE, int STAG_, int SLP>
__global__ __launch_bounds__(576) void kv_sweep_kernel(
    const unsigned short* q, const int* qk, const unsigned short* ksc,
    const unsigned short* kzp, const int* qv, const unsigned short* vsc,
    const unsigned short* vzp, const unsigned short* q_cos, const unsigned short* q_sin,
    const unsigned short* k_cos, const unsigned short* k_sin, unsigned char* ws, bf16* out, Pace pace)
{
    __shared__ __attribute__((aligned(16))) unsigned s_q[NH * DH];
    __shared__ __attribute__((aligned(16))) float s_sc[NH * 64 * 4];
    __shared__ __attribute__((aligned(16))) float s_part[8 * NI * DH];
    __shared__ __attribute__((aligned(16))) float s_red[8 * 28];
    __shared__ __attribute__((aligned(16))) float s_f[NH * 8];
    __shared__ __attribute__((aligned(16))) unsigned s_done[8];
    __shared__ __attribute__((aligned(16))) unsigned s_sync[8];
    __shared__ __attribute__((aligned(16))) unsigned s_act[16];

    constexpr bool TRABS = true;
    constexpr int ORD = 1, WID = 0, HOLD = GATE % 10 * 2, SLIP = GATE / 10 % 10 * 2560, SLIPT = GATE / 100;
    constexpr int PRO = PRO_ % 10, PF = PRO_ / 10, STAG = 0, LAY = 0, VS = 2, LDA = 16, STA = 16;
    constexpr unsigned NREG = 1u << STAG_;
    constexpr unsigned PSTR = LAY == 0 ? 64 : LAY == 1 ? UNITB + 256 : LAY == 2 ? UNITB + 128 : UNITB;
    constexpr unsigned USTR = LAY == 0 ? UNITB : 64;
    STAMP(0);
    unsigned long long pc0;
    asm volatile("s_getpc_b64 %0" : "=s"(pc0));
    const unsigned tstart = now32();
    const int tid = threadIdx.x;
    const int lane = tid & 63;
    const int wave = __builtin_amdgcn_readfirstlane(tid >> 6);
    const int r = lane >> 4;
    const int c = lane & 15;
    const int bid = blockIdx.x;
    const int m = (bid & 7) * 32 + (bid >> 3);
    const auto rw_ = __builtin_amdgcn_make_buffer_rsrc((void*)ws, 0, WS_BYTES, 0x00020000);

    auto merge = [&](int i, int hp, int c6, unsigned tag) {
        const f32x4 f = *reinterpret_cast<const f32x4*>(s_f + (2 * i + hp) * 8);
        const f32x2 zl = *reinterpret_cast<const f32x2*>(s_f + (2 * i + hp) * 8 + 4);
        const float zs = zl[0];
        f32x2 a[3];
#pragma unroll
        for (int k = 0; k < 3; ++k) a[k] = f32x2{-zs, -zs};
#pragma unroll
        for (int w = 0; w < 4; ++w) {
            const float* p = s_part + ((hp * 4 + w) * NI + i) * DH + c6 * 6;
#pragma unroll
            for (int k = 0; k < 3; ++k) a[k] += f[w] * *reinterpret_cast<const f32x2*>(p + 2 * k);
        }
        return u32x4{pk_bf16(a[0]), pk_bf16(a[1]), pk_bf16(a[2]), tag | ((__float_as_uint(zl[1]) >> (16 * (c6 & 1))) & 0xffffu)};
    };
    auto region = [&](unsigned tag) { return WS_PART + ((__builtin_amdgcn_readfirstlane(tag) >> 16) & (NREG - 1)) * (NWG * UNITB); };
    auto publish = [&](u32x4 v, unsigned reg, int i, int hp, int c6, bool on) {
        __builtin_amdgcn_raw_buffer_store_b128(v, rw_, on ? (hp * 8 + (c6 >> 2)) * USTR + (c6 & 3) * 16 : OOB, reg + i * 16 * USTR + m * PSTR, STA);
        asm volatile("s_nop 1" :: "v"(v) : "memory");
    };

    if (wave == 8) {
        unsigned long long pc1;
        asm volatile("s_getpc_b64 %0" : "=s"(pc1));
        pc0 &= ~255ull;
        const auto rcode = __builtin_amdgcn_make_buffer_rsrc((void*)pc0, 0, (unsigned)(pc1 - pc0), 0x00020000);
        auto warm_code = [&]() {
            unsigned x[2];
#pragma unroll
            for (int k = 0; k < 2; ++k) x[k] = __builtin_amdgcn_raw_buffer_load_b32(rcode, lane * 128 + k * 8192, 0, 0);
            asm volatile("s_waitcnt vmcnt(0)" :: "v"(x[0]), "v"(x[1]) : "memory");
        };
        if (PF) warm_code();
        const unsigned tag = ((unsigned)__builtin_amdgcn_raw_buffer_load_b16(rw_, (lane & 3) * 2, WS_EPOCH, 16) + 1u) << 16;
        unsigned origin = (tstart << 8) + pace.offset;
        if (lane < 8) { s_done[lane] = 0; s_sync[lane] = lane == 2 ? origin : lane < 4 ? 0 : tag; }
        PIN();
        BARRIER();
        const int mh = lane >> 5, mc = lane & 31;
        const unsigned rtag = __builtin_amdgcn_readlane(tag, 0) | __builtin_amdgcn_readlane(tag, 1) >> 16;
        constexpr int RPX = 2;
        constexpr int RSH = X / 100;
        constexpr unsigned RDLY = 200;
        constexpr int UP = X / 10 % 10 ? X / 10 % 10 * 82 : 1 << 24, DN = X % 10 ? X % 10 * 82 : 1 << 24;
        const int rj = ((bid >> 3) + 5 * (bid & 7)) & 31;
        const int rid = rj % (32 / RPX) == 0 ? rj / (32 / RPX) * 8 + (bid & 7) : -1;
        unsigned wround = now32() >> RSH, rround = wround, tick = 0, target = origin;
        bool warmed = false;
        int i = 0;
        auto kept_now = [&](unsigned now) {
            const u32x2 a = *(vlds_u32x2*)(s_act + 2 * (lane & 7));
            const int late = max((int)(a[0] - origin), (int)((now << 8) - a[1] - origin));
            return origin + (__builtin_amdgcn_readfirstlane(row16_sum(late)) >> 4);
        };
        while (i < NI - 1) {
            const unsigned now = now32();
            if ((now >> RSH) != wround && i < NI - 2) {
                wround = now >> RSH;
                if (rid >= 0 && (now & ((1u << RSH) - 1)) < 48)
                    __builtin_amdgcn_raw_buffer_store_b64(u32x2{kept_now(now), rtag + wround}, rw_, lane == 0 ? rid * 8 : OOB, WS_REP + (wround & 255) * 512, 16);
                PIN();
                continue;
            }
            if ((int)(((now - RDLY) >> RSH) - rround) > 0 && i < NI - 2) {
                rround = (now - RDLY) >> RSH;
                const u32x2 x = __builtin_amdgcn_raw_buffer_load_b64(rw_, c * 8, WS_REP + (rround & 255) * 512, 16);
                const int d = (int)(x[0] - origin);
                const bool ok = x[1] == rtag + rround && d < (32 << 16) && d > -(32 << 16);
                const int n = __builtin_popcountll(__builtin_amdgcn_ballot_w64(ok));
                if (n >= 48) {
                    target = origin + __builtin_amdgcn_readfirstlane((int)(row16_allsum(ok ? (float)d : 0.f) * 4.f / (float)n)) - pace.bias;
                    const int lost = min((int)(kept_now(now32()) - origin) - pace.bias, (int)(target - origin));
                    if (lost > 0) origin += lost;
                    if (SLIP && !SLIPT) { const int l2 = (int)(kept_now(now32()) - origin) - SLIP; if (l2 > 0) origin += l2; }
                }
                PIN();
                continue;
            }
            if ((now >> 5) != tick) {
                tick = now >> 5;
                origin += max(-DN, min(UP, (int)(target - origin)));
                if (SLIP && SLIPT) { const int l2 = (int)(kept_now(now) - origin) - SLIP; if (l2 > 0) origin += l2; }
                if (lane == 0) *(vlds_u32*)(s_sync + 2) = origin;
                PIN();
            }
            if (PF >= 2 && !warmed && (int)((now << 8) - origin) > 60 * (int)pace.step) { warm_code(); warmed = true; continue; }
            const u32x4 a = *(vlds_u32x4*)s_done, b = *(vlds_u32x4*)(s_done + 4);
            const unsigned mn = min(min(min(a[0], a[1]), min(a[2], a[3])), min(min(b[0], b[1]), min(b[2], b[3])));
            if (__builtin_amdgcn_readfirstlane(mn) <= (unsigned)i) { __builtin_amdgcn_s_sleep(2); continue; }
            PIN();
            if (i == 0) {
                f32x4 ml[4], f;
#pragma unroll
                for (int w = 0; w < 4; ++w) ml[w] = *reinterpret_cast<const f32x4*>(s_sc + (mc * 4 + w) * 64 + 16);
                const float M = fmaxf(fmaxf(ml[0][0], ml[1][0]), fmaxf(ml[2][0], ml[3][0]));
                float L = 0.f, zs = 0.f;
#pragma unroll
                for (int w = 0; w < 4; ++w) { f[w] = __builtin_amdgcn_exp2f(ml[w][0] - M); L += f[w] * ml[w][1]; zs += f[w] * ml[w][2]; }
                const float inv = 1.f / L;
                *reinterpret_cast<f32x4*>(s_f + mc * 8) = f * inv;
                *reinterpret_cast<f32x2*>(s_f + mc * 8 + 4) = f32x2{zs * inv, M + __builtin_amdgcn_logf(L)};
                PIN();
                if (lane == 0) *(vlds_u32*)(s_sync + 3) = 1;
                PIN();
            }
            if (PF >= 2 && i == NI - 3) warm_code();
            const u32x4 v = merge(i, mh, mc, tag);
            PIN();
            if (pace.pub < 0) publish(v, region(tag), i, mh, mc, true);
            else *reinterpret_cast<u32x4*>(s_part + (mh * 4 * NI + i) * DH + mc * 4) = v;
            PIN();
            ++i;
        }
        u32x4 v[NI - 1];
#pragma unroll
        for (int i = 0; i < NI - 1; ++i) { v[i] = *reinterpret_cast<const u32x4*>(s_part + (mh * 4 * NI + i) * DH + mc * 4); asm volatile("" : "+v"(v[i])); }
        STAMP(5);
        while ((int)((now32() << 8) - origin) < pace.pub) __builtin_amdgcn_s_sleep(1);
        PIN();
        STAMP(6);
#pragma unroll
        for (int i = 0; i < NI - 1; ++i) publish(v[i], region(tag), i, mh, mc, pace.pub >= 0);
        STAMP(7);
        return;
    }

    const int rw = wave & 3, hp = wave >> 2;
    const int row0 = m * 64 + rw * 16;

    const unsigned sidx = STAG == 0 ? hp * 1024 + m * 4 + rw : STAG == 1 ? m * 8 + wave : (hp * 1024 + (m & 31) * 32 + (m >> 5) * 4 + rw);
    unsigned off = pace.start + ((pace.step * sidx) >> 11);
    unsigned origin = tstart << 8, kept = 0;
    bool wide = false;
    unsigned prev = 0, hold = 0;
    int holdn = 0;
    const unsigned step = pace.step;
    auto pace_wait = [&]() {
        unsigned org = __builtin_amdgcn_readfirstlane(origin);
        if (HOLD && holdn > 0) { --holdn; if ((int)(hold - org) > 0) org = hold; }
        const unsigned tgt = org + off;
        prev = kept;
        unsigned now = now32() << 8;
        kept = ((int)(now - tgt) > 0 ? now : tgt) - off;
        wide = (int)(now - tgt) > 30 * 256;
        while ((int)(tgt - now) > 0) { if (SLP) __builtin_amdgcn_s_sleep(SLP); now = now32() << 8; }
        off += step;
    };
    auto report = [&]() {
        if (lane == 0) *(vlds_u32x2*)(s_act + 2 * wave) = u32x2{kept, off};
        origin = *(vlds_u32*)(s_sync + 2);
        PIN();
    };
#define TRV (TRABS ? kept - (tstart << 8) : kept - __builtin_amdgcn_readfirstlane(origin))

    const int qe = min(tid, 383);
    const int qh = qe / 12, qd = (qe % 12) * 8;
    u32x4 lq[2], lc[2], ls[2];
    auto load_qs = [&]() {
#pragma unroll
        for (int k = 0; k < 2; ++k) {
            lq[k] = *reinterpret_cast<const u32x4*>(q + qh * DH + k * HALF + qd);
            lc[k] = *reinterpret_cast<const u32x4*>(q_cos + k * HALF + qd);
            ls[k] = *reinterpret_cast<const u32x4*>(q_sin + k * HALF + qd);
        }
        PIN();
    };
    u32x2 lsc[4];
    auto load_sc = [&]() {
#pragma unroll
        for (int a = 0; a < 4; ++a) {
            const unsigned short* base = a == 0 ? ksc : a == 1 ? kzp : a == 2 ? vsc : vzp;
            lsc[a] = __builtin_nontemporal_load(reinterpret_cast<const u32x2*>(base + (size_t)(tid >> 4) * SKV + m * 64 + (tid & 15) * 4));
        }
        PIN();
    };

    const auto rc = __builtin_amdgcn_make_buffer_rsrc((void*)k_cos, 0, CSB, 0x00020000);
    const auto rs = __builtin_amdgcn_make_buffer_rsrc((void*)k_sin, 0, CSB, 0x00020000);
    u32x2 raw[2][6];
    auto load_cs = [&](int t) {
        const unsigned co = r * 384 + c * 8;
        const unsigned so1 = r * 384 + (c < 8 ? c * 8 + 320 : c * 8 - 64);
        const unsigned rb = (row0 + (t + 2 * hp) * 4) * 384u;
        raw[t][0] = __builtin_amdgcn_raw_buffer_load_b64(rc, co, rb, 2);
        raw[t][1] = __builtin_amdgcn_raw_buffer_load_b64(rc, co + 128, rb, 2);
        raw[t][2] = __builtin_amdgcn_raw_buffer_load_b64(rc, co + 256, rb, 2);
        raw[t][3] = __builtin_amdgcn_raw_buffer_load_b64(rs, co + 192, rb, 2);
        raw[t][4] = __builtin_amdgcn_raw_buffer_load_b64(rs, so1, rb, 2);
        raw[t][5] = __builtin_amdgcn_raw_buffer_load_b64(rs, co + 64, rb, 2);
        PIN();
    };
    u32x4 tab[4][3];
    auto pack_cs = [&](int t) {
#pragma unroll
        for (int j = 0; j < 3; ++j) {
#pragma unroll
            for (int k = 0; k < 2; ++k) {
                tab[t][j][2 * k] = __builtin_amdgcn_perm(raw[t][3 + j][k], raw[t][j][k], 0x05040100);
                tab[t][j][2 * k + 1] = __builtin_amdgcn_perm(raw[t][3 + j][k], raw[t][j][k], 0x07060302);
            }
            *reinterpret_cast<u32x4*>(s_part + (wave ^ 4) * (NI * DH) + ((t * 3 + j) * 64 + lane) * 4) = tab[t][j];
        }
        PIN();
    };
    const auto rk = __builtin_amdgcn_make_buffer_rsrc((void*)qk, 0, KVB, 0x00020000);
    const auto rv = __builtin_amdgcn_make_buffer_rsrc((void*)qv, 0, KVB, 0x00020000);
    const unsigned voff = r * ROWB + c * 16;
    const unsigned woff = row0 * ROWB + hp * HEADB;
    auto toff = [&](int t) { return (unsigned)(t ^ (2 * hp)) * (4 * ROWB); };
    u32x4 ring[4][3];
    auto ld = [&](int slot, int j, __amdgpu_buffer_rsrc_t rr, unsigned off) {
        ring[slot][j] = __builtin_amdgcn_raw_buffer_load_b128(rr, voff + j * 256, off, 2);
        PIN();
    };
    auto ld_tile = [&](int slot, __amdgpu_buffer_rsrc_t rr, unsigned off) { ld(slot, 0, rr, off); ld(slot, 1, rr, off); ld(slot, 2, rr, off); };
    auto refill = [&](int slot, int j, __amdgpu_buffer_rsrc_t rr, unsigned goff, bool on) {
        if (ORD == 0) {
            if (on) { if (WID && wide) VMCNT(2 + WID); else VMCNT(2); if (j == 0) pace_wait(); }
            ld(slot, j, rr, goff);
        } else {
            if (on) { if (j == 0) pace_wait(); if (WID && wide) VMCNT(2 + WID); else VMCNT(2); }
            ld(slot, j, rr, goff);
            if (on && j == 0) { const unsigned t = (now32() << 8) - 1024 - (off - step); if ((int)(t - kept) > 0) kept = t;
                if (HOLD && (int)(kept - prev) > 100 * 256) { hold = kept; holdn = HOLD; } }
        }
        if (j == 2) report();
    };
    auto q_to_lds = [&]() {
        float o[2][8];
#pragma unroll
        for (int k = 0; k < 2; ++k)
#pragma unroll
            for (int u = 0; u < 4; ++u) {
                const unsigned pp = lq[k ^ 1][u] ^ (k == 0 ? 0x80008000u : 0u);
                o[k][2 * u] = rbf(rbf(bflo(lq[k][u]) * bflo(lc[k][u])) + rbf(bflo(pp) * bflo(ls[k][u])));
                o[k][2 * u + 1] = rbf(rbf(bfhi(lq[k][u]) * bfhi(lc[k][u])) + rbf(bfhi(pp) * bfhi(ls[k][u])));
            }
#pragma unroll
        for (int v = 0; v < 2; ++v) {
            u32x4 lo, hi;
#pragma unroll
            for (int e = 0; e < 4; ++e) {
                lo[e] = __builtin_amdgcn_perm(__float_as_uint(o[1][4 * v + e]), __float_as_uint(o[0][4 * v + e]), 0x07060302);
                hi[e] = __builtin_amdgcn_perm(__float_as_uint(o[0][4 * v + e]) ^ 0x80000000u, __float_as_uint(o[1][4 * v + e]), 0x07060302);
            }
            *reinterpret_cast<u32x4*>(s_q + qh * DH + qd + 4 * v) = lo;
            *reinterpret_cast<u32x4*>(s_q + qh * DH + HALF + qd + 4 * v) = hi;
        }
        PIN();
    };
    auto sc_to_lds = [&]() {
        float* dst = s_sc + tid * 16;
#pragma unroll
        for (int u = 0; u < 2; ++u) {
            *reinterpret_cast<f32x4*>(dst + 8 * u) = f32x4{bflo(lsc[0][u]) * SCALE_LOG2, bflo(lsc[1][u]), bflo(lsc[2][u]), bflo(lsc[3][u])};
            *reinterpret_cast<f32x4*>(dst + 8 * u + 4) = f32x4{bfhi(lsc[0][u]) * SCALE_LOG2, bfhi(lsc[1][u]), bfhi(lsc[2][u]), bfhi(lsc[3][u])};
        }
        PIN();
    };

    auto k0 = [&](int t) { pace_wait(); ld_tile(t, rk, woff + toff(t)); };
    if (PRO == 0) {
        load_qs(); load_sc(); load_cs(0);
        pace_wait(); load_cs(1);
        q_to_lds(); sc_to_lds();
        STAMP(1);
        k0(0); pack_cs(0);
        k0(1); pack_cs(1);
        VMCNT(3); k0(2);
        STAMP(2);
        VMCNT(3); k0(3);
    } else if (PRO == 1) {
        load_cs(0); PSTAMP(70); load_sc(); PSTAMP(71);
        pace_wait(); load_cs(1); STAMP(1); if (wave < 6) load_qs();
        PSTAMP(72); pack_cs(0); PSTAMP(73); sc_to_lds(); PSTAMP(74);
        k0(0); pack_cs(1); PSTAMP(75); if (wave < 6) q_to_lds();
        PSTAMP(76); k0(1); PSTAMP(77);
        VMCNT(3); PSTAMP(78); k0(2);
        STAMP(2);
        VMCNT(3); k0(3);
    } else if (PRO == 2) {
        load_cs(0); load_sc();
        pace_wait(); load_cs(1);
        STAMP(1);
        pack_cs(0); sc_to_lds();
        k0(0); if (wave < 6) load_qs(); pack_cs(1);
        k0(1); if (wave < 6) q_to_lds();
        VMCNT(3); k0(2);
        STAMP(2);
        VMCNT(3); k0(3);
    } else if (PRO == 3) {
        load_cs(0); load_sc();
        pace_wait(); load_cs(1);
        STAMP(1);
        pack_cs(0); sc_to_lds();
        k0(0); pack_cs(1);
        k0(1); if (wave < 6) load_qs();
        VMCNT(9); k0(2); if (wave < 6) q_to_lds();
        STAMP(2);
        VMCNT(3); k0(3);
    }
    if (lane == 0) *(vlds_u32x2*)(s_act + 2 * wave) = u32x2{kept, off};
    BARRIER();
    origin = *(vlds_u32*)(s_sync + 2);
    STAMP(3);

    u32x4 qp[3];
    auto load_q = [&](int h, int j) { qp[j] = *reinterpret_cast<const u32x4*>(s_q + h * DH + 64 * j + 4 * c); };
    load_q(hp, 0); load_q(hp, 1); load_q(hp, 2);
#pragma unroll
    for (int t = 0; t < 2; ++t)
#pragma unroll
        for (int j = 0; j < 3; ++j) tab[2 + t][j] = *reinterpret_cast<const u32x4*>(s_part + wave * (NI * DH) + ((t * 3 + j) * 64 + lane) * 4);

    for (int i = 0; i < NI; ++i) {
        const bool last = i + 1 == NI;
        const int h = 2 * i + hp;
        const unsigned hnext = (i + 1) * (2 * HEADB) + woff;
        const auto rn = __builtin_amdgcn_make_buffer_rsrc((void*)(last ? qv : qk), 0, KVB, 0x00020000);
        float* scp = s_sc + (h * 4 + rw) * 64;
        f32x4 sc[4];
#pragma unroll
        for (int t = 0; t < 4; ++t) sc[t] = *reinterpret_cast<const f32x4*>(scp + (4 * (t ^ (2 * hp)) + r) * 4);
        float s[4];
#pragma unroll
        for (int t = 0; t < 4; ++t) {
            f32x2 dot, wsum;
#pragma unroll
            for (int j = 0; j < 3; ++j) {
                f32x2 w01, w23, k01, k23;
                dot_cvt(qp[j], tab[t][j], ring[t][j], w01, w23, k01, k23);
                if (j == 0) { wsum = w01 + w23; dot = k01 * w01; } else { wsum += w01; wsum += w23; dot += k01 * w01; }
                dot += k23 * w23;
                asm volatile("" : "+v"(dot), "+v"(wsum));
                PIN();
                if (t == 3) load_q(last ? h : h + 2, j);
                if (t == 0 || t == VS) refill(t, j, rn, last ? woff + toff(t == VS ? 1 : 0) : hnext + toff(t), true);
                else refill(t, j, rk, last ? OOB : hnext + toff(t), !last);
                if (t == 0 && j == 0) TRACE(i);
            }
            const float a = (dot[0] + dot[1]) - sc[t][1] * (wsum[0] + wsum[1]);
            s[t] = row16_allsum(a) * sc[t][0];
        }
        const float mx = xrow_max(fmaxf(fmaxf(s[0], s[1]), fmaxf(s[2], s[3])));
        float l = 0.f, z = 0.f;
#pragma unroll
        for (int t = 0; t < 4; ++t) {
            const float p = __builtin_amdgcn_exp2f(s[t] - mx);
            l += p;
            s[t] = p * sc[t][2];
            z += s[t] * sc[t][3];
        }
        l = xrow_sum(l);
        z = xrow_sum(z);
        if (c == 0) {
            *reinterpret_cast<f32x4*>(scp + r * 4) = f32x4{s[0], s[1], s[2], s[3]};
            *reinterpret_cast<f32x4*>(scp + 16) = f32x4{mx, l, z, 0.f};
        }
        PIN();
    }
    STAMP(4);

    const float* pwp = s_sc + (hp * 4 + rw) * 64 + r * 4;
    f32x4 pw = *reinterpret_cast<const f32x4*>(pwp);
    for (int i = 0; i < NI; ++i) {
        const bool last = i + 1 == NI;
        const unsigned hoff = i * (2 * HEADB) + woff;
        f32x2 acc[3][2];
#pragma unroll
        for (int t = 0; t < 4; ++t) {
            const f32x2 p2 = {pw[t], pw[t]};
#pragma unroll
            for (int j = 0; j < 3; ++j) {
                const u32x4 vv = ring[(t & 1) * VS][j];
                const f32x2 v01 = {(float)(int)vv[0], (float)(int)vv[1]}, v23 = {(float)(int)vv[2], (float)(int)vv[3]};
                if (t == 0) { acc[j][0] = p2 * v01; acc[j][1] = p2 * v23; }
                else { acc[j][0] += p2 * v01; acc[j][1] += p2 * v23; }
                asm volatile("" : "+v"(acc[j][0]), "+v"(acc[j][1]));
                PIN();
                if (t == 3 && j == 2) pw = *reinterpret_cast<const f32x4*>(pwp + (last ? i : i + 1) * 512);
                if (t < 2) refill((t & 1) * VS, j, rv, hoff + toff(t + 2), true);
                else refill((t & 1) * VS, j, rv, last ? OOB : hoff + 2 * HEADB + toff(t - 2), !last);
                if (t == 0 && j == 0) TRACE(16 + i);
            }
        }
        float* sl = s_part + (wave * NI + i) * DH;
#pragma unroll
        for (int j = 0; j < 3; ++j) {
            const float x = swap32_add(acc[j][0][0], acc[j][1][0]);
            const float y = swap32_add(acc[j][0][1], acc[j][1][1]);
            sl[64 * j + 4 * c + r] = swap16_add(x, y);
        }
        PIN();
        if (lane == 0) *(vlds_u32*)(s_done + wave) = i + 1;
        PIN();
    }
    STAMP(6);
    unsigned old = 0;
    if (lane == 0) old = __hip_atomic_fetch_add(s_sync + hp, 1u, __ATOMIC_RELAXED, __HIP_MEMORY_SCOPE_WORKGROUP);
    PIN();
    const unsigned tag = s_sync[4 + (lane & 3)];
    if (__builtin_amdgcn_readfirstlane(old) == 3) {
        while (__builtin_amdgcn_readfirstlane(*(vlds_u32*)(s_sync + 3)) == 0) __builtin_amdgcn_s_sleep(1);
        PIN();
        publish(merge(NI - 1, hp, lane & 31, tag), region(tag), NI - 1, hp, lane & 31, lane < 32);
        PIN();
    }
    STAMP(7);
    {
        const unsigned o = __builtin_amdgcn_readfirstlane(origin);
        while ((int)((now32() << 8) - o) < pace.rd) __builtin_amdgcn_s_sleep(1);
        PIN();
    }
    STAMP(8);
    {
        u32x4 x, y;
        unsigned xo = (tid >> 2) * PSTR + (tid & 3) * 16, yo = xo + 128 * PSTR;
        for (;;) {
            const u32x4 xn = __builtin_amdgcn_raw_buffer_load_b128(rw_, xo, region(tag) + bid * USTR, LDA);
            const u32x4 yn = __builtin_amdgcn_raw_buffer_load_b128(rw_, yo, region(tag) + bid * USTR, LDA);
            unsigned bad = (xo != OOB && (xn[3] ^ tag) > 0xffffu ? 1u : 0u) | (yo != OOB && (yn[3] ^ tag) > 0xffffu ? 2u : 0u);
            bad |= __builtin_amdgcn_mov_dpp(bad, 0xb1, 0xf, 0xf, true);
            bad |= __builtin_amdgcn_mov_dpp(bad, 0x4e, 0xf, 0xf, true);
            const bool tx = xo != OOB, ty = yo != OOB;
#pragma unroll
            for (int k = 0; k < 4; ++k) { x[k] = tx ? xn[k] : x[k]; y[k] = ty ? yn[k] : y[k]; }
            xo = bad & 1 ? xo : OOB;
            yo = bad & 2 ? yo : OOB;
            if (__builtin_amdgcn_ballot_w64(bad != 0) == 0 || now32() - tstart > SPIN_LIMIT) break;
            PIN();
        }
        STAMP(9);
        const unsigned sel = lane & 1 ? 0x01000504u : 0x05040100u;
        const float lx = __uint_as_float(__builtin_amdgcn_perm(__builtin_amdgcn_mov_dpp(x[3], 0xb1, 0xf, 0xf, true), x[3], sel));
        const float ly = __uint_as_float(__builtin_amdgcn_perm(__builtin_amdgcn_mov_dpp(y[3], 0xb1, 0xf, 0xf, true), y[3], sel));
        float M = fmaxf(lx, ly);
        M = fmaxf(M, dpp<0x128>(M));
        M = fmaxf(M, dpp<0x124>(M));
        M = xrow_max(M);
        const float w0 = __builtin_amdgcn_exp2f(lx - M), w1 = __builtin_amdgcn_exp2f(ly - M);
        float a[8];
#pragma unroll
        for (int k = 0; k < 3; ++k) {
            a[2 * k] = w0 * bflo(x[k]) + w1 * bflo(y[k]);
            a[2 * k + 1] = w0 * bfhi(x[k]) + w1 * bfhi(y[k]);
        }
        a[6] = a[7] = w0 + w1;
        float b[4], d[2];
#pragma unroll
        for (int k = 0; k < 4; ++k) b[k] = swap32_add(a[k], a[4 + k]);
#pragma unroll
        for (int k = 0; k < 2; ++k) d[k] = swap16_add(b[k], b[2 + k]);
#pragma unroll
        for (int k = 0; k < 2; ++k) { d[k] += dpp<0x128>(d[k]); d[k] += dpp<0x124>(d[k]); }
        if (c < 4) *reinterpret_cast<f32x2*>(s_red + wave * 28 + (r < 3 ? c * 6 + r * 2 : 24)) = f32x2{d[0], r < 3 ? d[1] : M};
        STAMP(10);
        BARRIER();
        if (tid < 24) {
            f32x2 sm[8];
            float ov[8];
#pragma unroll
            for (int w = 0; w < 8; ++w) { sm[w] = *reinterpret_cast<const f32x2*>(s_red + w * 28 + 24); ov[w] = s_red[w * 28 + tid]; }
            float MM = sm[0][1];
#pragma unroll
            for (int w = 1; w < 8; ++w) MM = fmaxf(MM, sm[w][1]);
            float S = 0.f, o = 0.f;
#pragma unroll
            for (int w = 0; w < 8; ++w) {
                const float ff = __builtin_amdgcn_exp2f(sm[w][1] - MM);
                S += ff * sm[w][0];
                o += ff * ov[w];
            }
            out[bid * 24 + tid] = __float2bfloat16(o * __builtin_amdgcn_rcpf(S));
        }
        if (bid == 0 && wave == 1) __builtin_amdgcn_raw_buffer_store_b16((unsigned short)(tag >> 16), rw_, lane < 4 ? lane * 2 : OOB, WS_EPOCH, 16);
    }
}

template <int X, int PRO, int GATE, int STAG>
static void launch_cfg(void* const* d_in, void* d_out, void* d_ws, hipStream_t stream, float t_us, float start_us, float off_us, float debt_us, float adj_pct, float pub_us) {
    Pace pace;
    pace.step = (unsigned)(t_us * 25600.f / 128.f);
    pace.start = (int)(start_us * 25600.f);
    pace.offset = (int)(off_us * 25600.f);
    pace.bias = (int)(debt_us * 25600.f);
    pace.rd = (int)((start_us + t_us * (129.f / 128.f) + adj_pct) * 25600.f);
    pace.pub = (int)((start_us + t_us * (129.f / 128.f) + pub_us) * 25600.f);
    kv_sweep_kernel<X, PRO, GATE, STAG, 1><<<NWG, 576, 0, stream>>>((const unsigned short*)d_in[0], (const int*)d_in[1], (const unsigned short*)d_in[2],
        (const unsigned short*)d_in[3], (const int*)d_in[4], (const unsigned short*)d_in[5], (const unsigned short*)d_in[6],
        (const unsigned short*)d_in[7], (const unsigned short*)d_in[8], (const unsigned short*)d_in[9], (const unsigned short*)d_in[10],
        (unsigned char*)d_ws, (bf16*)d_out, pace);
}

#define CFG 966, 21, 120, 0
#define PRM 102.5f, 0.5f, 1.0f, 0.05f, 1.8f, 0.8f
extern "C" void kernel_launch(void* const* d_in, const int* in_sizes, int n_in,
                              void* d_out, int out_size, void* d_ws, size_t ws_size,
                              hipStream_t stream) {
    launch_cfg<CFG>(d_in, d_out, d_ws, stream, PRM);
}
```

```cpp
#include <hip/hip_runtime.h>
#include <hip/hip_bf16.h>
#include <cstdint>
#include <type_traits>


using bf16 = __hip_bfloat16;
using u32x4 = __attribute__((ext_vector_type(4))) unsigned;
using u32x3 = __attribute__((ext_vector_type(3))) unsigned;
using u32x2 = __attribute__((ext_vector_type(2))) unsigned;
using u64x8 = __attribute__((ext_vector_type(8))) unsigned long long;
using f32x2 = __attribute__((ext_vector_type(2))) float;
using f32x4 = __attribute__((ext_vector_type(4))) float;
using bf16x2 = __attribute__((ext_vector_type(2))) __bf16;

constexpr int DH = 192;
constexpr int HALF = 96;
constexpr int NH = 32;
constexpr int SKV = 16384;
constexpr int NWG = 256;
constexpr int NI = 16;
constexpr unsigned ROWB = DH * 4;
constexpr unsigned HEADB = SKV * ROWB;
constexpr unsigned KVB = NH * HEADB;
constexpr unsigned CSB = SKV * DH * 2;
constexpr unsigned OOB = 0x80000000u;
constexpr float SCALE_LOG2 = 1.4426950408889634f * 0.07216878364870322f;
constexpr unsigned SPIN_LIMIT = 20000000u;

constexpr unsigned UNITB = NWG * 64;
constexpr unsigned GSTR = 16 * UNITB + 256;
constexpr unsigned WS_PART = 0;
constexpr unsigned WS_EPOCH = NWG * (UNITB + 1024);
constexpr unsigned WS_REP = WS_EPOCH + 256;
constexpr unsigned WS_BYTES = WS_REP + 256 * 512;

__device__ __forceinline__ float bflo(unsigned u) { return __uint_as_float(u << 16); }
__device__ __forceinline__ float bfhi(unsigned u) { return __uint_as_float(u & 0xffff0000u); }
__device__ __forceinline__ float rbf(float x) { return __bfloat162float(__float2bfloat16(x)); }
__device__ __forceinline__ unsigned pk_bf16(f32x2 a) { return __builtin_bit_cast(unsigned, __builtin_convertvector(a, bf16x2)); }
__device__ __forceinline__ void dot_cvt(const u32x4 q, const u32x4 t, const u32x4 ki, f32x2& w01, f32x2& w23, f32x2& k01, f32x2& k23) {
    float w0, w1, w2, w3;
    float k0 = __uint_as_float(ki[0]), k1 = __uint_as_float(ki[1]), k2 = __uint_as_float(ki[2]), k3 = __uint_as_float(ki[3]);
    asm("v_dot2_f32_bf16 %0, %8, %12, 0\n\tv_dot2_f32_bf16 %1, %9, %13, 0\n\tv_dot2_f32_bf16 %2, %10, %14, 0\n\tv_dot2_f32_bf16 %3, %11, %15, 0\n\t"
        "v_cvt_f32_i32 %4, %4\n\tv_cvt_f32_i32 %5, %5\n\tv_cvt_f32_i32 %6, %6\n\tv_cvt_f32_i32 %7, %7"
        : "=&v"(w0), "=&v"(w1), "=&v"(w2), "=&v"(w3), "+v"(k0), "+v"(k1), "+v"(k2), "+v"(k3)
        : "v"(q[0]), "v"(q[1]), "v"(q[2]), "v"(q[3]), "v"(t[0]), "v"(t[1]), "v"(t[2]), "v"(t[3]));
    w01 = f32x2{w0, w1}; w23 = f32x2{w2, w3}; k01 = f32x2{k0, k1}; k23 = f32x2{k2, k3};
}

template <int CTRL> __device__ __forceinline__ float dpp(float x) {
    return __builtin_bit_cast(float, __builtin_amdgcn_mov_dpp(__builtin_bit_cast(int, x), CTRL, 0xf, 0xf, true));
}
__device__ __forceinline__ float row16_allsum(float x) {
    x += dpp<0x128>(x);
    x += dpp<0x124>(x);
    x += dpp<0x122>(x);
    x += dpp<0x121>(x);
    return x;
}
__device__ __forceinline__ float row16_allmax(float x) {
    x = fmaxf(x, dpp<0x128>(x));
    x = fmaxf(x, dpp<0x124>(x));
    x = fmaxf(x, dpp<0x122>(x));
    x = fmaxf(x, dpp<0x121>(x));
    return x;
}
__device__ __forceinline__ float xrow_max(float x) {
    auto s = __builtin_amdgcn_permlane16_swap(__float_as_uint(x), __float_as_uint(x), false, false);
    x = fmaxf(__uint_as_float(s[0]), __uint_as_float(s[1]));
    auto t = __builtin_amdgcn_permlane32_swap(__float_as_uint(x), __float_as_uint(x), false, false);
    return fmaxf(__uint_as_float(t[0]), __uint_as_float(t[1]));
}
__device__ __forceinline__ float xrow_sum(float x) {
    auto s = __builtin_amdgcn_permlane16_swap(__float_as_uint(x), __float_as_uint(x), false, false);
    x = __uint_as_float(s[0]) + __uint_as_float(s[1]);
    auto t = __builtin_amdgcn_permlane32_swap(__float_as_uint(x), __float_as_uint(x), false, false);
    return __uint_as_float(t[0]) + __uint_as_float(t[1]);
}
__device__ __forceinline__ float swap32_add(float a, float b) {
    auto t = __builtin_amdgcn_permlane32_swap(__float_as_uint(a), __float_as_uint(b), false, false);
    return __uint_as_float(t[0]) + __uint_as_float(t[1]);
}
__device__ __forceinline__ float swap16_add(float a, float b) {
    auto t = __builtin_amdgcn_permlane16_swap(__float_as_uint(a), __float_as_uint(b), false, false);
    return __uint_as_float(t[0]) + __uint_as_float(t[1]);
}
__device__ __forceinline__ int row16_sum(int x) {
    x += __builtin_amdgcn_mov_dpp(x, 0x128, 0xf, 0xf, true);
    x += __builtin_amdgcn_mov_dpp(x, 0x124, 0xf, 0xf, true);
    x += __builtin_amdgcn_mov_dpp(x, 0x122, 0xf, 0xf, true);
    x += __builtin_amdgcn_mov_dpp(x, 0x121, 0xf, 0xf, true);
    return x;
}
__device__ __forceinline__ unsigned now32() { return (unsigned)__builtin_amdgcn_s_memrealtime(); }

#define PIN() do { asm volatile("" ::: "memory"); __builtin_amdgcn_sched_barrier(0); } while (0)
#define BARRIER() do { asm volatile("s_waitcnt lgkmcnt(0)" ::: "memory"); __builtin_amdgcn_s_barrier(); PIN(); } while (0)
#define VMCNT(n) asm volatile("s_waitcnt vmcnt(%0)" :: "i"(n) : "memory")

typedef volatile __attribute__((address_space(3))) unsigned vlds_u32;
typedef volatile __attribute__((address_space(3))) u32x2 vlds_u32x2;
typedef volatile __attribute__((address_space(3))) u32x4 vlds_u32x4;

struct Pace {
    unsigned step;
    int start;
    int offset;
    int bias;
    int rd;
    int pub;
};

template <int X, int OPT, int GATE, int TP, int SLP>
__global__ __launch_bounds__(576) __attribute__((aligned(4096))) void kv_sweep_kernel(
    const unsigned short* q, const int* qk, const unsigned short* ksc,
    const unsigned short* kzp, const int* qv, const unsigned short* vsc,
    const unsigned short* vzp, const unsigned short* q_cos, const unsigned short* q_sin,
    const unsigned short* k_cos, const unsigned short* k_sin, unsigned char* ws, unsigned short* out, Pace pace)
{
    __shared__ __attribute__((aligned(16))) unsigned s_q[NH * DH];
    __shared__ __attribute__((aligned(16))) float s_sc[NH * 64 * 4];
    __shared__ __attribute__((aligned(16))) float s_part[8 * NI * DH];
    __shared__ __attribute__((aligned(16))) float s_red[8 * 28];
    __shared__ __attribute__((aligned(16))) float s_f[NH * 8];
    __shared__ __attribute__((aligned(16))) unsigned s_done[8];
    __shared__ __attribute__((aligned(16))) unsigned s_sync[8];
    __shared__ __attribute__((aligned(16))) unsigned s_act[16];

    constexpr int SLIP = GATE / 10 % 10 * 2560, HOLD = GATE % 10;
    constexpr int DRY = OPT % 10 & 1, LS = 2, PAD = OPT % 10 / 2 & 1, PF = OPT / 10 % 10, RST = GATE / 1000 % 10 * 256, LDA = GATE / 10000 % 10 < 4 ? 16 + GATE / 10000 % 10 : GATE / 10000 % 10 - 4, STA = 16, WIN = GATE / 100000 % 10, OST = OPT / 100 % 10 == 0 ? 0 : OPT / 100 % 10 == 1 ? 16 : OPT / 100 % 10 == 2 ? 17 : 18, PRO = OPT / 1000;
    unsigned long long pc0;
    asm volatile("s_getpc_b64 %0\n.Lkv_beg_%1_%2_%3_%4:" : "=s"(pc0) : "i"(X), "i"(OPT), "i"(GATE), "i"(TP));
    const unsigned tstart = now32();
    const int tid = threadIdx.x;
    const int lane = tid & 63;
    const int wave = __builtin_amdgcn_readfirstlane(tid >> 6);
    const int r = lane >> 4;
    const int c = lane & 15;
    const int bid = blockIdx.x;
    unsigned clen;
    asm volatile("s_mov_b32 %0, .Lkv_end_%1_%2_%3_%4-.Lkv_beg_%1_%2_%3_%4" : "=s"(clen) : "i"(X), "i"(OPT), "i"(GATE), "i"(TP));
    const auto rcode = __builtin_amdgcn_make_buffer_rsrc((void*)(pc0 & ~127ull), 0, clen, 0x00020000);
    const unsigned xw = TP & 16 ? __builtin_amdgcn_raw_buffer_load_b32(rcode, (lane << 20) + ((bid >> 3) + wave * 32) * 64, 0, 0) : 0;
    asm volatile("" ::: "memory");
    const int m = (bid & 7) * 32 + (bid >> 3);
    const int rw = wave & 3, hp = wave >> 2;
    const int row0 = m * 64 + rw * 16;
    unsigned co = r * 384 + c * 8, so1 = r * 384 + (c < 8 ? c * 8 + 320 : c * 8 - 64), rb0 = (row0 + 8 * hp) * 384u;
    unsigned sco = ((tid >> 4) * SKV + m * 64 + (tid & 15) * 4) * 2;
    asm volatile("" : "+v"(co), "+v"(so1), "+v"(sco), "+s"(rb0));
    asm volatile("" :: "s"(q), "s"(qk), "s"(ksc), "s"(kzp), "s"(qv), "s"(vsc), "s"(vzp), "s"(q_cos), "s"(q_sin), "s"(k_cos), "s"(k_sin), "s"(ws), "s"(out),
                 "s"(pace.step), "s"(pace.start), "s"(pace.offset), "s"(pace.bias), "s"(pace.rd), "s"(pace.pub));
    const auto rw_ = __builtin_amdgcn_make_buffer_rsrc((void*)ws, 0, WS_BYTES, 0x00020000);
    const auto ro = __builtin_amdgcn_make_buffer_rsrc((void*)out, 0, NH * DH * 2, 0x00020000);

    auto merge = [&](const float* p, f32x4 f, float zs, unsigned w3) {
        f32x2 u[4][3], a[3];
#pragma unroll
        for (int w = 0; w < 4; ++w)
#pragma unroll
            for (int k = 0; k < 3; ++k) u[w][k] = *reinterpret_cast<const f32x2*>(p + w * NI * DH + 2 * k);
        asm volatile("" : "+v"(u[0][0]), "+v"(u[0][1]), "+v"(u[0][2]), "+v"(u[1][0]), "+v"(u[1][1]), "+v"(u[1][2]),
                          "+v"(u[2][0]), "+v"(u[2][1]), "+v"(u[2][2]), "+v"(u[3][0]), "+v"(u[3][1]), "+v"(u[3][2]));
#pragma unroll
        for (int k = 0; k < 3; ++k) a[k] = (f[0] * u[0][k] - zs + f[1] * u[1][k]) + (f[2] * u[2][k] + f[3] * u[3][k]);
        return u32x4{pk_bf16(a[0]), pk_bf16(a[1]), pk_bf16(a[2]), w3};
    };
    auto last_word = [&](unsigned tag, float ls, int c6) { return tag | ((__float_as_uint(ls) >> (16 * (c6 & 1))) & 0xffffu); };
    auto piece_off = [&](int hp, int c6) { return (hp * 8 + (c6 >> 2)) * UNITB + (c6 & 3) * 16; };
    auto publish = [&](u32x4 v, int i, unsigned vo) {
        __builtin_amdgcn_raw_buffer_store_b128(v, rw_, vo, WS_PART + i * GSTR + m * 64, STA);
        asm volatile("s_nop 1" :: "v"(v) : "memory");
    };

    if (wave == 8) {
        if (TP & 32) __builtin_amdgcn_s_setprio(3);
        const int jj = bid >> 3;
        auto warm_ld = [&](int n) {
            return __builtin_amdgcn_raw_buffer_load_b32(rcode, lane < n ? (jj * 9 + lane) * 64 : OOB, 0, 0);
        };
        auto warm_code = [&](int n) { if (n) { const unsigned x = warm_ld(n); asm volatile("" :: "v"(x)); } };
        constexpr int WN[10][4] = {{0, 0, 0, 0}, {64, 0, 0, 0}, {9, 0, 0, 0}, {64, 9, 9, 0}, {9, 9, 9, 0}, {18, 0, 0, 0}, {18, 18, 18, 0}, {9, 9, 0, 9}, {36, 0, 0, 0}, {9, 0, 9, 0}};
        auto touch_ld = [&](bool on) {
            return __builtin_amdgcn_raw_buffer_load_b32(__builtin_amdgcn_make_buffer_rsrc((void*)(jj < NI ? qk : qv), 0, KVB, 0x00020000),
                on && lane < 4 ? (lane >> 1) * HEADB + (lane & 1) * 2047 * ROWB : OOB, (jj & 15) * 2 * HEADB + (bid & 7) * 2048 * ROWB, 2);
        };
        auto touch_kv = [&]() { const unsigned x = touch_ld(true); asm volatile("" :: "v"(x)); };
        auto touch_ws = [&]() {
            const unsigned x = __builtin_amdgcn_raw_buffer_load_b32(rw_, c * GSTR, WS_PART + 16 * UNITB, 0);
            asm volatile("" :: "v"(x));
        };
        const unsigned x0 = TP & 16 ? xw : warm_ld(WN[PF][0]);
        PIN();
        if (TP >> 6) while (now32() - tstart < 50 + (TP >> 6) * 50u) __builtin_amdgcn_s_sleep(1);
        u32x3 lc[2], ls[2];
        u32x4 qraw[12];
        {
            const auto rq = __builtin_amdgcn_make_buffer_rsrc((void*)q, 0, NH * DH * 2, 0x00020000);
            const auto rqc = __builtin_amdgcn_make_buffer_rsrc((void*)q_cos, 0, DH * 2, 0x00020000);
            const auto rqs = __builtin_amdgcn_make_buffer_rsrc((void*)q_sin, 0, DH * 2, 0x00020000);
#pragma unroll
            for (int k = 0; k < 12; ++k) qraw[k] = __builtin_amdgcn_raw_buffer_load_b128(rq, lane * 16 + (k & 3) * 1024, (k >> 2) * 4096, 0);
#pragma unroll
            for (int k = 0; k < 2; ++k) {
                lc[k] = __builtin_amdgcn_raw_buffer_load_b96(rqc, c * 12 + k * 192, 0, 0);
                ls[k] = __builtin_amdgcn_raw_buffer_load_b96(rqs, c * 12 + k * 192, 0, 0);
            }
            PIN();
        }
        const unsigned tag = ((unsigned)__builtin_amdgcn_raw_buffer_load_b16(rw_, (lane & 3) * 2, WS_EPOCH, 16) + 1u) << 16;
        bool touched = !(TP & 1) || jj < 3;
        const unsigned x1 = TP & 1 ? touch_ld(jj < 3) : 0;
        PIN();
        unsigned* const s_raw = reinterpret_cast<unsigned*>(s_part) + NI * DH / 2;
#pragma unroll
        for (int k = 0; k < 12; ++k) *reinterpret_cast<u32x4*>(s_raw + k / 6 * (NI * DH) + k % 6 * 256 + lane * 4) = qraw[k];
        f32x2 fc[2][3], fs[2][3];
#pragma unroll
        for (int k = 0; k < 2; ++k)
#pragma unroll
            for (int u = 0; u < 3; ++u) { fc[k][u] = f32x2{bflo(lc[k][u]), bfhi(lc[k][u])}; fs[k][u] = f32x2{bflo(ls[k][u]), bfhi(ls[k][u])}; }
        PIN();
#pragma nounroll
        for (int it = 0; it < 4; ++it) {
            unsigned qa[2][3], qb[2][3];
#pragma unroll
            for (int g = 0; g < 2; ++g)
#pragma unroll
                for (int u = 0; u < 3; ++u) {
                    const unsigned* src = s_raw + g * (NI * DH) + (it * 4 + r) * HALF + 3 * c;
                    qa[g][u] = src[u]; qb[g][u] = src[HALF / 2 + u];
                }
#pragma unroll
            for (int g = 0; g < 2; ++g)
#pragma unroll
                for (int u = 0; u < 3; ++u) {
                    const f32x2 a = {bflo(qa[g][u]), bfhi(qa[g][u])}, b = {bflo(qb[g][u]), bfhi(qb[g][u])};
                    const f32x2 o0 = a * fc[0][u] - b * fs[0][u], o1 = b * fc[1][u] + a * fs[1][u];
                    const unsigned l0 = pk_bf16(f32x2{o0[0], o1[0]}), l1 = pk_bf16(f32x2{o0[1], o1[1]});
                    unsigned* dst = s_q + (16 * g + 4 * it + r) * DH + 6 * c + 2 * u;
                    *reinterpret_cast<u32x2*>(dst) = u32x2{l0, l1};
                    *reinterpret_cast<u32x2*>(dst + HALF) = u32x2{__builtin_amdgcn_alignbit(l0, l0, 16) ^ 0x80000000u, __builtin_amdgcn_alignbit(l1, l1, 16) ^ 0x80000000u};
                }
        }
        asm volatile("" :: "v"(x0), "v"(x1));
        if (TP & 32) __builtin_amdgcn_s_setprio(0);
        if (TP & 2) touch_ws();
        if (TP & 4) __builtin_amdgcn_raw_buffer_store_b16((unsigned short)0, ro, lane < 24 ? lane * 2 : OOB, bid * 48, OST);
        unsigned origin = (tstart << 8) + pace.offset;
        if (lane < 8) { s_done[lane] = 0; s_sync[lane] = lane < 3 ? origin : lane < 4 ? 0 : tag; }
        PIN();
        BARRIER();
        const int mh = lane >> 5, mc = lane & 31;
        const unsigned rtag = __builtin_amdgcn_readlane(tag, 0) | __builtin_amdgcn_readlane(tag, 1) >> 16;
        constexpr int RPX = 2;
        constexpr int KS = X / 1000 % 10, SR = X / 10000 % 10 * 2, EM = X / 100000 % 10 * 1280, EHI = X / 1000000 % 10 * 512, IB = X / 10000000 % 10 * 256, FR = X / 100000000 + 10;
        constexpr int RSH = X / 100 % 10;
        constexpr unsigned RDLY = 200;
        constexpr int UP = X / 10 % 10 ? X / 10 % 10 * 82 : 1 << 24, DN = X % 10 ? X % 10 * 82 : 1 << 24;
        const int rj = ((bid >> 3) + 5 * (bid & 7)) & 31;
        const int rid = rj % (32 / RPX) == 0 ? rj / (32 / RPX) * 8 + (bid & 7) : -1;
        unsigned wround = now32() >> RSH, rround = wround, tick = 0, target = origin;
        bool warmed = false, synced = false;
        int drift = 0, endx = 0;
        const unsigned round0 = rround;
        unsigned fin = origin;
        int i = 0;
        auto kept_now = [&](unsigned now) {
            const u32x2 a = *(vlds_u32x2*)(s_act + 2 * (lane & 7));
            const int late = max((int)(a[0] - origin), (int)((now << 8) - a[1] - origin));
            return origin + (__builtin_amdgcn_readfirstlane(row16_sum(late)) >> 4);
        };
        auto steer = [&](unsigned now) {
            if ((now >> 5) != tick) {
                tick = now >> 5;
                target += drift;
                origin += max(-DN, min(UP + drift, (int)(target - origin)));
                if (SLIP) { const int l2 = (int)(kept_now(now) - origin) - SLIP; if (l2 > 0) origin += l2; }
                if (HOLD && !synced) target = origin - (HOLD - 1) * 82;
                fin = origin + max(0, EM ? (int)(target + endx - origin) : 0);
                if (lane == 0) { *(vlds_u32*)(s_sync + 2) = origin; *(vlds_u32*)(s_sync + 1) = fin; }
                PIN();
            }
        };
        auto finished = [&]() {
            const u32x4 a = *(vlds_u32x4*)s_done, b = *(vlds_u32x4*)(s_done + 4);
            return __builtin_amdgcn_readfirstlane(min(min(min(a[0], a[1]), min(a[2], a[3])), min(min(b[0], b[1]), min(b[2], b[3]))));
        };
        while (i < NI - 2) {
            const unsigned now = now32();
            if ((now >> RSH) != wround) {
                wround = now >> RSH;
                if (rid >= 0 && (now & ((1u << RSH) - 1)) < 48)
                    __builtin_amdgcn_raw_buffer_store_b64(u32x2{kept_now(now), rtag + wround}, rw_, lane == 0 ? rid * 8 : OOB, WS_REP + (wround & 255) * 512, 16);
                PIN();
                continue;
            }
            if ((int)(((now - RDLY) >> RSH) - rround) > 0) {
                rround = (now - RDLY) >> RSH;
                const u32x2 x = __builtin_amdgcn_raw_buffer_load_b64(rw_, c * 8, WS_REP + (rround & 255) * 512, 16);
                const int d = (int)(x[0] - origin);
                const bool ok = x[1] == rtag + rround && d < (32 << 16) && d > -(32 << 16);
                const int n = __builtin_popcountll(__builtin_amdgcn_ballot_w64(ok));
                if (n >= 48) {
                    const unsigned nt = origin + __builtin_amdgcn_readfirstlane((int)(row16_allsum(ok ? (float)d : 0.f) * 4.f / (float)n)) - pace.bias;
                    if (KS && synced && rround - round0 > SR) drift = max(0, min(drift + ((max(-1024, min((int)(nt - target) + 6 * drift, EHI)) - (rround - round0 < FR ? IB : 0)) >> (3 + KS)), 400));
                    target = nt + 6 * drift;
                    if (EM) endx = max(0, __builtin_amdgcn_readfirstlane((int)row16_allmax(ok ? (float)d : -1e9f)) - (int)(target - origin) - EM);
                    synced = true;
                    const int lost = min((int)(kept_now(now32()) - origin) - pace.bias, (int)(target - origin));
                    if (lost > 0) origin += lost;
                }
                PIN();
                continue;
            }
            steer(now);
            if (!touched && (int)((now << 8) - origin) > (jj * 4 - 8) * (int)pace.step) { touch_kv(); touched = true; continue; }
            if (WN[PF][1] && !warmed && (int)((now << 8) - origin) > 60 * (int)pace.step) { warm_code(WN[PF][1]); warmed = true; continue; }
            if (finished() <= (unsigned)i) { __builtin_amdgcn_s_sleep(2); continue; }
            PIN();
            if (i == 0) {
                f32x4 ml[4], f;
#pragma unroll
                for (int w = 0; w < 4; ++w) ml[w] = *reinterpret_cast<const f32x4*>(s_sc + (mc * 4 + w) * 64 + 16);
                const float M = fmaxf(fmaxf(ml[0][0], ml[1][0]), fmaxf(ml[2][0], ml[3][0]));
                float L = 0.f, zs = 0.f;
#pragma unroll
                for (int w = 0; w < 4; ++w) { f[w] = __builtin_amdgcn_exp2f(ml[w][0] - M); L += f[w] * ml[w][1]; zs += f[w] * ml[w][2]; }
                const float inv = 1.f / L;
                *reinterpret_cast<f32x4*>(s_f + mc * 8) = f * inv;
                *reinterpret_cast<f32x2*>(s_f + mc * 8 + 4) = f32x2{zs * inv, M + __builtin_amdgcn_logf(L)};
                PIN();
                if (lane == 0) *(vlds_u32*)(s_sync + 3) = 1;
                PIN();
            }
            if (i == NI - 3) { warm_code(WN[PF][2]); if (TP & 8) touch_ws(); }
            const f32x2 zl = *reinterpret_cast<const f32x2*>(s_f + (2 * i + mh) * 8 + 4);
            const u32x4 v = merge(s_part + (mh * 4 * NI + i) * DH + mc * 6, *reinterpret_cast<const f32x4*>(s_f + (2 * i + mh) * 8), zl[0], last_word(tag, zl[1], mc));
            PIN();
            *reinterpret_cast<u32x4*>(s_part + (mh * 4 * NI + i) * DH + mc * 4) = v;
            PIN();
            ++i;
        }
        u32x4 v[NI - 1];
#pragma unroll
        for (int i = 0; i < NI - 2; ++i) v[i] = *reinterpret_cast<const u32x4*>(s_part + (mh * 4 * NI + i) * DH + mc * 4);
        f32x4 f = *reinterpret_cast<const f32x4*>(s_f + (2 * (NI - 2) + mh) * 8);
        f32x2 zl = *reinterpret_cast<const f32x2*>(s_f + (2 * (NI - 2) + mh) * 8 + 4);
        PIN();
#pragma unroll
        for (int i = 0; i < NI - 2; ++i) asm volatile("" : "+v"(v[i]));
        asm volatile("" : "+v"(f), "+v"(zl));
        v[NI - 2] = v[0];
        bool have = false, sent = false;
        while (!(have && sent)) {
            const unsigned now = now32();
            if (!have) {
                steer(now);
                if (finished() > NI - 2) {
                    PIN();
                    v[NI - 2] = merge(s_part + (mh * 4 * NI + NI - 2) * DH + mc * 6, f, zl[0], last_word(tag, zl[1], mc));
                    have = true;
                    warm_code(WN[PF][3]);
                }
            }
            if (!sent && (int)((now << 8) - fin) >= pace.pub) {
#pragma unroll
                for (int i = 0; i < NI - 2; ++i) publish(v[i], i, piece_off(mh, mc));
                sent = true;
            }
            if (!(have && sent)) __builtin_amdgcn_s_sleep(1);
            PIN();
        }
        publish(v[NI - 2], NI - 2, piece_off(mh, mc));
        asm volatile(".Lkv_end_%0_%1_%2_%3:" :: "i"(X), "i"(OPT), "i"(GATE), "i"(TP));
        return;
    }

    unsigned off = pace.start + ((pace.step * (hp * 1024 + m * 4 + rw)) >> 11);
    unsigned origin = tstart << 8, kept = 0;
    const unsigned step = pace.step;
    auto pace_wait = [&]() {
        const unsigned tgt = __builtin_amdgcn_readfirstlane(origin) + off;
        unsigned now = now32() << 8;
        kept = ((int)(now - tgt) > 0 ? now : tgt) - off;
        while ((int)(tgt - now) > 0) { if (SLP) __builtin_amdgcn_s_sleep(SLP); now = now32() << 8; }
        off += step;
    };
    auto report = [&]() {
        if (lane == 0) *(vlds_u32x2*)(s_act + 2 * wave) = u32x2{kept, off};
        origin = *(vlds_u32*)(s_sync + 2);
        PIN();
    };

    u32x2 lsc[4];
    auto load_sc = [&]() {
#pragma unroll
        for (int a = 0; a < 4; ++a) {
            const unsigned short* base = a == 0 ? ksc : a == 1 ? kzp : a == 2 ? vsc : vzp;
            lsc[a] = __builtin_nontemporal_load(reinterpret_cast<const u32x2*>((const char*)base + sco));
        }
        PIN();
    };

    const auto rc = __builtin_amdgcn_make_buffer_rsrc((void*)k_cos, 0, CSB, 0x00020000);
    const auto rs = __builtin_amdgcn_make_buffer_rsrc((void*)k_sin, 0, CSB, 0x00020000);
    u32x2 raw[2][6];
    auto load_cs = [&](int t) {
        const unsigned rb = rb0 + t * 4 * 384;
        raw[t][0] = __builtin_amdgcn_raw_buffer_load_b64(rc, co, rb, 2);
        raw[t][1] = __builtin_amdgcn_raw_buffer_load_b64(rc, co + 128, rb, 2);
        raw[t][2] = __builtin_amdgcn_raw_buffer_load_b64(rc, co + 256, rb, 2);
        raw[t][3] = __builtin_amdgcn_raw_buffer_load_b64(rs, co + 192, rb, 2);
        raw[t][4] = __builtin_amdgcn_raw_buffer_load_b64(rs, so1, rb, 2);
        raw[t][5] = __builtin_amdgcn_raw_buffer_load_b64(rs, co + 64, rb, 2);
        PIN();
    };
    u32x4 tab[4][3];
    auto pack_cs = [&](int t) {
#pragma unroll
        for (int j = 0; j < 3; ++j) {
#pragma unroll
            for (int k = 0; k < 2; ++k) {
                tab[t][j][2 * k] = __builtin_amdgcn_perm(raw[t][3 + j][k], raw[t][j][k], 0x05040100);
                tab[t][j][2 * k + 1] = __builtin_amdgcn_perm(raw[t][3 + j][k], raw[t][j][k], 0x07060302);
            }
            *reinterpret_cast<u32x4*>(s_part + (wave ^ 4) * (NI * DH) + ((t * 3 + j) * 64 + lane) * 4) = tab[t][j];
        }
        PIN();
    };
    const auto rk = __builtin_amdgcn_make_buffer_rsrc((void*)qk, 0, KVB, 0x00020000);
    const auto rv = __builtin_amdgcn_make_buffer_rsrc((void*)qv, 0, KVB, 0x00020000);
    const unsigned voff = r * ROWB + c * 16;
    const unsigned woff = row0 * ROWB + hp * HEADB;
    auto toff = [&](int t) { return (unsigned)(t ^ (2 * hp)) * (4 * ROWB); };
    u32x4 ring[4][3];
    auto ld = [&](int slot, int j, __amdgpu_buffer_rsrc_t rr, unsigned off) {
        ring[slot][j] = __builtin_amdgcn_raw_buffer_load_b128(rr, voff + j * 256, off, 2);
        PIN();
    };
    auto ld_tile = [&](int slot, __amdgpu_buffer_rsrc_t rr, unsigned off) { ld(slot, 0, rr, off); ld(slot, 1, rr, off); ld(slot, 2, rr, off); };
    auto refill = [&](int slot, int j, __amdgpu_buffer_rsrc_t rr, unsigned goff) {
        if (j == 0) pace_wait();
        if (WIN == 1) VMCNT(3);
        else if (WIN == 2) { if (j == 0) VMCNT(3); else VMCNT(2); }
        else if (WIN == 3) { if (j == 2) VMCNT(2); else VMCNT(3); }
        else if (WIN == 4) VMCNT(4);
        else VMCNT(2);
        ld(slot, j, rr, goff);
        if (j == 0) { const unsigned t = (now32() << 8) - 1024 - (off - step); if ((int)(t - kept) > 0) kept = t; }
        if (j == 2) report();
    };
    auto sc_to_lds = [&]() {
        float* dst = s_sc + tid * 16;
#pragma unroll
        for (int u = 0; u < 2; ++u) {
            *reinterpret_cast<f32x4*>(dst + 8 * u) = f32x4{bflo(lsc[0][u]) * SCALE_LOG2, bflo(lsc[1][u]), bflo(lsc[2][u]), bflo(lsc[3][u])};
            *reinterpret_cast<f32x4*>(dst + 8 * u + 4) = f32x4{bfhi(lsc[0][u]) * SCALE_LOG2, bfhi(lsc[1][u]), bfhi(lsc[2][u]), bfhi(lsc[3][u])};
        }
        PIN();
    };

    auto k0 = [&](int t) { pace_wait(); ld_tile(t, rk, woff + toff(t)); };
    if (PRO == 0) {
        load_cs(0); load_sc();
        pace_wait(); load_cs(1);
        pack_cs(0); sc_to_lds();
        k0(0); pack_cs(1);
        k0(1);
    } else if (PRO == 1) {
        load_cs(0);
        pace_wait(); load_cs(1);
        pack_cs(0);
        pace_wait(); load_sc();
        pack_cs(1);
        k0(0); sc_to_lds();
        k0(1);
    } else if (PRO == 3) {
        load_sc();
        pace_wait(); load_cs(0);
        sc_to_lds();
        pace_wait(); load_cs(1);
        pack_cs(0);
        k0(0); pack_cs(1);
        k0(1);
    } else if (PRO == 5) {
        load_cs(0);
        pace_wait(); load_cs(1);
        pace_wait(); load_sc();
        pack_cs(0);
        k0(0); pack_cs(1);
        k0(1); sc_to_lds();
    } else if (PRO == 7) {
        load_sc();
        pace_wait(); load_cs(0);
        pace_wait(); load_cs(1);
        sc_to_lds();
        k0(0); pack_cs(0);
        k0(1); pack_cs(1);
    } else if (PRO == 10) {
        load_cs(0); load_sc();
        pace_wait(); load_cs(1);
        pack_cs(0);
        k0(0); sc_to_lds(); pack_cs(1);
        k0(1);
    } else if (PRO == 20) {
        load_cs(0); load_sc();
        pace_wait(); load_cs(1);
        pack_cs(0); sc_to_lds(); pack_cs(1);
        k0(0);
        k0(1);
    }
    asm volatile("" :: "v"(xw));
    VMCNT(3); k0(2);
    VMCNT(3); k0(3);
    if (lane == 0) *(vlds_u32x2*)(s_act + 2 * wave) = u32x2{kept, off};
    BARRIER();
    origin = *(vlds_u32*)(s_sync + 2);

    u32x4 qp[3];
    auto load_q = [&](int h, int j) { qp[j] = *reinterpret_cast<const u32x4*>(s_q + h * DH + 64 * j + 4 * c); };
    load_q(hp, 0); load_q(hp, 1); load_q(hp, 2);
#pragma unroll
    for (int t = 0; t < 2; ++t)
#pragma unroll
        for (int j = 0; j < 3; ++j) tab[2 + t][j] = *reinterpret_cast<const u32x4*>(s_part + wave * (NI * DH) + ((t * 3 + j) * 64 + lane) * 4);

    for (int i = 0; i < NI; ++i) {
        const bool last = i + 1 == NI;
        const int h = 2 * i + hp;
        const unsigned hnext = (i + 1) * (2 * HEADB) + woff;
        const auto rn = __builtin_amdgcn_make_buffer_rsrc((void*)(last ? qv : qk), 0, KVB, 0x00020000);
        float* scp = s_sc + (h * 4 + rw) * 64;
        f32x4 sc[4];
#pragma unroll
        for (int t = 0; t < 4; ++t) sc[t] = *reinterpret_cast<const f32x4*>(scp + (4 * (t ^ (2 * hp)) + r) * 4);
        float s[4];
#pragma unroll
        for (int t = 0; t < 4; ++t) {
            f32x2 dot, wsum;
#pragma unroll
            for (int j = 0; j < 3; ++j) {
                f32x2 w01, w23, k01, k23;
                dot_cvt(qp[j], tab[t][j], ring[t][j], w01, w23, k01, k23);
                if (j == 0) { wsum = w01 + w23; dot = k01 * w01; } else { wsum += w01; wsum += w23; dot += k01 * w01; }
                dot += k23 * w23;
                asm volatile("" : "+v"(dot), "+v"(wsum));
                PIN();
                if (t == 3) load_q(last ? h : h + 2, j);
                refill(t, j, rn, (last ? woff : hnext) + toff(t));
            }
            const float a = (dot[0] + dot[1]) - sc[t][1] * (wsum[0] + wsum[1]);
            s[t] = row16_allsum(a) * sc[t][0];
        }
        const float mx = xrow_max(fmaxf(fmaxf(s[0], s[1]), fmaxf(s[2], s[3])));
        float l = 0.f, z = 0.f;
#pragma unroll
        for (int t = 0; t < 4; ++t) {
            const float p = __builtin_amdgcn_exp2f(s[t] - mx);
            l += p;
            s[t] = p * sc[t][2];
            z += s[t] * sc[t][3];
        }
        l = xrow_sum(l);
        z = xrow_sum(z);
        if (c == 0) {
            *reinterpret_cast<f32x4*>(scp + r * 4) = f32x4{s[0], s[1], s[2], s[3]};
            *reinterpret_cast<f32x4*>(scp + 16) = f32x4{mx, l, z, 0.f};
        }
        PIN();
    }

    const float* pwp = s_sc + (hp * 4 + rw) * 64 + r * 4;
    f32x4 pw = *reinterpret_cast<const f32x4*>(pwp);
    unsigned tag, lw, lo;
    f32x4 lf;
    f32x2 lz;
    unsigned lp = (hp * 4 * NI + NI - 1) * DH + (lane & 31) * 6;
    f32x2 acc[3][2];
    auto v_acc = [&](int t, int j) {
        const f32x2 p2 = {pw[t], pw[t]};
        const u32x4 vv = ring[t][j];
        const f32x2 v01 = {(float)(int)vv[0], (float)(int)vv[1]}, v23 = {(float)(int)vv[2], (float)(int)vv[3]};
        if (t == 0) { acc[j][0] = p2 * v01; acc[j][1] = p2 * v23; }
        else { acc[j][0] += p2 * v01; acc[j][1] += p2 * v23; }
        asm volatile("" : "+v"(acc[j][0]), "+v"(acc[j][1]));
        PIN();
    };
    auto v_fold = [&](int i, int j) {
        const float x = swap32_add(acc[j][0][0], acc[j][1][0]);
        const float y = swap32_add(acc[j][0][1], acc[j][1][1]);
        s_part[(wave * NI + i) * DH + 64 * j + 4 * c + r] = swap16_add(x, y);
    };
    auto v_done = [&](int i) {
        PIN();
        if (lane == 0) *(vlds_u32*)(s_done + wave) = i + 1;
        PIN();
    };
#pragma unroll
    for (int t = 0; t < LS; ++t)
#pragma unroll
        for (int j = 0; j < 3; ++j) v_acc(t, j);
    for (int i = 0; i < NI - 1; ++i) {
        const unsigned hnext = (i + 1) * (2 * HEADB) + woff;
#pragma unroll
        for (int k = 0; k < 4; ++k) {
            const int t = (k + LS) & 3;
#pragma unroll
            for (int j = 0; j < 3; ++j) {
                v_acc(t, j);
                if (t == 3 && j == 2) pw = *reinterpret_cast<const f32x4*>(pwp + (i + 1) * 512);
                refill(k, j, rv, hnext + toff(k));
            }
            if (t == 3) { v_fold(i, 0); v_fold(i, 1); v_fold(i, 2); v_done(i); }
        }
    }
    if (PAD) asm volatile("s_branch .Lkv_tail_%=\n\t.p2align 12\n.Lkv_tail_%=:" ::: "memory");
    while (__builtin_amdgcn_readfirstlane(*(vlds_u32*)(s_sync + 3)) == 0) __builtin_amdgcn_s_sleep(1);
    PIN();
    tag = s_sync[4 + (lane & 3)];
    lf = *reinterpret_cast<const f32x4*>(s_f + (2 * (NI - 1) + hp) * 8);
    lz = *reinterpret_cast<const f32x2*>(s_f + (2 * (NI - 1) + hp) * 8 + 4);
    origin = *(vlds_u32*)(s_sync + 1);
    lw = last_word(tag, lz[1], lane & 31);
    lo = lane < 32 ? piece_off(hp, lane & 31) : OOB;
    asm volatile("" : "+v"(tag), "+v"(lf), "+v"(lz), "+v"(lw), "+v"(lo), "+v"(lp), "+v"(origin));
    PIN();
#pragma unroll
    for (int t = LS; t < 4; ++t)
#pragma unroll
        for (int j = 0; j < 3; ++j) { v_acc(t, j); if (t == 3) { v_fold(NI - 1, j); PIN(); } }
    v_done(NI - 1);
    {
        const u32x4 dn = *(vlds_u32x4*)(s_done + 4 * hp);
        PIN();
        const u32x4 v = merge(s_part + lp, lf, lz[0], lw);
        if (__builtin_amdgcn_readfirstlane(dn[0] & dn[1] & dn[2] & dn[3]) == NI) publish(v, NI - 1, lo);
        PIN();
    }
    {
        const unsigned inbox = WS_PART + (bid >> 4) * GSTR + (bid & 15) * UNITB;
        const unsigned o = __builtin_amdgcn_readfirstlane(origin) - (15 - (bid >> 4)) * RST;
        while ((int)((now32() << 8) - o) < pace.rd) __builtin_amdgcn_s_sleep(1);
        PIN();
        u32x4 x = __builtin_amdgcn_raw_buffer_load_b128(rw_, tid * 16, inbox, LDA);
        u32x4 y = __builtin_amdgcn_raw_buffer_load_b128(rw_, tid * 16 + 8192, inbox, LDA);
        for (;;) {
            unsigned bad = ((x[3] ^ tag) > 0xffffu ? 1u : 0u) | ((y[3] ^ tag) > 0xffffu ? 2u : 0u);
            if (__builtin_amdgcn_ballot_w64(bad != 0) == 0 || now32() - tstart > SPIN_LIMIT) break;
            bad |= __builtin_amdgcn_mov_dpp(bad, 0xb1, 0xf, 0xf, true);
            bad |= __builtin_amdgcn_mov_dpp(bad, 0x4e, 0xf, 0xf, true);
            PIN();
            const u32x4 xn = __builtin_amdgcn_raw_buffer_load_b128(rw_, bad & 1 ? tid * 16 : OOB, inbox, 16);
            const u32x4 yn = __builtin_amdgcn_raw_buffer_load_b128(rw_, bad & 2 ? tid * 16 + 8192 : OOB, inbox, 16);
#pragma unroll
            for (int k = 0; k < 4; ++k) { x[k] = bad & 1 ? xn[k] : x[k]; y[k] = bad & 2 ? yn[k] : y[k]; }
        }
        if (DRY) { __builtin_amdgcn_raw_buffer_store_b32(0u, rw_, lane < 16 ? (lane & 7) * 128 + (lane >> 3) * 8192 : OOB, inbox + wave * 1024, 16); PIN(); }
        const unsigned sel = lane & 1 ? 0x01000504u : 0x05040100u;
        const float lx = __uint_as_float(__builtin_amdgcn_perm(__builtin_amdgcn_mov_dpp(x[3], 0xb1, 0xf, 0xf, true), x[3], sel));
        const float ly = __uint_as_float(__builtin_amdgcn_perm(__builtin_amdgcn_mov_dpp(y[3], 0xb1, 0xf, 0xf, true), y[3], sel));
        float M = fmaxf(lx, ly);
        M = fmaxf(M, dpp<0x128>(M));
        M = fmaxf(M, dpp<0x124>(M));
        M = xrow_max(M);
        const float w0 = __builtin_amdgcn_exp2f(lx - M), w1 = __builtin_amdgcn_exp2f(ly - M);
        float a[8];
#pragma unroll
        for (int k = 0; k < 3; ++k) {
            a[2 * k] = w0 * bflo(x[k]) + w1 * bflo(y[k]);
            a[2 * k + 1] = w0 * bfhi(x[k]) + w1 * bfhi(y[k]);
        }
        a[6] = a[7] = w0 + w1;
        float b[4], d[2];
#pragma unroll
        for (int k = 0; k < 4; ++k) b[k] = swap32_add(a[k], a[4 + k]);
#pragma unroll
        for (int k = 0; k < 2; ++k) d[k] = swap16_add(b[k], b[2 + k]);
#pragma unroll
        for (int k = 0; k < 2; ++k) { d[k] += dpp<0x128>(d[k]); d[k] += dpp<0x124>(d[k]); }
        if (c < 4) *reinterpret_cast<f32x2*>(s_red + wave * 28 + (r < 3 ? c * 6 + r * 2 : 24)) = f32x2{d[0], r < 3 ? d[1] : M};
        BARRIER();
        if (tid < 24) {
            f32x2 sm[8];
            float ov[8];
#pragma unroll
            for (int w = 0; w < 8; ++w) { sm[w] = *reinterpret_cast<const f32x2*>(s_red + w * 28 + 24); ov[w] = s_red[w * 28 + lane]; }
            float MM = sm[0][1];
#pragma unroll
            for (int w = 1; w < 8; ++w) MM = fmaxf(MM, sm[w][1]);
            float S = 0.f, o = 0.f;
#pragma unroll
            for (int w = 0; w < 8; ++w) {
                const float ff = __builtin_amdgcn_exp2f(sm[w][1] - MM);
                S += ff * sm[w][0];
                o += ff * ov[w];
            }
            unsigned res = pk_bf16(f32x2{o * __builtin_amdgcn_rcpf(S), 0.f});
            __builtin_amdgcn_raw_buffer_store_b16((unsigned short)res, ro, lane * 2, bid * 48, OST);
        }
        PIN();
        if (bid == 0 && wave == 1) __builtin_amdgcn_raw_buffer_store_b16((unsigned short)(tag >> 16), rw_, lane < 4 ? lane * 2 : OOB, WS_EPOCH, 16);
    }
    __builtin_amdgcn_endpgm();
}

#define VAR_XS(p) ((p) / 1000 % 2)
template <int X, int PRO, int GATE, int STAG>
static void launch_cfg(void* const* d_in, void* d_out, void* d_ws, hipStream_t stream, float t_us, float start_us, float off_us, float debt_us, float adj_pct, float pub_us) {
    Pace pace;
    pace.step = (unsigned)(t_us * 25600.f / 128.f);
    pace.start = (int)(start_us * 25600.f);
    pace.offset = (int)(off_us * 25600.f);
    pace.bias = (int)(debt_us * 25600.f);
    pace.rd = (int)((start_us + t_us * ((129.f + VAR_XS(PRO)) / 128.f) + adj_pct) * 25600.f);
    pace.pub = (int)((start_us + t_us * ((129.f + VAR_XS(PRO)) / 128.f) + pub_us) * 25600.f);
    kv_sweep_kernel<X, PRO, GATE, STAG, 1><<<NWG, 576, 0, stream>>>((const unsigned short*)d_in[0], (const int*)d_in[1], (const unsigned short*)d_in[2],
        (const unsigned short*)d_in[3], (const int*)d_in[4], (const unsigned short*)d_in[5], (const unsigned short*)d_in[6],
        (const unsigned short*)d_in[7], (const unsigned short*)d_in[8], (const unsigned short*)d_in[9], (const unsigned short*)d_in[10],
        (unsigned char*)d_ws, (unsigned short*)d_out, pace);
}

extern "C" void kernel_launch(void* const* d_in, const int* in_sizes, int n_in,
                              void* d_out, int out_size, void* d_ws, size_t ws_size,
                              hipStream_t stream) {
    launch_cfg<335022966, 1123, 2121, 225>(d_in, d_out, d_ws, stream, 102.5f, 0.5f, 0.0f, 0.05f, 1.6f, 0.6f);
}
```

```cpp
#define PIDX 99
#define PMAX 0
#define PSUB 0
#define PWAVE 0
#include <hip/hip_runtime.h>
#include <hip/hip_bf16.h>
#include <cstdint>
#include <type_traits>

using bf16 = __hip_bfloat16;
using u32x4 = __attribute__((ext_vector_type(4))) unsigned;
using u32x3 = __attribute__((ext_vector_type(3))) unsigned;
using u32x2 = __attribute__((ext_vector_type(2))) unsigned;
using u64x8 = __attribute__((ext_vector_type(8))) unsigned long long;
using f32x2 = __attribute__((ext_vector_type(2))) float;
using f32x4 = __attribute__((ext_vector_type(4))) float;
using bf16x2 = __attribute__((ext_vector_type(2))) __bf16;

constexpr int DH = 192;
constexpr int HALF = 96;
constexpr int NH = 32;
constexpr int SKV = 16384;
constexpr int NWG = 256;
constexpr int NI = 16;
constexpr unsigned ROWB = DH * 4;
constexpr unsigned HEADB = SKV * ROWB;
constexpr unsigned KVB = NH * HEADB;
constexpr unsigned CSB = SKV * DH * 2;
constexpr unsigned OOB = 0x80000000u;
constexpr float SCALE_LOG2 = 1.4426950408889634f * 0.07216878364870322f;
constexpr unsigned SPIN_LIMIT = 20000000u;

constexpr unsigned UNITB = NWG * 64;
constexpr unsigned GSTR = 16 * UNITB + 256;
constexpr unsigned WS_PART = 0;
constexpr unsigned WS_EPOCH = NWG * (UNITB + 1024);
constexpr unsigned WS_REP = WS_EPOCH + 256;
constexpr unsigned WS_PROBE = WS_REP + 256 * 512;
constexpr unsigned WS_BYTES = WS_PROBE + 4096;

__device__ __forceinline__ float bflo(unsigned u) { return __uint_as_float(u << 16); }
__device__ __forceinline__ float bfhi(unsigned u) { return __uint_as_float(u & 0xffff0000u); }
__device__ __forceinline__ float rbf(float x) { return __bfloat162float(__float2bfloat16(x)); }
__device__ __forceinline__ unsigned pk_bf16(f32x2 a) { return __builtin_bit_cast(unsigned, __builtin_convertvector(a, bf16x2)); }
__device__ __forceinline__ void dot_cvt(const u32x4 q, const u32x4 t, const u32x4 ki, f32x2& w01, f32x2& w23, f32x2& k01, f32x2& k23) {
    float w0, w1, w2, w3;
    float k0 = __uint_as_float(ki[0]), k1 = __uint_as_float(ki[1]), k2 = __uint_as_float(ki[2]), k3 = __uint_as_float(ki[3]);
    asm("v_dot2_f32_bf16 %0, %8, %12, 0\n\tv_dot2_f32_bf16 %1, %9, %13, 0\n\tv_dot2_f32_bf16 %2, %10, %14, 0\n\tv_dot2_f32_bf16 %3, %11, %15, 0\n\t"
        "v_cvt_f32_i32 %4, %4\n\tv_cvt_f32_i32 %5, %5\n\tv_cvt_f32_i32 %6, %6\n\tv_cvt_f32_i32 %7, %7"
        : "=&v"(w0), "=&v"(w1), "=&v"(w2), "=&v"(w3), "+v"(k0), "+v"(k1), "+v"(k2), "+v"(k3)
        : "v"(q[0]), "v"(q[1]), "v"(q[2]), "v"(q[3]), "v"(t[0]), "v"(t[1]), "v"(t[2]), "v"(t[3]));
    w01 = f32x2{w0, w1}; w23 = f32x2{w2, w3}; k01 = f32x2{k0, k1}; k23 = f32x2{k2, k3};
}

template <int CTRL> __device__ __forceinline__ float dpp(float x) {
    return __builtin_bit_cast(float, __builtin_amdgcn_mov_dpp(__builtin_bit_cast(int, x), CTRL, 0xf, 0xf, true));
}
__device__ __forceinline__ float row16_allsum(float x) {
    x += dpp<0x128>(x);
    x += dpp<0x124>(x);
    x += dpp<0x122>(x);
    x += dpp<0x121>(x);
    return x;
}
__device__ __forceinline__ float row16_allmax(float x) {
    x = fmaxf(x, dpp<0x128>(x));
    x = fmaxf(x, dpp<0x124>(x));
    x = fmaxf(x, dpp<0x122>(x));
    x = fmaxf(x, dpp<0x121>(x));
    return x;
}
__device__ __forceinline__ float xrow_max(float x) {
    auto s = __builtin_amdgcn_permlane16_swap(__float_as_uint(x), __float_as_uint(x), false, false);
    x = fmaxf(__uint_as_float(s[0]), __uint_as_float(s[1]));
    auto t = __builtin_amdgcn_permlane32_swap(__float_as_uint(x), __float_as_uint(x), false, false);
    return fmaxf(__uint_as_float(t[0]), __uint_as_float(t[1]));
}
__device__ __forceinline__ float xrow_sum(float x) {
    auto s = __builtin_amdgcn_permlane16_swap(__float_as_uint(x), __float_as_uint(x), false, false);
    x = __uint_as_float(s[0]) + __uint_as_float(s[1]);
    auto t = __builtin_amdgcn_permlane32_swap(__float_as_uint(x), __float_as_uint(x), false, false);
    return __uint_as_float(t[0]) + __uint_as_float(t[1]);
}
__device__ __forceinline__ float swap32_add(float a, float b) {
    auto t = __builtin_amdgcn_permlane32_swap(__float_as_uint(a), __float_as_uint(b), false, false);
    return __uint_as_float(t[0]) + __uint_as_float(t[1]);
}
__device__ __forceinline__ float swap16_add(float a, float b) {
    auto t = __builtin_amdgcn_permlane16_swap(__float_as_uint(a), __float_as_uint(b), false, false);
    return __uint_as_float(t[0]) + __uint_as_float(t[1]);
}
__device__ __forceinline__ int row16_sum(int x) {
    x += __builtin_amdgcn_mov_dpp(x, 0x128, 0xf, 0xf, true);
    x += __builtin_amdgcn_mov_dpp(x, 0x124, 0xf, 0xf, true);
    x += __builtin_amdgcn_mov_dpp(x, 0x122, 0xf, 0xf, true);
    x += __builtin_amdgcn_mov_dpp(x, 0x121, 0xf, 0xf, true);
    return x;
}
__device__ __forceinline__ unsigned now32() { return (unsigned)__builtin_amdgcn_s_memrealtime(); }

#define NSTAMP 12
#define STAMP(i) do { if ((i) == PIDX - 50) { __builtin_amdgcn_sched_barrier(0); probe = now32(); __builtin_amdgcn_sched_barrier(0); } } while (0)
#define PSTAMP(i) do { if ((i) == PIDX) { __builtin_amdgcn_sched_barrier(0); probe = now32(); asm volatile("" : "+s"(probe) :: "memory"); __builtin_amdgcn_sched_barrier(0); } } while (0)
#define TRACE(i) do { if ((i) == PIDX) probe = tstart + ((int)(kept - (tstart << 8)) >> 8); } while (0)
#define HSTAMP(i)
#define PIN() do { asm volatile("" ::: "memory"); __builtin_amdgcn_sched_barrier(0); } while (0)
#define BARRIER() do { asm volatile("s_waitcnt lgkmcnt(0)" ::: "memory"); __builtin_amdgcn_s_barrier(); PIN(); } while (0)
#define VMCNT(n) asm volatile("s_waitcnt vmcnt(%0)" :: "i"(n) : "memory")

typedef volatile __attribute__((address_space(3))) unsigned vlds_u32;
typedef volatile __attribute__((address_space(3))) u32x2 vlds_u32x2;
typedef volatile __attribute__((address_space(3))) u32x4 vlds_u32x4;

struct Pace {
    unsigned step;
    int start;
    int offset;
    int bias;
    int rd;
    int pub;
};

template <int X, int OPT, int GATE, int TP, int SLP>
__global__ __launch_bounds__(576) __attribute__((aligned(4096))) void kv_sweep_kernel(
    const void*, const void*, const void*, const void*, const void*, const void*, const void*, const void*,
    const void*, const void*, const void*, void*, void*, Pace)
{
    __shared__ __attribute__((aligned(16))) unsigned s_q[NH * DH];
    __shared__ __attribute__((aligned(16))) float s_sc[NH * 64 * 4];
    __shared__ __attribute__((aligned(16))) float s_part[8 * NI * DH];
    __shared__ __attribute__((aligned(16))) float s_red[8 * 28];
    __shared__ __attribute__((aligned(16))) float s_f[NH * 8];
    __shared__ __attribute__((aligned(16))) unsigned s_done[8];
    __shared__ __attribute__((aligned(16))) unsigned s_sync[8];
    __shared__ __attribute__((aligned(16))) unsigned s_act[16];

    unsigned probe = 0;
    constexpr int SLIP = GATE / 10 % 10 * 2560, HOLD = GATE % 10;
    constexpr int DRY = OPT % 10 & 1, LS = 2, PAD = OPT % 10 / 2 & 1, PF = OPT / 10 % 10, RST = GATE / 1000 % 10 * 256, LDA = GATE / 10000 % 10 < 4 ? 16 + GATE / 10000 % 10 : GATE / 10000 % 10 - 4, STA = 16, WIN = GATE / 100000 % 10, OST = OPT / 100 % 10 == 0 ? 0 : OPT / 100 % 10 == 1 ? 16 : OPT / 100 % 10 == 2 ? 17 : 18, PRO = OPT / 1000;
    unsigned long long pc0, t0;
    u64x8 ka, kb;
    asm volatile("s_load_dwordx16 %3, %4, 0x40\n\ts_load_dwordx16 %2, %4, 0x0\n\ts_memrealtime %1\n\ts_getpc_b64 %0\n.Lkv_beg_%5_%6_%7_%8:"
                 : "=&s"(pc0), "=&s"(t0), "=&s"(ka), "=&s"(kb) : "s"(__builtin_amdgcn_kernarg_segment_ptr()), "i"(X), "i"(OPT), "i"(GATE), "i"(TP));
    __builtin_amdgcn_sched_barrier(0);
    const int tid = threadIdx.x;
    const int lane = tid & 63;
    int wave = __builtin_amdgcn_readfirstlane(tid >> 6);
    const int r = lane >> 4;
    const int c = lane & 15;
    const int bid = blockIdx.x;
    const int m = (bid & 7) * 32 + (bid >> 3);
    const int rw = wave & 3, hp = wave >> 2;
    const int row0 = m * 64 + rw * 16;
    unsigned co = r * 384 + c * 8, so1 = r * 384 + (c < 8 ? c * 8 + 320 : c * 8 - 64), rb0 = (row0 + 8 * hp) * 384u;
    asm volatile("s_waitcnt lgkmcnt(0)" : "+s"(t0), "+s"(ka), "+s"(kb), "+v"(co), "+v"(so1), "+s"(rb0), "+s"(wave));
    typedef const __attribute__((address_space(1))) char* gptr;
    void* const q = (void*)ka[0]; void* const qk = (void*)ka[1]; void* const qv = (void*)ka[4]; void* const q_cos = (void*)ka[7];
    const gptr ksc = (gptr)ka[2], kzp = (gptr)ka[3], vsc = (gptr)ka[5], vzp = (gptr)ka[6];
    void* const q_sin = (void*)kb[0]; void* const k_cos = (void*)kb[1]; void* const k_sin = (void*)kb[2]; void* const out = (void*)kb[4];
    unsigned char* const ws = (unsigned char*)(__attribute__((address_space(1))) unsigned char*)kb[3];
    const Pace pace = {(unsigned)kb[5], (int)(kb[5] >> 32), (int)kb[6], (int)(kb[6] >> 32), (int)kb[7], (int)(kb[7] >> 32)};
    const unsigned tstart = (unsigned)t0;
    const unsigned tsched = TP & 16 ? now32() : tstart;
    PSTAMP(69);
    const auto rw_ = __builtin_amdgcn_make_buffer_rsrc((void*)ws, 0, WS_BYTES, 0x00020000);
    const auto ro = __builtin_amdgcn_make_buffer_rsrc((void*)out, 0, NH * DH * 2, 0x00020000);

    auto merge = [&](const float* p, f32x4 f, float zs, unsigned w3) {
        f32x2 u[4][3], a[3];
#pragma unroll
        for (int w = 0; w < 4; ++w)
#pragma unroll
            for (int k = 0; k < 3; ++k) u[w][k] = *reinterpret_cast<const f32x2*>(p + w * NI * DH + 2 * k);
        asm volatile("" : "+v"(u[0][0]), "+v"(u[0][1]), "+v"(u[0][2]), "+v"(u[1][0]), "+v"(u[1][1]), "+v"(u[1][2]),
                          "+v"(u[2][0]), "+v"(u[2][1]), "+v"(u[2][2]), "+v"(u[3][0]), "+v"(u[3][1]), "+v"(u[3][2]));
#pragma unroll
        for (int k = 0; k < 3; ++k) a[k] = (f[0] * u[0][k] - zs + f[1] * u[1][k]) + (f[2] * u[2][k] + f[3] * u[3][k]);
        return u32x4{pk_bf16(a[0]), pk_bf16(a[1]), pk_bf16(a[2]), w3};
    };
    auto last_word = [&](unsigned tag, float ls, int c6) { return tag | ((__float_as_uint(ls) >> (16 * (c6 & 1))) & 0xffffu); };
    auto piece_off = [&](int hp, int c6) { return (hp * 8 + (c6 >> 2)) * UNITB + (c6 & 3) * 16; };
    auto publish = [&](u32x4 v, int i, unsigned vo) {
        __builtin_amdgcn_raw_buffer_store_b128(v, rw_, vo, WS_PART + i * GSTR + m * 64, STA);
        asm volatile("s_nop 1" :: "v"(v) : "memory");
    };

    if (wave == 8) {
        if (TP & 32) __builtin_amdgcn_s_setprio(3);
        HSTAMP(11);
        unsigned clen;
        asm volatile("s_mov_b32 %0, .Lkv_end_%1_%2_%3_%4-.Lkv_beg_%1_%2_%3_%4" : "=s"(clen) : "i"(X), "i"(OPT), "i"(GATE), "i"(TP));
        const auto rcode = __builtin_amdgcn_make_buffer_rsrc((void*)(pc0 & ~127ull), 0, clen, 0x00020000);
        const int jj = bid >> 3;
        auto warm_ld = [&](int n) {
            return __builtin_amdgcn_raw_buffer_load_b32(rcode, lane < n ? (jj * 9 + lane) * 64 : OOB, 0, 0);
        };
        auto warm_code = [&](int n) { if (n) { const unsigned x = warm_ld(n); asm volatile("" :: "v"(x)); } };
        constexpr int WN[10][4] = {{0, 0, 0, 0}, {64, 0, 0, 0}, {9, 0, 0, 0}, {64, 9, 9, 0}, {9, 9, 9, 0}, {18, 0, 0, 0}, {18, 18, 18, 0}, {9, 9, 0, 9}, {36, 0, 0, 0}, {9, 0, 9, 0}};
        auto touch_ld = [&](bool on) {
            return __builtin_amdgcn_raw_buffer_load_b32(__builtin_amdgcn_make_buffer_rsrc((void*)(jj < NI ? qk : qv), 0, KVB, 0x00020000),
                on && lane < 4 ? (lane >> 1) * HEADB + (lane & 1) * 2047 * ROWB : OOB, (jj & 15) * 2 * HEADB + (bid & 7) * 2048 * ROWB, 2);
        };
        auto touch_kv = [&]() { const unsigned x = touch_ld(true); asm volatile("" :: "v"(x)); };
        auto touch_ws = [&]() {
            const unsigned x = __builtin_amdgcn_raw_buffer_load_b32(rw_, c * GSTR, WS_PART + 16 * UNITB, 0);
            asm volatile("" :: "v"(x));
        };
        const unsigned x0 = warm_ld(WN[PF][0]);
        PIN();
        if (TP >> 6) while (now32() - tsched < 50 + (TP >> 6) * 50u) __builtin_amdgcn_s_sleep(1);
        u32x3 lc[2], ls[2];
        u32x4 qraw[12];
        {
            const auto rq = __builtin_amdgcn_make_buffer_rsrc((void*)q, 0, NH * DH * 2, 0x00020000);
            const auto rqc = __builtin_amdgcn_make_buffer_rsrc((void*)q_cos, 0, DH * 2, 0x00020000);
            const auto rqs = __builtin_amdgcn_make_buffer_rsrc((void*)q_sin, 0, DH * 2, 0x00020000);
#pragma unroll
            for (int k = 0; k < 12; ++k) qraw[k] = __builtin_amdgcn_raw_buffer_load_b128(rq, lane * 16 + (k & 3) * 1024, (k >> 2) * 4096, 0);
#pragma unroll
            for (int k = 0; k < 2; ++k) {
                lc[k] = __builtin_amdgcn_raw_buffer_load_b96(rqc, c * 12 + k * 192, 0, 0);
                ls[k] = __builtin_amdgcn_raw_buffer_load_b96(rqs, c * 12 + k * 192, 0, 0);
            }
            PIN();
        }
        const unsigned tag = ((unsigned)__builtin_amdgcn_raw_buffer_load_b16(rw_, (lane & 3) * 2, WS_EPOCH, 16) + 1u) << 16;
        bool touched = !(TP & 1) || jj < 3;
        const unsigned x1 = TP & 1 ? touch_ld(jj < 3) : 0;
        PIN();
        HSTAMP(2);
        unsigned* const s_raw = reinterpret_cast<unsigned*>(s_part) + NI * DH / 2;
#pragma unroll
        for (int k = 0; k < 12; ++k) *reinterpret_cast<u32x4*>(s_raw + k / 6 * (NI * DH) + k % 6 * 256 + lane * 4) = qraw[k];
        f32x2 fc[2][3], fs[2][3];
#pragma unroll
        for (int k = 0; k < 2; ++k)
#pragma unroll
            for (int u = 0; u < 3; ++u) { fc[k][u] = f32x2{bflo(lc[k][u]), bfhi(lc[k][u])}; fs[k][u] = f32x2{bflo(ls[k][u]), bfhi(ls[k][u])}; }
        PIN();
        HSTAMP(3);
#pragma nounroll
        for (int it = 0; it < 4; ++it) {
            unsigned qa[2][3], qb[2][3];
#pragma unroll
            for (int g = 0; g < 2; ++g)
#pragma unroll
                for (int u = 0; u < 3; ++u) {
                    const unsigned* src = s_raw + g * (NI * DH) + (it * 4 + r) * HALF + 3 * c;
                    qa[g][u] = src[u]; qb[g][u] = src[HALF / 2 + u];
                }
#pragma unroll
            for (int g = 0; g < 2; ++g)
#pragma unroll
                for (int u = 0; u < 3; ++u) {
                    const f32x2 a = {bflo(qa[g][u]), bfhi(qa[g][u])}, b = {bflo(qb[g][u]), bfhi(qb[g][u])};
                    const f32x2 o0 = a * fc[0][u] - b * fs[0][u], o1 = b * fc[1][u] + a * fs[1][u];
                    const unsigned l0 = pk_bf16(f32x2{o0[0], o1[0]}), l1 = pk_bf16(f32x2{o0[1], o1[1]});
                    unsigned* dst = s_q + (16 * g + 4 * it + r) * DH + 6 * c + 2 * u;
                    *reinterpret_cast<u32x2*>(dst) = u32x2{l0, l1};
                    *reinterpret_cast<u32x2*>(dst + HALF) = u32x2{__builtin_amdgcn_alignbit(l0, l0, 16) ^ 0x80000000u, __builtin_amdgcn_alignbit(l1, l1, 16) ^ 0x80000000u};
                }
        }
        HSTAMP(1);
        asm volatile("" :: "v"(x0), "v"(x1));
        if (TP & 32) __builtin_amdgcn_s_setprio(0);
        if (TP & 2) touch_ws();
        if (TP & 4) __builtin_amdgcn_raw_buffer_store_b16((unsigned short)0, ro, lane < 24 ? lane * 2 : OOB, bid * 48, OST);
        unsigned origin = (tsched << 8) + pace.offset;
        if (lane < 8) { s_done[lane] = 0; s_sync[lane] = lane < 3 ? origin : lane < 4 ? 0 : tag; }
        PIN();
        BARRIER();
        const int mh = lane >> 5, mc = lane & 31;
        const unsigned rtag = __builtin_amdgcn_readlane(tag, 0) | __builtin_amdgcn_readlane(tag, 1) >> 16;
        constexpr int RPX = 2;
        constexpr int KS = X / 1000 % 10, SR = X / 10000 % 10 * 2, IB2 = X / 100000 % 10 * 128, EHI = X / 1000000 % 10 * 512, IB = X / 10000000 % 10 * 256, FR = X / 100000000 + 10;
        constexpr int RSH = X / 100 % 10;
        constexpr unsigned RDLY = 200;
        constexpr int UP = X / 10 % 10 ? X / 10 % 10 * 82 : 1 << 24, DN = X % 10 ? X % 10 * 82 : 1 << 24;
        const int rj = ((bid >> 3) + 5 * (bid & 7)) & 31;
        const int rid = rj % (32 / RPX) == 0 ? rj / (32 / RPX) * 8 + (bid & 7) : -1;
        unsigned wround = now32() >> RSH, rround = wround, tick = 0, target = origin;
        bool warmed = false, synced = false;
        int drift = 0;
        const unsigned round0 = rround;
        int i = 0;
        auto kept_now = [&](unsigned now) {
            const u32x2 a = *(vlds_u32x2*)(s_act + 2 * (lane & 7));
            const int late = max((int)(a[0] - origin), (int)((now << 8) - a[1] - origin));
            return origin + (__builtin_amdgcn_readfirstlane(row16_sum(late)) >> 4);
        };
        auto steer = [&](unsigned now) {
            if ((now >> 5) != tick) {
                tick = now >> 5;
                target += drift;
                origin += max(-DN, min(UP + drift, (int)(target - origin)));
                if (SLIP) { const int l2 = (int)(kept_now(now) - origin) - SLIP; if (l2 > 0) origin += l2; }
                if (HOLD && !synced) target = origin - (HOLD - 1) * 82;
                if (lane == 0) *(vlds_u32*)(s_sync + 2) = origin;
                PIN();
            }
        };
        auto finished = [&]() {
            const u32x4 a = *(vlds_u32x4*)s_done, b = *(vlds_u32x4*)(s_done + 4);
            return __builtin_amdgcn_readfirstlane(min(min(min(a[0], a[1]), min(a[2], a[3])), min(min(b[0], b[1]), min(b[2], b[3]))));
        };
        while (i < NI - 2) {
            const unsigned now = now32();
            if ((now >> RSH) != wround) {
                wround = now >> RSH;
                if (rid >= 0 && (now & ((1u << RSH) - 1)) < 48)
                    __builtin_amdgcn_raw_buffer_store_b64(u32x2{kept_now(now), rtag + wround}, rw_, lane == 0 ? rid * 8 : OOB, WS_REP + (wround & 255) * 512, 16);
                PIN();
                continue;
            }
            if ((int)(((now - RDLY) >> RSH) - rround) > 0) {
                rround = (now - RDLY) >> RSH;
                const u32x2 x = __builtin_amdgcn_raw_buffer_load_b64(rw_, c * 8, WS_REP + (rround & 255) * 512, 16);
                const int d = (int)(x[0] - origin);
                const bool ok = x[1] == rtag + rround && d < (32 << 16) && d > -(32 << 16);
                const int n = __builtin_popcountll(__builtin_amdgcn_ballot_w64(ok));
                if (n >= 48) {
                    const unsigned nt = origin + __builtin_amdgcn_readfirstlane((int)(row16_allsum(ok ? (float)d : 0.f) * 4.f / (float)n)) - pace.bias;
                    if (KS && synced && rround - round0 > SR) drift = max(0, min(drift + ((max(-1024, min((int)(nt - target) + 6 * drift, EHI)) - (rround - round0 < FR ? IB : IB2)) >> (3 + KS)), 400));
                    target = nt + 6 * drift;
                    synced = true;
                    const int lost = min((int)(kept_now(now32()) - origin) - pace.bias, (int)(target - origin));
                    if (lost > 0) origin += lost;
                }
                PIN();
                continue;
            }
            steer(now);
            if (!touched && (int)((now << 8) - origin) > (jj * 4 - 8) * (int)pace.step) { touch_kv(); touched = true; continue; }
            if (WN[PF][1] && !warmed && (int)((now << 8) - origin) > 60 * (int)pace.step) { warm_code(WN[PF][1]); warmed = true; continue; }
            if (finished() <= (unsigned)i) { __builtin_amdgcn_s_sleep(2); continue; }
            PIN();
            if (i == 0) {
                f32x4 ml[4], f;
#pragma unroll
                for (int w = 0; w < 4; ++w) ml[w] = *reinterpret_cast<const f32x4*>(s_sc + (mc * 4 + w) * 64 + 16);
                const float M = fmaxf(fmaxf(ml[0][0], ml[1][0]), fmaxf(ml[2][0], ml[3][0]));
                float L = 0.f, zs = 0.f;
#pragma unroll
                for (int w = 0; w < 4; ++w) { f[w] = __builtin_amdgcn_exp2f(ml[w][0] - M); L += f[w] * ml[w][1]; zs += f[w] * ml[w][2]; }
                const float inv = 1.f / L;
                *reinterpret_cast<f32x4*>(s_f + mc * 8) = f * inv;
                *reinterpret_cast<f32x2*>(s_f + mc * 8 + 4) = f32x2{zs * inv, M + __builtin_amdgcn_logf(L)};
                PIN();
                if (lane == 0) *(vlds_u32*)(s_sync + 3) = 1;
                PIN();
            }
            if (i == NI - 3) { warm_code(WN[PF][2]); if (TP & 8) touch_ws(); }
            const f32x2 zl = *reinterpret_cast<const f32x2*>(s_f + (2 * i + mh) * 8 + 4);
            const u32x4 v = merge(s_part + (mh * 4 * NI + i) * DH + mc * 6, *reinterpret_cast<const f32x4*>(s_f + (2 * i + mh) * 8), zl[0], last_word(tag, zl[1], mc));
            PIN();
            *reinterpret_cast<u32x4*>(s_part + (mh * 4 * NI + i) * DH + mc * 4) = v;
            PIN();
            ++i;
        }
        u32x4 v[NI - 1];
#pragma unroll
        for (int i = 0; i < NI - 2; ++i) v[i] = *reinterpret_cast<const u32x4*>(s_part + (mh * 4 * NI + i) * DH + mc * 4);
        f32x4 f = *reinterpret_cast<const f32x4*>(s_f + (2 * (NI - 2) + mh) * 8);
        f32x2 zl = *reinterpret_cast<const f32x2*>(s_f + (2 * (NI - 2) + mh) * 8 + 4);
        PIN();
#pragma unroll
        for (int i = 0; i < NI - 2; ++i) asm volatile("" : "+v"(v[i]));
        asm volatile("" : "+v"(f), "+v"(zl));
        v[NI - 2] = v[0];
        bool have = false, sent = false;
        while (!(have && sent)) {
            const unsigned now = now32();
            if (!have) {
                steer(now);
                if (finished() > NI - 2) {
                    PIN();
                    v[NI - 2] = merge(s_part + (mh * 4 * NI + NI - 2) * DH + mc * 6, f, zl[0], last_word(tag, zl[1], mc));
                    have = true;
                    warm_code(WN[PF][3]);
                    HSTAMP(5);
                }
            }
            if (!sent && (int)((now << 8) - origin) >= pace.pub) {
                HSTAMP(6);
#pragma unroll
                for (int i = 0; i < NI - 2; ++i) publish(v[i], i, piece_off(mh, mc));
                sent = true;
            }
            if (!(have && sent)) __builtin_amdgcn_s_sleep(1);
            PIN();
        }
        publish(v[NI - 2], NI - 2, piece_off(mh, mc));
        HSTAMP(7);
        asm volatile(".Lkv_end_%0_%1_%2_%3:" :: "i"(X), "i"(OPT), "i"(GATE), "i"(TP));
        return;
    }

    unsigned off = pace.start + ((pace.step * (hp * 1024 + m * 4 + rw)) >> 11);
    unsigned origin = tsched << 8, kept = 0;
    const unsigned step = pace.step;
    auto pace_wait = [&]() {
        const unsigned tgt = __builtin_amdgcn_readfirstlane(origin) + off;
        unsigned now = now32() << 8;
        kept = ((int)(now - tgt) > 0 ? now : tgt) - off;
        while ((int)(tgt - now) > 0) { if (SLP) __builtin_amdgcn_s_sleep(SLP); now = now32() << 8; }
        off += step;
    };
    auto report = [&]() {
        if (lane == 0) *(vlds_u32x2*)(s_act + 2 * wave) = u32x2{kept, off};
        origin = *(vlds_u32*)(s_sync + 2);
        PIN();
    };
#define TRV (kept - (tstart << 8))

    u32x2 lsc[4];
    auto load_sc = [&]() {
#pragma unroll
        for (int a = 0; a < 4; ++a) {
            const gptr base = a == 0 ? ksc : a == 1 ? kzp : a == 2 ? vsc : vzp;
            lsc[a] = __builtin_nontemporal_load((const __attribute__((address_space(1))) u32x2*)(base + (unsigned)(((tid >> 4) * SKV + m * 64 + (tid & 15) * 4) * 2)));
        }
        PIN();
    };

    const auto rc = __builtin_amdgcn_make_buffer_rsrc((void*)k_cos, 0, CSB, 0x00020000);
    const auto rs = __builtin_amdgcn_make_buffer_rsrc((void*)k_sin, 0, CSB, 0x00020000);
    u32x2 raw[2][6];
    auto load_cs = [&](int t) {
        const unsigned rb = rb0 + t * 4 * 384;
        raw[t][0] = __builtin_amdgcn_raw_buffer_load_b64(rc, co, rb, 2);
        raw[t][1] = __builtin_amdgcn_raw_buffer_load_b64(rc, co + 128, rb, 2);
        raw[t][2] = __builtin_amdgcn_raw_buffer_load_b64(rc, co + 256, rb, 2);
        raw[t][3] = __builtin_amdgcn_raw_buffer_load_b64(rs, co + 192, rb, 2);
        raw[t][4] = __builtin_amdgcn_raw_buffer_load_b64(rs, so1, rb, 2);
        raw[t][5] = __builtin_amdgcn_raw_buffer_load_b64(rs, co + 64, rb, 2);
        PIN();
    };
    u32x4 tab[4][3];
    auto pack_cs = [&](int t) {
#pragma unroll
        for (int j = 0; j < 3; ++j) {
#pragma unroll
            for (int k = 0; k < 2; ++k) {
                tab[t][j][2 * k] = __builtin_amdgcn_perm(raw[t][3 + j][k], raw[t][j][k], 0x05040100);
                tab[t][j][2 * k + 1] = __builtin_amdgcn_perm(raw[t][3 + j][k], raw[t][j][k], 0x07060302);
            }
            *reinterpret_cast<u32x4*>(s_part + (wave ^ 4) * (NI * DH) + ((t * 3 + j) * 64 + lane) * 4) = tab[t][j];
        }
        PIN();
    };
    const auto rk = __builtin_amdgcn_make_buffer_rsrc((void*)qk, 0, KVB, 0x00020000);
    const auto rv = __builtin_amdgcn_make_buffer_rsrc((void*)qv, 0, KVB, 0x00020000);
    const unsigned voff = r * ROWB + c * 16;
    const unsigned woff = row0 * ROWB + hp * HEADB;
    auto toff = [&](int t) { return (unsigned)(t ^ (2 * hp)) * (4 * ROWB); };
    u32x4 ring[4][3];
    auto ld = [&](int slot, int j, __amdgpu_buffer_rsrc_t rr, unsigned off) {
        ring[slot][j] = __builtin_amdgcn_raw_buffer_load_b128(rr, voff + j * 256, off, 2);
        PIN();
    };
    auto ld_tile = [&](int slot, __amdgpu_buffer_rsrc_t rr, unsigned off) { ld(slot, 0, rr, off); ld(slot, 1, rr, off); ld(slot, 2, rr, off); };
    auto refill = [&](int slot, int j, __amdgpu_buffer_rsrc_t rr, unsigned goff) {
        if (j == 0) pace_wait();
        if (WIN == 1) VMCNT(3);
        else if (WIN == 2) { if (j == 0) VMCNT(3); else VMCNT(2); }
        else if (WIN == 3) { if (j == 2) VMCNT(2); else VMCNT(3); }
        else if (WIN == 4) VMCNT(4);
        else VMCNT(2);
        ld(slot, j, rr, goff);
        if (j == 0) { const unsigned t = (now32() << 8) - 1024 - (off - step); if ((int)(t - kept) > 0) kept = t; }
        if (j == 2) report();
    };
    auto sc_to_lds = [&]() {
        float* dst = s_sc + tid * 16;
#pragma unroll
        for (int u = 0; u < 2; ++u) {
            *reinterpret_cast<f32x4*>(dst + 8 * u) = f32x4{bflo(lsc[0][u]) * SCALE_LOG2, bflo(lsc[1][u]), bflo(lsc[2][u]), bflo(lsc[3][u])};
            *reinterpret_cast<f32x4*>(dst + 8 * u + 4) = f32x4{bfhi(lsc[0][u]) * SCALE_LOG2, bfhi(lsc[1][u]), bfhi(lsc[2][u]), bfhi(lsc[3][u])};
        }
        PIN();
    };

    auto k0 = [&](int t) { pace_wait(); ld_tile(t, rk, woff + toff(t)); };
    if (PRO == 0) {
        load_cs(0); PSTAMP(70); load_sc(); PSTAMP(71);
        pace_wait(); load_cs(1); STAMP(1);
        PSTAMP(72); pack_cs(0); PSTAMP(73); sc_to_lds(); PSTAMP(74);
        k0(0); pack_cs(1); PSTAMP(75);
        PSTAMP(76); k0(1); PSTAMP(77);
    } else if (PRO == 1) {
        load_cs(0);
        pace_wait(); load_cs(1); STAMP(1);
        pack_cs(0);
        pace_wait(); load_sc();
        pack_cs(1);
        k0(0); sc_to_lds();
        k0(1);
    } else if (PRO == 3) {
        load_sc();
        pace_wait(); load_cs(0); STAMP(1);
        sc_to_lds();
        pace_wait(); load_cs(1);
        pack_cs(0);
        k0(0); pack_cs(1);
        k0(1);
    } else if (PRO == 5) {
        load_cs(0);
        pace_wait(); load_cs(1); STAMP(1);
        pace_wait(); load_sc();
        pack_cs(0);
        k0(0); pack_cs(1);
        k0(1); sc_to_lds();
    } else if (PRO == 7) {
        load_sc();
        pace_wait(); load_cs(0); STAMP(1);
        pace_wait(); load_cs(1);
        sc_to_lds();
        k0(0); pack_cs(0);
        k0(1); pack_cs(1);
    } else if (PRO == 10) {
        load_cs(0); load_sc();
        pace_wait(); load_cs(1); STAMP(1);
        pack_cs(0);
        k0(0); sc_to_lds(); pack_cs(1);
        k0(1);
    } else if (PRO == 20) {
        load_cs(0); load_sc();
        pace_wait(); load_cs(1); STAMP(1);
        pack_cs(0); sc_to_lds(); pack_cs(1);
        k0(0);
        k0(1);
    }
    VMCNT(3); PSTAMP(78); k0(2);
    STAMP(2);
    VMCNT(3); k0(3);
    if (lane == 0) *(vlds_u32x2*)(s_act + 2 * wave) = u32x2{kept, off};
    BARRIER();
    origin = *(vlds_u32*)(s_sync + 2);
    STAMP(3);

    u32x4 qp[3];
    auto load_q = [&](int h, int j) { qp[j] = *reinterpret_cast<const u32x4*>(s_q + h * DH + 64 * j + 4 * c); };
    load_q(hp, 0); load_q(hp, 1); load_q(hp, 2);
#pragma unroll
    for (int t = 0; t < 2; ++t)
#pragma unroll
        for (int j = 0; j < 3; ++j) tab[2 + t][j] = *reinterpret_cast<const u32x4*>(s_part + wave * (NI * DH) + ((t * 3 + j) * 64 + lane) * 4);

    for (int i = 0; i < NI; ++i) {
        const bool last = i + 1 == NI;
        const int h = 2 * i + hp;
        const unsigned hnext = (i + 1) * (2 * HEADB) + woff;
        const auto rn = __builtin_amdgcn_make_buffer_rsrc((void*)(last ? qv : qk), 0, KVB, 0x00020000);
        float* scp = s_sc + (h * 4 + rw) * 64;
        f32x4 sc[4];
#pragma unroll
        for (int t = 0; t < 4; ++t) sc[t] = *reinterpret_cast<const f32x4*>(scp + (4 * (t ^ (2 * hp)) + r) * 4);
        float s[4];
#pragma unroll
        for (int t = 0; t < 4; ++t) {
            f32x2 dot, wsum;
#pragma unroll
            for (int j = 0; j < 3; ++j) {
                f32x2 w01, w23, k01, k23;
                dot_cvt(qp[j], tab[t][j], ring[t][j], w01, w23, k01, k23);
                if (j == 0) { wsum = w01 + w23; dot = k01 * w01; } else { wsum += w01; wsum += w23; dot += k01 * w01; }
                dot += k23 * w23;
                asm volatile("" : "+v"(dot), "+v"(wsum));
                PIN();
                if (t == 3) load_q(last ? h : h + 2, j);
                refill(t, j, rn, (last ? woff : hnext) + toff(t));
                if (t == 0 && j == 0) TRACE(i);
            }
            const float a = (dot[0] + dot[1]) - sc[t][1] * (wsum[0] + wsum[1]);
            s[t] = row16_allsum(a) * sc[t][0];
        }
        const float mx = xrow_max(fmaxf(fmaxf(s[0], s[1]), fmaxf(s[2], s[3])));
        float l = 0.f, z = 0.f;
#pragma unroll
        for (int t = 0; t < 4; ++t) {
            const float p = __builtin_amdgcn_exp2f(s[t] - mx);
            l += p;
            s[t] = p * sc[t][2];
            z += s[t] * sc[t][3];
        }
        l = xrow_sum(l);
        z = xrow_sum(z);
        if (c == 0) {
            *reinterpret_cast<f32x4*>(scp + r * 4) = f32x4{s[0], s[1], s[2], s[3]};
            *reinterpret_cast<f32x4*>(scp + 16) = f32x4{mx, l, z, 0.f};
        }
        PIN();
    }
    STAMP(4);

    const float* pwp = s_sc + (hp * 4 + rw) * 64 + r * 4;
    f32x4 pw = *reinterpret_cast<const f32x4*>(pwp);
    unsigned tag, lw, lo;
    f32x4 lf;
    f32x2 lz;
    unsigned lp = (hp * 4 * NI + NI - 1) * DH + (lane & 31) * 6;
    f32x2 acc[3][2];
    auto v_acc = [&](int t, int j) {
        const f32x2 p2 = {pw[t], pw[t]};
        const u32x4 vv = ring[t][j];
        const f32x2 v01 = {(float)(int)vv[0], (float)(int)vv[1]}, v23 = {(float)(int)vv[2], (float)(int)vv[3]};
        if (t == 0) { acc[j][0] = p2 * v01; acc[j][1] = p2 * v23; }
        else { acc[j][0] += p2 * v01; acc[j][1] += p2 * v23; }
        asm volatile("" : "+v"(acc[j][0]), "+v"(acc[j][1]));
        PIN();
    };
    auto v_fold = [&](int i, int j) {
        const float x = swap32_add(acc[j][0][0], acc[j][1][0]);
        const float y = swap32_add(acc[j][0][1], acc[j][1][1]);
        s_part[(wave * NI + i) * DH + 64 * j + 4 * c + r] = swap16_add(x, y);
    };
    auto v_done = [&](int i) {
        PIN();
        if (lane == 0) *(vlds_u32*)(s_done + wave) = i + 1;
        PIN();
    };
#pragma unroll
    for (int t = 0; t < LS; ++t)
#pragma unroll
        for (int j = 0; j < 3; ++j) v_acc(t, j);
    for (int i = 0; i < NI - 1; ++i) {
        const unsigned hnext = (i + 1) * (2 * HEADB) + woff;
#pragma unroll
        for (int k = 0; k < 4; ++k) {
            const int t = (k + LS) & 3;
#pragma unroll
            for (int j = 0; j < 3; ++j) {
                v_acc(t, j);
                if (t == 3 && j == 2) pw = *reinterpret_cast<const f32x4*>(pwp + (i + 1) * 512);
                refill(k, j, rv, hnext + toff(k));
                if (k == 0 && j == 0) TRACE(16 + i);
            }
            if (t == 3) { v_fold(i, 0); v_fold(i, 1); v_fold(i, 2); v_done(i); }
        }
    }
    if (PAD) asm volatile("s_branch .Lkv_tail_%=\n\t.p2align 12\n.Lkv_tail_%=:" ::: "memory");
    STAMP(5);
    TRACE(31);
    while (__builtin_amdgcn_readfirstlane(*(vlds_u32*)(s_sync + 3)) == 0) __builtin_amdgcn_s_sleep(1);
    PIN();
    tag = s_sync[4 + (lane & 3)];
    lf = *reinterpret_cast<const f32x4*>(s_f + (2 * (NI - 1) + hp) * 8);
    lz = *reinterpret_cast<const f32x2*>(s_f + (2 * (NI - 1) + hp) * 8 + 4);
    origin = *(vlds_u32*)(s_sync + 2);
    lw = last_word(tag, lz[1], lane & 31);
    lo = lane < 32 ? piece_off(hp, lane & 31) : OOB;
    asm volatile("" : "+v"(tag), "+v"(lf), "+v"(lz), "+v"(lw), "+v"(lo), "+v"(lp), "+v"(origin));
    PIN();
#pragma unroll
    for (int t = LS; t < 4; ++t)
#pragma unroll
        for (int j = 0; j < 3; ++j) { v_acc(t, j); if (t == 3) { v_fold(NI - 1, j); PIN(); } }
    v_done(NI - 1);
    STAMP(6);
    {
        const u32x4 dn = *(vlds_u32x4*)(s_done + 4 * hp);
        PIN();
        const u32x4 v = merge(s_part + lp, lf, lz[0], lw);
        if (__builtin_amdgcn_readfirstlane(dn[0] & dn[1] & dn[2] & dn[3]) == NI) publish(v, NI - 1, lo);
        PIN();
    }
    STAMP(7);
    {
        unsigned inbox = WS_PART + (bid >> 4) * GSTR + (bid & 15) * UNITB, obase = bid * 48;
        unsigned xo = tid * 16, yo = tid * 16 + 8192, sel = lane & 1 ? 0x01000504u : 0x05040100u;
        unsigned bo = lane < 16 ? wave * 1024 + (lane & 7) * 128 + (lane >> 3) * 8192 : OOB;
        typedef __attribute__((address_space(3))) float lds_f;
        lds_f* wo = (lds_f*)s_red + wave * 28 + (r < 3 ? c * 6 + r * 2 : 24);
        lds_f* fo = (lds_f*)s_red + (lane & 7) * 28 + 24;
        lds_f* go = (lds_f*)s_red + (lane & 7) * 28 + min(lane >> 3, 5) * 4;
        vlds_u32* dw = (vlds_u32*)(s_done + wave);
        vlds_u32* dr = (vlds_u32*)(s_done + (lane & 7));
        unsigned oo = (lane & 7) == 0 && lane < 48 ? lane : OOB, eo = bid == 0 && lane < 4 ? lane * 2 : OOB;
        asm volatile("" : "+s"(inbox), "+s"(obase), "+v"(xo), "+v"(yo), "+v"(sel), "+v"(bo), "+v"(wo), "+v"(fo), "+v"(go), "+v"(oo), "+v"(eo), "+v"(dw), "+v"(dr));
        const unsigned o = __builtin_amdgcn_readfirstlane(origin) - (15 - (bid >> 4)) * RST;
        while ((int)((now32() << 8) - o) < pace.rd) __builtin_amdgcn_s_sleep(1);
        PIN();
        STAMP(8);
        u32x4 x, y;
        do {
            x = __builtin_amdgcn_raw_buffer_load_b128(rw_, xo, inbox, 16);
            y = __builtin_amdgcn_raw_buffer_load_b128(rw_, yo, inbox, 16);
            const unsigned age = now32() - tstart;
            PIN();
            if (age > SPIN_LIMIT) break;
        } while (__builtin_amdgcn_ballot_w64(((x[3] ^ tag) | (y[3] ^ tag)) > 0xffffu) != 0);
        if (DRY) { __builtin_amdgcn_raw_buffer_store_b32(0u, rw_, bo, inbox, 16); PIN(); }
        STAMP(9);
        const float lx = __uint_as_float(__builtin_amdgcn_perm(__builtin_amdgcn_mov_dpp(x[3], 0xb1, 0xf, 0xf, true), x[3], sel));
        const float ly = __uint_as_float(__builtin_amdgcn_perm(__builtin_amdgcn_mov_dpp(y[3], 0xb1, 0xf, 0xf, true), y[3], sel));
        float M = fmaxf(lx, ly);
        M = fmaxf(M, dpp<0x128>(M));
        M = fmaxf(M, dpp<0x124>(M));
        M = xrow_max(M);
        const float w0 = __builtin_amdgcn_exp2f(lx - M), w1 = __builtin_amdgcn_exp2f(ly - M);
        float a[8];
#pragma unroll
        for (int k = 0; k < 3; ++k) {
            a[2 * k] = w0 * bflo(x[k]) + w1 * bflo(y[k]);
            a[2 * k + 1] = w0 * bfhi(x[k]) + w1 * bfhi(y[k]);
        }
        a[6] = a[7] = w0 + w1;
        float b[4], d[2];
#pragma unroll
        for (int k = 0; k < 4; ++k) b[k] = swap32_add(a[k], a[4 + k]);
#pragma unroll
        for (int k = 0; k < 2; ++k) d[k] = swap16_add(b[k], b[2 + k]);
#pragma unroll
        for (int k = 0; k < 2; ++k) { d[k] += dpp<0x128>(d[k]); d[k] += dpp<0x124>(d[k]); }
        if (c < 4) {
            *(__attribute__((address_space(3))) f32x2*)wo = f32x2{d[0], r < 3 ? d[1] : M};
            PIN();
            *dw = 3 * NI;
        }
        PIN();
        STAMP(10);
        const unsigned fin = *dr;
        PIN();
        f32x2 sm = *(__attribute__((address_space(3))) f32x2*)fo;
        f32x4 ov = *(__attribute__((address_space(3))) f32x4*)go;
        asm volatile("" : "+v"(sm), "+v"(ov));
        if (__builtin_amdgcn_ballot_w64(fin != 3 * NI) == 0) {
            float MM = fmaxf(sm[1], dpp<0xb1>(sm[1]));
            MM = fmaxf(MM, dpp<0x4e>(MM));
            MM = fmaxf(MM, dpp<0x141>(MM));
            const float ff = __builtin_amdgcn_exp2f(sm[1] - MM);
            float e[5] = {ff * ov[0], ff * ov[1], ff * ov[2], ff * ov[3], ff * sm[0]};
#pragma unroll
            for (int k = 0; k < 5; ++k) e[k] += dpp<0xb1>(e[k]);
#pragma unroll
            for (int k = 0; k < 5; ++k) e[k] += dpp<0x4e>(e[k]);
#pragma unroll
            for (int k = 0; k < 5; ++k) e[k] += dpp<0x141>(e[k]);
            const float inv = __builtin_amdgcn_rcpf(e[4]);
            __builtin_amdgcn_raw_buffer_store_b64(u32x2{pk_bf16(f32x2{e[0] * inv, e[1] * inv}), pk_bf16(f32x2{e[2] * inv, e[3] * inv})}, ro, oo, obase, OST);
            if (bid == 0) __builtin_amdgcn_raw_buffer_store_b16((unsigned short)(tag >> 16), rw_, eo, WS_EPOCH, 16);
        }
    }
    if (wave == PWAVE) {
        if (PIDX == 90) probe = __builtin_amdgcn_readfirstlane(s_sync[0]);
        if (PIDX == 91 || PIDX == 92) probe = tstart + ((int)(__builtin_amdgcn_readfirstlane(s_sync[PIDX - 90]) - (tstart << 8)) >> 8);
        if (PIDX == 100) { asm volatile("s_waitcnt vmcnt(0)" ::: "memory"); probe = now32(); }
        const unsigned ptag = __builtin_amdgcn_readlane(tag, 0) | __builtin_amdgcn_readlane(tag, 1) >> 16;
        __builtin_amdgcn_raw_buffer_store_b128(u32x4{tstart, probe, ptag, 0}, rw_, lane == 0 ? bid * 16 : OOB, WS_PROBE, 16);
        asm volatile("s_nop 1" ::: "memory");
        if (bid == 100) {
            while ((int)(now32() - tstart) < 12500) __builtin_amdgcn_s_sleep(1);
            u32x4 p[4];
            for (;;) {
                bool ok = true;
                for (int k = 0; k < 4; ++k) { p[k] = __builtin_amdgcn_raw_buffer_load_b128(rw_, lane * 16 + k * 1024, WS_PROBE, 16); ok = ok && p[k][2] == ptag; }
                if (__builtin_amdgcn_ballot_w64(!ok) == 0 || now32() - tstart > SPIN_LIMIT) break;
                PIN();
            }
            float mn = 1e9f, mx = -1e9f, av = 0.f;
            for (int k = 0; k < 4; ++k) { mn = fminf(mn, (float)(int)(p[k][0] - tstart)); const float v = (float)(int)(p[k][1] - tstart); mx = fmaxf(mx, v); av += v; }
            mn = -xrow_max(row16_allmax(-mn)); mx = xrow_max(row16_allmax(mx)); av = xrow_sum(row16_allsum(av)) / 256.f;
            float val = (PMAX & 1 ? mx : av) - (PMAX & 2 ? (float)((int)(s_sync[2] - (tstart << 8)) >> 8) : mn) - PSUB;
            if (PIDX == 99) val = 0.f;
            val = fminf(fmaxf(val, 0.f), 3000.f);
            const unsigned endt = tstart + __builtin_amdgcn_readfirstlane((int)(mn + 14000.f + 5.f * val));
            while ((int)(endt - now32()) > 0) __builtin_amdgcn_s_sleep(1);
        }
    }
}

#define VAR_XS(p) ((p) / 1000 % 2)
template <int X, int PRO, int GATE, int STAG>
static void launch_cfg(void* const* d_in, void* d_out, void* d_ws, hipStream_t stream, float t_us, float start_us, float off_us, float debt_us, float adj_pct, float pub_us) {
    Pace pace;
    pace.step = (unsigned)(t_us * 25600.f / 128.f);
    pace.start = (int)(start_us * 25600.f);
    pace.offset = (int)(off_us * 25600.f);
    pace.bias = (int)(debt_us * 25600.f);
    pace.rd = (int)((start_us + t_us * ((129.f + VAR_XS(PRO)) / 128.f) + adj_pct) * 25600.f);
    pace.pub = (int)((start_us + t_us * ((129.f + VAR_XS(PRO)) / 128.f) + pub_us) * 25600.f);
    kv_sweep_kernel<X, PRO, GATE, STAG, 1><<<NWG, 576, 0, stream>>>(d_in[0], d_in[1], d_in[2], d_in[3], d_in[4], d_in[5], d_in[6], d_in[7],
        d_in[8], d_in[9], d_in[10], d_ws, d_out, pace);
}

#define CFG 335022966, 1023, 2121, 241
#define PRM 102.5f, 0.5f, 0.0f, 0.05f, 1.6f, 0.6f
extern "C" void kernel_launch(void* const* d_in, const int* in_sizes, int n_in,
                              void* d_out, int out_size, void* d_ws, size_t ws_size,
                              hipStream_t stream) {
    launch_cfg<CFG>(d_in, d_out, d_ws, stream, PRM);
}
```

```cpp
#include <hip/hip_runtime.h>
#include <hip/hip_bf16.h>
#include <cstdint>
#include <type_traits>

using bf16 = __hip_bfloat16;
using u32x4 = __attribute__((ext_vector_type(4))) unsigned;
using u32x3 = __attribute__((ext_vector_type(3))) unsigned;
using u32x2 = __attribute__((ext_vector_type(2))) unsigned;
using u64x8 = __attribute__((ext_vector_type(8))) unsigned long long;
using f32x2 = __attribute__((ext_vector_type(2))) float;
using f32x4 = __attribute__((ext_vector_type(4))) float;
using bf16x2 = __attribute__((ext_vector_type(2))) __bf16;

constexpr int DH = 192;
constexpr int HALF = 96;
constexpr int NH = 32;
constexpr int SKV = 16384;
constexpr int NWG = 256;
constexpr int NI = 16;
constexpr unsigned ROWB = DH * 4;
constexpr unsigned HEADB = SKV * ROWB;
constexpr unsigned KVB = NH * HEADB;
constexpr unsigned CSB = SKV * DH * 2;
constexpr unsigned OOB = 0x80000000u;
constexpr float SCALE_LOG2 = 1.4426950408889634f * 0.07216878364870322f;
constexpr unsigned SPIN_LIMIT = 20000000u;

constexpr unsigned UNITB = NWG * 64;
constexpr unsigned GSTR = 16 * UNITB + 256;
constexpr unsigned WS_PART = 0;
constexpr unsigned WS_EPOCH = NWG * (UNITB + 1024);
constexpr unsigned WS_REP = WS_EPOCH + 256;
constexpr unsigned WS_PROBE = WS_REP + 256 * 512;
constexpr unsigned WS_BYTES = WS_PROBE + 4096;

__device__ __forceinline__ float bflo(unsigned u) { return __uint_as_float(u << 16); }
__device__ __forceinline__ float bfhi(unsigned u) { return __uint_as_float(u & 0xffff0000u); }
__device__ __forceinline__ float rbf(float x) { return __bfloat162float(__float2bfloat16(x)); }
__device__ __forceinline__ unsigned pk_bf16(f32x2 a) { return __builtin_bit_cast(unsigned, __builtin_convertvector(a, bf16x2)); }
__device__ __forceinline__ void dot_cvt(const u32x4 q, const u32x4 t, const u32x4 ki, f32x2& w01, f32x2& w23, f32x2& k01, f32x2& k23) {
    float w0, w1, w2, w3;
    float k0 = __uint_as_float(ki[0]), k1 = __uint_as_float(ki[1]), k2 = __uint_as_float(ki[2]), k3 = __uint_as_float(ki[3]);
    asm("v_dot2_f32_bf16 %0, %8, %12, 0\n\tv_dot2_f32_bf16 %1, %9, %13, 0\n\tv_dot2_f32_bf16 %2, %10, %14, 0\n\tv_dot2_f32_bf16 %3, %11, %15, 0\n\t"
        "v_cvt_f32_i32 %4, %4\n\tv_cvt_f32_i32 %5, %5\n\tv_cvt_f32_i32 %6, %6\n\tv_cvt_f32_i32 %7, %7"
        : "=&v"(w0), "=&v"(w1), "=&v"(w2), "=&v"(w3), "+v"(k0), "+v"(k1), "+v"(k2), "+v"(k3)
        : "v"(q[0]), "v"(q[1]), "v"(q[2]), "v"(q[3]), "v"(t[0]), "v"(t[1]), "v"(t[2]), "v"(t[3]));
    w01 = f32x2{w0, w1}; w23 = f32x2{w2, w3}; k01 = f32x2{k0, k1}; k23 = f32x2{k2, k3};
}

template <int CTRL> __device__ __forceinline__ float dpp(float x) {
    return __builtin_bit_cast(float, __builtin_amdgcn_mov_dpp(__builtin_bit_cast(int, x), CTRL, 0xf, 0xf, true));
}
__device__ __forceinline__ float row16_allsum(float x) {
    x += dpp<0x128>(x);
    x += dpp<0x124>(x);
    x += dpp<0x122>(x);
    x += dpp<0x121>(x);
    return x;
}
__device__ __forceinline__ float row16_allmax(float x) {
    x = fmaxf(x, dpp<0x128>(x));
    x = fmaxf(x, dpp<0x124>(x));
    x = fmaxf(x, dpp<0x122>(x));
    x = fmaxf(x, dpp<0x121>(x));
    return x;
}
__device__ __forceinline__ float xrow_max(float x) {
    auto s = __builtin_amdgcn_permlane16_swap(__float_as_uint(x), __float_as_uint(x), false, false);
    x = fmaxf(__uint_as_float(s[0]), __uint_as_float(s[1]));
    auto t = __builtin_amdgcn_permlane32_swap(__float_as_uint(x), __float_as_uint(x), false, false);
    return fmaxf(__uint_as_float(t[0]), __uint_as_float(t[1]));
}
__device__ __forceinline__ float xrow_sum(float x) {
    auto s = __builtin_amdgcn_permlane16_swap(__float_as_uint(x), __float_as_uint(x), false, false);
    x = __uint_as_float(s[0]) + __uint_as_float(s[1]);
    auto t = __builtin_amdgcn_permlane32_swap(__float_as_uint(x), __float_as_uint(x), false, false);
    return __uint_as_float(t[0]) + __uint_as_float(t[1]);
}
__device__ __forceinline__ float swap32_add(float a, float b) {
    auto t = __builtin_amdgcn_permlane32_swap(__float_as_uint(a), __float_as_uint(b), false, false);
    return __uint_as_float(t[0]) + __uint_as_float(t[1]);
}
__device__ __forceinline__ float swap16_add(float a, float b) {
    auto t = __builtin_amdgcn_permlane16_swap(__float_as_uint(a), __float_as_uint(b), false, false);
    return __uint_as_float(t[0]) + __uint_as_float(t[1]);
}
__device__ __forceinline__ int row16_sum(int x) {
    x += __builtin_amdgcn_mov_dpp(x, 0x128, 0xf, 0xf, true);
    x += __builtin_amdgcn_mov_dpp(x, 0x124, 0xf, 0xf, true);
    x += __builtin_amdgcn_mov_dpp(x, 0x122, 0xf, 0xf, true);
    x += __builtin_amdgcn_mov_dpp(x, 0x121, 0xf, 0xf, true);
    return x;
}
__device__ __forceinline__ unsigned now32() { return (unsigned)__builtin_amdgcn_s_memrealtime(); }

#define NSTAMP 12
#define STAMP(i)
#define TRACE(i)
#define HSTAMP(i)
#define PSTAMP(i)
#define PIN() do { asm volatile("" ::: "memory"); __builtin_amdgcn_sched_barrier(0); } while (0)
#define BARRIER() do { asm volatile("s_waitcnt lgkmcnt(0)" ::: "memory"); __builtin_amdgcn_s_barrier(); PIN(); } while (0)
#define VMCNT(n) asm volatile("s_waitcnt vmcnt(%0)" :: "i"(n) : "memory")

typedef volatile __attribute__((address_space(3))) unsigned vlds_u32;
typedef volatile __attribute__((address_space(3))) u32x2 vlds_u32x2;
typedef volatile __attribute__((address_space(3))) u32x4 vlds_u32x4;

struct Pace {
    unsigned step;
    int start;
    int offset;
    int bias;
    int rd;
    int pub;
};

template <int X, int OPT, int GATE, int TP, int SLP>
__global__ __launch_bounds__(576) __attribute__((aligned(4096))) void kv_sweep_kernel(
    const void*, const void*, const void*, const void*, const void*, const void*, const void*, const void*,
    const void*, const void*, const void*, void*, void*, Pace)
{
    __shared__ __attribute__((aligned(16))) unsigned s_q[NH * DH];
    __shared__ __attribute__((aligned(16))) float s_sc[NH * 64 * 4];
    __shared__ __attribute__((aligned(16))) float s_part[8 * NI * DH];
    __shared__ __attribute__((aligned(16))) float s_red[8 * 28];
    __shared__ __attribute__((aligned(16))) float s_f[NH * 8];
    __shared__ __attribute__((aligned(16))) unsigned s_done[8];
    __shared__ __attribute__((aligned(16))) unsigned s_sync[8];
    __shared__ __attribute__((aligned(16))) unsigned s_act[16];

    constexpr int SLIP = GATE / 10 % 10 * 2560, HOLD = GATE % 10;
    constexpr int DRY = OPT % 10 & 1, LS = 2, PAD = OPT % 10 / 2 & 1, PF = OPT / 10 % 10, RST = GATE / 1000 % 10 * 256, LDA = GATE / 10000 % 10 < 4 ? 16 + GATE / 10000 % 10 : GATE / 10000 % 10 - 4, STA = 16, WIN = GATE / 100000 % 10, OST = OPT / 100 % 10 == 0 ? 0 : OPT / 100 % 10 == 1 ? 16 : OPT / 100 % 10 == 2 ? 17 : 18, PRO = OPT / 1000;
    unsigned long long pc0, t0;
    u64x8 ka, kb;
    asm volatile("s_load_dwordx16 %3, %4, 0x40\n\ts_load_dwordx16 %2, %4, 0x0\n\ts_memrealtime %1\n\ts_getpc_b64 %0\n.Lkv_beg_%5_%6_%7_%8:"
                 : "=&s"(pc0), "=&s"(t0), "=&s"(ka), "=&s"(kb) : "s"(__builtin_amdgcn_kernarg_segment_ptr()), "i"(X), "i"(OPT), "i"(GATE), "i"(TP));
    __builtin_amdgcn_sched_barrier(0);
    const int tid = threadIdx.x;
    const int lane = tid & 63;
    int wave = __builtin_amdgcn_readfirstlane(tid >> 6);
    const int r = lane >> 4;
    const int c = lane & 15;
    const int bid = blockIdx.x;
    const int m = (bid & 7) * 32 + (bid >> 3);
    const int rw = wave & 3, hp = wave >> 2;
    const int row0 = m * 64 + rw * 16;
    unsigned co = r * 384 + c * 8, so1 = r * 384 + (c < 8 ? c * 8 + 320 : c * 8 - 64), rb0 = (row0 + 8 * hp) * 384u;
    asm volatile("s_waitcnt lgkmcnt(0)" : "+s"(t0), "+s"(ka), "+s"(kb), "+v"(co), "+v"(so1), "+s"(rb0), "+s"(wave));
    typedef const __attribute__((address_space(1))) char* gptr;
    void* const q = (void*)ka[0]; void* const qk = (void*)ka[1]; void* const qv = (void*)ka[4]; void* const q_cos = (void*)ka[7];
    const gptr ksc = (gptr)ka[2], kzp = (gptr)ka[3], vsc = (gptr)ka[5], vzp = (gptr)ka[6];
    void* const q_sin = (void*)kb[0]; void* const k_cos = (void*)kb[1]; void* const k_sin = (void*)kb[2]; void* const out = (void*)kb[4];
    unsigned char* const ws = (unsigned char*)(__attribute__((address_space(1))) unsigned char*)kb[3];
    const Pace pace = {(unsigned)kb[5], (int)(kb[5] >> 32), (int)kb[6], (int)(kb[6] >> 32), (int)kb[7], (int)(kb[7] >> 32)};
    const unsigned tstart = (unsigned)t0;
    const unsigned tsched = TP & 16 ? now32() : tstart;
    PSTAMP(69);
    const auto rw_ = __builtin_amdgcn_make_buffer_rsrc((void*)ws, 0, WS_BYTES, 0x00020000);
    const auto ro = __builtin_amdgcn_make_buffer_rsrc((void*)out, 0, NH * DH * 2, 0x00020000);

    auto merge = [&](const float* p, f32x4 f, float zs, unsigned w3) {
        f32x2 u[4][3], a[3];
#pragma unroll
        for (int w = 0; w < 4; ++w)
#pragma unroll
            for (int k = 0; k < 3; ++k) u[w][k] = *reinterpret_cast<const f32x2*>(p + w * NI * DH + 2 * k);
        asm volatile("" : "+v"(u[0][0]), "+v"(u[0][1]), "+v"(u[0][2]), "+v"(u[1][0]), "+v"(u[1][1]), "+v"(u[1][2]),
                          "+v"(u[2][0]), "+v"(u[2][1]), "+v"(u[2][2]), "+v"(u[3][0]), "+v"(u[3][1]), "+v"(u[3][2]));
#pragma unroll
        for (int k = 0; k < 3; ++k) a[k] = (f[0] * u[0][k] - zs + f[1] * u[1][k]) + (f[2] * u[2][k] + f[3] * u[3][k]);
        return u32x4{pk_bf16(a[0]), pk_bf16(a[1]), pk_bf16(a[2]), w3};
    };
    auto last_word = [&](unsigned tag, float ls, int c6) { return tag | ((__float_as_uint(ls) >> (16 * (c6 & 1))) & 0xffffu); };
    auto piece_off = [&](int hp, int c6) { return (hp * 8 + (c6 >> 2)) * UNITB + (c6 & 3) * 16; };
    auto publish = [&](u32x4 v, int i, unsigned vo) {
        __builtin_amdgcn_raw_buffer_store_b128(v, rw_, vo, WS_PART + i * GSTR + m * 64, STA);
        asm volatile("s_nop 1" :: "v"(v) : "memory");
    };

    if (wave == 8) {
        if (TP & 32) __builtin_amdgcn_s_setprio(3);
        HSTAMP(11);
        unsigned clen;
        asm volatile("s_mov_b32 %0, .Lkv_end_%1_%2_%3_%4-.Lkv_beg_%1_%2_%3_%4" : "=s"(clen) : "i"(X), "i"(OPT), "i"(GATE), "i"(TP));
        const auto rcode = __builtin_amdgcn_make_buffer_rsrc((void*)(pc0 & ~127ull), 0, clen, 0x00020000);
        const int jj = bid >> 3;
        auto warm_ld = [&](int n) {
            return __builtin_amdgcn_raw_buffer_load_b32(rcode, lane < n ? (jj * 9 + lane) * 64 : OOB, 0, 0);
        };
        auto warm_code = [&](int n) { if (n) { const unsigned x = warm_ld(n); asm volatile("" :: "v"(x)); } };
        constexpr int WN[10][4] = {{0, 0, 0, 0}, {64, 0, 0, 0}, {9, 0, 0, 0}, {64, 9, 9, 0}, {9, 9, 9, 0}, {18, 0, 0, 0}, {18, 18, 18, 0}, {9, 9, 0, 9}, {36, 0, 0, 0}, {9, 0, 9, 0}};
        auto touch_ld = [&](bool on) {
            return __builtin_amdgcn_raw_buffer_load_b32(__builtin_amdgcn_make_buffer_rsrc((void*)(jj < NI ? qk : qv), 0, KVB, 0x00020000),
                on && lane < 4 ? (lane >> 1) * HEADB + (lane & 1) * 2047 * ROWB : OOB, (jj & 15) * 2 * HEADB + (bid & 7) * 2048 * ROWB, 2);
        };
        auto touch_kv = [&]() { const unsigned x = touch_ld(true); asm volatile("" :: "v"(x)); };
        auto touch_ws = [&]() {
            const unsigned x = __builtin_amdgcn_raw_buffer_load_b32(rw_, c * GSTR, WS_PART + 16 * UNITB, 0);
            asm volatile("" :: "v"(x));
        };
        const unsigned x0 = warm_ld(WN[PF][0]);
        PIN();
        if (TP >> 6) while (now32() - tsched < 50 + (TP >> 6) * 50u) __builtin_amdgcn_s_sleep(1);
        u32x3 lc[2], ls[2];
        u32x4 qraw[12];
        {
            const auto rq = __builtin_amdgcn_make_buffer_rsrc((void*)q, 0, NH * DH * 2, 0x00020000);
            const auto rqc = __builtin_amdgcn_make_buffer_rsrc((void*)q_cos, 0, DH * 2, 0x00020000);
            const auto rqs = __builtin_amdgcn_make_buffer_rsrc((void*)q_sin, 0, DH * 2, 0x00020000);
#pragma unroll
            for (int k = 0; k < 12; ++k) qraw[k] = __builtin_amdgcn_raw_buffer_load_b128(rq, lane * 16 + (k & 3) * 1024, (k >> 2) * 4096, 0);
#pragma unroll
            for (int k = 0; k < 2; ++k) {
                lc[k] = __builtin_amdgcn_raw_buffer_load_b96(rqc, c * 12 + k * 192, 0, 0);
                ls[k] = __builtin_amdgcn_raw_buffer_load_b96(rqs, c * 12 + k * 192, 0, 0);
            }
            PIN();
        }
        const unsigned tag = ((unsigned)__builtin_amdgcn_raw_buffer_load_b16(rw_, (lane & 3) * 2, WS_EPOCH, 16) + 1u) << 16;
        bool touched = !(TP & 1) || jj < 3;
        const unsigned x1 = TP & 1 ? touch_ld(jj < 3) : 0;
        PIN();
        HSTAMP(2);
        unsigned* const s_raw = reinterpret_cast<unsigned*>(s_part) + NI * DH / 2;
#pragma unroll
        for (int k = 0; k < 12; ++k) *reinterpret_cast<u32x4*>(s_raw + k / 6 * (NI * DH) + k % 6 * 256 + lane * 4) = qraw[k];
        f32x2 fc[2][3], fs[2][3];
#pragma unroll
        for (int k = 0; k < 2; ++k)
#pragma unroll
            for (int u = 0; u < 3; ++u) { fc[k][u] = f32x2{bflo(lc[k][u]), bfhi(lc[k][u])}; fs[k][u] = f32x2{bflo(ls[k][u]), bfhi(ls[k][u])}; }
        PIN();
        HSTAMP(3);
#pragma nounroll
        for (int it = 0; it < 4; ++it) {
            unsigned qa[2][3], qb[2][3];
#pragma unroll
            for (int g = 0; g < 2; ++g)
#pragma unroll
                for (int u = 0; u < 3; ++u) {
                    const unsigned* src = s_raw + g * (NI * DH) + (it * 4 + r) * HALF + 3 * c;
                    qa[g][u] = src[u]; qb[g][u] = src[HALF / 2 + u];
                }
#pragma unroll
            for (int g = 0; g < 2; ++g)
#pragma unroll
                for (int u = 0; u < 3; ++u) {
                    const f32x2 a = {bflo(qa[g][u]), bfhi(qa[g][u])}, b = {bflo(qb[g][u]), bfhi(qb[g][u])};
                    const f32x2 o0 = a * fc[0][u] - b * fs[0][u], o1 = b * fc[1][u] + a * fs[1][u];
                    const unsigned l0 = pk_bf16(f32x2{o0[0], o1[0]}), l1 = pk_bf16(f32x2{o0[1], o1[1]});
                    unsigned* dst = s_q + (16 * g + 4 * it + r) * DH + 6 * c + 2 * u;
                    *reinterpret_cast<u32x2*>(dst) = u32x2{l0, l1};
                    *reinterpret_cast<u32x2*>(dst + HALF) = u32x2{__builtin_amdgcn_alignbit(l0, l0, 16) ^ 0x80000000u, __builtin_amdgcn_alignbit(l1, l1, 16) ^ 0x80000000u};
                }
        }
        HSTAMP(1);
        asm volatile("" :: "v"(x0), "v"(x1));
        if (TP & 32) __builtin_amdgcn_s_setprio(0);
        if (TP & 2) touch_ws();
        if (TP & 4) __builtin_amdgcn_raw_buffer_store_b16((unsigned short)0, ro, lane < 24 ? lane * 2 : OOB, bid * 48, OST);
        unsigned origin = (tsched << 8) + pace.offset;
        if (lane < 8) { s_done[lane] = 0; s_sync[lane] = lane < 3 ? origin : lane < 4 ? 0 : tag; }
        PIN();
        BARRIER();
        const int mh = lane >> 5, mc = lane & 31;
        const unsigned rtag = __builtin_amdgcn_readlane(tag, 0) | __builtin_amdgcn_readlane(tag, 1) >> 16;
        constexpr int RPX = 2;
        constexpr int KS = X / 1000 % 10, SR = X / 10000 % 10 * 2, IB2 = X / 100000 % 10 * 128, EHI = X / 1000000 % 10 * 512, IB = X / 10000000 % 10 * 256, FR = X / 100000000 + 10;
        constexpr int RSH = X / 100 % 10;
        constexpr unsigned RDLY = 200;
        constexpr int UP = X / 10 % 10 ? X / 10 % 10 * 82 : 1 << 24, DN = X % 10 ? X % 10 * 82 : 1 << 24;
        const int rj = ((bid >> 3) + 5 * (bid & 7)) & 31;
        const int rid = rj % (32 / RPX) == 0 ? rj / (32 / RPX) * 8 + (bid & 7) : -1;
        unsigned wround = now32() >> RSH, rround = wround, tick = 0, target = origin;
        bool warmed = false, synced = false;
        int drift = 0;
        const unsigned round0 = rround;
        int i = 0;
        auto kept_now = [&](unsigned now) {
            const u32x2 a = *(vlds_u32x2*)(s_act + 2 * (lane & 7));
            const int late = max((int)(a[0] - origin), (int)((now << 8) - a[1] - origin));
            return origin + (__builtin_amdgcn_readfirstlane(row16_sum(late)) >> 4);
        };
        auto steer = [&](unsigned now) {
            if ((now >> 5) != tick) {
                tick = now >> 5;
                target += drift;
                origin += max(-DN, min(UP + drift, (int)(target - origin)));
                if (SLIP) { const int l2 = (int)(kept_now(now) - origin) - SLIP; if (l2 > 0) origin += l2; }
                if (HOLD && !synced) target = origin - (HOLD - 1) * 82;
                if (lane == 0) *(vlds_u32*)(s_sync + 2) = origin;
                PIN();
            }
        };
        auto finished = [&]() {
            const u32x4 a = *(vlds_u32x4*)s_done, b = *(vlds_u32x4*)(s_done + 4);
            return __builtin_amdgcn_readfirstlane(min(min(min(a[0], a[1]), min(a[2], a[3])), min(min(b[0], b[1]), min(b[2], b[3]))));
        };
        while (i < NI - 2) {
            const unsigned now = now32();
            if ((now >> RSH) != wround) {
                wround = now >> RSH;
                if (rid >= 0 && (now & ((1u << RSH) - 1)) < 48)
                    __builtin_amdgcn_raw_buffer_store_b64(u32x2{kept_now(now), rtag + wround}, rw_, lane == 0 ? rid * 8 : OOB, WS_REP + (wround & 255) * 512, 16);
                PIN();
                continue;
            }
            if ((int)(((now - RDLY) >> RSH) - rround) > 0) {
                rround = (now - RDLY) >> RSH;
                const u32x2 x = __builtin_amdgcn_raw_buffer_load_b64(rw_, c * 8, WS_REP + (rround & 255) * 512, 16);
                const int d = (int)(x[0] - origin);
                const bool ok = x[1] == rtag + rround && d < (32 << 16) && d > -(32 << 16);
                const int n = __builtin_popcountll(__builtin_amdgcn_ballot_w64(ok));
                if (n >= 48) {
                    const unsigned nt = origin + __builtin_amdgcn_readfirstlane((int)(row16_allsum(ok ? (float)d : 0.f) * 4.f / (float)n)) - pace.bias;
                    if (KS && synced && rround - round0 > SR) drift = max(0, min(drift + ((max(-1024, min((int)(nt - target) + 6 * drift, EHI)) - (rround - round0 < FR ? IB : IB2)) >> (3 + KS)), 400));
                    target = nt + 6 * drift;
                    synced = true;
                    const int lost = min((int)(kept_now(now32()) - origin) - pace.bias, (int)(target - origin));
                    if (lost > 0) origin += lost;
                }
                PIN();
                continue;
            }
            steer(now);
            if (!touched && (int)((now << 8) - origin) > (jj * 4 - 8) * (int)pace.step) { touch_kv(); touched = true; continue; }
            if (WN[PF][1] && !warmed && (int)((now << 8) - origin) > 60 * (int)pace.step) { warm_code(WN[PF][1]); warmed = true; continue; }
            if (finished() <= (unsigned)i) { __builtin_amdgcn_s_sleep(2); continue; }
            PIN();
            if (i == 0) {
                f32x4 ml[4], f;
#pragma unroll
                for (int w = 0; w < 4; ++w) ml[w] = *reinterpret_cast<const f32x4*>(s_sc + (mc * 4 + w) * 64 + 16);
                const float M = fmaxf(fmaxf(ml[0][0], ml[1][0]), fmaxf(ml[2][0], ml[3][0]));
                float L = 0.f, zs = 0.f;
#pragma unroll
                for (int w = 0; w < 4; ++w) { f[w] = __builtin_amdgcn_exp2f(ml[w][0] - M); L += f[w] * ml[w][1]; zs += f[w] * ml[w][2]; }
                const float inv = 1.f / L;
                *reinterpret_cast<f32x4*>(s_f + mc * 8) = f * inv;
                *reinterpret_cast<f32x2*>(s_f + mc * 8 + 4) = f32x2{zs * inv, M + __builtin_amdgcn_logf(L)};
                PIN();
                if (lane == 0) *(vlds_u32*)(s_sync + 3) = 1;
                PIN();
            }
            if (i == NI - 3) { warm_code(WN[PF][2]); if (TP & 8) touch_ws(); }
            const f32x2 zl = *reinterpret_cast<const f32x2*>(s_f + (2 * i + mh) * 8 + 4);
            const u32x4 v = merge(s_part + (mh * 4 * NI + i) * DH + mc * 6, *reinterpret_cast<const f32x4*>(s_f + (2 * i + mh) * 8), zl[0], last_word(tag, zl[1], mc));
            PIN();
            *reinterpret_cast<u32x4*>(s_part + (mh * 4 * NI + i) * DH + mc * 4) = v;
            PIN();
            ++i;
        }
        u32x4 v[NI - 1];
#pragma unroll
        for (int i = 0; i < NI - 2; ++i) v[i] = *reinterpret_cast<const u32x4*>(s_part + (mh * 4 * NI + i) * DH + mc * 4);
        f32x4 f = *reinterpret_cast<const f32x4*>(s_f + (2 * (NI - 2) + mh) * 8);
        f32x2 zl = *reinterpret_cast<const f32x2*>(s_f + (2 * (NI - 2) + mh) * 8 + 4);
        PIN();
#pragma unroll
        for (int i = 0; i < NI - 2; ++i) asm volatile("" : "+v"(v[i]));
        asm volatile("" : "+v"(f), "+v"(zl));
        v[NI - 2] = v[0];
        bool have = false, sent = false;
        while (!(have && sent)) {
            const unsigned now = now32();
            if (!have) {
                steer(now);
                if (finished() > NI - 2) {
                    PIN();
                    v[NI - 2] = merge(s_part + (mh * 4 * NI + NI - 2) * DH + mc * 6, f, zl[0], last_word(tag, zl[1], mc));
                    have = true;
                    warm_code(WN[PF][3]);
                    HSTAMP(5);
                }
            }
            if (!sent && (int)((now << 8) - origin) >= pace.pub) {
                HSTAMP(6);
#pragma unroll
                for (int i = 0; i < NI - 2; ++i) publish(v[i], i, piece_off(mh, mc));
                sent = true;
            }
            if (!(have && sent)) __builtin_amdgcn_s_sleep(1);
            PIN();
        }
        publish(v[NI - 2], NI - 2, piece_off(mh, mc));
        HSTAMP(7);
        asm volatile(".Lkv_end_%0_%1_%2_%3:" :: "i"(X), "i"(OPT), "i"(GATE), "i"(TP));
        return;
    }

    unsigned off = pace.start + ((pace.step * (hp * 1024 + m * 4 + rw)) >> 11);
    unsigned origin = tsched << 8, kept = 0;
    const unsigned step = pace.step;
    auto pace_wait = [&]() {
        const unsigned tgt = __builtin_amdgcn_readfirstlane(origin) + off;
        unsigned now = now32() << 8;
        kept = ((int)(now - tgt) > 0 ? now : tgt) - off;
        while ((int)(tgt - now) > 0) { if (SLP) __builtin_amdgcn_s_sleep(SLP); now = now32() << 8; }
        off += step;
    };
    auto report = [&]() {
        if (lane == 0) *(vlds_u32x2*)(s_act + 2 * wave) = u32x2{kept, off};
        origin = *(vlds_u32*)(s_sync + 2);
        PIN();
    };
#define TRV (kept - (tstart << 8))

    u32x2 lsc[4];
    auto load_sc = [&]() {
#pragma unroll
        for (int a = 0; a < 4; ++a) {
            const gptr base = a == 0 ? ksc : a == 1 ? kzp : a == 2 ? vsc : vzp;
            lsc[a] = __builtin_nontemporal_load((const __attribute__((address_space(1))) u32x2*)(base + (unsigned)(((tid >> 4) * SKV + m * 64 + (tid & 15) * 4) * 2)));
        }
        PIN();
    };

    const auto rc = __builtin_amdgcn_make_buffer_rsrc((void*)k_cos, 0, CSB, 0x00020000);
    const auto rs = __builtin_amdgcn_make_buffer_rsrc((void*)k_sin, 0, CSB, 0x00020000);
    u32x2 raw[2][6];
    auto load_cs = [&](int t) {
        const unsigned rb = rb0 + t * 4 * 384;
        raw[t][0] = __builtin_amdgcn_raw_buffer_load_b64(rc, co, rb, 2);
        raw[t][1] = __builtin_amdgcn_raw_buffer_load_b64(rc, co + 128, rb, 2);
        raw[t][2] = __builtin_amdgcn_raw_buffer_load_b64(rc, co + 256, rb, 2);
        raw[t][3] = __builtin_amdgcn_raw_buffer_load_b64(rs, co + 192, rb, 2);
        raw[t][4] = __builtin_amdgcn_raw_buffer_load_b64(rs, so1, rb, 2);
        raw[t][5] = __builtin_amdgcn_raw_buffer_load_b64(rs, co + 64, rb, 2);
        PIN();
    };
    u32x4 tab[4][3];
    auto pack_cs = [&](int t) {
#pragma unroll
        for (int j = 0; j < 3; ++j) {
#pragma unroll
            for (int k = 0; k < 2; ++k) {
                tab[t][j][2 * k] = __builtin_amdgcn_perm(raw[t][3 + j][k], raw[t][j][k], 0x05040100);
                tab[t][j][2 * k + 1] = __builtin_amdgcn_perm(raw[t][3 + j][k], raw[t][j][k], 0x07060302);
            }
            *reinterpret_cast<u32x4*>(s_part + (wave ^ 4) * (NI * DH) + ((t * 3 + j) * 64 + lane) * 4) = tab[t][j];
        }
        PIN();
    };
    const auto rk = __builtin_amdgcn_make_buffer_rsrc((void*)qk, 0, KVB, 0x00020000);
    const auto rv = __builtin_amdgcn_make_buffer_rsrc((void*)qv, 0, KVB, 0x00020000);
    const unsigned voff = r * ROWB + c * 16;
    const unsigned woff = row0 * ROWB + hp * HEADB;
    auto toff = [&](int t) { return (unsigned)(t ^ (2 * hp)) * (4 * ROWB); };
    u32x4 ring[4][3];
    auto ld = [&](int slot, int j, __amdgpu_buffer_rsrc_t rr, unsigned off) {
        ring[slot][j] = __builtin_amdgcn_raw_buffer_load_b128(rr, voff + j * 256, off, 2);
        PIN();
    };
    auto ld_tile = [&](int slot, __amdgpu_buffer_rsrc_t rr, unsigned off) { ld(slot, 0, rr, off); ld(slot, 1, rr, off); ld(slot, 2, rr, off); };
    auto refill = [&](int slot, int j, __amdgpu_buffer_rsrc_t rr, unsigned goff) {
        if (j == 0) pace_wait();
        if (WIN == 1) VMCNT(3);
        else if (WIN == 2) { if (j == 0) VMCNT(3); else VMCNT(2); }
        else if (WIN == 3) { if (j == 2) VMCNT(2); else VMCNT(3); }
        else if (WIN == 4) VMCNT(4);
        else VMCNT(2);
        ld(slot, j, rr, goff);
        if (j == 0) { const unsigned t = (now32() << 8) - 1024 - (off - step); if ((int)(t - kept) > 0) kept = t; }
        if (j == 2) report();
    };
    auto sc_to_lds = [&]() {
        float* dst = s_sc + tid * 16;
#pragma unroll
        for (int u = 0; u < 2; ++u) {
            *reinterpret_cast<f32x4*>(dst + 8 * u) = f32x4{bflo(lsc[0][u]) * SCALE_LOG2, bflo(lsc[1][u]), bflo(lsc[2][u]), bflo(lsc[3][u])};
            *reinterpret_cast<f32x4*>(dst + 8 * u + 4) = f32x4{bfhi(lsc[0][u]) * SCALE_LOG2, bfhi(lsc[1][u]), bfhi(lsc[2][u]), bfhi(lsc[3][u])};
        }
        PIN();
    };

    auto k0 = [&](int t) { pace_wait(); ld_tile(t, rk, woff + toff(t)); };
    if (PRO == 0) {
        load_cs(0); PSTAMP(70); load_sc(); PSTAMP(71);
        pace_wait(); load_cs(1); STAMP(1);
        PSTAMP(72); pack_cs(0); PSTAMP(73); sc_to_lds(); PSTAMP(74);
        k0(0); pack_cs(1); PSTAMP(75);
        PSTAMP(76); k0(1); PSTAMP(77);
    } else if (PRO == 1) {
        load_cs(0);
        pace_wait(); load_cs(1); STAMP(1);
        pack_cs(0);
        pace_wait(); load_sc();
        pack_cs(1);
        k0(0); sc_to_lds();
        k0(1);
    } else if (PRO == 3) {
        load_sc();
        pace_wait(); load_cs(0); STAMP(1);
        sc_to_lds();
        pace_wait(); load_cs(1);
        pack_cs(0);
        k0(0); pack_cs(1);
        k0(1);
    } else if (PRO == 5) {
        load_cs(0);
        pace_wait(); load_cs(1); STAMP(1);
        pace_wait(); load_sc();
        pack_cs(0);
        k0(0); pack_cs(1);
        k0(1); sc_to_lds();
    } else if (PRO == 7) {
        load_sc();
        pace_wait(); load_cs(0); STAMP(1);
        pace_wait(); load_cs(1);
        sc_to_lds();
        k0(0); pack_cs(0);
        k0(1); pack_cs(1);
    } else if (PRO == 10) {
        load_cs(0); load_sc();
        pace_wait(); load_cs(1); STAMP(1);
        pack_cs(0);
        k0(0); sc_to_lds(); pack_cs(1);
        k0(1);
    } else if (PRO == 20) {
        load_cs(0); load_sc();
        pace_wait(); load_cs(1); STAMP(1);
        pack_cs(0); sc_to_lds(); pack_cs(1);
        k0(0);
        k0(1);
    }
    VMCNT(3); PSTAMP(78); k0(2);
    STAMP(2);
    VMCNT(3); k0(3);
    if (lane == 0) *(vlds_u32x2*)(s_act + 2 * wave) = u32x2{kept, off};
    BARRIER();
    origin = *(vlds_u32*)(s_sync + 2);
    STAMP(3);

    u32x4 qp[3];
    auto load_q = [&](int h, int j) { qp[j] = *reinterpret_cast<const u32x4*>(s_q + h * DH + 64 * j + 4 * c); };
    load_q(hp, 0); load_q(hp, 1); load_q(hp, 2);
#pragma unroll
    for (int t = 0; t < 2; ++t)
#pragma unroll
        for (int j = 0; j < 3; ++j) tab[2 + t][j] = *reinterpret_cast<const u32x4*>(s_part + wave * (NI * DH) + ((t * 3 + j) * 64 + lane) * 4);

    for (int i = 0; i < NI; ++i) {
        const bool last = i + 1 == NI;
        const int h = 2 * i + hp;
        const unsigned hnext = (i + 1) * (2 * HEADB) + woff;
        const auto rn = __builtin_amdgcn_make_buffer_rsrc((void*)(last ? qv : qk), 0, KVB, 0x00020000);
        float* scp = s_sc + (h * 4 + rw) * 64;
        f32x4 sc[4];
#pragma unroll
        for (int t = 0; t < 4; ++t) sc[t] = *reinterpret_cast<const f32x4*>(scp + (4 * (t ^ (2 * hp)) + r) * 4);
        float s[4];
#pragma unroll
        for (int t = 0; t < 4; ++t) {
            f32x2 dot, wsum;
#pragma unroll
            for (int j = 0; j < 3; ++j) {
                f32x2 w01, w23, k01, k23;
                dot_cvt(qp[j], tab[t][j], ring[t][j], w01, w23, k01, k23);
                if (j == 0) { wsum = w01 + w23; dot = k01 * w01; } else { wsum += w01; wsum += w23; dot += k01 * w01; }
                dot += k23 * w23;
                asm volatile("" : "+v"(dot), "+v"(wsum));
                PIN();
                if (t == 3) load_q(last ? h : h + 2, j);
                refill(t, j, rn, (last ? woff : hnext) + toff(t));
                if (t == 0 && j == 0) TRACE(i);
            }
            const float a = (dot[0] + dot[1]) - sc[t][1] * (wsum[0] + wsum[1]);
            s[t] = row16_allsum(a) * sc[t][0];
        }
        const float mx = xrow_max(fmaxf(fmaxf(s[0], s[1]), fmaxf(s[2], s[3])));
        float l = 0.f, z = 0.f;
#pragma unroll
        for (int t = 0; t < 4; ++t) {
            const float p = __builtin_amdgcn_exp2f(s[t] - mx);
            l += p;
            s[t] = p * sc[t][2];
            z += s[t] * sc[t][3];
        }
        l = xrow_sum(l);
        z = xrow_sum(z);
        if (c == 0) {
            *reinterpret_cast<f32x4*>(scp + r * 4) = f32x4{s[0], s[1], s[2], s[3]};
            *reinterpret_cast<f32x4*>(scp + 16) = f32x4{mx, l, z, 0.f};
        }
        PIN();
    }
    STAMP(4);

    const float* pwp = s_sc + (hp * 4 + rw) * 64 + r * 4;
    f32x4 pw = *reinterpret_cast<const f32x4*>(pwp);
    unsigned tag, lw, lo;
    f32x4 lf;
    f32x2 lz;
    unsigned lp = (hp * 4 * NI + NI - 1) * DH + (lane & 31) * 6;
    f32x2 acc[3][2];
    auto v_acc = [&](int t, int j) {
        const f32x2 p2 = {pw[t], pw[t]};
        const u32x4 vv = ring[t][j];
        const f32x2 v01 = {(float)(int)vv[0], (float)(int)vv[1]}, v23 = {(float)(int)vv[2], (float)(int)vv[3]};
        if (t == 0) { acc[j][0] = p2 * v01; acc[j][1] = p2 * v23; }
        else { acc[j][0] += p2 * v01; acc[j][1] += p2 * v23; }
        asm volatile("" : "+v"(acc[j][0]), "+v"(acc[j][1]));
        PIN();
    };
    auto v_fold = [&](int i, int j) {
        const float x = swap32_add(acc[j][0][0], acc[j][1][0]);
        const float y = swap32_add(acc[j][0][1], acc[j][1][1]);
        s_part[(wave * NI + i) * DH + 64 * j + 4 * c + r] = swap16_add(x, y);
    };
    auto v_done = [&](int i) {
        PIN();
        if (lane == 0) *(vlds_u32*)(s_done + wave) = i + 1;
        PIN();
    };
#pragma unroll
    for (int t = 0; t < LS; ++t)
#pragma unroll
        for (int j = 0; j < 3; ++j) v_acc(t, j);
    for (int i = 0; i < NI - 1; ++i) {
        const unsigned hnext = (i + 1) * (2 * HEADB) + woff;
#pragma unroll
        for (int k = 0; k < 4; ++k) {
            const int t = (k + LS) & 3;
#pragma unroll
            for (int j = 0; j < 3; ++j) {
                v_acc(t, j);
                if (t == 3 && j == 2) pw = *reinterpret_cast<const f32x4*>(pwp + (i + 1) * 512);
                refill(k, j, rv, hnext + toff(k));
                if (k == 0 && j == 0) TRACE(16 + i);
            }
            if (t == 3) { v_fold(i, 0); v_fold(i, 1); v_fold(i, 2); v_done(i); }
        }
    }
    if (PAD) asm volatile("s_branch .Lkv_tail_%=\n\t.p2align 12\n.Lkv_tail_%=:" ::: "memory");
    STAMP(5);
    TRACE(31);
    while (__builtin_amdgcn_readfirstlane(*(vlds_u32*)(s_sync + 3)) == 0) __builtin_amdgcn_s_sleep(1);
    PIN();
    tag = s_sync[4 + (lane & 3)];
    lf = *reinterpret_cast<const f32x4*>(s_f + (2 * (NI - 1) + hp) * 8);
    lz = *reinterpret_cast<const f32x2*>(s_f + (2 * (NI - 1) + hp) * 8 + 4);
    origin = *(vlds_u32*)(s_sync + 2);
    lw = last_word(tag, lz[1], lane & 31);
    lo = lane < 32 ? piece_off(hp, lane & 31) : OOB;
    asm volatile("" : "+v"(tag), "+v"(lf), "+v"(lz), "+v"(lw), "+v"(lo), "+v"(lp), "+v"(origin));
    PIN();
#pragma unroll
    for (int t = LS; t < 4; ++t)
#pragma unroll
        for (int j = 0; j < 3; ++j) { v_acc(t, j); if (t == 3) { v_fold(NI - 1, j); PIN(); } }
    v_done(NI - 1);
    STAMP(6);
    {
        const u32x4 dn = *(vlds_u32x4*)(s_done + 4 * hp);
        PIN();
        const u32x4 v = merge(s_part + lp, lf, lz[0], lw);
        if (__builtin_amdgcn_readfirstlane(dn[0] & dn[1] & dn[2] & dn[3]) == NI) publish(v, NI - 1, lo);
        PIN();
    }
    STAMP(7);
    {
        unsigned inbox = WS_PART + (bid >> 4) * GSTR + (bid & 15) * UNITB, obase = bid * 48;
        unsigned xo = tid * 16, yo = tid * 16 + 8192, sel = lane & 1 ? 0x01000504u : 0x05040100u;
        unsigned bo = lane < 16 ? wave * 1024 + (lane & 7) * 128 + (lane >> 3) * 8192 : OOB;
        typedef __attribute__((address_space(3))) float lds_f;
        lds_f* wo = (lds_f*)s_red + wave * 28 + (r < 3 ? c * 6 + r * 2 : 24);
        lds_f* fo = (lds_f*)s_red + (lane & 7) * 28 + 24;
        lds_f* go = (lds_f*)s_red + (lane & 7) * 28 + min(lane >> 3, 5) * 4;
        vlds_u32* dw = (vlds_u32*)(s_done + wave);
        vlds_u32* dr = (vlds_u32*)(s_done + (lane & 7));
        unsigned oo = (lane & 7) == 0 && lane < 48 ? lane : OOB, eo = bid == 0 && lane < 4 ? lane * 2 : OOB;
        asm volatile("" : "+s"(inbox), "+s"(obase), "+v"(xo), "+v"(yo), "+v"(sel), "+v"(bo), "+v"(wo), "+v"(fo), "+v"(go), "+v"(oo), "+v"(eo), "+v"(dw), "+v"(dr));
        const unsigned o = __builtin_amdgcn_readfirstlane(origin) - (15 - (bid >> 4)) * RST;
        while ((int)((now32() << 8) - o) < pace.rd) __builtin_amdgcn_s_sleep(1);
        PIN();
        STAMP(8);
        u32x4 x, y;
        do {
            x = __builtin_amdgcn_raw_buffer_load_b128(rw_, xo, inbox, 16);
            y = __builtin_amdgcn_raw_buffer_load_b128(rw_, yo, inbox, 16);
            const unsigned age = now32() - tstart;
            PIN();
            if (age > SPIN_LIMIT) break;
        } while (__builtin_amdgcn_ballot_w64(((x[3] ^ tag) | (y[3] ^ tag)) > 0xffffu) != 0);
        if (DRY) { __builtin_amdgcn_raw_buffer_store_b32(0u, rw_, bo, inbox, 16); PIN(); }
        STAMP(9);
        const float lx = __uint_as_float(__builtin_amdgcn_perm(__builtin_amdgcn_mov_dpp(x[3], 0xb1, 0xf, 0xf, true), x[3], sel));
        const float ly = __uint_as_float(__builtin_amdgcn_perm(__builtin_amdgcn_mov_dpp(y[3], 0xb1, 0xf, 0xf, true), y[3], sel));
        float M = fmaxf(lx, ly);
        M = fmaxf(M, dpp<0x128>(M));
        M = fmaxf(M, dpp<0x124>(M));
        M = xrow_max(M);
        const float w0 = __builtin_amdgcn_exp2f(lx - M), w1 = __builtin_amdgcn_exp2f(ly - M);
        float a[8];
#pragma unroll
        for (int k = 0; k < 3; ++k) {
            a[2 * k] = w0 * bflo(x[k]) + w1 * bflo(y[k]);
            a[2 * k + 1] = w0 * bfhi(x[k]) + w1 * bfhi(y[k]);
        }
        a[6] = a[7] = w0 + w1;
        float b[4], d[2];
#pragma unroll
        for (int k = 0; k < 4; ++k) b[k] = swap32_add(a[k], a[4 + k]);
#pragma unroll
        for (int k = 0; k < 2; ++k) d[k] = swap16_add(b[k], b[2 + k]);
#pragma unroll
        for (int k = 0; k < 2; ++k) { d[k] += dpp<0x128>(d[k]); d[k] += dpp<0x124>(d[k]); }
        if (c < 4) {
            *(__attribute__((address_space(3))) f32x2*)wo = f32x2{d[0], r < 3 ? d[1] : M};
            PIN();
            *dw = 3 * NI;
        }
        PIN();
        STAMP(10);
        const unsigned fin = *dr;
        PIN();
        f32x2 sm = *(__attribute__((address_space(3))) f32x2*)fo;
        f32x4 ov = *(__attribute__((address_space(3))) f32x4*)go;
        asm volatile("" : "+v"(sm), "+v"(ov));
        if (__builtin_amdgcn_ballot_w64(fin != 3 * NI) == 0) {
            float MM = fmaxf(sm[1], dpp<0xb1>(sm[1]));
            MM = fmaxf(MM, dpp<0x4e>(MM));
            MM = fmaxf(MM, dpp<0x141>(MM));
            const float ff = __builtin_amdgcn_exp2f(sm[1] - MM);
            float e[5] = {ff * ov[0], ff * ov[1], ff * ov[2], ff * ov[3], ff * sm[0]};
#pragma unroll
            for (int k = 0; k < 5; ++k) e[k] += dpp<0xb1>(e[k]);
#pragma unroll
            for (int k = 0; k < 5; ++k) e[k] += dpp<0x4e>(e[k]);
#pragma unroll
            for (int k = 0; k < 5; ++k) e[k] += dpp<0x141>(e[k]);
            const float inv = __builtin_amdgcn_rcpf(e[4]);
            __builtin_amdgcn_raw_buffer_store_b64(u32x2{pk_bf16(f32x2{e[0] * inv, e[1] * inv}), pk_bf16(f32x2{e[2] * inv, e[3] * inv})}, ro, oo, obase, OST);
            if (bid == 0) __builtin_amdgcn_raw_buffer_store_b16((unsigned short)(tag >> 16), rw_, eo, WS_EPOCH, 16);
        }
    }
    __builtin_amdgcn_endpgm();
}

#define VAR_XS(p) ((p) / 1000 % 2)
template <int X, int PRO, int GATE, int STAG>
static void launch_cfg(void* const* d_in, void* d_out, void* d_ws, hipStream_t stream, float t_us, float start_us, float off_us, float debt_us, float adj_pct, float pub_us) {
    Pace pace;
    pace.step = (unsigned)(t_us * 25600.f / 128.f);
    pace.start = (int)(start_us * 25600.f);
    pace.offset = (int)(off_us * 25600.f);
    pace.bias = (int)(debt_us * 25600.f);
    pace.rd = (int)((start_us + t_us * ((129.f + VAR_XS(PRO)) / 128.f) + adj_pct) * 25600.f);
    pace.pub = (int)((start_us + t_us * ((129.f + VAR_XS(PRO)) / 128.f) + pub_us) * 25600.f);
    kv_sweep_kernel<X, PRO, GATE, STAG, 1><<<NWG, 576, 0, stream>>>(d_in[0], d_in[1], d_in[2], d_in[3], d_in[4], d_in[5], d_in[6], d_in[7],
        d_in[8], d_in[9], d_in[10], d_ws, d_out, pace);
}

#define CFG 335022966, 1023, 2121, 241
#define PRM 102.5f, 0.5f, 0.0f, 0.05f, 1.6f, 0.6f
extern "C" void kernel_launch(void* const* d_in, const int* in_sizes, int n_in,
                              void* d_out, int out_size, void* d_ws, size_t ws_size,
                              hipStream_t stream) {
    launch_cfg<CFG>(d_in, d_out, d_ws, stream, PRM);
}
```

```cpp
#include <hip/hip_runtime.h>
#include <cstdint>


using u32x4 = __attribute__((ext_vector_type(4))) unsigned;
using u32x3 = __attribute__((ext_vector_type(3))) unsigned;
using u32x2 = __attribute__((ext_vector_type(2))) unsigned;
using u64x8 = __attribute__((ext_vector_type(8))) unsigned long long;
using f32x2 = __attribute__((ext_vector_type(2))) float;
using f32x4 = __attribute__((ext_vector_type(4))) float;
using bf16x2 = __attribute__((ext_vector_type(2))) __bf16;

constexpr int DH = 192;
constexpr int HALF = 96;
constexpr int NH = 32;
constexpr int SKV = 16384;
constexpr int NWG = 256;
constexpr int NI = 16;
constexpr unsigned ROWB = DH * 4;
constexpr unsigned HEADB = SKV * ROWB;
constexpr unsigned KVB = NH * HEADB;
constexpr unsigned CSB = SKV * DH * 2;
constexpr unsigned OOB = 0x80000000u;
constexpr int DEV = 16, NT = 2;
constexpr float SCALE_LOG2 = 1.4426950408889634f * 0.07216878364870322f;
constexpr unsigned SPIN_LIMIT = 20000000u;

constexpr unsigned UNITB = NWG * 64;
constexpr unsigned GSTR = 16 * UNITB + 256;
constexpr unsigned WS_PART = 0;
constexpr unsigned WS_EPOCH = NWG * (UNITB + 1024);
constexpr unsigned WS_REP = WS_EPOCH + 256;
constexpr unsigned WS_BYTES = WS_REP + 256 * 512;

__device__ __forceinline__ float bflo(unsigned u) { return __uint_as_float(u << 16); }
__device__ __forceinline__ float bfhi(unsigned u) { return __uint_as_float(u & 0xffff0000u); }
__device__ __forceinline__ unsigned pk_bf16(f32x2 a) { return __builtin_bit_cast(unsigned, __builtin_convertvector(a, bf16x2)); }
__device__ __forceinline__ void dot_cvt(const u32x4 q, const u32x4 t, const u32x4 ki, f32x2& w01, f32x2& w23, f32x2& k01, f32x2& k23) {
    float w0, w1, w2, w3;
    float k0 = __uint_as_float(ki[0]), k1 = __uint_as_float(ki[1]), k2 = __uint_as_float(ki[2]), k3 = __uint_as_float(ki[3]);
    asm("v_dot2_f32_bf16 %0, %8, %12, 0\n\tv_dot2_f32_bf16 %1, %9, %13, 0\n\tv_dot2_f32_bf16 %2, %10, %14, 0\n\tv_dot2_f32_bf16 %3, %11, %15, 0\n\t"
        "v_cvt_f32_i32 %4, %4\n\tv_cvt_f32_i32 %5, %5\n\tv_cvt_f32_i32 %6, %6\n\tv_cvt_f32_i32 %7, %7"
        : "=&v"(w0), "=&v"(w1), "=&v"(w2), "=&v"(w3), "+v"(k0), "+v"(k1), "+v"(k2), "+v"(k3)
        : "v"(q[0]), "v"(q[1]), "v"(q[2]), "v"(q[3]), "v"(t[0]), "v"(t[1]), "v"(t[2]), "v"(t[3]));
    w01 = f32x2{w0, w1}; w23 = f32x2{w2, w3}; k01 = f32x2{k0, k1}; k23 = f32x2{k2, k3};
}

template <int CTRL> __device__ __forceinline__ float dpp(float x) {
    return __builtin_bit_cast(float, __builtin_amdgcn_mov_dpp(__builtin_bit_cast(int, x), CTRL, 0xf, 0xf, true));
}
__device__ __forceinline__ float row16_allsum(float x) {
    x += dpp<0x128>(x);
    x += dpp<0x124>(x);
    x += dpp<0x122>(x);
    x += dpp<0x121>(x);
    return x;
}
__device__ __forceinline__ float xrow_max(float x) {
    auto s = __builtin_amdgcn_permlane16_swap(__float_as_uint(x), __float_as_uint(x), false, false);
    x = fmaxf(__uint_as_float(s[0]), __uint_as_float(s[1]));
    auto t = __builtin_amdgcn_permlane32_swap(__float_as_uint(x), __float_as_uint(x), false, false);
    return fmaxf(__uint_as_float(t[0]), __uint_as_float(t[1]));
}
__device__ __forceinline__ float xrow_sum(float x) {
    auto s = __builtin_amdgcn_permlane16_swap(__float_as_uint(x), __float_as_uint(x), false, false);
    x = __uint_as_float(s[0]) + __uint_as_float(s[1]);
    auto t = __builtin_amdgcn_permlane32_swap(__float_as_uint(x), __float_as_uint(x), false, false);
    return __uint_as_float(t[0]) + __uint_as_float(t[1]);
}
__device__ __forceinline__ float swap32_add(float a, float b) {
    auto t = __builtin_amdgcn_permlane32_swap(__float_as_uint(a), __float_as_uint(b), false, false);
    return __uint_as_float(t[0]) + __uint_as_float(t[1]);
}
__device__ __forceinline__ float swap16_add(float a, float b) {
    auto t = __builtin_amdgcn_permlane16_swap(__float_as_uint(a), __float_as_uint(b), false, false);
    return __uint_as_float(t[0]) + __uint_as_float(t[1]);
}
__device__ __forceinline__ int row16_sum(int x) {
    x += __builtin_amdgcn_mov_dpp(x, 0x128, 0xf, 0xf, true);
    x += __builtin_amdgcn_mov_dpp(x, 0x124, 0xf, 0xf, true);
    x += __builtin_amdgcn_mov_dpp(x, 0x122, 0xf, 0xf, true);
    x += __builtin_amdgcn_mov_dpp(x, 0x121, 0xf, 0xf, true);
    return x;
}
__device__ __forceinline__ unsigned now32() { return (unsigned)__builtin_amdgcn_s_memrealtime(); }

#define PIN() do { asm volatile("" ::: "memory"); __builtin_amdgcn_sched_barrier(0); } while (0)
#define BARRIER() do { asm volatile("s_waitcnt lgkmcnt(0)" ::: "memory"); __builtin_amdgcn_s_barrier(); PIN(); } while (0)
#define VMCNT(n) asm volatile("s_waitcnt vmcnt(%0)" :: "i"(n) : "memory")

typedef volatile __attribute__((address_space(3))) unsigned vlds_u32;
typedef volatile __attribute__((address_space(3))) u32x2 vlds_u32x2;
typedef volatile __attribute__((address_space(3))) u32x4 vlds_u32x4;

struct Pace {
    unsigned step;
    int start;
    int offset;
    int bias;
    int rd;
    int pub;
};

__global__ __launch_bounds__(576, 3) __attribute__((aligned(4096))) void kv_sweep_kernel(
    const void*, const void*, const void*, const void*, const void*, const void*, const void*, const void*,
    const void*, const void*, const void*, void*, void*, Pace)
{
    __shared__ __attribute__((aligned(16))) unsigned s_q[NH * DH];
    __shared__ __attribute__((aligned(16))) float s_sc[NH * 64 * 4];
    __shared__ __attribute__((aligned(16))) float s_part[8 * NI * DH];
    __shared__ __attribute__((aligned(16))) float s_red[8 * 28];
    __shared__ __attribute__((aligned(16))) float s_f[NH * 8];
    __shared__ __attribute__((aligned(16))) unsigned s_done[8];
    __shared__ __attribute__((aligned(16))) unsigned s_sync[8];
    __shared__ __attribute__((aligned(16))) unsigned s_act[16];

    unsigned long long pc0, t0;
    u64x8 ka, kb;
    asm volatile("s_load_dwordx16 %3, %4, 0x40\n\ts_load_dwordx16 %2, %4, 0x0\n\ts_memrealtime %1\n\ts_getpc_b64 %0\n.Lkv_beg:"
                 : "=&s"(pc0), "=&s"(t0), "=&s"(ka), "=&s"(kb) : "s"(__builtin_amdgcn_kernarg_segment_ptr()));
    __builtin_amdgcn_sched_barrier(0);
    const int tid = threadIdx.x;
    const int lane = tid & 63;
    int wave = __builtin_amdgcn_readfirstlane(tid >> 6);
    const int r = lane >> 4;
    const int c = lane & 15;
    const int bid = blockIdx.x;
    const int m = (bid & 7) * 32 + (bid >> 3);
    const int rw = wave & 3, hp = wave >> 2;
    const int row0 = m * 64 + rw * 16;
    unsigned co = r * 384 + c * 8, so1 = r * 384 + (c < 8 ? c * 8 + 320 : c * 8 - 64), rb0 = (row0 + 8 * hp) * 384u;
    asm volatile("s_waitcnt lgkmcnt(0)" : "+s"(t0), "+s"(ka), "+s"(kb), "+v"(co), "+v"(so1), "+s"(rb0), "+s"(wave));
    typedef const __attribute__((address_space(1))) char* gptr;
    void* const q = (void*)ka[0]; void* const qk = (void*)ka[1]; void* const qv = (void*)ka[4]; void* const q_cos = (void*)ka[7];
    const gptr ksc = (gptr)ka[2], kzp = (gptr)ka[3], vsc = (gptr)ka[5], vzp = (gptr)ka[6];
    void* const q_sin = (void*)kb[0]; void* const k_cos = (void*)kb[1]; void* const k_sin = (void*)kb[2]; void* const out = (void*)kb[4];
    unsigned char* const ws = (unsigned char*)(__attribute__((address_space(1))) unsigned char*)kb[3];
    const Pace pace = {(unsigned)kb[5], (int)(kb[5] >> 32), (int)kb[6], (int)(kb[6] >> 32), (int)kb[7], (int)(kb[7] >> 32)};
    const unsigned tstart = (unsigned)t0;
    const unsigned tsched = now32();
    const auto rw_ = __builtin_amdgcn_make_buffer_rsrc((void*)ws, 0, WS_BYTES, 0x00020000);
    const auto ro = __builtin_amdgcn_make_buffer_rsrc((void*)out, 0, NH * DH * 2, 0x00020000);

    auto merge = [&](const float* p, f32x4 f, float zs, unsigned w3) {
        f32x2 u[4][3], a[3];
#pragma unroll
        for (int w = 0; w < 4; ++w)
#pragma unroll
            for (int k = 0; k < 3; ++k) u[w][k] = *reinterpret_cast<const f32x2*>(p + w * NI * DH + 2 * k);
        asm volatile("" : "+v"(u[0][0]), "+v"(u[0][1]), "+v"(u[0][2]), "+v"(u[1][0]), "+v"(u[1][1]), "+v"(u[1][2]),
                          "+v"(u[2][0]), "+v"(u[2][1]), "+v"(u[2][2]), "+v"(u[3][0]), "+v"(u[3][1]), "+v"(u[3][2]));
#pragma unroll
        for (int k = 0; k < 3; ++k) a[k] = (f[0] * u[0][k] - zs + f[1] * u[1][k]) + (f[2] * u[2][k] + f[3] * u[3][k]);
        return u32x4{pk_bf16(a[0]), pk_bf16(a[1]), pk_bf16(a[2]), w3};
    };
    auto last_word = [&](unsigned tag, float ls, int c6) { return tag | ((__float_as_uint(ls) >> (16 * (c6 & 1))) & 0xffffu); };
    auto piece_off = [&](int hp, int c6) { return (hp * 8 + (c6 >> 2)) * UNITB + (c6 & 3) * 16; };
    auto publish = [&](u32x4 v, int i, unsigned vo) {
        __builtin_amdgcn_raw_buffer_store_b128(v, rw_, vo, WS_PART + i * GSTR + m * 64, DEV);
        asm volatile("s_nop 1" :: "v"(v) : "memory");
    };

    if (wave == 8) {
        __builtin_amdgcn_s_setprio(3);
        unsigned clen;
        asm volatile("s_mov_b32 %0, .Lkv_end-.Lkv_beg" : "=s"(clen));
        const auto rcode = __builtin_amdgcn_make_buffer_rsrc((void*)(pc0 & ~127ull), 0, clen, 0x00020000);
        const int jj = bid >> 3;
        auto touch_ld = [&](bool on) {
            return __builtin_amdgcn_raw_buffer_load_b32(__builtin_amdgcn_make_buffer_rsrc((void*)(jj < NI ? qk : qv), 0, KVB, 0x00020000),
                on && lane < 4 ? (lane >> 1) * HEADB + (lane & 1) * 2047 * ROWB : OOB, (jj & 15) * 2 * HEADB + (bid & 7) * 2048 * ROWB, NT);
        };
        auto touch_kv = [&]() { const unsigned x = touch_ld(true); asm volatile("" :: "v"(x)); };
        const unsigned x0 = __builtin_amdgcn_raw_buffer_load_b32(rcode, lane < 9 ? (jj * 9 + lane) * 64 : OOB, 0, 0);
        PIN();
        while (now32() - tsched < 200) __builtin_amdgcn_s_sleep(1);
        u32x3 lc[2], ls[2];
        u32x4 qraw[12];
        {
            const auto rq = __builtin_amdgcn_make_buffer_rsrc((void*)q, 0, NH * DH * 2, 0x00020000);
            const auto rqc = __builtin_amdgcn_make_buffer_rsrc((void*)q_cos, 0, DH * 2, 0x00020000);
            const auto rqs = __builtin_amdgcn_make_buffer_rsrc((void*)q_sin, 0, DH * 2, 0x00020000);
#pragma unroll
            for (int k = 0; k < 12; ++k) qraw[k] = __builtin_amdgcn_raw_buffer_load_b128(rq, lane * 16 + (k & 3) * 1024, (k >> 2) * 4096, 0);
#pragma unroll
            for (int k = 0; k < 2; ++k) {
                lc[k] = __builtin_amdgcn_raw_buffer_load_b96(rqc, c * 12 + k * 192, 0, 0);
                ls[k] = __builtin_amdgcn_raw_buffer_load_b96(rqs, c * 12 + k * 192, 0, 0);
            }
            PIN();
        }
        const unsigned tag = ((unsigned)__builtin_amdgcn_raw_buffer_load_b16(rw_, (lane & 3) * 2, WS_EPOCH, DEV) + 1u) << 16;
        bool touched = jj < 3;
        const unsigned x1 = touch_ld(jj < 3);
        PIN();
        unsigned* const s_raw = reinterpret_cast<unsigned*>(s_part) + NI * DH / 2;
#pragma unroll
        for (int k = 0; k < 12; ++k) *reinterpret_cast<u32x4*>(s_raw + k / 6 * (NI * DH) + k % 6 * 256 + lane * 4) = qraw[k];
        f32x2 fc[2][3], fs[2][3];
#pragma unroll
        for (int k = 0; k < 2; ++k)
#pragma unroll
            for (int u = 0; u < 3; ++u) { fc[k][u] = f32x2{bflo(lc[k][u]), bfhi(lc[k][u])}; fs[k][u] = f32x2{bflo(ls[k][u]), bfhi(ls[k][u])}; }
        PIN();
#pragma nounroll
        for (int it = 0; it < 4; ++it) {
            unsigned qa[2][3], qb[2][3];
#pragma unroll
            for (int g = 0; g < 2; ++g)
#pragma unroll
                for (int u = 0; u < 3; ++u) {
                    const unsigned* src = s_raw + g * (NI * DH) + (it * 4 + r) * HALF + 3 * c;
                    qa[g][u] = src[u]; qb[g][u] = src[HALF / 2 + u];
                }
#pragma unroll
            for (int g = 0; g < 2; ++g)
#pragma unroll
                for (int u = 0; u < 3; ++u) {
                    const f32x2 a = {bflo(qa[g][u]), bfhi(qa[g][u])}, b = {bflo(qb[g][u]), bfhi(qb[g][u])};
                    const f32x2 o0 = a * fc[0][u] - b * fs[0][u], o1 = b * fc[1][u] + a * fs[1][u];
                    const unsigned l0 = pk_bf16(f32x2{o0[0], o1[0]}), l1 = pk_bf16(f32x2{o0[1], o1[1]});
                    unsigned* dst = s_q + (16 * g + 4 * it + r) * DH + 6 * c + 2 * u;
                    *reinterpret_cast<u32x2*>(dst) = u32x2{l0, l1};
                    *reinterpret_cast<u32x2*>(dst + HALF) = u32x2{__builtin_amdgcn_alignbit(l0, l0, 16) ^ 0x80000000u, __builtin_amdgcn_alignbit(l1, l1, 16) ^ 0x80000000u};
                }
        }
        asm volatile("" :: "v"(x0), "v"(x1));
        __builtin_amdgcn_s_setprio(0);
        unsigned origin = (tsched << 8) + pace.offset;
        if (lane < 8) { s_done[lane] = 0; s_sync[lane] = lane < 3 ? origin : lane < 4 ? 0 : tag; }
        PIN();
        BARRIER();
        const int mh = lane >> 5, mc = lane & 31;
        const unsigned rtag = __builtin_amdgcn_readlane(tag, 0) | __builtin_amdgcn_readlane(tag, 1) >> 16;
        constexpr int RPX = 2;
        constexpr int RSH = 9;
        constexpr unsigned RDLY = 200;
        constexpr int SLEW = 492;
        constexpr int SLIP = 5120;
        const int rj = ((bid >> 3) + 5 * (bid & 7)) & 31;
        const int rid = rj % (32 / RPX) == 0 ? rj / (32 / RPX) * 8 + (bid & 7) : -1;
        unsigned wround = now32() >> RSH, rround = wround, tick = 0, target = origin;
        bool synced = false;
        int drift = 0;
        const unsigned round0 = rround;
        int i = 0;
        auto kept_now = [&](unsigned now) {
            const u32x2 a = *(vlds_u32x2*)(s_act + 2 * (lane & 7));
            const int late = max((int)(a[0] - origin), (int)((now << 8) - a[1] - origin));
            return origin + (__builtin_amdgcn_readfirstlane(row16_sum(late)) >> 4);
        };
        auto steer = [&](unsigned now) {
            if ((now >> 5) != tick) {
                tick = now >> 5;
                target += drift;
                origin += max(-SLEW, min(SLEW + drift, (int)(target - origin)));
                const int l2 = (int)(kept_now(now) - origin) - SLIP;
                if (l2 > 0) origin += l2;
                if (!synced) target = origin;
                if (lane == 0) *(vlds_u32*)(s_sync + 2) = origin;
                PIN();
            }
        };
        auto finished = [&]() {
            const u32x4 a = *(vlds_u32x4*)s_done, b = *(vlds_u32x4*)(s_done + 4);
            return __builtin_amdgcn_readfirstlane(min(min(min(a[0], a[1]), min(a[2], a[3])), min(min(b[0], b[1]), min(b[2], b[3]))));
        };
        while (i < NI - 2) {
            const unsigned now = now32();
            if ((now >> RSH) != wround) {
                wround = now >> RSH;
                if (rid >= 0 && (now & ((1u << RSH) - 1)) < 48)
                    __builtin_amdgcn_raw_buffer_store_b64(u32x2{kept_now(now), rtag + wround}, rw_, lane == 0 ? rid * 8 : OOB, WS_REP + (wround & 255) * 512, DEV);
                PIN();
                continue;
            }
            if ((int)(((now - RDLY) >> RSH) - rround) > 0) {
                rround = (now - RDLY) >> RSH;
                const u32x2 x = __builtin_amdgcn_raw_buffer_load_b64(rw_, c * 8, WS_REP + (rround & 255) * 512, DEV);
                const int d = (int)(x[0] - origin);
                const bool ok = x[1] == rtag + rround && d < (32 << 16) && d > -(32 << 16);
                const int n = __builtin_popcountll(__builtin_amdgcn_ballot_w64(ok));
                if (n >= 48) {
                    const unsigned nt = origin + __builtin_amdgcn_readfirstlane((int)(row16_allsum(ok ? (float)d : 0.f) * 4.f / (float)n)) - pace.bias;
                    if (synced && rround - round0 > 4) drift = max(0, min(drift + ((max(-1024, min((int)(nt - target) + 6 * drift, 2560)) - (rround - round0 < 13 ? 768 : 0)) >> 5), 400));
                    target = nt + 6 * drift;
                    synced = true;
                    const int lost = min((int)(kept_now(now32()) - origin) - pace.bias, (int)(target - origin));
                    if (lost > 0) origin += lost;
                }
                PIN();
                continue;
            }
            steer(now);
            if (!touched && (int)((now << 8) - origin) > (jj * 4 - 8) * (int)pace.step) { touch_kv(); touched = true; continue; }
            if (finished() <= (unsigned)i) { __builtin_amdgcn_s_sleep(2); continue; }
            PIN();
            if (i == 0) {
                f32x4 ml[4], f;
#pragma unroll
                for (int w = 0; w < 4; ++w) ml[w] = *reinterpret_cast<const f32x4*>(s_sc + (mc * 4 + w) * 64 + 16);
                const float M = fmaxf(fmaxf(ml[0][0], ml[1][0]), fmaxf(ml[2][0], ml[3][0]));
                float L = 0.f, zs = 0.f;
#pragma unroll
                for (int w = 0; w < 4; ++w) { f[w] = __builtin_amdgcn_exp2f(ml[w][0] - M); L += f[w] * ml[w][1]; zs += f[w] * ml[w][2]; }
                const float inv = 1.f / L;
                *reinterpret_cast<f32x4*>(s_f + mc * 8) = f * inv;
                *reinterpret_cast<f32x2*>(s_f + mc * 8 + 4) = f32x2{zs * inv, M + __builtin_amdgcn_logf(L)};
                PIN();
                if (lane == 0) *(vlds_u32*)(s_sync + 3) = 1;
                PIN();
            }
            const f32x2 zl = *reinterpret_cast<const f32x2*>(s_f + (2 * i + mh) * 8 + 4);
            const u32x4 v = merge(s_part + (mh * 4 * NI + i) * DH + mc * 6, *reinterpret_cast<const f32x4*>(s_f + (2 * i + mh) * 8), zl[0], last_word(tag, zl[1], mc));
            PIN();
            *reinterpret_cast<u32x4*>(s_part + (mh * 4 * NI + i) * DH + mc * 4) = v;
            PIN();
            ++i;
        }
        u32x4 v[NI - 1];
#pragma unroll
        for (int i = 0; i < NI - 2; ++i) v[i] = *reinterpret_cast<const u32x4*>(s_part + (mh * 4 * NI + i) * DH + mc * 4);
        f32x4 f = *reinterpret_cast<const f32x4*>(s_f + (2 * (NI - 2) + mh) * 8);
        f32x2 zl = *reinterpret_cast<const f32x2*>(s_f + (2 * (NI - 2) + mh) * 8 + 4);
        PIN();
#pragma unroll
        for (int i = 0; i < NI - 2; ++i) asm volatile("" : "+v"(v[i]));
        asm volatile("" : "+v"(f), "+v"(zl));
        v[NI - 2] = v[0];
        bool have = false, sent = false;
        while (!(have && sent)) {
            const unsigned now = now32();
            if (!have) {
                steer(now);
                if (finished() > NI - 2) {
                    PIN();
                    v[NI - 2] = merge(s_part + (mh * 4 * NI + NI - 2) * DH + mc * 6, f, zl[0], last_word(tag, zl[1], mc));
                    have = true;
                }
            }
            if (!sent && (int)((now << 8) - origin) >= pace.pub) {
#pragma unroll
                for (int i = 0; i < NI - 2; ++i) publish(v[i], i, piece_off(mh, mc));
                sent = true;
            }
            if (!(have && sent)) __builtin_amdgcn_s_sleep(1);
            PIN();
        }
        publish(v[NI - 2], NI - 2, piece_off(mh, mc));
        asm volatile(".Lkv_end:");
        return;
    }

    unsigned off = pace.start + ((pace.step * (hp * 1024 + m * 4 + rw)) >> 11);
    unsigned origin = tsched << 8, kept = 0;
    const unsigned step = pace.step;
    auto pace_wait = [&]() {
        const unsigned tgt = __builtin_amdgcn_readfirstlane(origin) + off;
        unsigned now = now32() << 8;
        kept = ((int)(now - tgt) > 0 ? now : tgt) - off;
        while ((int)(tgt - now) > 0) { __builtin_amdgcn_s_sleep(1); now = now32() << 8; }
        off += step;
    };
    auto report = [&]() {
        if (lane == 0) *(vlds_u32x2*)(s_act + 2 * wave) = u32x2{kept, off};
        origin = *(vlds_u32*)(s_sync + 2);
        PIN();
    };

    u32x2 lsc[4];
    auto load_sc = [&]() {
#pragma unroll
        for (int a = 0; a < 4; ++a) {
            const gptr base = a == 0 ? ksc : a == 1 ? kzp : a == 2 ? vsc : vzp;
            lsc[a] = __builtin_nontemporal_load((const __attribute__((address_space(1))) u32x2*)(base + (unsigned)(((tid >> 4) * SKV + m * 64 + (tid & 15) * 4) * 2)));
        }
        PIN();
    };

    const auto rc = __builtin_amdgcn_make_buffer_rsrc((void*)k_cos, 0, CSB, 0x00020000);
    const auto rs = __builtin_amdgcn_make_buffer_rsrc((void*)k_sin, 0, CSB, 0x00020000);
    u32x2 raw[2][6];
    auto load_cs = [&](int t) {
        const unsigned rb = rb0 + t * 4 * 384;
        raw[t][0] = __builtin_amdgcn_raw_buffer_load_b64(rc, co, rb, NT);
        raw[t][1] = __builtin_amdgcn_raw_buffer_load_b64(rc, co + 128, rb, NT);
        raw[t][2] = __builtin_amdgcn_raw_buffer_load_b64(rc, co + 256, rb, NT);
        raw[t][3] = __builtin_amdgcn_raw_buffer_load_b64(rs, co + 192, rb, NT);
        raw[t][4] = __builtin_amdgcn_raw_buffer_load_b64(rs, so1, rb, NT);
        raw[t][5] = __builtin_amdgcn_raw_buffer_load_b64(rs, co + 64, rb, NT);
        PIN();
    };
    u32x4 tab[4][3];
    auto pack_cs = [&](int t) {
#pragma unroll
        for (int j = 0; j < 3; ++j) {
#pragma unroll
            for (int k = 0; k < 2; ++k) {
                tab[t][j][2 * k] = __builtin_amdgcn_perm(raw[t][3 + j][k], raw[t][j][k], 0x05040100);
                tab[t][j][2 * k + 1] = __builtin_amdgcn_perm(raw[t][3 + j][k], raw[t][j][k], 0x07060302);
            }
            *reinterpret_cast<u32x4*>(s_part + (wave ^ 4) * (NI * DH) + ((t * 3 + j) * 64 + lane) * 4) = tab[t][j];
        }
        PIN();
    };
    const auto rk = __builtin_amdgcn_make_buffer_rsrc((void*)qk, 0, KVB, 0x00020000);
    const auto rv = __builtin_amdgcn_make_buffer_rsrc((void*)qv, 0, KVB, 0x00020000);
    const unsigned voff = r * ROWB + c * 16;
    const unsigned woff = row0 * ROWB + hp * HEADB;
    auto toff = [&](int t) { return (unsigned)(t ^ (2 * hp)) * (4 * ROWB); };
    u32x4 ring[4][3];
    auto ld = [&](int slot, int j, __amdgpu_buffer_rsrc_t rr, unsigned off) {
        ring[slot][j] = __builtin_amdgcn_raw_buffer_load_b128(rr, voff + j * 256, off, NT);
        PIN();
    };
    auto ld_tile = [&](int slot, __amdgpu_buffer_rsrc_t rr, unsigned off) { ld(slot, 0, rr, off); ld(slot, 1, rr, off); ld(slot, 2, rr, off); };
    auto refill = [&](int slot, int j, __amdgpu_buffer_rsrc_t rr, unsigned goff) {
        if (j == 0) pace_wait();
        VMCNT(2);
        ld(slot, j, rr, goff);
        if (j == 0) { const unsigned t = (now32() << 8) - 1024 - (off - step); if ((int)(t - kept) > 0) kept = t; }
        if (j == 2) report();
    };
    auto sc_to_lds = [&]() {
        float* dst = s_sc + tid * 16;
#pragma unroll
        for (int u = 0; u < 2; ++u) {
            *reinterpret_cast<f32x4*>(dst + 8 * u) = f32x4{bflo(lsc[0][u]) * SCALE_LOG2, bflo(lsc[1][u]), bflo(lsc[2][u]), bflo(lsc[3][u])};
            *reinterpret_cast<f32x4*>(dst + 8 * u + 4) = f32x4{bfhi(lsc[0][u]) * SCALE_LOG2, bfhi(lsc[1][u]), bfhi(lsc[2][u]), bfhi(lsc[3][u])};
        }
        PIN();
    };

    auto k0 = [&](int t) { pace_wait(); ld_tile(t, rk, woff + toff(t)); };
    load_cs(0);
    pace_wait(); load_cs(1);
    pack_cs(0);
    pace_wait(); load_sc();
    pack_cs(1);
    k0(0); sc_to_lds();
    k0(1);
    VMCNT(3); k0(2);
    VMCNT(3); k0(3);
    if (lane == 0) *(vlds_u32x2*)(s_act + 2 * wave) = u32x2{kept, off};
    BARRIER();
    origin = *(vlds_u32*)(s_sync + 2);

    u32x4 qp[3];
    auto load_q = [&](int h, int j) { qp[j] = *reinterpret_cast<const u32x4*>(s_q + h * DH + 64 * j + 4 * c); };
    load_q(hp, 0); load_q(hp, 1); load_q(hp, 2);
#pragma unroll
    for (int t = 0; t < 2; ++t)
#pragma unroll
        for (int j = 0; j < 3; ++j) tab[2 + t][j] = *reinterpret_cast<const u32x4*>(s_part + wave * (NI * DH) + ((t * 3 + j) * 64 + lane) * 4);

    for (int i = 0; i < NI; ++i) {
        const bool last = i + 1 == NI;
        const int h = 2 * i + hp;
        const unsigned hnext = (i + 1) * (2 * HEADB) + woff;
        const auto rn = __builtin_amdgcn_make_buffer_rsrc((void*)(last ? qv : qk), 0, KVB, 0x00020000);
        float* scp = s_sc + (h * 4 + rw) * 64;
        f32x4 sc[4];
#pragma unroll
        for (int t = 0; t < 4; ++t) sc[t] = *reinterpret_cast<const f32x4*>(scp + (4 * (t ^ (2 * hp)) + r) * 4);
        float s[4];
#pragma unroll
        for (int t = 0; t < 4; ++t) {
            f32x2 dot, wsum;
#pragma unroll
            for (int j = 0; j < 3; ++j) {
                f32x2 w01, w23, k01, k23;
                dot_cvt(qp[j], tab[t][j], ring[t][j], w01, w23, k01, k23);
                if (j == 0) { wsum = w01 + w23; dot = k01 * w01; } else { wsum += w01; wsum += w23; dot += k01 * w01; }
                dot += k23 * w23;
                asm volatile("" : "+v"(dot), "+v"(wsum));
                PIN();
                if (t == 3) load_q(last ? h : h + 2, j);
                refill(t, j, rn, (last ? woff : hnext) + toff(t));
            }
            const float a = (dot[0] + dot[1]) - sc[t][1] * (wsum[0] + wsum[1]);
            s[t] = row16_allsum(a) * sc[t][0];
        }
        const float mx = xrow_max(fmaxf(fmaxf(s[0], s[1]), fmaxf(s[2], s[3])));
        float l = 0.f, z = 0.f;
#pragma unroll
        for (int t = 0; t < 4; ++t) {
            const float p = __builtin_amdgcn_exp2f(s[t] - mx);
            l += p;
            s[t] = p * sc[t][2];
            z += s[t] * sc[t][3];
        }
        l = xrow_sum(l);
        z = xrow_sum(z);
        if (c == 0) {
            *reinterpret_cast<f32x4*>(scp + r * 4) = f32x4{s[0], s[1], s[2], s[3]};
            *reinterpret_cast<f32x4*>(scp + 16) = f32x4{mx, l, z, 0.f};
        }
        PIN();
    }

    const float* pwp = s_sc + (hp * 4 + rw) * 64 + r * 4;
    f32x4 pw = *reinterpret_cast<const f32x4*>(pwp);
    unsigned tag, lw, lo;
    f32x4 lf;
    f32x2 lz;
    unsigned lp = (hp * 4 * NI + NI - 1) * DH + (lane & 31) * 6;
    f32x2 acc[3][2];
    auto v_acc = [&](int t, int j) {
        const f32x2 p2 = {pw[t], pw[t]};
        const u32x4 vv = ring[t][j];
        const f32x2 v01 = {(float)(int)vv[0], (float)(int)vv[1]}, v23 = {(float)(int)vv[2], (float)(int)vv[3]};
        if (t == 0) { acc[j][0] = p2 * v01; acc[j][1] = p2 * v23; }
        else { acc[j][0] += p2 * v01; acc[j][1] += p2 * v23; }
        asm volatile("" : "+v"(acc[j][0]), "+v"(acc[j][1]));
        PIN();
    };
    auto v_fold = [&](int i, int j) {
        const float x = swap32_add(acc[j][0][0], acc[j][1][0]);
        const float y = swap32_add(acc[j][0][1], acc[j][1][1]);
        s_part[(wave * NI + i) * DH + 64 * j + 4 * c + r] = swap16_add(x, y);
    };
    auto v_done = [&](int i) {
        PIN();
        if (lane == 0) *(vlds_u32*)(s_done + wave) = i + 1;
        PIN();
    };
    constexpr int LS = 2;
#pragma unroll
    for (int t = 0; t < LS; ++t)
#pragma unroll
        for (int j = 0; j < 3; ++j) v_acc(t, j);
    for (int i = 0; i < NI - 1; ++i) {
        const unsigned hnext = (i + 1) * (2 * HEADB) + woff;
#pragma unroll
        for (int k = 0; k < 4; ++k) {
            const int t = (k + LS) & 3;
#pragma unroll
            for (int j = 0; j < 3; ++j) {
                v_acc(t, j);
                if (t == 3 && j == 2) pw = *reinterpret_cast<const f32x4*>(pwp + (i + 1) * 512);
                refill(k, j, rv, hnext + toff(k));
            }
            if (t == 3) { v_fold(i, 0); v_fold(i, 1); v_fold(i, 2); v_done(i); }
        }
    }
    asm volatile("s_branch .Lkv_tail_%=\n\t.p2align 12\n.Lkv_tail_%=:" ::: "memory");
    while (__builtin_amdgcn_readfirstlane(*(vlds_u32*)(s_sync + 3)) == 0) __builtin_amdgcn_s_sleep(1);
    PIN();
    tag = s_sync[4 + (lane & 3)];
    lf = *reinterpret_cast<const f32x4*>(s_f + (2 * (NI - 1) + hp) * 8);
    lz = *reinterpret_cast<const f32x2*>(s_f + (2 * (NI - 1) + hp) * 8 + 4);
    origin = *(vlds_u32*)(s_sync + 2);
    lw = last_word(tag, lz[1], lane & 31);
    lo = lane < 32 ? piece_off(hp, lane & 31) : OOB;
    asm volatile("" : "+v"(tag), "+v"(lf), "+v"(lz), "+v"(lw), "+v"(lo), "+v"(lp), "+v"(origin));
    PIN();
#pragma unroll
    for (int t = LS; t < 4; ++t)
#pragma unroll
        for (int j = 0; j < 3; ++j) { v_acc(t, j); if (t == 3) { v_fold(NI - 1, j); PIN(); } }
    v_done(NI - 1);
    {
        const u32x4 dn = *(vlds_u32x4*)(s_done + 4 * hp);
        PIN();
        const u32x4 v = merge(s_part + lp, lf, lz[0], lw);
        if (__builtin_amdgcn_readfirstlane(dn[0] & dn[1] & dn[2] & dn[3]) == NI) publish(v, NI - 1, lo);
        PIN();
    }
    {
        unsigned inbox = WS_PART + (bid >> 4) * GSTR + (bid & 15) * UNITB, obase = bid * 48;
        unsigned xo = tid * 16, yo = tid * 16 + 8192, sel = lane & 1 ? 0x01000504u : 0x05040100u;
        unsigned bo = lane < 16 ? wave * 1024 + (lane & 7) * 128 + (lane >> 3) * 8192 : OOB;
        typedef __attribute__((address_space(3))) float lds_f;
        lds_f* wo = (lds_f*)s_red + wave * 28 + (r < 3 ? c * 6 + r * 2 : 24);
        lds_f* fo = (lds_f*)s_red + (lane & 7) * 28 + 24;
        lds_f* go = (lds_f*)s_red + (lane & 7) * 28 + min(lane >> 3, 5) * 4;
        vlds_u32* dw = (vlds_u32*)(s_done + wave);
        vlds_u32* dr = (vlds_u32*)(s_done + (lane & 7));
        unsigned oo = (lane & 7) == 0 && lane < 48 ? lane : OOB, eo = bid == 0 && lane < 4 ? lane * 2 : OOB;
        asm volatile("" : "+s"(inbox), "+s"(obase), "+v"(xo), "+v"(yo), "+v"(sel), "+v"(bo), "+v"(wo), "+v"(fo), "+v"(go), "+v"(oo), "+v"(eo), "+v"(dw), "+v"(dr));
        const unsigned o = __builtin_amdgcn_readfirstlane(origin) - (15 - (bid >> 4)) * 512;
        while ((int)((now32() << 8) - o) < pace.rd) __builtin_amdgcn_s_sleep(1);
        PIN();
        u32x4 x, y;
        do {
            x = __builtin_amdgcn_raw_buffer_load_b128(rw_, xo, inbox, DEV);
            y = __builtin_amdgcn_raw_buffer_load_b128(rw_, yo, inbox, DEV);
            const unsigned age = now32() - tstart;
            PIN();
            if (age > SPIN_LIMIT) break;
        } while (__builtin_amdgcn_ballot_w64(((x[3] ^ tag) | (y[3] ^ tag)) > 0xffffu) != 0);
        __builtin_amdgcn_raw_buffer_store_b32(0u, rw_, bo, inbox, DEV);
        PIN();
        const float lx = __uint_as_float(__builtin_amdgcn_perm(__builtin_amdgcn_mov_dpp(x[3], 0xb1, 0xf, 0xf, true), x[3], sel));
        const float ly = __uint_as_float(__builtin_amdgcn_perm(__builtin_amdgcn_mov_dpp(y[3], 0xb1, 0xf, 0xf, true), y[3], sel));
        float M = fmaxf(lx, ly);
        M = fmaxf(M, dpp<0x128>(M));
        M = fmaxf(M, dpp<0x124>(M));
        M = xrow_max(M);
        const float w0 = __builtin_amdgcn_exp2f(lx - M), w1 = __builtin_amdgcn_exp2f(ly - M);
        float a[8];
#pragma unroll
        for (int k = 0; k < 3; ++k) {
            a[2 * k] = w0 * bflo(x[k]) + w1 * bflo(y[k]);
            a[2 * k + 1] = w0 * bfhi(x[k]) + w1 * bfhi(y[k]);
        }
        a[6] = a[7] = w0 + w1;
        float b[4], d[2];
#pragma unroll
        for (int k = 0; k < 4; ++k) b[k] = swap32_add(a[k], a[4 + k]);
#pragma unroll
        for (int k = 0; k < 2; ++k) d[k] = swap16_add(b[k], b[2 + k]);
#pragma unroll
        for (int k = 0; k < 2; ++k) { d[k] += dpp<0x128>(d[k]); d[k] += dpp<0x124>(d[k]); }
        if (c < 4) {
            *(__attribute__((address_space(3))) f32x2*)wo = f32x2{d[0], r < 3 ? d[1] : M};
            PIN();
            *dw = 3 * NI;
        }
        PIN();
        const unsigned fin = *dr;
        PIN();
        f32x2 sm = *(__attribute__((address_space(3))) f32x2*)fo;
        f32x4 ov = *(__attribute__((address_space(3))) f32x4*)go;
        asm volatile("" : "+v"(sm), "+v"(ov));
        if (__builtin_amdgcn_ballot_w64(fin != 3 * NI) == 0) {
            float MM = fmaxf(sm[1], dpp<0xb1>(sm[1]));
            MM = fmaxf(MM, dpp<0x4e>(MM));
            MM = fmaxf(MM, dpp<0x141>(MM));
            const float ff = __builtin_amdgcn_exp2f(sm[1] - MM);
            float e[5] = {ff * ov[0], ff * ov[1], ff * ov[2], ff * ov[3], ff * sm[0]};
#pragma unroll
            for (int k = 0; k < 5; ++k) e[k] += dpp<0xb1>(e[k]);
#pragma unroll
            for (int k = 0; k < 5; ++k) e[k] += dpp<0x4e>(e[k]);
#pragma unroll
            for (int k = 0; k < 5; ++k) e[k] += dpp<0x141>(e[k]);
            const float inv = __builtin_amdgcn_rcpf(e[4]);
            __builtin_amdgcn_raw_buffer_store_b64(u32x2{pk_bf16(f32x2{e[0] * inv, e[1] * inv}), pk_bf16(f32x2{e[2] * inv, e[3] * inv})}, ro, oo, obase, 0);
            if (bid == 0) __builtin_amdgcn_raw_buffer_store_b16((unsigned short)(tag >> 16), rw_, eo, WS_EPOCH, DEV);
        }
    }
    __builtin_amdgcn_endpgm();
}

extern "C" void kernel_launch(void* const* d_in, const int* in_sizes, int n_in,
                              void* d_out, int out_size, void* d_ws, size_t ws_size,
                              hipStream_t stream) {
    const float t_us = 102.5f, start_us = 0.5f, end_us = start_us + t_us * (130.f / 128.f);
    Pace pace;
    pace.step = (unsigned)(t_us * 25600.f / 128.f);
    pace.start = (int)(start_us * 25600.f);
    pace.offset = 0;
    pace.bias = (int)(0.05f * 25600.f);
    pace.rd = (int)((end_us + 1.6f) * 25600.f);
    pace.pub = (int)((end_us + 0.6f) * 25600.f);
    kv_sweep_kernel<<<NWG, 576, 0, stream>>>(d_in[0], d_in[1], d_in[2], d_in[3], d_in[4], d_in[5], d_in[6], d_in[7],
        d_in[8], d_in[9], d_in[10], d_ws, d_out, pace);
}
```

```cpp
#include <hip/hip_runtime.h>
#include <hip/hip_bf16.h>
#include <cstdint>
#include <type_traits>

using bf16 = __hip_bfloat16;
using u32x4 = __attribute__((ext_vector_type(4))) unsigned;
using u32x3 = __attribute__((ext_vector_type(3))) unsigned;
using u32x2 = __attribute__((ext_vector_type(2))) unsigned;
using u64x8 = __attribute__((ext_vector_type(8))) unsigned long long;
using f32x2 = __attribute__((ext_vector_type(2))) float;
using f32x4 = __attribute__((ext_vector_type(4))) float;
using bf16x2 = __attribute__((ext_vector_type(2))) __bf16;

constexpr int DH = 192;
constexpr int HALF = 96;
constexpr int NH = 32;
constexpr int SKV = 16384;
constexpr int NWG = 256;
constexpr int NI = 16;
constexpr unsigned ROWB = DH * 4;
constexpr unsigned HEADB = SKV * ROWB;
constexpr unsigned KVB = NH * HEADB;
constexpr unsigned CSB = SKV * DH * 2;
constexpr unsigned OOB = 0x80000000u;
constexpr float SCALE_LOG2 = 1.4426950408889634f * 0.07216878364870322f;
constexpr unsigned SPIN_LIMIT = 20000000u;

constexpr unsigned UNITB = NWG * 64;
constexpr unsigned GSTR = 16 * UNITB + 256;
constexpr unsigned WS_PART = 0;
constexpr unsigned WS_EPOCH = NWG * (UNITB + 1024);
constexpr unsigned WS_REP = WS_EPOCH + 256;
constexpr unsigned WS_PROBE = WS_REP + 256 * 512;
constexpr unsigned WS_BYTES = WS_PROBE + 4096;

__device__ __forceinline__ float bflo(unsigned u) { return __uint_as_float(u << 16); }
__device__ __forceinline__ float bfhi(unsigned u) { return __uint_as_float(u & 0xffff0000u); }
__device__ __forceinline__ float rbf(float x) { return __bfloat162float(__float2bfloat16(x)); }
__device__ __forceinline__ unsigned pk_bf16(f32x2 a) { return __builtin_bit_cast(unsigned, __builtin_convertvector(a, bf16x2)); }
__device__ __forceinline__ void dot_cvt(const u32x4 q, const u32x4 t, const u32x4 ki, f32x2& w01, f32x2& w23, f32x2& k01, f32x2& k23) {
    float w0, w1, w2, w3;
    float k0 = __uint_as_float(ki[0]), k1 = __uint_as_float(ki[1]), k2 = __uint_as_float(ki[2]), k3 = __uint_as_float(ki[3]);
    asm("v_dot2_f32_bf16 %0, %8, %12, 0\n\tv_dot2_f32_bf16 %1, %9, %13, 0\n\tv_dot2_f32_bf16 %2, %10, %14, 0\n\tv_dot2_f32_bf16 %3, %11, %15, 0\n\t"
        "v_cvt_f32_i32 %4, %4\n\tv_cvt_f32_i32 %5, %5\n\tv_cvt_f32_i32 %6, %6\n\tv_cvt_f32_i32 %7, %7"
        : "=&v"(w0), "=&v"(w1), "=&v"(w2), "=&v"(w3), "+v"(k0), "+v"(k1), "+v"(k2), "+v"(k3)
        : "v"(q[0]), "v"(q[1]), "v"(q[2]), "v"(q[3]), "v"(t[0]), "v"(t[1]), "v"(t[2]), "v"(t[3]));
    w01 = f32x2{w0, w1}; w23 = f32x2{w2, w3}; k01 = f32x2{k0, k1}; k23 = f32x2{k2, k3};
}

template <int CTRL> __device__ __forceinline__ float dpp(float x) {
    return __builtin_bit_cast(float, __builtin_amdgcn_mov_dpp(__builtin_bit_cast(int, x), CTRL, 0xf, 0xf, true));
}
__device__ __forceinline__ float row16_allsum(float x) {
    x += dpp<0x128>(x);
    x += dpp<0x124>(x);
    x += dpp<0x122>(x);
    x += dpp<0x121>(x);
    return x;
}
__device__ __forceinline__ float row16_allmax(float x) {
    x = fmaxf(x, dpp<0x128>(x));
    x = fmaxf(x, dpp<0x124>(x));
    x = fmaxf(x, dpp<0x122>(x));
    x = fmaxf(x, dpp<0x121>(x));
    return x;
}
__device__ __forceinline__ float xrow_max(float x) {
    auto s = __builtin_amdgcn_permlane16_swap(__float_as_uint(x), __float_as_uint(x), false, false);
    x = fmaxf(__uint_as_float(s[0]), __uint_as_float(s[1]));
    auto t = __builtin_amdgcn_permlane32_swap(__float_as_uint(x), __float_as_uint(x), false, false);
    return fmaxf(__uint_as_float(t[0]), __uint_as_float(t[1]));
}
__device__ __forceinline__ float xrow_sum(float x) {
    auto s = __builtin_amdgcn_permlane16_swap(__float_as_uint(x), __float_as_uint(x), false, false);
    x = __uint_as_float(s[0]) + __uint_as_float(s[1]);
    auto t = __builtin_amdgcn_permlane32_swap(__float_as_uint(x), __float_as_uint(x), false, false);
    return __uint_as_float(t[0]) + __uint_as_float(t[1]);
}
__device__ __forceinline__ float swap32_add(float a, float b) {
    auto t = __builtin_amdgcn_permlane32_swap(__float_as_uint(a), __float_as_uint(b), false, false);
    return __uint_as_float(t[0]) + __uint_as_float(t[1]);
}
__device__ __forceinline__ float swap16_add(float a, float b) {
    auto t = __builtin_amdgcn_permlane16_swap(__float_as_uint(a), __float_as_uint(b), false, false);
    return __uint_as_float(t[0]) + __uint_as_float(t[1]);
}
__device__ __forceinline__ int row16_sum(int x) {
    x += __builtin_amdgcn_mov_dpp(x, 0x128, 0xf, 0xf, true);
    x += __builtin_amdgcn_mov_dpp(x, 0x124, 0xf, 0xf, true);
    x += __builtin_amdgcn_mov_dpp(x, 0x122, 0xf, 0xf, true);
    x += __builtin_amdgcn_mov_dpp(x, 0x121, 0xf, 0xf, true);
    return x;
}
__device__ __forceinline__ unsigned now32() { return (unsigned)__builtin_amdgcn_s_memrealtime(); }

#define PLAN_EXTRA
#define NSTAMP 12
#define STAMP(i)
#define TRACE(i)
#define HSTAMP(i)
#define PSTAMP(i)
#define PIN() do { asm volatile("" ::: "memory"); __builtin_amdgcn_sched_barrier(0); } while (0)
#define BARRIER() do { asm volatile("s_waitcnt lgkmcnt(0)" ::: "memory"); __builtin_amdgcn_s_barrier(); PIN(); } while (0)
#define VMCNT(n) asm volatile("s_waitcnt vmcnt(%0)" :: "i"(n) : "memory")

typedef volatile __attribute__((address_space(3))) unsigned vlds_u32;
typedef volatile __attribute__((address_space(3))) u32x2 vlds_u32x2;
typedef volatile __attribute__((address_space(3))) u32x4 vlds_u32x4;

struct Plan { const char* rows; const char* phase; int hx; char sx, tx; };
constexpr Plan PLANS[] = {
    {"1234tcab0s", "vwabc01234h", 2}, {"432cb1ts0a", "1vwacb4320h", 0}, {"123cb0ts4a", "vwabc01234h", 0}, {"103cb2ts4a", "vwabc01234h", 0},
    {"0234catb1s", "vwabc01234h", 2}, {"013cb2ta4s", "vwabc01234h", 0}, {"1234tcab0s", "vwabc01234h", 0}, {"123cb0st4a", "vwabc01234h", 0},
    {"1234tcab0s", "vwabc01234h", 2, 'c'}, {"432cb1ts0a", "1vwacb4320h", 0, 'b', 'c'}, {"123cb0ts4a", "vwabc01234h", 0, 'b', 'c'}, {"0123t4cabs", "vwabc01234h", 2},
    {"1234t0cabs", "vwabc01234h", 2}, {"1234tcab0s", "vwabc01234h", 2, 'c', 'a'},
    PLAN_EXTRA
};
constexpr int sym_pos(const char* s, char c) { for (int i = 0; s[i]; ++i) if (s[i] == c) return i; return -1; }
template <int N> __device__ __forceinline__ int place(const char* dst, const char (&src)[N], int x) {
    int y = 0;
#pragma unroll
    for (int i = 0; i < N - 1; ++i) y |= (x >> i & 1) << sym_pos(dst, src[i]);
    return y;
}
struct Pace {
    unsigned step;
    int start;
    int offset;
    int bias;
    int rd;
    int pub;
};

template <int X, int OPT, int GATE, int TP, int SLP>
__global__ __launch_bounds__(576) __attribute__((aligned(4096))) void kv_sweep_kernel(
    const void*, const void*, const void*, const void*, const void*, const void*, const void*, const void*,
    const void*, const void*, const void*, void*, void*, Pace)
{
    __shared__ __attribute__((aligned(16))) unsigned s_q[NH * DH];
    __shared__ __attribute__((aligned(16))) unsigned short s_sc[NH * 256];
    __shared__ __attribute__((aligned(16))) float s_w[NH * 4 * 20];
    __shared__ __attribute__((aligned(16))) float s_part[8 * NI * DH];
    __shared__ __attribute__((aligned(16))) float s_red[8 * 28];
    __shared__ __attribute__((aligned(16))) float s_f[NH * 8];
    __shared__ __attribute__((aligned(16))) unsigned s_done[8];
    __shared__ __attribute__((aligned(16))) unsigned s_sync[8];
    __shared__ __attribute__((aligned(16))) unsigned s_act[16];

    constexpr int SLIP = GATE / 10 % 10 * 2560, HOLD = GATE % 10;
    constexpr int DRY = OPT % 10 & 1, LS = 2, PAD = OPT % 10 / 2 & 1, PF = OPT / 10 % 10, RST = GATE / 1000 % 10 * 256, SLEAD = GATE / 10000 % 10, NB = GATE / 100 % 10 * 2, PER = GATE / 100000 % 10, STA = 16, WIN = 0, OST = OPT / 100 % 10 == 0 ? 0 : OPT / 100 % 10 == 1 ? 16 : OPT / 100 % 10 == 2 ? 17 : 18, PRO = OPT / 1000;
    unsigned long long pc0, t0;
    u64x8 ka, kb;
    asm volatile("s_load_dwordx16 %3, %4, 0x40\n\ts_load_dwordx16 %2, %4, 0x0\n\ts_memrealtime %1\n\ts_getpc_b64 %0\n.Lkv_beg_%5_%6_%7_%8:"
                 : "=&s"(pc0), "=&s"(t0), "=&s"(ka), "=&s"(kb) : "s"(__builtin_amdgcn_kernarg_segment_ptr()), "i"(X), "i"(OPT), "i"(GATE), "i"(TP));
    __builtin_amdgcn_sched_barrier(0);
    const int tid = threadIdx.x;
    const int lane = tid & 63;
    int wave = __builtin_amdgcn_readfirstlane(tid >> 6);
    const int r = lane >> 4;
    const int c = lane & 15;
    const int bid = blockIdx.x;
    const int m = (bid & 7) * 32 + (bid >> 3);
    const int rw = wave & 3, hp = wave >> 2;
    constexpr Plan PL = PLANS[GATE / 1000000];
    constexpr int HX = PL.hx, RS = 16 << sym_pos(PL.rows, 's') | (PL.sx ? 16 << sym_pos(PL.rows, PL.sx) : 0), RT = 16 << sym_pos(PL.rows, 't') | (PL.tx ? 16 << sym_pos(PL.rows, PL.tx) : 0);
    const int grow = place(PL.rows, "abc01234", bid) * 16;
    auto srow = [&](int t) { return (t & 1) * RS ^ (t >> 1) * RT; };
    const int row0 = grow + rw * 4;
    unsigned co = r * 384 + c * 8, so1 = r * 384 + (c < 8 ? c * 8 + 320 : c * 8 - 64), rb0 = (row0 ^ RT * hp) * 384u;
    asm volatile("s_waitcnt lgkmcnt(0)" : "+s"(t0), "+s"(ka), "+s"(kb), "+v"(co), "+v"(so1), "+s"(rb0), "+s"(wave));
    typedef const __attribute__((address_space(1))) char* gptr;
    void* const q = (void*)ka[0]; void* const qk = (void*)ka[1]; void* const qv = (void*)ka[4]; void* const q_cos = (void*)ka[7];
    const gptr ksc = (gptr)ka[2], kzp = (gptr)ka[3], vsc = (gptr)ka[5], vzp = (gptr)ka[6];
    void* const q_sin = (void*)kb[0]; void* const k_cos = (void*)kb[1]; void* const k_sin = (void*)kb[2]; void* const out = (void*)kb[4];
    unsigned char* const ws = (unsigned char*)(__attribute__((address_space(1))) unsigned char*)kb[3];
    const Pace pace = {(unsigned)kb[5], (int)(kb[5] >> 32), (int)kb[6], (int)(kb[6] >> 32), (int)kb[7], (int)(kb[7] >> 32)};
    const unsigned tstart = (unsigned)t0;
    const unsigned tsched = TP & 16 ? now32() : tstart;
    PSTAMP(69);
    const auto rw_ = __builtin_amdgcn_make_buffer_rsrc((void*)ws, 0, WS_BYTES, 0x00020000);
    const auto ro = __builtin_amdgcn_make_buffer_rsrc((void*)out, 0, NH * DH * 2, 0x00020000);

    auto merge = [&](const float* p, f32x4 f, float zs, unsigned w3) {
        f32x2 u[4][3], a[3];
#pragma unroll
        for (int w = 0; w < 4; ++w)
#pragma unroll
            for (int k = 0; k < 3; ++k) u[w][k] = *reinterpret_cast<const f32x2*>(p + w * NI * DH + 2 * k);
        asm volatile("" : "+v"(u[0][0]), "+v"(u[0][1]), "+v"(u[0][2]), "+v"(u[1][0]), "+v"(u[1][1]), "+v"(u[1][2]),
                          "+v"(u[2][0]), "+v"(u[2][1]), "+v"(u[2][2]), "+v"(u[3][0]), "+v"(u[3][1]), "+v"(u[3][2]));
#pragma unroll
        for (int k = 0; k < 3; ++k) a[k] = (f[0] * u[0][k] - zs + f[1] * u[1][k]) + (f[2] * u[2][k] + f[3] * u[3][k]);
        return u32x4{pk_bf16(a[0]), pk_bf16(a[1]), pk_bf16(a[2]), w3};
    };
    auto last_word = [&](unsigned tag, float ls, int c6) { return tag | ((__float_as_uint(ls) >> (16 * (c6 & 1))) & 0xffffu); };
    auto piece_off = [&](int hp, int c6) { return (hp * 8 + (c6 >> 2)) * UNITB + (c6 & 3) * 16; };
    auto publish = [&](u32x4 v, int i, unsigned vo) {
        __builtin_amdgcn_raw_buffer_store_b128(v, rw_, vo, WS_PART + i * GSTR + m * 64, STA);
        asm volatile("s_nop 1" :: "v"(v) : "memory");
    };

    if (wave == 8) {
        if (TP & 32) __builtin_amdgcn_s_setprio(3);
        HSTAMP(11);
        unsigned clen;
        asm volatile("s_mov_b32 %0, .Lkv_end_%1_%2_%3_%4-.Lkv_beg_%1_%2_%3_%4" : "=s"(clen) : "i"(X), "i"(OPT), "i"(GATE), "i"(TP));
        const auto rcode = __builtin_amdgcn_make_buffer_rsrc((void*)(pc0 & ~127ull), 0, clen, 0x00020000);
        const int jj = bid >> 3;
        auto warm_ld = [&](int n) {
            return __builtin_amdgcn_raw_buffer_load_b32(rcode, lane < n ? (jj * 9 + lane) * 64 : OOB, 0, 0);
        };
        auto warm_code = [&](int n) { if (n) { const unsigned x = warm_ld(n); asm volatile("" :: "v"(x)); } };
        constexpr int WN[10][4] = {{0, 0, 0, 0}, {64, 0, 0, 0}, {9, 0, 0, 0}, {64, 9, 9, 0}, {9, 9, 9, 0}, {18, 0, 0, 0}, {18, 18, 18, 0}, {9, 9, 0, 9}, {36, 0, 0, 0}, {9, 0, 9, 0}};
        auto touch_ld = [&](bool on) {
            return __builtin_amdgcn_raw_buffer_load_b32(__builtin_amdgcn_make_buffer_rsrc((void*)(jj < NI ? qk : qv), 0, KVB, 0x00020000),
                on && lane < 13 ? min(lane * (2u << 20) + (bid & 7) * 4096, 2 * HEADB - 4) : OOB, (jj & 15) * 2 * HEADB, 2);
        };
        auto touch_kv = [&]() { const unsigned x = touch_ld(true); asm volatile("" :: "v"(x)); };
        auto touch_ws = [&]() {
            const unsigned x = __builtin_amdgcn_raw_buffer_load_b32(rw_, c * GSTR, WS_PART + 16 * UNITB, 0);
            asm volatile("" :: "v"(x));
        };
        gptr scp = ((lane & 4) ? ((lane & 2) ? vzp : vsc) : ((lane & 2) ? kzp : ksc)) + ((lane >> 5) * SKV + (grow ^ srow(lane >> 3 & 3)) + (lane & 1) * 8) * 2;
        const unsigned x0 = warm_ld(WN[PF][0]);
        PIN();
        constexpr int SI = SLEAD == 9 ? 320 : SLEAD == 8 ? 160 : SLEAD * 10;
        unsigned sdst = (unsigned)(unsigned long long)(__attribute__((address_space(3))) unsigned short*)s_sc;
        auto sc_dma = [&]() {
            asm volatile("s_mov_b32 m0, %1\n\ts_nop 0\n\tglobal_load_lds_dwordx4 %0, off nt" :: "v"(scp), "s"(sdst) : "memory");
            scp += 4 * SKV; sdst += 1024;
        };
        if (TP >> 6) while (now32() - tsched < 50 + (TP >> 6) * 50u) __builtin_amdgcn_s_sleep(1);
        u32x3 lc[2], ls[2];
        u32x4 qraw[12];
        {
            const auto rq = __builtin_amdgcn_make_buffer_rsrc((void*)q, 0, NH * DH * 2, 0x00020000);
            const auto rqc = __builtin_amdgcn_make_buffer_rsrc((void*)q_cos, 0, DH * 2, 0x00020000);
            const auto rqs = __builtin_amdgcn_make_buffer_rsrc((void*)q_sin, 0, DH * 2, 0x00020000);
#pragma unroll
            for (int k = 0; k < 12; ++k) qraw[k] = __builtin_amdgcn_raw_buffer_load_b128(rq, lane * 16 + (k & 3) * 1024, (k >> 2) * 4096, 0);
#pragma unroll
            for (int k = 0; k < 2; ++k) {
                lc[k] = __builtin_amdgcn_raw_buffer_load_b96(rqc, c * 12 + k * 192, 0, 0);
                ls[k] = __builtin_amdgcn_raw_buffer_load_b96(rqs, c * 12 + k * 192, 0, 0);
            }
            PIN();
        }
        const unsigned tag = ((unsigned)__builtin_amdgcn_raw_buffer_load_b16(rw_, (lane & 3) * 2, WS_EPOCH, 16) + 1u) << 16;
        bool touched = !(TP & 1) || jj < 3;
        const unsigned x1 = TP & 1 ? touch_ld(jj < 3) : 0;
        PIN();
        HSTAMP(2);
        unsigned* const s_raw = reinterpret_cast<unsigned*>(s_part) + NI * DH / 2;
#pragma unroll
        for (int k = 0; k < 12; ++k) *reinterpret_cast<u32x4*>(s_raw + k / 6 * (NI * DH) + k % 6 * 256 + lane * 4) = qraw[k];
        f32x2 fc[2][3], fs[2][3];
#pragma unroll
        for (int k = 0; k < 2; ++k)
#pragma unroll
            for (int u = 0; u < 3; ++u) { fc[k][u] = f32x2{bflo(lc[k][u]), bfhi(lc[k][u])}; fs[k][u] = f32x2{bflo(ls[k][u]), bfhi(ls[k][u])}; }
        PIN();
        HSTAMP(3);
#pragma nounroll
        for (int it = 0; it < 4; ++it) {
            if (it < NB) sc_dma();
            if (it + 4 < NB) sc_dma();
            if (it + 8 < NB) sc_dma();
            if (it + 12 < NB) sc_dma();
            unsigned qa[2][3], qb[2][3];
#pragma unroll
            for (int g = 0; g < 2; ++g)
#pragma unroll
                for (int u = 0; u < 3; ++u) {
                    const unsigned* src = s_raw + g * (NI * DH) + (it * 4 + r) * HALF + 3 * c;
                    qa[g][u] = src[u]; qb[g][u] = src[HALF / 2 + u];
                }
#pragma unroll
            for (int g = 0; g < 2; ++g)
#pragma unroll
                for (int u = 0; u < 3; ++u) {
                    const f32x2 a = {bflo(qa[g][u]), bfhi(qa[g][u])}, b = {bflo(qb[g][u]), bfhi(qb[g][u])};
                    const f32x2 o0 = a * fc[0][u] - b * fs[0][u], o1 = b * fc[1][u] + a * fs[1][u];
                    const unsigned l0 = pk_bf16(f32x2{o0[0], o1[0]}), l1 = pk_bf16(f32x2{o0[1], o1[1]});
                    unsigned* dst = s_q + (16 * g + 4 * it + r) * DH + 6 * c + 2 * u;
                    *reinterpret_cast<u32x2*>(dst) = u32x2{l0, l1};
                    *reinterpret_cast<u32x2*>(dst + HALF) = u32x2{__builtin_amdgcn_alignbit(l0, l0, 16) ^ 0x80000000u, __builtin_amdgcn_alignbit(l1, l1, 16) ^ 0x80000000u};
                }
        }
        HSTAMP(1);
        asm volatile("" :: "v"(x0), "v"(x1));
        if (TP & 32) __builtin_amdgcn_s_setprio(0);
        if (TP & 2) touch_ws();
        if (TP & 4) __builtin_amdgcn_raw_buffer_store_b16((unsigned short)0, ro, lane < 24 ? lane * 2 : OOB, bid * 48, OST);
        unsigned origin = tsched << 8;
        if (lane < 8) { s_done[lane] = 0; s_sync[lane] = lane < 3 ? origin : lane < 4 ? NB : tag; }
        PIN();
        VMCNT(0);
        BARRIER();
        const int mh = lane >> 5, mc = lane & 31;
        const unsigned rtag = __builtin_amdgcn_readlane(tag, 0) | __builtin_amdgcn_readlane(tag, 1) >> 16;
        constexpr int RPX = 2;
        constexpr int KS = X / 1000 % 10, SR = X / 10000 % 10 * 2, IB2 = X / 100000 % 10 * 128, EHI = X / 1000000 % 10 * 512, IB = X / 10000000 % 10 * 256, FR = X / 100000000 + 10;
        constexpr int RSH = X / 100 % 10;
        constexpr unsigned RDLY = 200;
        constexpr int UP = X / 10 % 10 ? X / 10 % 10 * 82 : 1 << 24, DN = X % 10 ? X % 10 * 82 : 1 << 24;
        const int rj = ((bid >> 3) + 5 * (bid & 7)) & 31;
        const int rid = rj % (32 / RPX) == 0 ? rj / (32 / RPX) * 8 + (bid & 7) : -1;
        unsigned wround = now32() >> RSH, rround = wround, tick = 0, target = origin;
        bool warmed = false, synced = false;
        int drift = 0;
        const unsigned round0 = rround;
        int i = 0, sci = NB;
        unsigned sct = now32() - SI;
        auto kept_now = [&](unsigned now) {
            const u32x2 a = *(vlds_u32x2*)(s_act + 2 * (lane & 7));
            const int late = max((int)(a[0] - origin), (int)((now << 8) - a[1] - origin));
            return origin + (__builtin_amdgcn_readfirstlane(row16_sum(late)) >> 4);
        };
        auto steer = [&](unsigned now) {
            if ((now >> 5) != tick) {
                tick = now >> 5;
                target += drift;
                origin += max(-DN, min(UP + drift, (int)(target - origin)));
                if (SLIP) { const int l2 = (int)(kept_now(now) - origin) - SLIP; if (l2 > 0) origin += l2; }
                if (HOLD && !synced) target = origin - (HOLD - 1) * 82;
                if (lane == 0) *(vlds_u32*)(s_sync + 2) = origin;
                PIN();
            }
        };
        auto finished = [&]() {
            const u32x4 a = *(vlds_u32x4*)s_done, b = *(vlds_u32x4*)(s_done + 4);
            return __builtin_amdgcn_readfirstlane(min(min(min(a[0], a[1]), min(a[2], a[3])), min(min(b[0], b[1]), min(b[2], b[3]))));
        };
        while (i < NI - 2) {
            const unsigned now = now32();
            if ((now >> RSH) != wround) {
                wround = now >> RSH;
                if (rid >= 0 && (now & ((1u << RSH) - 1)) < 48)
                    __builtin_amdgcn_raw_buffer_store_b64(u32x2{kept_now(now), rtag + wround}, rw_, lane == 0 ? rid * 8 : OOB, WS_REP + (wround & 255) * 512, 16);
                PIN();
                continue;
            }
            if ((int)(((now - RDLY) >> RSH) - rround) > 0) {
                rround = (now - RDLY) >> RSH;
                const u32x2 x = __builtin_amdgcn_raw_buffer_load_b64(rw_, c * 8, WS_REP + (rround & 255) * 512, 16);
                const int d = (int)(x[0] - origin);
                const bool ok = x[1] == rtag + rround && d < (32 << 16) && d > -(32 << 16);
                const int n = __builtin_popcountll(__builtin_amdgcn_ballot_w64(ok));
                if (n >= 48) {
                    const unsigned nt = origin + __builtin_amdgcn_readfirstlane((int)(row16_allsum(ok ? (float)d : 0.f) * 4.f / (float)n)) - pace.bias;
                    if (KS && synced && rround - round0 > SR) drift = max(0, min(drift + ((max(-1024, min((int)(nt - target) + 6 * drift, EHI)) - (rround - round0 < FR ? IB : IB2)) >> (3 + KS)), 400));
                    target = nt + 6 * drift;
                    synced = true;
                    const int lost = min((int)(kept_now(now32()) - origin) - pace.bias, (int)(target - origin));
                    if (lost > 0) origin += lost;
                }
                PIN();
                continue;
            }
            steer(now);
            if (NB < NI && sci <= NI && now - sct >= SI) {
                VMCNT(0);
                if (lane == 0) *(vlds_u32*)(s_sync + 3) = sci;
                if (sci < NI) for (int k = 0; k < PER; ++k) sc_dma();
                sci += PER; sct = now;
                PIN();
                continue;
            }
            if (!touched && (int)((now << 8) - origin) > (jj * 4 - 8) * (int)pace.step) { touch_kv(); touched = true; continue; }
            if (WN[PF][1] && !warmed && (int)((now << 8) - origin) > 60 * (int)pace.step) { warm_code(WN[PF][1]); warmed = true; continue; }
            if (finished() <= (unsigned)i) { __builtin_amdgcn_s_sleep(2); continue; }
            PIN();
            if (i == 0) {
                f32x4 ml[4], f;
#pragma unroll
                for (int w = 0; w < 4; ++w) ml[w] = *reinterpret_cast<const f32x4*>(s_w + (mc * 4 + w) * 20 + 16);
                const float M = fmaxf(fmaxf(ml[0][0], ml[1][0]), fmaxf(ml[2][0], ml[3][0]));
                float L = 0.f, zs = 0.f;
#pragma unroll
                for (int w = 0; w < 4; ++w) { f[w] = __builtin_amdgcn_exp2f(ml[w][0] - M); L += f[w] * ml[w][1]; zs += f[w] * ml[w][2]; }
                const float inv = 1.f / L;
                *reinterpret_cast<f32x4*>(s_f + mc * 8) = f * inv;
                *reinterpret_cast<f32x2*>(s_f + mc * 8 + 4) = f32x2{zs * inv, M + __builtin_amdgcn_logf(L)};
                PIN();
                if (lane == 0) *(vlds_u32*)(s_sync + 3) = NI + 1;
                PIN();
            }
            if (i == NI - 3) { warm_code(WN[PF][2]); if (TP & 8) touch_ws(); }
            const f32x2 zl = *reinterpret_cast<const f32x2*>(s_f + (2 * i + mh) * 8 + 4);
            const u32x4 v = merge(s_part + (mh * 4 * NI + i) * DH + mc * 6, *reinterpret_cast<const f32x4*>(s_f + (2 * i + mh) * 8), zl[0], last_word(tag, zl[1], mc));
            PIN();
            *reinterpret_cast<u32x4*>(s_part + (mh * 4 * NI + i) * DH + mc * 4) = v;
            PIN();
            ++i;
        }
        u32x4 v[NI - 1];
#pragma unroll
        for (int i = 0; i < NI - 2; ++i) v[i] = *reinterpret_cast<const u32x4*>(s_part + (mh * 4 * NI + i) * DH + mc * 4);
        f32x4 f = *reinterpret_cast<const f32x4*>(s_f + (2 * (NI - 2) + mh) * 8);
        f32x2 zl = *reinterpret_cast<const f32x2*>(s_f + (2 * (NI - 2) + mh) * 8 + 4);
        PIN();
#pragma unroll
        for (int i = 0; i < NI - 2; ++i) asm volatile("" : "+v"(v[i]));
        asm volatile("" : "+v"(f), "+v"(zl));
        v[NI - 2] = v[0];
        bool have = false, sent = false;
        while (!(have && sent)) {
            const unsigned now = now32();
            if (!have) {
                steer(now);
                if (finished() > NI - 2) {
                    PIN();
                    v[NI - 2] = merge(s_part + (mh * 4 * NI + NI - 2) * DH + mc * 6, f, zl[0], last_word(tag, zl[1], mc));
                    have = true;
                    warm_code(WN[PF][3]);
                    HSTAMP(5);
                }
            }
            if (!sent && (int)((now << 8) - origin) >= pace.pub) {
                HSTAMP(6);
#pragma unroll
                for (int i = 0; i < NI - 2; ++i) publish(v[i], i, piece_off(mh, mc));
                sent = true;
            }
            if (!(have && sent)) __builtin_amdgcn_s_sleep(1);
            PIN();
        }
        publish(v[NI - 2], NI - 2, piece_off(mh, mc));
        HSTAMP(7);
        asm volatile(".Lkv_end_%0_%1_%2_%3:" :: "i"(X), "i"(OPT), "i"(GATE), "i"(TP));
        return;
    }

    const int ph = place(PL.phase, "abc01234", bid) + place(PL.phase, "vw", rw) + place(PL.phase, "h", hp);
    unsigned off = pace.start + ((pace.step * ph) >> 11);
    unsigned origin = tsched << 8, kept = 0;
    const unsigned step = pace.step;
    auto pace_wait = [&]() {
        const unsigned tgt = __builtin_amdgcn_readfirstlane(origin) + off;
        unsigned now = now32() << 8;
        kept = ((int)(now - tgt) > 0 ? now : tgt) - off;
        while ((int)(tgt - now) > 0) { if (SLP) __builtin_amdgcn_s_sleep(SLP); now = now32() << 8; }
        off += step;
    };
    auto report = [&]() {
        if (lane == 0) *(vlds_u32x2*)(s_act + 2 * wave) = u32x2{kept, off};
        origin = *(vlds_u32*)(s_sync + 2);
        PIN();
    };
#define TRV (kept - (tstart << 8))

    u32x4 psc[2];
    auto pre_sc = [&]() {
        const int g = grow >> 4 & 3;
        const gptr base = g == 0 ? ksc : g == 1 ? kzp : g == 2 ? vsc : vzp;
        const auto rp = __builtin_amdgcn_make_buffer_rsrc((void*)base, 0, NH * SKV * 2, 0x00020000);
#pragma unroll
        for (int k = 0; k < 2; ++k) psc[k] = __builtin_amdgcn_raw_buffer_load_b128(rp, ((tid >> 5) * SKV + (grow & ~63 ^ srow(tid >> 3 & 3))) * 2 + (tid & 7) * 16, k * 16 * SKV * 2, 2);
        PIN();
    };
    auto pre_done = [&]() { asm volatile("" :: "v"(psc[0]), "v"(psc[1])); PIN(); };
    const auto rc = __builtin_amdgcn_make_buffer_rsrc((void*)k_cos, 0, CSB, 0x00020000);
    const auto rs = __builtin_amdgcn_make_buffer_rsrc((void*)k_sin, 0, CSB, 0x00020000);
    u32x2 raw[2][6];
    auto load_cs = [&](int t) {
        const unsigned rb = t == 0 ? rb0 : (row0 ^ RT * hp ^ RS) * 384u;
        raw[t][0] = __builtin_amdgcn_raw_buffer_load_b64(rc, co, rb, 2);
        raw[t][1] = __builtin_amdgcn_raw_buffer_load_b64(rc, co + 128, rb, 2);
        raw[t][2] = __builtin_amdgcn_raw_buffer_load_b64(rc, co + 256, rb, 2);
        raw[t][3] = __builtin_amdgcn_raw_buffer_load_b64(rs, co + 192, rb, 2);
        raw[t][4] = __builtin_amdgcn_raw_buffer_load_b64(rs, so1, rb, 2);
        raw[t][5] = __builtin_amdgcn_raw_buffer_load_b64(rs, co + 64, rb, 2);
        PIN();
    };
    u32x4 tab[4][3];
    auto pack_cs = [&](int t) {
#pragma unroll
        for (int j = 0; j < 3; ++j) {
#pragma unroll
            for (int k = 0; k < 2; ++k) {
                tab[t][j][2 * k] = __builtin_amdgcn_perm(raw[t][3 + j][k], raw[t][j][k], 0x05040100);
                tab[t][j][2 * k + 1] = __builtin_amdgcn_perm(raw[t][3 + j][k], raw[t][j][k], 0x07060302);
            }
            *reinterpret_cast<u32x4*>(s_part + (wave ^ 4) * (NI * DH) + ((t * 3 + j) * 64 + lane) * 4) = tab[t][j];
        }
        PIN();
    };
    const auto rk = __builtin_amdgcn_make_buffer_rsrc((void*)qk, 0, KVB, 0x00020000);
    const auto rv = __builtin_amdgcn_make_buffer_rsrc((void*)qv, 0, KVB, 0x00020000);
    const unsigned voff = r * ROWB + c * 16;
    const unsigned woff = hp * HEADB;
    auto toff = [&](int t) { return (unsigned)(row0 ^ srow(t ^ (HX * hp))) * ROWB; };
    u32x4 ring[4][3];
    auto ld = [&](int slot, int j, __amdgpu_buffer_rsrc_t rr, unsigned off) {
        ring[slot][j] = __builtin_amdgcn_raw_buffer_load_b128(rr, voff + j * 256, off, 2);
        PIN();
    };
    auto ld_tile = [&](int slot, __amdgpu_buffer_rsrc_t rr, unsigned off) { ld(slot, 0, rr, off); ld(slot, 1, rr, off); ld(slot, 2, rr, off); };
    auto refill = [&](int slot, int j, __amdgpu_buffer_rsrc_t rr, unsigned goff) {
        if (j == 0) pace_wait();
        if (WIN == 1) VMCNT(3);
        else if (WIN == 2) { if (j == 0) VMCNT(3); else VMCNT(2); }
        else if (WIN == 3) { if (j == 2) VMCNT(2); else VMCNT(3); }
        else if (WIN == 4) VMCNT(4);
        else VMCNT(2);
        ld(slot, j, rr, goff);
        if (j == 0) { const unsigned t = (now32() << 8) - 1024 - (off - step); if ((int)(t - kept) > 0) kept = t; }
        if (j == 2) report();
    };
    auto k0 = [&](int t) { pace_wait(); ld_tile(t, rk, woff + toff(t)); };
    if (PRO == 0) {
        load_cs(0);
        pace_wait(); load_cs(1); STAMP(1);
        pack_cs(0);
        off += pace.offset;
        k0(0); pack_cs(1);
    } else if (PRO == 2) {
        load_cs(0);
        pace_wait(); load_cs(1); STAMP(1);
        pack_cs(0);
        pace_wait(); pre_sc(); off += pace.offset - step;
        pack_cs(1);
        k0(0); pre_done();
    } else if (PRO == 4) {
        load_cs(0);
        pace_wait(); pre_sc(); off += pace.offset - step; STAMP(1);
        pack_cs(0);
        pace_wait(); load_cs(1);
        pre_done();
        k0(0); pack_cs(1);
    } else {
        pre_sc();
        pace_wait(); load_cs(0); STAMP(1);
        pre_done();
        pace_wait(); load_cs(1); off += pace.offset - step;
        pack_cs(0);
        k0(0); pack_cs(1);
    }
    k0(1);
    VMCNT(3); k0(2);
    STAMP(2);
    VMCNT(3); k0(3);
    if (lane == 0) *(vlds_u32x2*)(s_act + 2 * wave) = u32x2{kept, off};
    BARRIER();
    origin = *(vlds_u32*)(s_sync + 2);
    STAMP(3);

    u32x4 qp[3];
    auto load_q = [&](int h, int j) { qp[j] = *reinterpret_cast<const u32x4*>(s_q + h * DH + 64 * j + 4 * c); };
    load_q(hp, 0); load_q(hp, 1); load_q(hp, 2);
#pragma unroll
    for (int t = 0; t < 2; ++t)
#pragma unroll
        for (int j = 0; j < 3; ++j) tab[2 + t][j] = *reinterpret_cast<const u32x4*>(s_part + wave * (NI * DH) + ((t * 3 + j) * 64 + lane) * 4);
    if (HX == 0 && hp) {
#pragma unroll
        for (int t = 0; t < 2; ++t)
#pragma unroll
            for (int j = 0; j < 3; ++j)
#pragma unroll
                for (int k = 0; k < 4; ++k) asm volatile("v_swap_b32 %0, %1" : "+v"(tab[t][j][k]), "+v"(tab[2 + t][j][k]));
    }

    unsigned ready = NB;
    for (int i = 0; i < NI; ++i) {
        const bool last = i + 1 == NI;
        const int h = 2 * i + hp;
        const unsigned hnext = (i + 1) * (2 * HEADB) + woff;
        const auto rn = __builtin_amdgcn_make_buffer_rsrc((void*)(last ? qv : qk), 0, KVB, 0x00020000);
        float* scp = s_w + (h * 4 + rw) * 20;
        f32x4 sc[4];
        if (NB < NI) while (__builtin_amdgcn_readfirstlane(ready) <= i) { __builtin_amdgcn_s_sleep(1); ready = *(vlds_u32*)(s_sync + 3); }
        PIN();
#pragma unroll
        for (int t = 0; t < 4; ++t)
#pragma unroll
            for (int a = 0; a < 4; ++a) sc[t][a] = bflo(s_sc[h * 256 + (t ^ (HX * hp)) * 64 + a * 16 + rw * 4 + r]);
        float s[4];
#pragma unroll
        for (int t = 0; t < 4; ++t) {
            f32x2 dot, wsum;
#pragma unroll
            for (int j = 0; j < 3; ++j) {
                f32x2 w01, w23, k01, k23;
                dot_cvt(qp[j], tab[t][j], ring[t][j], w01, w23, k01, k23);
                if (j == 0) { wsum = w01 + w23; dot = k01 * w01; } else { wsum += w01; wsum += w23; dot += k01 * w01; }
                dot += k23 * w23;
                asm volatile("" : "+v"(dot), "+v"(wsum));
                PIN();
                if (t == 3) load_q(last ? h : h + 2, j);
                if (NB < NI && t == 3 && j == 0) ready = *(vlds_u32*)(s_sync + 3);
                refill(t, j, rn, (last ? woff : hnext) + toff(t));
                if (t == 0 && j == 0) TRACE(i);
            }
            const float a = (dot[0] + dot[1]) - sc[t][1] * (wsum[0] + wsum[1]);
            s[t] = row16_allsum(a) * (sc[t][0] * SCALE_LOG2);
        }
        const float mx = xrow_max(fmaxf(fmaxf(s[0], s[1]), fmaxf(s[2], s[3])));
        float l = 0.f, z = 0.f;
#pragma unroll
        for (int t = 0; t < 4; ++t) {
            const float p = __builtin_amdgcn_exp2f(s[t] - mx);
            l += p;
            s[t] = p * sc[t][2];
            z += s[t] * sc[t][3];
        }
        l = xrow_sum(l);
        z = xrow_sum(z);
        if (c == 0) {
            *reinterpret_cast<f32x4*>(scp + r * 4) = f32x4{s[0], s[1], s[2], s[3]};
            *reinterpret_cast<f32x4*>(scp + 16) = f32x4{mx, l, z, 0.f};
        }
        PIN();
    }
    STAMP(4);

    const float* pwp = s_w + (hp * 4 + rw) * 20 + r * 4;
    f32x4 pw = *reinterpret_cast<const f32x4*>(pwp);
    unsigned tag, lw, lo;
    f32x4 lf;
    f32x2 lz;
    unsigned lp = (hp * 4 * NI + NI - 1) * DH + (lane & 31) * 6;
    f32x2 acc[3][2];
    auto v_acc = [&](int t, int j) {
        const f32x2 p2 = {pw[t], pw[t]};
        const u32x4 vv = ring[t][j];
        const f32x2 v01 = {(float)(int)vv[0], (float)(int)vv[1]}, v23 = {(float)(int)vv[2], (float)(int)vv[3]};
        if (t == 0) { acc[j][0] = p2 * v01; acc[j][1] = p2 * v23; }
        else { acc[j][0] += p2 * v01; acc[j][1] += p2 * v23; }
        asm volatile("" : "+v"(acc[j][0]), "+v"(acc[j][1]));
        PIN();
    };
    auto v_fold = [&](int i, int j) {
        const float x = swap32_add(acc[j][0][0], acc[j][1][0]);
        const float y = swap32_add(acc[j][0][1], acc[j][1][1]);
        s_part[(wave * NI + i) * DH + 64 * j + 4 * c + r] = swap16_add(x, y);
    };
    auto v_done = [&](int i) {
        PIN();
        if (lane == 0) *(vlds_u32*)(s_done + wave) = i + 1;
        PIN();
    };
#pragma unroll
    for (int t = 0; t < LS; ++t)
#pragma unroll
        for (int j = 0; j < 3; ++j) v_acc(t, j);
    for (int i = 0; i < NI - 1; ++i) {
        const unsigned hnext = (i + 1) * (2 * HEADB) + woff;
#pragma unroll
        for (int k = 0; k < 4; ++k) {
            const int t = (k + LS) & 3;
#pragma unroll
            for (int j = 0; j < 3; ++j) {
                v_acc(t, j);
                if (t == 3 && j == 2) pw = *reinterpret_cast<const f32x4*>(pwp + (i + 1) * 160);
                refill(k, j, rv, hnext + toff(k));
                if (k == 0 && j == 0) TRACE(16 + i);
            }
            if (t == 3) { v_fold(i, 0); v_fold(i, 1); v_fold(i, 2); v_done(i); }
        }
    }
    if (PAD) asm volatile("s_branch .Lkv_tail_%=\n\t.p2align 12\n.Lkv_tail_%=:" ::: "memory");
    STAMP(5);
    TRACE(31);
    while (__builtin_amdgcn_readfirstlane(*(vlds_u32*)(s_sync + 3)) <= NI) __builtin_amdgcn_s_sleep(1);
    PIN();
    tag = s_sync[4 + (lane & 3)];
    lf = *reinterpret_cast<const f32x4*>(s_f + (2 * (NI - 1) + hp) * 8);
    lz = *reinterpret_cast<const f32x2*>(s_f + (2 * (NI - 1) + hp) * 8 + 4);
    origin = *(vlds_u32*)(s_sync + 2);
    lw = last_word(tag, lz[1], lane & 31);
    lo = lane < 32 ? piece_off(hp, lane & 31) : OOB;
    asm volatile("" : "+v"(tag), "+v"(lf), "+v"(lz), "+v"(lw), "+v"(lo), "+v"(lp), "+v"(origin));
    PIN();
#pragma unroll
    for (int t = LS; t < 4; ++t)
#pragma unroll
        for (int j = 0; j < 3; ++j) { v_acc(t, j); if (t == 3) { v_fold(NI - 1, j); PIN(); } }
    v_done(NI - 1);
    STAMP(6);
    {
        const u32x4 dn = *(vlds_u32x4*)(s_done + 4 * hp);
        PIN();
        const u32x4 v = merge(s_part + lp, lf, lz[0], lw);
        if (__builtin_amdgcn_readfirstlane(dn[0] & dn[1] & dn[2] & dn[3]) == NI) publish(v, NI - 1, lo);
        PIN();
    }
    STAMP(7);
    {
        unsigned inbox = WS_PART + (bid >> 4) * GSTR + (bid & 15) * UNITB, obase = bid * 48;
        unsigned xo = tid * 16, yo = tid * 16 + 8192, sel = lane & 1 ? 0x01000504u : 0x05040100u;
        unsigned bo = lane < 16 ? wave * 1024 + (lane & 7) * 128 + (lane >> 3) * 8192 : OOB;
        typedef __attribute__((address_space(3))) float lds_f;
        lds_f* wo = (lds_f*)s_red + wave * 28 + (r < 3 ? c * 6 + r * 2 : 24);
        lds_f* fo = (lds_f*)s_red + (lane & 7) * 28 + 24;
        lds_f* go = (lds_f*)s_red + (lane & 7) * 28 + min(lane >> 3, 5) * 4;
        vlds_u32* dw = (vlds_u32*)(s_done + wave);
        vlds_u32* dr = (vlds_u32*)(s_done + (lane & 7));
        unsigned oo = (lane & 7) == 0 && lane < 48 ? lane : OOB, eo = bid == 0 && lane < 4 ? lane * 2 : OOB;
        asm volatile("" : "+s"(inbox), "+s"(obase), "+v"(xo), "+v"(yo), "+v"(sel), "+v"(bo), "+v"(wo), "+v"(fo), "+v"(go), "+v"(oo), "+v"(eo), "+v"(dw), "+v"(dr));
        const unsigned o = __builtin_amdgcn_readfirstlane(origin) - (15 - (bid >> 4)) * RST;
        while ((int)((now32() << 8) - o) < pace.rd) __builtin_amdgcn_s_sleep(1);
        PIN();
        STAMP(8);
        u32x4 x, y;
        do {
            x = __builtin_amdgcn_raw_buffer_load_b128(rw_, xo, inbox, 16);
            y = __builtin_amdgcn_raw_buffer_load_b128(rw_, yo, inbox, 16);
            const unsigned age = now32() - tstart;
            PIN();
            if (age > SPIN_LIMIT) break;
        } while (__builtin_amdgcn_ballot_w64(((x[3] ^ tag) | (y[3] ^ tag)) > 0xffffu) != 0);
        if (DRY) { __builtin_amdgcn_raw_buffer_store_b32(0u, rw_, bo, inbox, 16); PIN(); }
        STAMP(9);
        const float lx = __uint_as_float(__builtin_amdgcn_perm(__builtin_amdgcn_mov_dpp(x[3], 0xb1, 0xf, 0xf, true), x[3], sel));
        const float ly = __uint_as_float(__builtin_amdgcn_perm(__builtin_amdgcn_mov_dpp(y[3], 0xb1, 0xf, 0xf, true), y[3], sel));
        float M = fmaxf(lx, ly);
        M = fmaxf(M, dpp<0x128>(M));
        M = fmaxf(M, dpp<0x124>(M));
        M = xrow_max(M);
        const float w0 = __builtin_amdgcn_exp2f(lx - M), w1 = __builtin_amdgcn_exp2f(ly - M);
        float a[8];
#pragma unroll
        for (int k = 0; k < 3; ++k) {
            a[2 * k] = w0 * bflo(x[k]) + w1 * bflo(y[k]);
            a[2 * k + 1] = w0 * bfhi(x[k]) + w1 * bfhi(y[k]);
        }
        a[6] = a[7] = w0 + w1;
        float b[4], d[2];
#pragma unroll
        for (int k = 0; k < 4; ++k) b[k] = swap32_add(a[k], a[4 + k]);
#pragma unroll
        for (int k = 0; k < 2; ++k) d[k] = swap16_add(b[k], b[2 + k]);
#pragma unroll
        for (int k = 0; k < 2; ++k) { d[k] += dpp<0x128>(d[k]); d[k] += dpp<0x124>(d[k]); }
        if (c < 4) {
            *(__attribute__((address_space(3))) f32x2*)wo = f32x2{d[0], r < 3 ? d[1] : M};
            PIN();
            *dw = 3 * NI;
        }
        PIN();
        STAMP(10);
        const unsigned fin = *dr;
        PIN();
        f32x2 sm = *(__attribute__((address_space(3))) f32x2*)fo;
        f32x4 ov = *(__attribute__((address_space(3))) f32x4*)go;
        asm volatile("" : "+v"(sm), "+v"(ov));
        if (__builtin_amdgcn_ballot_w64(fin != 3 * NI) == 0) {
            float MM = fmaxf(sm[1], dpp<0xb1>(sm[1]));
            MM = fmaxf(MM, dpp<0x4e>(MM));
            MM = fmaxf(MM, dpp<0x141>(MM));
            const float ff = __builtin_amdgcn_exp2f(sm[1] - MM);
            float e[5] = {ff * ov[0], ff * ov[1], ff * ov[2], ff * ov[3], ff * sm[0]};
#pragma unroll
            for (int k = 0; k < 5; ++k) e[k] += dpp<0xb1>(e[k]);
#pragma unroll
            for (int k = 0; k < 5; ++k) e[k] += dpp<0x4e>(e[k]);
#pragma unroll
            for (int k = 0; k < 5; ++k) e[k] += dpp<0x141>(e[k]);
            const float inv = __builtin_amdgcn_rcpf(e[4]);
            __builtin_amdgcn_raw_buffer_store_b64(u32x2{pk_bf16(f32x2{e[0] * inv, e[1] * inv}), pk_bf16(f32x2{e[2] * inv, e[3] * inv})}, ro, oo, obase, OST);
            if (bid == 0) __builtin_amdgcn_raw_buffer_store_b16((unsigned short)(tag >> 16), rw_, eo, WS_EPOCH, 16);
        }
    }
    __builtin_amdgcn_endpgm();
}

#define VAR_XS(p) ((p) / 1000 % 2)
template <int X, int PRO, int GATE, int STAG>
static void launch_cfg(void* const* d_in, void* d_out, void* d_ws, hipStream_t stream, float t_us, float start_us, float off_us, float debt_us, float adj_pct, float pub_us) {
    Pace pace;
    pace.step = (unsigned)(t_us * 25600.f / 128.f);
    pace.start = (int)(start_us * 25600.f);
    pace.offset = (int)(off_us * 25600.f);
    pace.bias = (int)(debt_us * 25600.f);
    pace.rd = (int)((start_us + off_us + t_us * ((129.f + VAR_XS(PRO)) / 128.f) + adj_pct) * 25600.f);
    pace.pub = (int)((start_us + off_us + t_us * ((129.f + VAR_XS(PRO)) / 128.f) + pub_us) * 25600.f);
    kv_sweep_kernel<X, PRO, GATE, STAG, 1><<<NWG, 576, 0, stream>>>(d_in[0], d_in[1], d_in[2], d_in[3], d_in[4], d_in[5], d_in[6], d_in[7],
        d_in[8], d_in[9], d_in[10], d_ws, d_out, pace);
}

#define CFG 335022966, 2023, 242221, 240
#define PRM 101.0f, 0.5f, 0.8f, 0.05f, 1.6f, 0.6f
extern "C" void kernel_launch(void* const* d_in, const int* in_sizes, int n_in,
                              void* d_out, int out_size, void* d_ws, size_t ws_size,
                              hipStream_t stream) {
    launch_cfg<CFG>(d_in, d_out, d_ws, stream, PRM);
}
```

```cpp
#include <hip/hip_runtime.h>
#include <hip/hip_bf16.h>
#include <cstdint>
#include <type_traits>

using bf16 = __hip_bfloat16;
using u32x4 = __attribute__((ext_vector_type(4))) unsigned;
using u32x3 = __attribute__((ext_vector_type(3))) unsigned;
using u32x2 = __attribute__((ext_vector_type(2))) unsigned;
using u64x8 = __attribute__((ext_vector_type(8))) unsigned long long;
using f32x2 = __attribute__((ext_vector_type(2))) float;
using f32x4 = __attribute__((ext_vector_type(4))) float;
using bf16x2 = __attribute__((ext_vector_type(2))) __bf16;

constexpr int DH = 192;
constexpr int HALF = 96;
constexpr int NH = 32;
constexpr int SKV = 16384;
constexpr int NWG = 256;
constexpr int NI = 16;
constexpr unsigned ROWB = DH * 4;
constexpr unsigned HEADB = SKV * ROWB;
constexpr unsigned KVB = NH * HEADB;
constexpr unsigned CSB = SKV * DH * 2;
constexpr unsigned OOB = 0x80000000u;
constexpr float SCALE_LOG2 = 1.4426950408889634f * 0.07216878364870322f;
constexpr unsigned SPIN_LIMIT = 20000000u;

constexpr unsigned UNITB = NWG * 64;
constexpr unsigned GSTR = 16 * UNITB + 256;
constexpr unsigned WS_PART = 0;
constexpr unsigned WS_EPOCH = NWG * (UNITB + 1024);
constexpr unsigned WS_REP = WS_EPOCH + 256;
constexpr unsigned WS_PROBE = WS_REP + 256 * 512;
constexpr unsigned WS_BYTES = WS_PROBE + 4096;

__device__ __forceinline__ float bflo(unsigned u) { return __uint_as_float(u << 16); }
__device__ __forceinline__ float bfhi(unsigned u) { return __uint_as_float(u & 0xffff0000u); }
__device__ __forceinline__ float rbf(float x) { return __bfloat162float(__float2bfloat16(x)); }
__device__ __forceinline__ unsigned pk_bf16(f32x2 a) { return __builtin_bit_cast(unsigned, __builtin_convertvector(a, bf16x2)); }
__device__ __forceinline__ void dot_cvt(const u32x4 q, const u32x4 t, const u32x4 ki, f32x2& w01, f32x2& w23, f32x2& k01, f32x2& k23) {
    float w0, w1, w2, w3;
    float k0 = __uint_as_float(ki[0]), k1 = __uint_as_float(ki[1]), k2 = __uint_as_float(ki[2]), k3 = __uint_as_float(ki[3]);
    asm("v_dot2_f32_bf16 %0, %8, %12, 0\n\tv_dot2_f32_bf16 %1, %9, %13, 0\n\tv_dot2_f32_bf16 %2, %10, %14, 0\n\tv_dot2_f32_bf16 %3, %11, %15, 0\n\t"
        "v_cvt_f32_i32 %4, %4\n\tv_cvt_f32_i32 %5, %5\n\tv_cvt_f32_i32 %6, %6\n\tv_cvt_f32_i32 %7, %7"
        : "=&v"(w0), "=&v"(w1), "=&v"(w2), "=&v"(w3), "+v"(k0), "+v"(k1), "+v"(k2), "+v"(k3)
        : "v"(q[0]), "v"(q[1]), "v"(q[2]), "v"(q[3]), "v"(t[0]), "v"(t[1]), "v"(t[2]), "v"(t[3]));
    w01 = f32x2{w0, w1}; w23 = f32x2{w2, w3}; k01 = f32x2{k0, k1}; k23 = f32x2{k2, k3};
}

template <int CTRL> __device__ __forceinline__ float dpp(float x) {
    return __builtin_bit_cast(float, __builtin_amdgcn_mov_dpp(__builtin_bit_cast(int, x), CTRL, 0xf, 0xf, true));
}
__device__ __forceinline__ float row16_allsum(float x) {
    x += dpp<0x128>(x);
    x += dpp<0x124>(x);
    x += dpp<0x122>(x);
    x += dpp<0x121>(x);
    return x;
}
__device__ __forceinline__ float row16_allmax(float x) {
    x = fmaxf(x, dpp<0x128>(x));
    x = fmaxf(x, dpp<0x124>(x));
    x = fmaxf(x, dpp<0x122>(x));
    x = fmaxf(x, dpp<0x121>(x));
    return x;
}
__device__ __forceinline__ float xrow_max(float x) {
    auto s = __builtin_amdgcn_permlane16_swap(__float_as_uint(x), __float_as_uint(x), false, false);
    x = fmaxf(__uint_as_float(s[0]), __uint_as_float(s[1]));
    auto t = __builtin_amdgcn_permlane32_swap(__float_as_uint(x), __float_as_uint(x), false, false);
    return fmaxf(__uint_as_float(t[0]), __uint_as_float(t[1]));
}
__device__ __forceinline__ float xrow_sum(float x) {
    auto s = __builtin_amdgcn_permlane16_swap(__float_as_uint(x), __float_as_uint(x), false, false);
    x = __uint_as_float(s[0]) + __uint_as_float(s[1]);
    auto t = __builtin_amdgcn_permlane32_swap(__float_as_uint(x), __float_as_uint(x), false, false);
    return __uint_as_float(t[0]) + __uint_as_float(t[1]);
}
__device__ __forceinline__ float swap32_add(float a, float b) {
    auto t = __builtin_amdgcn_permlane32_swap(__float_as_uint(a), __float_as_uint(b), false, false);
    return __uint_as_float(t[0]) + __uint_as_float(t[1]);
}
__device__ __forceinline__ float swap16_add(float a, float b) {
    auto t = __builtin_amdgcn_permlane16_swap(__float_as_uint(a), __float_as_uint(b), false, false);
    return __uint_as_float(t[0]) + __uint_as_float(t[1]);
}
__device__ __forceinline__ int row16_sum(int x) {
    x += __builtin_amdgcn_mov_dpp(x, 0x128, 0xf, 0xf, true);
    x += __builtin_amdgcn_mov_dpp(x, 0x124, 0xf, 0xf, true);
    x += __builtin_amdgcn_mov_dpp(x, 0x122, 0xf, 0xf, true);
    x += __builtin_amdgcn_mov_dpp(x, 0x121, 0xf, 0xf, true);
    return x;
}
__device__ __forceinline__ unsigned now32() { return (unsigned)__builtin_amdgcn_s_memrealtime(); }

#define PLAN_EXTRA
#define GNT " nt"
#define GSA 0x22222222
#define GSB 0x55445544
#define CTRL 2, 4, 0, 2560, 768, 13, 9, 200, 984, 2000, 0, 6, 400, -1024
#define NSTAMP 12
#define STAMP(i)
#define TRACE(i)
#define HSTAMP(i)
#define PSTAMP(i)
#define PIN() do { asm volatile("" ::: "memory"); __builtin_amdgcn_sched_barrier(0); } while (0)
#define BARRIER() do { asm volatile("s_waitcnt lgkmcnt(0)" ::: "memory"); __builtin_amdgcn_s_barrier(); PIN(); } while (0)
#define VMCNT(n) asm volatile("s_waitcnt vmcnt(%0)" :: "i"(n) : "memory")

typedef volatile __attribute__((address_space(3))) unsigned vlds_u32;
typedef volatile __attribute__((address_space(3))) u32x2 vlds_u32x2;
typedef volatile __attribute__((address_space(3))) u32x4 vlds_u32x4;

struct Plan { const char* rows; const char* phase; int hx; char sx, tx; };
constexpr Plan PLANS[] = {
    {"1234tcab0s", "vwabc01234h", 2}, {"432cb1ts0a", "1vwacb4320h", 0}, {"123cb0ts4a", "vwabc01234h", 0}, {"103cb2ts4a", "vwabc01234h", 0},
    {"0234catb1s", "vwabc01234h", 2}, {"013cb2ta4s", "vwabc01234h", 0}, {"1234tcab0s", "vwabc01234h", 0}, {"123cb0st4a", "vwabc01234h", 0},
    {"1234tcab0s", "vwabc01234h", 2, 'c'}, {"432cb1ts0a", "1vwacb4320h", 0, 'b', 'c'}, {"123cb0ts4a", "vwabc01234h", 0, 'b', 'c'}, {"0123t4cabs", "vwabc01234h", 2},
    {"1234t0cabs", "vwabc01234h", 2}, {"1234tcab0s", "vwabc01234h", 2, 'c', 'a'},
    PLAN_EXTRA
};
constexpr int sym_pos(const char* s, char c) { for (int i = 0; s[i]; ++i) if (s[i] == c) return i; return -1; }
template <int N> __device__ __forceinline__ int place(const char* dst, const char (&src)[N], int x) {
    int y = 0;
#pragma unroll
    for (int i = 0; i < N - 1; ++i) y |= (x >> i & 1) << sym_pos(dst, src[i]);
    return y;
}
#define EXTARG
#define EXTVAL
struct Pace {
    unsigned step;
    int start;
    int offset;
    int bias;
    int rd;
    int pub;
};

template <int X, int OPT, int GATE, int TP, int SLP>
__global__ __launch_bounds__(576) __attribute__((aligned(4096))) void kv_sweep_kernel(
    const void*, const void*, const void*, const void*, const void*, const void*, const void*, const void*,
    const void*, const void*, const void*, void*, void*, Pace EXTARG)
{
    __shared__ __attribute__((aligned(16))) unsigned s_q[NH * DH];
    __shared__ __attribute__((aligned(16))) unsigned short s_sc[NH * 256];
    __shared__ __attribute__((aligned(16))) float s_w[NH * 4 * 20];
    __shared__ __attribute__((aligned(16))) float s_part[8 * NI * DH];
    __shared__ __attribute__((aligned(16))) float s_red[8 * 28];
    __shared__ __attribute__((aligned(16))) float s_f[NH * 8];
    __shared__ __attribute__((aligned(16))) unsigned s_done[8];
    __shared__ __attribute__((aligned(16))) unsigned s_junk[16];
    __shared__ __attribute__((aligned(16))) unsigned s_sync[8];
    __shared__ __attribute__((aligned(16))) unsigned s_act[16];

    constexpr int HOLD = GATE % 10;
    constexpr int DRY = OPT % 10 & 1, LS = 2, PAD = OPT % 10 / 2 & 1, PF = OPT / 10 % 10, RST = GATE / 1000 % 10 * 256, SLEAD = GATE / 10000 % 10, STA = 16, WIDE = OPT / 100 % 10, OST = 0, PRO = OPT / 1000;
    unsigned long long pc0, t0;
    u64x8 ka, kb;
    asm volatile("s_load_dwordx16 %3, %4, 0x40\n\ts_load_dwordx16 %2, %4, 0x0\n\ts_memrealtime %1\n\ts_getpc_b64 %0\n.Lkv_beg_%5_%6_%7_%8:"
                 : "=&s"(pc0), "=&s"(t0), "=&s"(ka), "=&s"(kb) : "s"(__builtin_amdgcn_kernarg_segment_ptr()), "i"(X), "i"(OPT), "i"(GATE), "i"(TP));
    __builtin_amdgcn_sched_barrier(0);
    const int tid = threadIdx.x;
    const int lane = tid & 63;
    int wave = __builtin_amdgcn_readfirstlane(tid >> 6);
    const int r = lane >> 4;
    const int c = lane & 15;
    const int bid = blockIdx.x;
    const int m = (bid & 7) * 32 + (bid >> 3);
    const int rw = wave & 3, hp = wave >> 2;
    constexpr Plan PL = PLANS[GATE / 1000000];
    constexpr unsigned SA = GSA, SB = GSB, QD = TP >> 6 ? 50 + (TP >> 6) * 50u : 0, PM = GATE / 100 % 10 & 3, BM = GATE / 100 % 10 >> 2;
    constexpr int HX = PL.hx, RS = 16 << sym_pos(PL.rows, 's') | (PL.sx ? 16 << sym_pos(PL.rows, PL.sx) : 0), RT = 16 << sym_pos(PL.rows, 't') | (PL.tx ? 16 << sym_pos(PL.rows, PL.tx) : 0);
    const int grow = place(PL.rows, "abc01234", bid) * 16;
    auto srow = [&](int t) { return (t & 1) * RS ^ (t >> 1) * RT; };
    const int row0 = grow + rw * 4;
    unsigned co = r * 384 + c * 8, so1 = r * 384 + (c < 8 ? c * 8 + 320 : c * 8 - 64), rb0 = (row0 ^ RT * hp) * 384u;
    asm volatile("s_waitcnt lgkmcnt(0)" : "+s"(t0), "+s"(ka), "+s"(kb), "+v"(co), "+v"(so1), "+s"(rb0), "+s"(wave));
    typedef const __attribute__((address_space(1))) char* gptr;
    void* const q = (void*)ka[0]; void* const qk = (void*)ka[1]; void* const qv = (void*)ka[4]; void* const q_cos = (void*)ka[7];
    const gptr ksc = (gptr)ka[2], kzp = (gptr)ka[3], vsc = (gptr)ka[5], vzp = (gptr)ka[6];
    void* const q_sin = (void*)kb[0]; void* const k_cos = (void*)kb[1]; void* const k_sin = (void*)kb[2]; void* const out = (void*)kb[4];
    unsigned char* const ws = (unsigned char*)(__attribute__((address_space(1))) unsigned char*)kb[3];
    const Pace pace = {(unsigned)kb[5], (int)(kb[5] >> 32), (int)kb[6], (int)(kb[6] >> 32), (int)kb[7], (int)(kb[7] >> 32)};
    const unsigned tstart = (unsigned)t0;
    const unsigned tsched = TP & 16 ? now32() : tstart;
    PSTAMP(69);
    const auto rw_ = __builtin_amdgcn_make_buffer_rsrc((void*)ws, 0, WS_BYTES, 0x00020000);
    const auto ro = __builtin_amdgcn_make_buffer_rsrc((void*)out, 0, NH * DH * 2, 0x00020000);

    auto merge = [&](const float* p, f32x4 f, float zs, unsigned w3) {
        f32x2 u[4][3], a[3];
#pragma unroll
        for (int w = 0; w < 4; ++w)
#pragma unroll
            for (int k = 0; k < 3; ++k) u[w][k] = *reinterpret_cast<const f32x2*>(p + w * NI * DH + 2 * k);
        asm volatile("" : "+v"(u[0][0]), "+v"(u[0][1]), "+v"(u[0][2]), "+v"(u[1][0]), "+v"(u[1][1]), "+v"(u[1][2]),
                          "+v"(u[2][0]), "+v"(u[2][1]), "+v"(u[2][2]), "+v"(u[3][0]), "+v"(u[3][1]), "+v"(u[3][2]));
#pragma unroll
        for (int k = 0; k < 3; ++k) a[k] = (f[0] * u[0][k] - zs + f[1] * u[1][k]) + (f[2] * u[2][k] + f[3] * u[3][k]);
        return u32x4{pk_bf16(a[0]), pk_bf16(a[1]), pk_bf16(a[2]), w3};
    };
    auto last_word = [&](unsigned tag, float ls, int c6) { return tag | ((__float_as_uint(ls) >> (16 * (c6 & 1))) & 0xffffu); };
    auto piece_off = [&](int hp, int c6) { return (hp * 8 + (c6 >> 2)) * UNITB + (c6 & 3) * 16; };
    auto publish = [&](u32x4 v, int i, unsigned vo) {
        __builtin_amdgcn_raw_buffer_store_b128(v, rw_, vo, WS_PART + i * GSTR + m * 64, STA);
        asm volatile("s_nop 1" :: "v"(v) : "memory");
    };

    if (__builtin_expect(wave == 8, 0)) {
        if (TP & 32) __builtin_amdgcn_s_setprio(3);
        HSTAMP(11);
        unsigned clen;
        asm volatile("s_mov_b32 %0, .Lkv_end_%1_%2_%3_%4-.Lkv_beg_%1_%2_%3_%4" : "=s"(clen) : "i"(X), "i"(OPT), "i"(GATE), "i"(TP));
        const auto rcode = __builtin_amdgcn_make_buffer_rsrc((void*)(pc0 & ~127ull), 0, clen, 0x00020000);
        const int jj = bid >> 3;
        auto warm_ld = [&](int n) {
            return __builtin_amdgcn_raw_buffer_load_b32(rcode, lane < n ? (jj * 9 + lane) * 64 : OOB, 0, 0);
        };
        auto warm_code = [&](int n) { if (n) { const unsigned x = warm_ld(n); asm volatile("" :: "v"(x)); } };
        constexpr int WN[10][4] = {{0, 0, 0, 0}, {64, 0, 0, 0}, {9, 0, 0, 0}, {64, 9, 9, 0}, {9, 9, 9, 0}, {18, 0, 0, 0}, {18, 18, 18, 0}, {9, 9, 0, 9}, {36, 0, 0, 0}, {9, 0, 9, 0}};
        constexpr int FREE = 0x3fff & ~(16 << sym_pos(PL.rows, 'a') | 16 << sym_pos(PL.rows, 'b') | 16 << sym_pos(PL.rows, 'c'));
        constexpr int F1 = 1 << (31 - __builtin_clz(FREE)), F2 = 1 << (31 - __builtin_clz(FREE - F1));
        const unsigned trow = (grow & ~FREE) + (jj & 1 ? FREE - F1 - F2 : 0) + (jj & 2 ? F2 : 0) + (jj & 4 ? F1 : 0) + (jj & 8 ? SKV : 0);
        int tp = jj >> 4;
        auto touch = [&]() {
            const unsigned long long a = (unsigned long long)(tp < NI ? qk : qv) + ((tp & 15) * 2 * SKV + trow) * ROWB;
            if (lane == 0)
                asm volatile("s_mov_b32 m0, %1\n\ts_nop 0\n\tglobal_load_lds_dword %0, off nt" :: "v"(a), "s"((unsigned)(unsigned long long)(__attribute__((address_space(3))) unsigned*)s_junk) : "memory");
            tp += 2;
            PIN();
        };
        const unsigned x0 = warm_ld(WN[PF][0]);
        if (TP & 4) touch(); else tp += 2;
        PIN();
        if (QD) while (now32() - tsched < QD) __builtin_amdgcn_s_sleep(1);
        u32x3 lc[2], ls[2];
        u32x4 qraw[12];
        {
            const auto rq = __builtin_amdgcn_make_buffer_rsrc((void*)q, 0, NH * DH * 2, 0x00020000);
            const auto rqc = __builtin_amdgcn_make_buffer_rsrc((void*)q_cos, 0, DH * 2, 0x00020000);
            const auto rqs = __builtin_amdgcn_make_buffer_rsrc((void*)q_sin, 0, DH * 2, 0x00020000);
#pragma unroll
            for (int k = 0; k < 12; ++k) qraw[k] = __builtin_amdgcn_raw_buffer_load_b128(rq, lane * 16 + (k & 3) * 1024, (k >> 2) * 4096, 0);
#pragma unroll
            for (int k = 0; k < 2; ++k) {
                lc[k] = __builtin_amdgcn_raw_buffer_load_b96(rqc, c * 12 + k * 192, 0, 0);
                ls[k] = __builtin_amdgcn_raw_buffer_load_b96(rqs, c * 12 + k * 192, 0, 0);
            }
            PIN();
        }
        const unsigned tag = ((unsigned)__builtin_amdgcn_raw_buffer_load_b16(rw_, (lane & 3) * 2, WS_EPOCH, 16) + 1u) << 16;
        PIN();
        HSTAMP(2);
        unsigned* const s_raw = reinterpret_cast<unsigned*>(s_part) + NI * DH / 2;
#pragma unroll
        for (int k = 0; k < 12; ++k) *reinterpret_cast<u32x4*>(s_raw + k / 6 * (NI * DH) + k % 6 * 256 + lane * 4) = qraw[k];
        f32x2 fc[2][3], fs[2][3];
#pragma unroll
        for (int k = 0; k < 2; ++k)
#pragma unroll
            for (int u = 0; u < 3; ++u) { fc[k][u] = f32x2{bflo(lc[k][u]), bfhi(lc[k][u])}; fs[k][u] = f32x2{bflo(ls[k][u]), bfhi(ls[k][u])}; }
        PIN();
        HSTAMP(3);
#pragma nounroll
        for (int it = 0; it < 4; ++it) {
            unsigned qa[2][3], qb[2][3];
#pragma unroll
            for (int g = 0; g < 2; ++g)
#pragma unroll
                for (int u = 0; u < 3; ++u) {
                    const unsigned* src = s_raw + g * (NI * DH) + (it * 4 + r) * HALF + 3 * c;
                    qa[g][u] = src[u]; qb[g][u] = src[HALF / 2 + u];
                }
#pragma unroll
            for (int g = 0; g < 2; ++g)
#pragma unroll
                for (int u = 0; u < 3; ++u) {
                    const f32x2 a = {bflo(qa[g][u]), bfhi(qa[g][u])}, b = {bflo(qb[g][u]), bfhi(qb[g][u])};
                    const f32x2 o0 = a * fc[0][u] - b * fs[0][u], o1 = b * fc[1][u] + a * fs[1][u];
                    const unsigned l0 = pk_bf16(f32x2{o0[0], o1[0]}), l1 = pk_bf16(f32x2{o0[1], o1[1]});
                    unsigned* dst = s_q + (16 * g + 4 * it + r) * DH + 6 * c + 2 * u;
                    *reinterpret_cast<u32x2*>(dst) = u32x2{l0, l1};
                    *reinterpret_cast<u32x2*>(dst + HALF) = u32x2{__builtin_amdgcn_alignbit(l0, l0, 16) ^ 0x80000000u, __builtin_amdgcn_alignbit(l1, l1, 16) ^ 0x80000000u};
                }
        }
        HSTAMP(1);
        asm volatile("" :: "v"(x0));
        if (TP & 32) __builtin_amdgcn_s_setprio(0);
        unsigned origin = tsched << 8;
        if (lane < 8) { s_done[lane] = 0; s_sync[lane] = lane < 3 ? origin : lane < 4 ? 0 : tag; }
        PIN();
        BARRIER();
        const int mh = lane >> 5, mc = lane & 31;
        const unsigned rtag = __builtin_amdgcn_readlane(tag, 0) | __builtin_amdgcn_readlane(tag, 1) >> 16;
        constexpr int RPX = 2;
        constexpr int gp[] = {CTRL};
        constexpr int KS = gp[0], SR = gp[1], IB2 = gp[2], EHI = gp[3], IB = gp[4], FR = gp[5], RSH = gp[6], UP = gp[8], DN = gp[9], SLIP = gp[10], FF = gp[11], DMAX = gp[12], ELO = gp[13];
        constexpr unsigned RDLY = gp[7];
        const int rj = ((bid >> 3) + 5 * (bid & 7)) & 31;
        const int rid = rj % (32 / RPX) == 0 ? rj / (32 / RPX) * 8 + (bid & 7) : -1;
        unsigned wround = now32() >> RSH, rround = wround, tick = 0, target = origin;
        bool warmed = false, synced = false;
        int drift = 0;
        const unsigned round0 = rround;
        int i = 0;
        auto kept_now = [&](unsigned now) {
            const u32x2 a = *(vlds_u32x2*)(s_act + 2 * (lane & 7));
            const int late = max((int)(a[0] - origin), (int)((now << 8) - a[1] - origin));
            return origin + (__builtin_amdgcn_readfirstlane(row16_sum(late)) >> 4);
        };
        auto steer = [&](unsigned now) {
            if ((now >> 5) != tick) {
                tick = now >> 5;
                target += drift;
                origin += max(-DN, min(UP + drift, (int)(target - origin)));
                if (SLIP) { const int l2 = (int)(kept_now(now) - origin) - SLIP; if (l2 > 0) origin += l2; }
                if (HOLD && !synced) target = origin - (HOLD - 1) * 82;
                if (lane == 0) *(vlds_u32*)(s_sync + 2) = origin;
                PIN();
            }
        };
        auto finished = [&]() {
            const u32x4 a = *(vlds_u32x4*)s_done, b = *(vlds_u32x4*)(s_done + 4);
            return __builtin_amdgcn_readfirstlane(min(min(min(a[0], a[1]), min(a[2], a[3])), min(min(b[0], b[1]), min(b[2], b[3]))));
        };
        while (i < NI - 2) {
            const unsigned now = now32();
            if ((now >> RSH) != wround) {
                wround = now >> RSH;
                if (rid >= 0 && (now & ((1u << RSH) - 1)) < 48)
                    __builtin_amdgcn_raw_buffer_store_b64(u32x2{kept_now(now), rtag + wround}, rw_, lane == 0 ? rid * 8 : OOB, WS_REP + (wround & 255) * 512, 16);
                PIN();
                continue;
            }
            if ((int)(((now - RDLY) >> RSH) - rround) > 0) {
                rround = (now - RDLY) >> RSH;
                const u32x2 x = __builtin_amdgcn_raw_buffer_load_b64(rw_, c * 8, WS_REP + (rround & 255) * 512, 16);
                const int d = (int)(x[0] - origin);
                const bool ok = x[1] == rtag + rround && d < (32 << 16) && d > -(32 << 16);
                const int n = __builtin_popcountll(__builtin_amdgcn_ballot_w64(ok));
                if (n >= 48) {
                    const unsigned nt = origin + __builtin_amdgcn_readfirstlane((int)(row16_allsum(ok ? (float)d : 0.f) * 4.f / (float)n)) - pace.bias;
                    if (KS && synced && rround - round0 > SR) drift = max(0, min(drift + ((max(ELO, min((int)(nt - target) + FF * drift, EHI)) - (rround - round0 < FR ? IB : IB2)) >> (3 + KS)), DMAX));
                    target = nt + FF * drift;
                    synced = true;
                    const int lost = min((int)(kept_now(now32()) - origin) - pace.bias, (int)(target - origin));
                    if (lost > 0) origin += lost;
                }
                PIN();
                continue;
            }
            steer(now);
            if ((TP & 1) && tp < 2 * NI && 4 * (int)((now << 8) - origin) > (16 * tp + (jj & 15) - (TP & 8 ? 40 : 24)) * (int)pace.step) { touch(); continue; }
            if (WN[PF][1] && !warmed && (int)((now << 8) - origin) > 60 * (int)pace.step) { warm_code(WN[PF][1]); warmed = true; continue; }
            if (finished() <= (unsigned)i) { __builtin_amdgcn_s_sleep(2); continue; }
            PIN();
            if (i == 0) {
                f32x4 ml[4], f;
#pragma unroll
                for (int w = 0; w < 4; ++w) ml[w] = *reinterpret_cast<const f32x4*>(s_w + (mc * 4 + w) * 20 + 16);
                const float M = fmaxf(fmaxf(ml[0][0], ml[1][0]), fmaxf(ml[2][0], ml[3][0]));
                float L = 0.f, zs = 0.f;
#pragma unroll
                for (int w = 0; w < 4; ++w) { f[w] = __builtin_amdgcn_exp2f(ml[w][0] - M); L += f[w] * ml[w][1]; zs += f[w] * ml[w][2]; }
                const float inv = 1.f / L;
                *reinterpret_cast<f32x4*>(s_f + mc * 8) = f * inv;
                *reinterpret_cast<f32x2*>(s_f + mc * 8 + 4) = f32x2{zs * inv, M + __builtin_amdgcn_logf(L)};
                PIN();
                if (lane == 0) *(vlds_u32*)(s_sync + 3) = NI + 1;
                PIN();
            }
            if (i == NI - 3) warm_code(WN[PF][2]);
            const f32x2 zl = *reinterpret_cast<const f32x2*>(s_f + (2 * i + mh) * 8 + 4);
            const u32x4 v = merge(s_part + (mh * 4 * NI + i) * DH + mc * 6, *reinterpret_cast<const f32x4*>(s_f + (2 * i + mh) * 8), zl[0], last_word(tag, zl[1], mc));
            PIN();
            *reinterpret_cast<u32x4*>(s_part + (mh * 4 * NI + i) * DH + mc * 4) = v;
            PIN();
            ++i;
        }
        u32x4 v[NI - 1];
#pragma unroll
        for (int i = 0; i < NI - 2; ++i) v[i] = *reinterpret_cast<const u32x4*>(s_part + (mh * 4 * NI + i) * DH + mc * 4);
        f32x4 f = *reinterpret_cast<const f32x4*>(s_f + (2 * (NI - 2) + mh) * 8);
        f32x2 zl = *reinterpret_cast<const f32x2*>(s_f + (2 * (NI - 2) + mh) * 8 + 4);
        PIN();
#pragma unroll
        for (int i = 0; i < NI - 2; ++i) asm volatile("" : "+v"(v[i]));
        asm volatile("" : "+v"(f), "+v"(zl));
        v[NI - 2] = v[0];
        bool have = false, sent = false;
        while (!(have && sent)) {
            const unsigned now = now32();
            if (!have) {
                steer(now);
                if (finished() > NI - 2) {
                    PIN();
                    v[NI - 2] = merge(s_part + (mh * 4 * NI + NI - 2) * DH + mc * 6, f, zl[0], last_word(tag, zl[1], mc));
                    have = true;
                    warm_code(WN[PF][3]);
                    HSTAMP(5);
                }
            }
            if (!sent && (int)((now << 8) - origin) >= pace.pub) {
                HSTAMP(6);
#pragma unroll
                for (int i = 0; i < NI - 2; ++i) publish(v[i], i, piece_off(mh, mc));
                sent = true;
            }
            if (!(have && sent)) __builtin_amdgcn_s_sleep(1);
            PIN();
        }
        publish(v[NI - 2], NI - 2, piece_off(mh, mc));
        HSTAMP(7);
        asm volatile(".Lkv_end_%0_%1_%2_%3:" :: "i"(X), "i"(OPT), "i"(GATE), "i"(TP));
        return;
    }

    const int ph = place(PL.phase, "abc01234", bid) + place(PL.phase, "vw", rw) + place(PL.phase, "h", hp);
    unsigned off = pace.start + ((pace.step * ph) >> 11);
    unsigned origin = tsched << 8, kept = 0;
    unsigned step = pace.step;
    auto pace_wait = [&]() {
        const unsigned tgt = __builtin_amdgcn_readfirstlane(origin) + off;
        unsigned now = now32() << 8;
        kept = ((int)(now - tgt) > 0 ? now : tgt) - off;
        while ((int)(tgt - now) > 0) { if (SLP) __builtin_amdgcn_s_sleep(SLP); now = now32() << 8; }
        off += step;
    };
    auto report = [&]() {
        if (lane == 0) *(vlds_u32x2*)(s_act + 2 * wave) = u32x2{kept, off};
        origin = *(vlds_u32*)(s_sync + 2);
        PIN();
    };
#define TRV (kept - (tstart << 8))

    auto pick = [](int n, gptr p0, gptr p1, gptr p2, gptr p3) { return (n & 2) ? ((n & 1) ? p3 : p2) : ((n & 1) ? p1 : p0); };
    auto dma = [](gptr a, unsigned dst) { asm volatile("s_mov_b32 m0, %1\n\ts_nop 0\n\tglobal_load_lds_dwordx4 %0, off nt" :: "v"(a), "s"(__builtin_amdgcn_readfirstlane(dst)) : "memory"); };
    const int w8 = rw + 4 * hp;
    const unsigned sdst = (unsigned)(unsigned long long)(__attribute__((address_space(3))) unsigned short*)s_sc + w8 * 1024;
    auto gather = [&](unsigned slot) {
        const gptr a = pick(lane >> 1, ksc, kzp, vsc, vzp) + ((w8 * 2 + (lane >> 5)) * SKV + (grow ^ srow(lane >> 3 & 3)) + (lane & 1) * 8) * 2;
        if ((SA >> 4 * w8 & 15) == slot) dma(a, sdst);
        if ((SB >> 4 * w8 & 15) == slot) dma(a + 32 * SKV, sdst + 8192);
        PIN();
    };
    auto pre_sc = [&]() {
        const gptr a = pick(grow >> 4, ksc, kzp, vsc, vzp) + ((w8 * 2 + (lane >> 5)) * SKV + (grow & ~63 ^ srow(lane >> 3 & 3))) * 2 + (lane & 7) * 16;
        const unsigned junk = (unsigned)(unsigned long long)(__attribute__((address_space(3))) float*)s_part + (3 * NI * DH - 256) * 4;
        if (PM >= 2) dma(a, junk);
        if (PM >= 1) dma(a + 32 * SKV, junk);
        PIN();
    };
    const auto rc = __builtin_amdgcn_make_buffer_rsrc((void*)k_cos, 0, CSB, 0x00020000);
    const auto rs = __builtin_amdgcn_make_buffer_rsrc((void*)k_sin, 0, CSB, 0x00020000);
    u32x2 raw[2][6];
    auto load_cs = [&](int t) {
        const unsigned rb = t == 0 ? rb0 : (row0 ^ RT * hp ^ RS) * 384u;
        raw[t][0] = __builtin_amdgcn_raw_buffer_load_b64(rc, co, rb, 2);
        raw[t][1] = __builtin_amdgcn_raw_buffer_load_b64(rc, co + 128, rb, 2);
        raw[t][2] = __builtin_amdgcn_raw_buffer_load_b64(rc, co + 256, rb, 2);
        raw[t][3] = __builtin_amdgcn_raw_buffer_load_b64(rs, co + 192, rb, 2);
        raw[t][4] = __builtin_amdgcn_raw_buffer_load_b64(rs, so1, rb, 2);
        raw[t][5] = __builtin_amdgcn_raw_buffer_load_b64(rs, co + 64, rb, 2);
        PIN();
    };
    u32x4 tab[4][3];
    auto pack_cs = [&](int t) {
#pragma unroll
        for (int j = 0; j < 3; ++j) {
#pragma unroll
            for (int k = 0; k < 2; ++k) {
                tab[t][j][2 * k] = __builtin_amdgcn_perm(raw[t][3 + j][k], raw[t][j][k], 0x05040100);
                tab[t][j][2 * k + 1] = __builtin_amdgcn_perm(raw[t][3 + j][k], raw[t][j][k], 0x07060302);
            }
            *reinterpret_cast<u32x4*>(s_part + (wave ^ 4) * (NI * DH) + ((t * 3 + j) * 64 + lane) * 4) = tab[t][j];
        }
        PIN();
    };
    const auto rk = __builtin_amdgcn_make_buffer_rsrc((void*)qk, 0, KVB, 0x00020000);
    const auto rv = __builtin_amdgcn_make_buffer_rsrc((void*)qv, 0, KVB, 0x00020000);
    const unsigned voff = r * ROWB + c * 16;
    const unsigned woff = hp * HEADB;
    auto toff = [&](int t) { return (unsigned)(row0 ^ srow(t ^ (HX * hp))) * ROWB; };
    u32x4 ring[4][3];
    auto ld = [&](int slot, int j, __amdgpu_buffer_rsrc_t rr, unsigned off) {
        ring[slot][j] = __builtin_amdgcn_raw_buffer_load_b128(rr, voff + j * 256, off, 2);
        PIN();
    };
    auto ld_tile = [&](int slot, __amdgpu_buffer_rsrc_t rr, unsigned off) { ld(slot, 0, rr, off); ld(slot, 1, rr, off); ld(slot, 2, rr, off); };
    unsigned post = 0;
    auto enter = [&](int i) {
        if (WIDE && (i == 9 || i == 10)) {
            const unsigned hs = pace.step >> 1, z = (unsigned)pace.offset >> 16;
            off += i == 10 ? post : hp * 3 * hs;
            step = i == 10 ? pace.step : hs + hp * z;
            post = __builtin_amdgcn_readfirstlane(i == 10 ? 0 : hp ? hs : 4 * (hs + z));
            if (lane == 0) *(vlds_u32x2*)(s_act + 2 * wave) = u32x2{kept, off};
            PIN();
        }
    };
    auto refill = [&](int slot, int j, __amdgpu_buffer_rsrc_t rr, unsigned goff) {
        if (j == 0) pace_wait();
        if (!WIDE) VMCNT(2);
        else if (j == 0) asm volatile("s_cmp_lg_u32 %0, 0\n\ts_waitcnt vmcnt(3)\n\ts_cbranch_scc1 1\n\ts_waitcnt vmcnt(2)" :: "s"(post) : "scc", "memory");
        else asm volatile("s_cmp_lg_u32 %0, 0\n\ts_cbranch_scc1 1\n\ts_waitcnt vmcnt(2)" :: "s"(post) : "scc", "memory");
        ld(slot, j, rr, goff);
        if (j == 0) { const unsigned t = (now32() << 8) - 1024 - (off - step); if ((int)(t - kept) > 0) kept = t; }
        if (j == 2) report();
    };
    auto k0 = [&](int t) { pace_wait(); if (t < 3) gather(3 + t); ld_tile(t, rk, woff + toff(t)); };
    load_cs(0); gather(0);
    pace_wait(); gather(1); load_cs(1); STAMP(1);
    pack_cs(0);
    pace_wait(); pre_sc(); gather(2); off += (pace.offset & 0xffff) - step;
    if (PRO == 0) { k0(0); pack_cs(1); } else { pack_cs(1); k0(0); }
    k0(1);
    VMCNT(3); k0(2);
    STAMP(2);
    VMCNT(3);
    if (BM && hp) { if (lane == 0) *(vlds_u32x2*)(s_act + 2 * wave) = u32x2{kept, off}; BARRIER(); }
    k0(3);
    if (lane == 0) *(vlds_u32x2*)(s_act + 2 * wave) = u32x2{kept, off};
    if (!(BM && hp)) BARRIER();
    origin = *(vlds_u32*)(s_sync + 2);
    STAMP(3);

    u32x4 qp[3];
    auto load_q = [&](int h, int j) { qp[j] = *reinterpret_cast<const u32x4*>(s_q + h * DH + 64 * j + 4 * c); };
    load_q(hp, 0); load_q(hp, 1); load_q(hp, 2);
#pragma unroll
    for (int t = 0; t < 2; ++t)
#pragma unroll
        for (int j = 0; j < 3; ++j) tab[2 + t][j] = *reinterpret_cast<const u32x4*>(s_part + wave * (NI * DH) + ((t * 3 + j) * 64 + lane) * 4);
    if (HX == 0 && hp) {
#pragma unroll
        for (int t = 0; t < 2; ++t)
#pragma unroll
            for (int j = 0; j < 3; ++j)
#pragma unroll
                for (int k = 0; k < 4; ++k) asm volatile("v_swap_b32 %0, %1" : "+v"(tab[t][j][k]), "+v"(tab[2 + t][j][k]));
    }

    for (int i = 0; i < NI; ++i) {
        const bool last = i + 1 == NI;
        enter(i);
        const int h = 2 * i + hp;
        const unsigned hnext = (i + 1) * (2 * HEADB) + woff;
        const auto rn = __builtin_amdgcn_make_buffer_rsrc((void*)(last ? qv : qk), 0, KVB, 0x00020000);
        float* scp = s_w + (h * 4 + rw) * 20;
        f32x4 sc[4];
#pragma unroll
        for (int t = 0; t < 4; ++t)
#pragma unroll
            for (int a = 0; a < 4; ++a) sc[t][a] = bflo(s_sc[h * 256 + (t ^ (HX * hp)) * 64 + a * 16 + rw * 4 + r]);
        float s[4];
#pragma unroll
        for (int t = 0; t < 4; ++t) {
            f32x2 dot, wsum;
#pragma unroll
            for (int j = 0; j < 3; ++j) {
                f32x2 w01, w23, k01, k23;
                dot_cvt(qp[j], tab[t][j], ring[t][j], w01, w23, k01, k23);
                if (j == 0) { wsum = w01 + w23; dot = k01 * w01; } else { wsum += w01; wsum += w23; dot += k01 * w01; }
                dot += k23 * w23;
                asm volatile("" : "+v"(dot), "+v"(wsum));
                PIN();
                if (t == 3) load_q(last ? h : h + 2, j);
                refill(t, j, rn, (last ? woff : hnext) + toff(t));
                if (t == 0 && j == 0) TRACE(i);
            }
            const float a = (dot[0] + dot[1]) - sc[t][1] * (wsum[0] + wsum[1]);
            s[t] = row16_allsum(a) * (sc[t][0] * SCALE_LOG2);
        }
        const float mx = xrow_max(fmaxf(fmaxf(s[0], s[1]), fmaxf(s[2], s[3])));
        float l = 0.f, z = 0.f;
#pragma unroll
        for (int t = 0; t < 4; ++t) {
            const float p = __builtin_amdgcn_exp2f(s[t] - mx);
            l += p;
            s[t] = p * sc[t][2];
            z += s[t] * sc[t][3];
        }
        l = xrow_sum(l);
        z = xrow_sum(z);
        if (c == 0) {
            *reinterpret_cast<f32x4*>(scp + r * 4) = f32x4{s[0], s[1], s[2], s[3]};
            *reinterpret_cast<f32x4*>(scp + 16) = f32x4{mx, l, z, 0.f};
        }
        PIN();
    }
    STAMP(4);

    const float* pwp = s_w + (hp * 4 + rw) * 20 + r * 4;
    f32x4 pw = *reinterpret_cast<const f32x4*>(pwp);
    unsigned tag, lw, lo;
    f32x4 lf;
    f32x2 lz;
    unsigned lp = (hp * 4 * NI + NI - 1) * DH + (lane & 31) * 6;
    f32x2 acc[3][2];
    auto v_acc = [&](int t, int j) {
        const f32x2 p2 = {pw[t], pw[t]};
        const u32x4 vv = ring[t][j];
        const f32x2 v01 = {(float)(int)vv[0], (float)(int)vv[1]}, v23 = {(float)(int)vv[2], (float)(int)vv[3]};
        if (t == 0) { acc[j][0] = p2 * v01; acc[j][1] = p2 * v23; }
        else { acc[j][0] += p2 * v01; acc[j][1] += p2 * v23; }
        asm volatile("" : "+v"(acc[j][0]), "+v"(acc[j][1]));
        PIN();
    };
    auto v_fold = [&](int i, int j) {
        const float x = swap32_add(acc[j][0][0], acc[j][1][0]);
        const float y = swap32_add(acc[j][0][1], acc[j][1][1]);
        s_part[(wave * NI + i) * DH + 64 * j + 4 * c + r] = swap16_add(x, y);
    };
    auto v_done = [&](int i) {
        PIN();
        if (lane == 0) *(vlds_u32*)(s_done + wave) = i + 1;
        PIN();
    };
#pragma unroll
    for (int t = 0; t < LS; ++t)
#pragma unroll
        for (int j = 0; j < 3; ++j) v_acc(t, j);
    for (int i = 0; i < NI - 1; ++i) {
        const unsigned hnext = (i + 1) * (2 * HEADB) + woff;
        enter(i);
#pragma unroll
        for (int k = 0; k < 4; ++k) {
            const int t = (k + LS) & 3;
#pragma unroll
            for (int j = 0; j < 3; ++j) {
                v_acc(t, j);
                if (t == 3 && j == 2) pw = *reinterpret_cast<const f32x4*>(pwp + (i + 1) * 160);
                refill(k, j, rv, hnext + toff(k));
                if (k == 0 && j == 0) TRACE(16 + i);
            }
            if (t == 3) { v_fold(i, 0); v_fold(i, 1); v_fold(i, 2); v_done(i); }
        }
    }
    if (PAD) asm volatile("s_branch .Lkv_tail_%=\n\t.p2align 12\n.Lkv_tail_%=:" ::: "memory");
    STAMP(5);
    TRACE(31);
    while (__builtin_amdgcn_readfirstlane(*(vlds_u32*)(s_sync + 3)) <= NI) __builtin_amdgcn_s_sleep(1);
    PIN();
    tag = s_sync[4 + (lane & 3)];
    lf = *reinterpret_cast<const f32x4*>(s_f + (2 * (NI - 1) + hp) * 8);
    lz = *reinterpret_cast<const f32x2*>(s_f + (2 * (NI - 1) + hp) * 8 + 4);
    origin = *(vlds_u32*)(s_sync + 2);
    lw = last_word(tag, lz[1], lane & 31);
    lo = lane < 32 ? piece_off(hp, lane & 31) : OOB;
    asm volatile("" : "+v"(tag), "+v"(lf), "+v"(lz), "+v"(lw), "+v"(lo), "+v"(lp), "+v"(origin));
    PIN();
#pragma unroll
    for (int t = LS; t < 4; ++t)
#pragma unroll
        for (int j = 0; j < 3; ++j) { v_acc(t, j); if (t == 3) { v_fold(NI - 1, j); PIN(); } }
    v_done(NI - 1);
    STAMP(6);
    {
        const u32x4 dn = *(vlds_u32x4*)(s_done + 4 * hp);
        PIN();
        const u32x4 v = merge(s_part + lp, lf, lz[0], lw);
        if (__builtin_amdgcn_readfirstlane(dn[0] & dn[1] & dn[2] & dn[3]) == NI) publish(v, NI - 1, lo);
        PIN();
    }
    STAMP(7);
    {
        unsigned inbox = WS_PART + (bid >> 4) * GSTR + (bid & 15) * UNITB, obase = bid * 48;
        unsigned xo = tid * 16, yo = tid * 16 + 8192, sel = lane & 1 ? 0x01000504u : 0x05040100u;
        unsigned bo = lane < 16 ? wave * 1024 + (lane & 7) * 128 + (lane >> 3) * 8192 : OOB;
        typedef __attribute__((address_space(3))) float lds_f;
        lds_f* wo = (lds_f*)s_red + wave * 28 + (r < 3 ? c * 6 + r * 2 : 24);
        lds_f* fo = (lds_f*)s_red + (lane & 7) * 28 + 24;
        lds_f* go = (lds_f*)s_red + (lane & 7) * 28 + min(lane >> 3, 5) * 4;
        vlds_u32* dw = (vlds_u32*)(s_done + wave);
        vlds_u32* dr = (vlds_u32*)(s_done + (lane & 7));
        unsigned oo = (lane & 7) == 0 && lane < 48 ? lane : OOB, eo = bid == 0 && lane < 4 ? lane * 2 : OOB;
        asm volatile("" : "+s"(inbox), "+s"(obase), "+v"(xo), "+v"(yo), "+v"(sel), "+v"(bo), "+v"(wo), "+v"(fo), "+v"(go), "+v"(oo), "+v"(eo), "+v"(dw), "+v"(dr));
        const unsigned o = __builtin_amdgcn_readfirstlane(origin) - (15 - (bid >> 4)) * RST;
        while ((int)((now32() << 8) - o) < pace.rd) __builtin_amdgcn_s_sleep(1);
        PIN();
        STAMP(8);
        u32x4 x, y;
        do {
            x = __builtin_amdgcn_raw_buffer_load_b128(rw_, xo, inbox, 16);
            y = __builtin_amdgcn_raw_buffer_load_b128(rw_, yo, inbox, 16);
            const unsigned age = now32() - tstart;
            PIN();
            if (age > SPIN_LIMIT) break;
        } while (__builtin_amdgcn_ballot_w64(((x[3] ^ tag) | (y[3] ^ tag)) > 0xffffu) != 0);
        if (DRY) { __builtin_amdgcn_raw_buffer_store_b32(0u, rw_, bo, inbox, 16); PIN(); }
        STAMP(9);
        const float lx = __uint_as_float(__builtin_amdgcn_perm(__builtin_amdgcn_mov_dpp(x[3], 0xb1, 0xf, 0xf, true), x[3], sel));
        const float ly = __uint_as_float(__builtin_amdgcn_perm(__builtin_amdgcn_mov_dpp(y[3], 0xb1, 0xf, 0xf, true), y[3], sel));
        float M = fmaxf(lx, ly);
        M = fmaxf(M, dpp<0x128>(M));
        M = fmaxf(M, dpp<0x124>(M));
        M = xrow_max(M);
        const float w0 = __builtin_amdgcn_exp2f(lx - M), w1 = __builtin_amdgcn_exp2f(ly - M);
        float a[8];
#pragma unroll
        for (int k = 0; k < 3; ++k) {
            a[2 * k] = w0 * bflo(x[k]) + w1 * bflo(y[k]);
            a[2 * k + 1] = w0 * bfhi(x[k]) + w1 * bfhi(y[k]);
        }
        a[6] = a[7] = w0 + w1;
        float b[4], d[2];
#pragma unroll
        for (int k = 0; k < 4; ++k) b[k] = swap32_add(a[k], a[4 + k]);
#pragma unroll
        for (int k = 0; k < 2; ++k) d[k] = swap16_add(b[k], b[2 + k]);
#pragma unroll
        for (int k = 0; k < 2; ++k) { d[k] += dpp<0x128>(d[k]); d[k] += dpp<0x124>(d[k]); }
        if (c < 4) {
            *(__attribute__((address_space(3))) f32x2*)wo = f32x2{d[0], r < 3 ? d[1] : M};
            PIN();
            *dw = 3 * NI;
        }
        PIN();
        STAMP(10);
        const unsigned fin = *dr;
        PIN();
        f32x2 sm = *(__attribute__((address_space(3))) f32x2*)fo;
        f32x4 ov = *(__attribute__((address_space(3))) f32x4*)go;
        asm volatile("" : "+v"(sm), "+v"(ov));
        if (__builtin_amdgcn_ballot_w64(fin != 3 * NI) == 0) {
            float MM = fmaxf(sm[1], dpp<0xb1>(sm[1]));
            MM = fmaxf(MM, dpp<0x4e>(MM));
            MM = fmaxf(MM, dpp<0x141>(MM));
            const float ff = __builtin_amdgcn_exp2f(sm[1] - MM);
            float e[5] = {ff * ov[0], ff * ov[1], ff * ov[2], ff * ov[3], ff * sm[0]};
#pragma unroll
            for (int k = 0; k < 5; ++k) e[k] += dpp<0xb1>(e[k]);
#pragma unroll
            for (int k = 0; k < 5; ++k) e[k] += dpp<0x4e>(e[k]);
#pragma unroll
            for (int k = 0; k < 5; ++k) e[k] += dpp<0x141>(e[k]);
            const float inv = __builtin_amdgcn_rcpf(e[4]);
            __builtin_amdgcn_raw_buffer_store_b64(u32x2{pk_bf16(f32x2{e[0] * inv, e[1] * inv}), pk_bf16(f32x2{e[2] * inv, e[3] * inv})}, ro, oo, obase, OST);
            if (bid == 0) __builtin_amdgcn_raw_buffer_store_b16((unsigned short)(tag >> 16), rw_, eo, WS_EPOCH, 16);
        }
    }
    __builtin_amdgcn_endpgm();
}

#define VAR_XS(p) ((p) / 1000 % 2)
template <int X, int PRO, int GATE, int STAG>
static void launch_cfg(void* const* d_in, void* d_out, void* d_ws, hipStream_t stream, float t_us, float start_us, float off_us, float debt_us, float adj_pct, float pub_us) {
    Pace pace;
    pace.step = (unsigned)(t_us * 25600.f / 128.f);
    pace.start = (int)(start_us * 25600.f);
    const float zone = PRO / 100 % 10 ? (int)off_us * 0.01f : 0.f; off_us -= (int)off_us;
    pace.offset = (int)(off_us * 25600.f) | (int)(zone * 6400.f) << 16; off_us += 2 * zone;
    pace.bias = (int)(debt_us * 25600.f);
    pace.rd = (int)((start_us + off_us + t_us * ((129.f + VAR_XS(PRO)) / 128.f) + adj_pct) * 25600.f);
    pace.pub = (int)((start_us + off_us + t_us * ((129.f + VAR_XS(PRO)) / 128.f) + pub_us) * 25600.f);
    kv_sweep_kernel<X, PRO, GATE, STAG, 1><<<NWG, 576, 0, stream>>>(d_in[0], d_in[1], d_in[2], d_in[3], d_in[4], d_in[5], d_in[6], d_in[7],
        d_in[8], d_in[9], d_in[10], d_ws, d_out, pace EXTVAL);
}

#define CFG 0, 123, 5002501, 112
#define PRM 100.75f, 0.5f, 10.8f, 0.055f, 1.6f, 0.6f
extern "C" void kernel_launch(void* const* d_in, const int* in_sizes, int n_in,
                              void* d_out, int out_size, void* d_ws, size_t ws_size,
                              hipStream_t stream) {
    launch_cfg<CFG>(d_in, d_out, d_ws, stream, PRM);
}
```

```cpp
#define PIDX 68
#define PMAX 1
#define PSUB 0
#define PWAVE 0
#include <hip/hip_runtime.h>
#include <hip/hip_bf16.h>
#include <cstdint>
#include <type_traits>

using bf16 = __hip_bfloat16;
using u32x4 = __attribute__((ext_vector_type(4))) unsigned;
using u32x3 = __attribute__((ext_vector_type(3))) unsigned;
using u32x2 = __attribute__((ext_vector_type(2))) unsigned;
using u64x8 = __attribute__((ext_vector_type(8))) unsigned long long;
using f32x2 = __attribute__((ext_vector_type(2))) float;
using f32x4 = __attribute__((ext_vector_type(4))) float;
using bf16x2 = __attribute__((ext_vector_type(2))) __bf16;

constexpr int DH = 192;
constexpr int HALF = 96;
constexpr int NH = 32;
constexpr int SKV = 16384;
constexpr int NWG = 256;
constexpr int NI = 16;
constexpr unsigned ROWB = DH * 4;
constexpr unsigned HEADB = SKV * ROWB;
constexpr unsigned KVB = NH * HEADB;
constexpr unsigned CSB = SKV * DH * 2;
constexpr unsigned OOB = 0x80000000u;
constexpr float SCALE_LOG2 = 1.4426950408889634f * 0.07216878364870322f;
constexpr unsigned SPIN_LIMIT = 20000000u;

constexpr unsigned UNITB = NWG * 64;
constexpr unsigned GSTR = 16 * UNITB + 256;
constexpr unsigned WS_PART = 0;
constexpr unsigned WS_EPOCH = NWG * (UNITB + 1024);
constexpr unsigned WS_REP = WS_EPOCH + 256;
constexpr unsigned WS_PROBE = WS_REP + 256 * 512;
constexpr unsigned WS_BYTES = WS_PROBE + 4096;

__device__ __forceinline__ float bflo(unsigned u) { return __uint_as_float(u << 16); }
__device__ __forceinline__ float bfhi(unsigned u) { return __uint_as_float(u & 0xffff0000u); }
__device__ __forceinline__ float rbf(float x) { return __bfloat162float(__float2bfloat16(x)); }
__device__ __forceinline__ unsigned pk_bf16(f32x2 a) { return __builtin_bit_cast(unsigned, __builtin_convertvector(a, bf16x2)); }
__device__ __forceinline__ void dot_cvt(const u32x4 q, const u32x4 t, const u32x4 ki, f32x2& w01, f32x2& w23, f32x2& k01, f32x2& k23) {
    float w0, w1, w2, w3;
    float k0 = __uint_as_float(ki[0]), k1 = __uint_as_float(ki[1]), k2 = __uint_as_float(ki[2]), k3 = __uint_as_float(ki[3]);
    asm("v_dot2_f32_bf16 %0, %8, %12, 0\n\tv_dot2_f32_bf16 %1, %9, %13, 0\n\tv_dot2_f32_bf16 %2, %10, %14, 0\n\tv_dot2_f32_bf16 %3, %11, %15, 0\n\t"
        "v_cvt_f32_i32 %4, %4\n\tv_cvt_f32_i32 %5, %5\n\tv_cvt_f32_i32 %6, %6\n\tv_cvt_f32_i32 %7, %7"
        : "=&v"(w0), "=&v"(w1), "=&v"(w2), "=&v"(w3), "+v"(k0), "+v"(k1), "+v"(k2), "+v"(k3)
        : "v"(q[0]), "v"(q[1]), "v"(q[2]), "v"(q[3]), "v"(t[0]), "v"(t[1]), "v"(t[2]), "v"(t[3]));
    w01 = f32x2{w0, w1}; w23 = f32x2{w2, w3}; k01 = f32x2{k0, k1}; k23 = f32x2{k2, k3};
}

template <int CTRL> __device__ __forceinline__ float dpp(float x) {
    return __builtin_bit_cast(float, __builtin_amdgcn_mov_dpp(__builtin_bit_cast(int, x), CTRL, 0xf, 0xf, true));
}
__device__ __forceinline__ float row16_allsum(float x) {
    x += dpp<0x128>(x);
    x += dpp<0x124>(x);
    x += dpp<0x122>(x);
    x += dpp<0x121>(x);
    return x;
}
__device__ __forceinline__ float row16_allmax(float x) {
    x = fmaxf(x, dpp<0x128>(x));
    x = fmaxf(x, dpp<0x124>(x));
    x = fmaxf(x, dpp<0x122>(x));
    x = fmaxf(x, dpp<0x121>(x));
    return x;
}
__device__ __forceinline__ float xrow_max(float x) {
    auto s = __builtin_amdgcn_permlane16_swap(__float_as_uint(x), __float_as_uint(x), false, false);
    x = fmaxf(__uint_as_float(s[0]), __uint_as_float(s[1]));
    auto t = __builtin_amdgcn_permlane32_swap(__float_as_uint(x), __float_as_uint(x), false, false);
    return fmaxf(__uint_as_float(t[0]), __uint_as_float(t[1]));
}
__device__ __forceinline__ float xrow_sum(float x) {
    auto s = __builtin_amdgcn_permlane16_swap(__float_as_uint(x), __float_as_uint(x), false, false);
    x = __uint_as_float(s[0]) + __uint_as_float(s[1]);
    auto t = __builtin_amdgcn_permlane32_swap(__float_as_uint(x), __float_as_uint(x), false, false);
    return __uint_as_float(t[0]) + __uint_as_float(t[1]);
}
__device__ __forceinline__ float swap32_add(float a, float b) {
    auto t = __builtin_amdgcn_permlane32_swap(__float_as_uint(a), __float_as_uint(b), false, false);
    return __uint_as_float(t[0]) + __uint_as_float(t[1]);
}
__device__ __forceinline__ float swap16_add(float a, float b) {
    auto t = __builtin_amdgcn_permlane16_swap(__float_as_uint(a), __float_as_uint(b), false, false);
    return __uint_as_float(t[0]) + __uint_as_float(t[1]);
}
__device__ __forceinline__ int row16_sum(int x) {
    x += __builtin_amdgcn_mov_dpp(x, 0x128, 0xf, 0xf, true);
    x += __builtin_amdgcn_mov_dpp(x, 0x124, 0xf, 0xf, true);
    x += __builtin_amdgcn_mov_dpp(x, 0x122, 0xf, 0xf, true);
    x += __builtin_amdgcn_mov_dpp(x, 0x121, 0xf, 0xf, true);
    return x;
}
__device__ __forceinline__ unsigned now32() { return (unsigned)__builtin_amdgcn_s_memrealtime(); }

#define PLAN_EXTRA
#define GNT " nt"
#define GSA 0x22222222
#define GSB 0x55445544
#define CTRL 2, 4, 0, 2560, 768, 13, 9, 200, 984, 2000, 0, 6, 400, -1024
#define NSTAMP 12
#define STAMP(i) do { if ((i) == PIDX - 50) { __builtin_amdgcn_sched_barrier(0); probe = now32(); __builtin_amdgcn_sched_barrier(0); } } while (0)
#define PSTAMP(i) do { if ((i) == PIDX) { __builtin_amdgcn_sched_barrier(0); probe = now32(); asm volatile("" : "+s"(probe) :: "memory"); __builtin_amdgcn_sched_barrier(0); } } while (0)
#define TRACE(i) do { if ((i) == PIDX) probe = tstart + ((int)(kept - (tstart << 8)) >> 8); } while (0)
#define HSTAMP(i)
#define PIN() do { asm volatile("" ::: "memory"); __builtin_amdgcn_sched_barrier(0); } while (0)
#define BARRIER() do { asm volatile("s_waitcnt lgkmcnt(0)" ::: "memory"); __builtin_amdgcn_s_barrier(); PIN(); } while (0)
#define VMCNT(n) asm volatile("s_waitcnt vmcnt(%0)" :: "i"(n) : "memory")

typedef volatile __attribute__((address_space(3))) unsigned vlds_u32;
typedef volatile __attribute__((address_space(3))) u32x2 vlds_u32x2;
typedef volatile __attribute__((address_space(3))) u32x4 vlds_u32x4;

struct Plan { const char* rows; const char* phase; int hx; char sx, tx; };
constexpr Plan PLANS[] = {
    {"1234tcab0s", "vwabc01234h", 2}, {"432cb1ts0a", "1vwacb4320h", 0}, {"123cb0ts4a", "vwabc01234h", 0}, {"103cb2ts4a", "vwabc01234h", 0},
    {"0234catb1s", "vwabc01234h", 2}, {"013cb2ta4s", "vwabc01234h", 0}, {"1234tcab0s", "vwabc01234h", 0}, {"123cb0st4a", "vwabc01234h", 0},
    {"1234tcab0s", "vwabc01234h", 2, 'c'}, {"432cb1ts0a", "1vwacb4320h", 0, 'b', 'c'}, {"123cb0ts4a", "vwabc01234h", 0, 'b', 'c'}, {"0123t4cabs", "vwabc01234h", 2},
    {"1234t0cabs", "vwabc01234h", 2}, {"1234tcab0s", "vwabc01234h", 2, 'c', 'a'},
    PLAN_EXTRA
};
constexpr int sym_pos(const char* s, char c) { for (int i = 0; s[i]; ++i) if (s[i] == c) return i; return -1; }
template <int N> __device__ __forceinline__ int place(const char* dst, const char (&src)[N], int x) {
    int y = 0;
#pragma unroll
    for (int i = 0; i < N - 1; ++i) y |= (x >> i & 1) << sym_pos(dst, src[i]);
    return y;
}
#define EXTARG
#define EXTVAL
struct Pace {
    unsigned step;
    int start;
    int offset;
    int bias;
    int rd;
    int pub;
};

template <int X, int OPT, int GATE, int TP, int SLP>
__global__ __launch_bounds__(576) __attribute__((aligned(4096))) void kv_sweep_kernel(
    const void*, const void*, const void*, const void*, const void*, const void*, const void*, const void*,
    const void*, const void*, const void*, void*, void*, Pace EXTARG)
{
    __shared__ __attribute__((aligned(16))) unsigned s_q[NH * DH];
    __shared__ __attribute__((aligned(16))) unsigned short s_sc[NH * 256];
    __shared__ __attribute__((aligned(16))) float s_w[NH * 4 * 20];
    __shared__ __attribute__((aligned(16))) float s_part[8 * NI * DH];
    __shared__ __attribute__((aligned(16))) float s_red[8 * 28];
    __shared__ __attribute__((aligned(16))) float s_f[NH * 8];
    __shared__ __attribute__((aligned(16))) unsigned s_done[8];
    __shared__ __attribute__((aligned(16))) unsigned s_junk[16];
    __shared__ __attribute__((aligned(16))) unsigned s_sync[8];
    __shared__ __attribute__((aligned(16))) unsigned s_act[16];

    unsigned probe = 0;
    constexpr int HOLD = GATE % 10;
    constexpr int DRY = OPT % 10 & 1, LS = 2, PAD = OPT % 10 / 2 & 1, PF = OPT / 10 % 10, RST = GATE / 1000 % 10 * 256, SLEAD = GATE / 10000 % 10, STA = 16, WIDE = OPT / 100 % 10, OST = 0, PRO = OPT / 1000;
    unsigned long long pc0, t0;
    u64x8 ka, kb;
    asm volatile("s_load_dwordx16 %3, %4, 0x40\n\ts_load_dwordx16 %2, %4, 0x0\n\ts_memrealtime %1\n\ts_getpc_b64 %0\n.Lkv_beg_%5_%6_%7_%8:"
                 : "=&s"(pc0), "=&s"(t0), "=&s"(ka), "=&s"(kb) : "s"(__builtin_amdgcn_kernarg_segment_ptr()), "i"(X), "i"(OPT), "i"(GATE), "i"(TP));
    __builtin_amdgcn_sched_barrier(0);
    const int tid = threadIdx.x;
    const int lane = tid & 63;
    int wave = __builtin_amdgcn_readfirstlane(tid >> 6);
    const int r = lane >> 4;
    const int c = lane & 15;
    const int bid = blockIdx.x;
    const int m = (bid & 7) * 32 + (bid >> 3);
    const int rw = wave & 3, hp = wave >> 2;
    constexpr Plan PL = PLANS[GATE / 1000000];
    constexpr unsigned SA = GSA, SB = GSB, QD = TP >> 6 ? 50 + (TP >> 6) * 50u : 0, PM = GATE / 100 % 10 & 3, BM = GATE / 100 % 10 >> 2;
    constexpr int HX = PL.hx, RS = 16 << sym_pos(PL.rows, 's') | (PL.sx ? 16 << sym_pos(PL.rows, PL.sx) : 0), RT = 16 << sym_pos(PL.rows, 't') | (PL.tx ? 16 << sym_pos(PL.rows, PL.tx) : 0);
    const int grow = place(PL.rows, "abc01234", bid) * 16;
    auto srow = [&](int t) { return (t & 1) * RS ^ (t >> 1) * RT; };
    const int row0 = grow + rw * 4;
    unsigned co = r * 384 + c * 8, so1 = r * 384 + (c < 8 ? c * 8 + 320 : c * 8 - 64), rb0 = (row0 ^ RT * hp) * 384u;
    asm volatile("s_waitcnt lgkmcnt(0)" : "+s"(t0), "+s"(ka), "+s"(kb), "+v"(co), "+v"(so1), "+s"(rb0), "+s"(wave));
    typedef const __attribute__((address_space(1))) char* gptr;
    void* const q = (void*)ka[0]; void* const qk = (void*)ka[1]; void* const qv = (void*)ka[4]; void* const q_cos = (void*)ka[7];
    const gptr ksc = (gptr)ka[2], kzp = (gptr)ka[3], vsc = (gptr)ka[5], vzp = (gptr)ka[6];
    void* const q_sin = (void*)kb[0]; void* const k_cos = (void*)kb[1]; void* const k_sin = (void*)kb[2]; void* const out = (void*)kb[4];
    unsigned char* const ws = (unsigned char*)(__attribute__((address_space(1))) unsigned char*)kb[3];
    const Pace pace = {(unsigned)kb[5], (int)(kb[5] >> 32), (int)kb[6], (int)(kb[6] >> 32), (int)kb[7], (int)(kb[7] >> 32)};
    const unsigned tstart = (unsigned)t0;
    const unsigned tsched = TP & 16 ? now32() : tstart;
    PSTAMP(69);
    probe = tstart;
    const auto rw_ = __builtin_amdgcn_make_buffer_rsrc((void*)ws, 0, WS_BYTES, 0x00020000);
    const auto ro = __builtin_amdgcn_make_buffer_rsrc((void*)out, 0, NH * DH * 2, 0x00020000);

    auto merge = [&](const float* p, f32x4 f, float zs, unsigned w3) {
        f32x2 u[4][3], a[3];
#pragma unroll
        for (int w = 0; w < 4; ++w)
#pragma unroll
            for (int k = 0; k < 3; ++k) u[w][k] = *reinterpret_cast<const f32x2*>(p + w * NI * DH + 2 * k);
        asm volatile("" : "+v"(u[0][0]), "+v"(u[0][1]), "+v"(u[0][2]), "+v"(u[1][0]), "+v"(u[1][1]), "+v"(u[1][2]),
                          "+v"(u[2][0]), "+v"(u[2][1]), "+v"(u[2][2]), "+v"(u[3][0]), "+v"(u[3][1]), "+v"(u[3][2]));
#pragma unroll
        for (int k = 0; k < 3; ++k) a[k] = (f[0] * u[0][k] - zs + f[1] * u[1][k]) + (f[2] * u[2][k] + f[3] * u[3][k]);
        return u32x4{pk_bf16(a[0]), pk_bf16(a[1]), pk_bf16(a[2]), w3};
    };
    auto last_word = [&](unsigned tag, float ls, int c6) { return tag | ((__float_as_uint(ls) >> (16 * (c6 & 1))) & 0xffffu); };
    auto piece_off = [&](int hp, int c6) { return (hp * 8 + (c6 >> 2)) * UNITB + (c6 & 3) * 16; };
    auto publish = [&](u32x4 v, int i, unsigned vo) {
        __builtin_amdgcn_raw_buffer_store_b128(v, rw_, vo, WS_PART + i * GSTR + m * 64, STA);
        asm volatile("s_nop 1" :: "v"(v) : "memory");
    };

    if (__builtin_expect_with_probability(wave == 8, 1, 0.45)) {
        if (TP & 32) __builtin_amdgcn_s_setprio(3);
        HSTAMP(11);
        unsigned clen;
        asm volatile("s_mov_b32 %0, .Lkv_end_%1_%2_%3_%4-.Lkv_beg_%1_%2_%3_%4" : "=s"(clen) : "i"(X), "i"(OPT), "i"(GATE), "i"(TP));
        const auto rcode = __builtin_amdgcn_make_buffer_rsrc((void*)(pc0 & ~127ull), 0, clen, 0x00020000);
        const int jj = bid >> 3;
        auto warm_ld = [&](int n) {
            return __builtin_amdgcn_raw_buffer_load_b32(rcode, lane < n ? (jj * 9 + lane) * 64 : OOB, 0, 0);
        };
        auto warm_code = [&](int n) { if (n) { const unsigned x = warm_ld(n); asm volatile("" :: "v"(x)); } };
        constexpr int WN[10][4] = {{0, 0, 0, 0}, {64, 0, 0, 0}, {9, 0, 0, 0}, {64, 9, 9, 0}, {9, 9, 9, 0}, {18, 0, 0, 0}, {18, 18, 18, 0}, {9, 9, 0, 9}, {36, 0, 0, 0}, {9, 0, 9, 0}};
        constexpr int FREE = 0x3fff & ~(16 << sym_pos(PL.rows, 'a') | 16 << sym_pos(PL.rows, 'b') | 16 << sym_pos(PL.rows, 'c'));
        constexpr int F1 = 1 << (31 - __builtin_clz(FREE)), F2 = 1 << (31 - __builtin_clz(FREE - F1));
        const unsigned trow = (grow & ~FREE) + (jj & 1 ? FREE - F1 - F2 : 0) + (jj & 2 ? F2 : 0) + (jj & 4 ? F1 : 0) + (jj & 8 ? SKV : 0);
        int tp = jj >> 4;
        auto touch = [&]() {
            const unsigned long long a = (unsigned long long)(tp < NI ? qk : qv) + ((tp & 15) * 2 * SKV + trow) * ROWB;
            if (lane == 0)
                asm volatile("s_mov_b32 m0, %1\n\ts_nop 0\n\tglobal_load_lds_dword %0, off nt" :: "v"(a), "s"((unsigned)(unsigned long long)(__attribute__((address_space(3))) unsigned*)s_junk) : "memory");
            tp += 2;
            PIN();
        };
        const unsigned x0 = warm_ld(WN[PF][0]);
        if (TP & 4) touch(); else tp += 2;
        PIN();
        if (QD) while (now32() - tsched < QD) __builtin_amdgcn_s_sleep(1);
        u32x3 lc[2], ls[2];
        u32x4 qraw[12];
        {
            const auto rq = __builtin_amdgcn_make_buffer_rsrc((void*)q, 0, NH * DH * 2, 0x00020000);
            const auto rqc = __builtin_amdgcn_make_buffer_rsrc((void*)q_cos, 0, DH * 2, 0x00020000);
            const auto rqs = __builtin_amdgcn_make_buffer_rsrc((void*)q_sin, 0, DH * 2, 0x00020000);
#pragma unroll
            for (int k = 0; k < 12; ++k) qraw[k] = __builtin_amdgcn_raw_buffer_load_b128(rq, lane * 16 + (k & 3) * 1024, (k >> 2) * 4096, 0);
#pragma unroll
            for (int k = 0; k < 2; ++k) {
                lc[k] = __builtin_amdgcn_raw_buffer_load_b96(rqc, c * 12 + k * 192, 0, 0);
                ls[k] = __builtin_amdgcn_raw_buffer_load_b96(rqs, c * 12 + k * 192, 0, 0);
            }
            PIN();
        }
        const unsigned tag = ((unsigned)__builtin_amdgcn_raw_buffer_load_b16(rw_, (lane & 3) * 2, WS_EPOCH, 16) + 1u) << 16;
        PIN();
        HSTAMP(2);
        unsigned* const s_raw = reinterpret_cast<unsigned*>(s_part) + NI * DH / 2;
#pragma unroll
        for (int k = 0; k < 12; ++k) *reinterpret_cast<u32x4*>(s_raw + k / 6 * (NI * DH) + k % 6 * 256 + lane * 4) = qraw[k];
        f32x2 fc[2][3], fs[2][3];
#pragma unroll
        for (int k = 0; k < 2; ++k)
#pragma unroll
            for (int u = 0; u < 3; ++u) { fc[k][u] = f32x2{bflo(lc[k][u]), bfhi(lc[k][u])}; fs[k][u] = f32x2{bflo(ls[k][u]), bfhi(ls[k][u])}; }
        PIN();
        HSTAMP(3);
#pragma nounroll
        for (int it = 0; it < 4; ++it) {
            unsigned qa[2][3], qb[2][3];
#pragma unroll
            for (int g = 0; g < 2; ++g)
#pragma unroll
                for (int u = 0; u < 3; ++u) {
                    const unsigned* src = s_raw + g * (NI * DH) + (it * 4 + r) * HALF + 3 * c;
                    qa[g][u] = src[u]; qb[g][u] = src[HALF / 2 + u];
                }
#pragma unroll
            for (int g = 0; g < 2; ++g)
#pragma unroll
                for (int u = 0; u < 3; ++u) {
                    const f32x2 a = {bflo(qa[g][u]), bfhi(qa[g][u])}, b = {bflo(qb[g][u]), bfhi(qb[g][u])};
                    const f32x2 o0 = a * fc[0][u] - b * fs[0][u], o1 = b * fc[1][u] + a * fs[1][u];
                    const unsigned l0 = pk_bf16(f32x2{o0[0], o1[0]}), l1 = pk_bf16(f32x2{o0[1], o1[1]});
                    unsigned* dst = s_q + (16 * g + 4 * it + r) * DH + 6 * c + 2 * u;
                    *reinterpret_cast<u32x2*>(dst) = u32x2{l0, l1};
                    *reinterpret_cast<u32x2*>(dst + HALF) = u32x2{__builtin_amdgcn_alignbit(l0, l0, 16) ^ 0x80000000u, __builtin_amdgcn_alignbit(l1, l1, 16) ^ 0x80000000u};
                }
        }
        HSTAMP(1);
        asm volatile("" :: "v"(x0));
        if (TP & 32) __builtin_amdgcn_s_setprio(0);
        unsigned origin = tsched << 8;
        if (lane < 8) { s_done[lane] = 0; s_sync[lane] = lane < 3 ? origin : lane < 4 ? 0 : tag; }
        PIN();
        BARRIER();
        const int mh = lane >> 5, mc = lane & 31;
        const unsigned rtag = __builtin_amdgcn_readlane(tag, 0) | __builtin_amdgcn_readlane(tag, 1) >> 16;
        constexpr int RPX = 2;
        constexpr int gp[] = {CTRL};
        constexpr int KS = gp[0], SR = gp[1], IB2 = gp[2], EHI = gp[3], IB = gp[4], FR = gp[5], RSH = gp[6], UP = gp[8], DN = gp[9], SLIP = gp[10], FF = gp[11], DMAX = gp[12], ELO = gp[13];
        constexpr unsigned RDLY = gp[7];
        const int rj = ((bid >> 3) + 5 * (bid & 7)) & 31;
        const int rid = rj % (32 / RPX) == 0 ? rj / (32 / RPX) * 8 + (bid & 7) : -1;
        unsigned wround = now32() >> RSH, rround = wround, tick = 0, target = origin;
        bool warmed = false, synced = false;
        int drift = 0;
        const unsigned round0 = rround;
        int i = 0;
        auto kept_now = [&](unsigned now) {
            const u32x2 a = *(vlds_u32x2*)(s_act + 2 * (lane & 7));
            const int late = max((int)(a[0] - origin), (int)((now << 8) - a[1] - origin));
            return origin + (__builtin_amdgcn_readfirstlane(row16_sum(late)) >> 4);
        };
        auto steer = [&](unsigned now) {
            if ((now >> 5) != tick) {
                tick = now >> 5;
                target += drift;
                origin += max(-DN, min(UP + drift, (int)(target - origin)));
                if (SLIP) { const int l2 = (int)(kept_now(now) - origin) - SLIP; if (l2 > 0) origin += l2; }
                if (HOLD && !synced) target = origin - (HOLD - 1) * 82;
                if (lane == 0) *(vlds_u32*)(s_sync + 2) = origin;
                PIN();
            }
        };
        auto finished = [&]() {
            const u32x4 a = *(vlds_u32x4*)s_done, b = *(vlds_u32x4*)(s_done + 4);
            return __builtin_amdgcn_readfirstlane(min(min(min(a[0], a[1]), min(a[2], a[3])), min(min(b[0], b[1]), min(b[2], b[3]))));
        };
        while (i < NI - 2) {
            const unsigned now = now32();
            if ((now >> RSH) != wround) {
                wround = now >> RSH;
                if (rid >= 0 && (now & ((1u << RSH) - 1)) < 48)
                    __builtin_amdgcn_raw_buffer_store_b64(u32x2{kept_now(now), rtag + wround}, rw_, lane == 0 ? rid * 8 : OOB, WS_REP + (wround & 255) * 512, 16);
                PIN();
                continue;
            }
            if ((int)(((now - RDLY) >> RSH) - rround) > 0) {
                rround = (now - RDLY) >> RSH;
                const u32x2 x = __builtin_amdgcn_raw_buffer_load_b64(rw_, c * 8, WS_REP + (rround & 255) * 512, 16);
                const int d = (int)(x[0] - origin);
                const bool ok = x[1] == rtag + rround && d < (32 << 16) && d > -(32 << 16);
                const int n = __builtin_popcountll(__builtin_amdgcn_ballot_w64(ok));
                if (n >= 48) {
                    const unsigned nt = origin + __builtin_amdgcn_readfirstlane((int)(row16_allsum(ok ? (float)d : 0.f) * 4.f / (float)n)) - pace.bias;
                    if (KS && synced && rround - round0 > SR) drift = max(0, min(drift + ((max(ELO, min((int)(nt - target) + FF * drift, EHI)) - (rround - round0 < FR ? IB : IB2)) >> (3 + KS)), DMAX));
                    target = nt + FF * drift;
                    synced = true;
                    const int lost = min((int)(kept_now(now32()) - origin) - pace.bias, (int)(target - origin));
                    if (lost > 0) origin += lost;
                }
                PIN();
                continue;
            }
            steer(now);
            if ((TP & 1) && tp < 2 * NI && 4 * (int)((now << 8) - origin) > (16 * tp + (jj & 15) - (TP & 8 ? 40 : 24)) * (int)pace.step) { touch(); continue; }
            if (WN[PF][1] && !warmed && (int)((now << 8) - origin) > 60 * (int)pace.step) { warm_code(WN[PF][1]); warmed = true; continue; }
            if (finished() <= (unsigned)i) { __builtin_amdgcn_s_sleep(2); continue; }
            PIN();
            if (i == 0) {
                f32x4 ml[4], f;
#pragma unroll
                for (int w = 0; w < 4; ++w) ml[w] = *reinterpret_cast<const f32x4*>(s_w + (mc * 4 + w) * 20 + 16);
                const float M = fmaxf(fmaxf(ml[0][0], ml[1][0]), fmaxf(ml[2][0], ml[3][0]));
                float L = 0.f, zs = 0.f;
#pragma unroll
                for (int w = 0; w < 4; ++w) { f[w] = __builtin_amdgcn_exp2f(ml[w][0] - M); L += f[w] * ml[w][1]; zs += f[w] * ml[w][2]; }
                const float inv = 1.f / L;
                *reinterpret_cast<f32x4*>(s_f + mc * 8) = f * inv;
                *reinterpret_cast<f32x2*>(s_f + mc * 8 + 4) = f32x2{zs * inv, M + __builtin_amdgcn_logf(L)};
                PIN();
                if (lane == 0) *(vlds_u32*)(s_sync + 3) = NI + 1;
                PIN();
            }
            if (i == NI - 3) warm_code(WN[PF][2]);
            const f32x2 zl = *reinterpret_cast<const f32x2*>(s_f + (2 * i + mh) * 8 + 4);
            const u32x4 v = merge(s_part + (mh * 4 * NI + i) * DH + mc * 6, *reinterpret_cast<const f32x4*>(s_f + (2 * i + mh) * 8), zl[0], last_word(tag, zl[1], mc));
            PIN();
            *reinterpret_cast<u32x4*>(s_part + (mh * 4 * NI + i) * DH + mc * 4) = v;
            PIN();
            ++i;
        }
        u32x4 v[NI - 1];
#pragma unroll
        for (int i = 0; i < NI - 2; ++i) v[i] = *reinterpret_cast<const u32x4*>(s_part + (mh * 4 * NI + i) * DH + mc * 4);
        f32x4 f = *reinterpret_cast<const f32x4*>(s_f + (2 * (NI - 2) + mh) * 8);
        f32x2 zl = *reinterpret_cast<const f32x2*>(s_f + (2 * (NI - 2) + mh) * 8 + 4);
        PIN();
#pragma unroll
        for (int i = 0; i < NI - 2; ++i) asm volatile("" : "+v"(v[i]));
        asm volatile("" : "+v"(f), "+v"(zl));
        v[NI - 2] = v[0];
        bool have = false, sent = false;
        while (!(have && sent)) {
            const unsigned now = now32();
            if (!have) {
                steer(now);
                if (finished() > NI - 2) {
                    PIN();
                    v[NI - 2] = merge(s_part + (mh * 4 * NI + NI - 2) * DH + mc * 6, f, zl[0], last_word(tag, zl[1], mc));
                    have = true;
                    warm_code(WN[PF][3]);
                    HSTAMP(5);
                }
            }
            if (!sent && (int)((now << 8) - origin) >= pace.pub) {
                HSTAMP(6);
#pragma unroll
                for (int i = 0; i < NI - 2; ++i) publish(v[i], i, piece_off(mh, mc));
                sent = true;
            }
            if (!(have && sent)) __builtin_amdgcn_s_sleep(1);
            PIN();
        }
        publish(v[NI - 2], NI - 2, piece_off(mh, mc));
        HSTAMP(7);
        asm volatile(".Lkv_end_%0_%1_%2_%3:" :: "i"(X), "i"(OPT), "i"(GATE), "i"(TP));
        return;
    }

    const int ph = place(PL.phase, "abc01234", bid) + place(PL.phase, "vw", rw) + place(PL.phase, "h", hp);
    unsigned off = pace.start + ((pace.step * ph) >> 11);
    unsigned origin = tsched << 8, kept = 0;
    unsigned step = pace.step;
    auto pace_wait = [&]() {
        const unsigned tgt = __builtin_amdgcn_readfirstlane(origin) + off;
        unsigned now = now32() << 8;
        kept = ((int)(now - tgt) > 0 ? now : tgt) - off;
        while ((int)(tgt - now) > 0) { if (SLP) __builtin_amdgcn_s_sleep(SLP); now = now32() << 8; }
        off += step;
    };
    auto report = [&]() {
        if (lane == 0) *(vlds_u32x2*)(s_act + 2 * wave) = u32x2{kept, off};
        origin = *(vlds_u32*)(s_sync + 2);
        PIN();
    };
#define TRV (kept - (tstart << 8))

    auto pick = [](int n, gptr p0, gptr p1, gptr p2, gptr p3) { return (n & 2) ? ((n & 1) ? p3 : p2) : ((n & 1) ? p1 : p0); };
    auto dma = [](gptr a, unsigned dst) { asm volatile("s_mov_b32 m0, %1\n\ts_nop 0\n\tglobal_load_lds_dwordx4 %0, off nt" :: "v"(a), "s"(__builtin_amdgcn_readfirstlane(dst)) : "memory"); };
    const int w8 = rw + 4 * hp;
    const unsigned sdst = (unsigned)(unsigned long long)(__attribute__((address_space(3))) unsigned short*)s_sc + w8 * 1024;
    auto gather = [&](unsigned slot) {
        const gptr a = pick(lane >> 1, ksc, kzp, vsc, vzp) + ((w8 * 2 + (lane >> 5)) * SKV + (grow ^ srow(lane >> 3 & 3)) + (lane & 1) * 8) * 2;
        if ((SA >> 4 * w8 & 15) == slot) dma(a, sdst);
        if ((SB >> 4 * w8 & 15) == slot) dma(a + 32 * SKV, sdst + 8192);
        PIN();
    };
    auto pre_sc = [&]() {
        const gptr a = pick(grow >> 4, ksc, kzp, vsc, vzp) + ((w8 * 2 + (lane >> 5)) * SKV + (grow & ~63 ^ srow(lane >> 3 & 3))) * 2 + (lane & 7) * 16;
        const unsigned junk = (unsigned)(unsigned long long)(__attribute__((address_space(3))) float*)s_part + (3 * NI * DH - 256) * 4;
        if (PM >= 2) dma(a, junk);
        if (PM >= 1) dma(a + 32 * SKV, junk);
        PIN();
    };
    const auto rc = __builtin_amdgcn_make_buffer_rsrc((void*)k_cos, 0, CSB, 0x00020000);
    const auto rs = __builtin_amdgcn_make_buffer_rsrc((void*)k_sin, 0, CSB, 0x00020000);
    u32x2 raw[2][6];
    auto load_cs = [&](int t) {
        const unsigned rb = t == 0 ? rb0 : (row0 ^ RT * hp ^ RS) * 384u;
        raw[t][0] = __builtin_amdgcn_raw_buffer_load_b64(rc, co, rb, 2);
        raw[t][1] = __builtin_amdgcn_raw_buffer_load_b64(rc, co + 128, rb, 2);
        raw[t][2] = __builtin_amdgcn_raw_buffer_load_b64(rc, co + 256, rb, 2);
        raw[t][3] = __builtin_amdgcn_raw_buffer_load_b64(rs, co + 192, rb, 2);
        raw[t][4] = __builtin_amdgcn_raw_buffer_load_b64(rs, so1, rb, 2);
        raw[t][5] = __builtin_amdgcn_raw_buffer_load_b64(rs, co + 64, rb, 2);
        PIN();
    };
    u32x4 tab[4][3];
    auto pack_cs = [&](int t) {
#pragma unroll
        for (int j = 0; j < 3; ++j) {
#pragma unroll
            for (int k = 0; k < 2; ++k) {
                tab[t][j][2 * k] = __builtin_amdgcn_perm(raw[t][3 + j][k], raw[t][j][k], 0x05040100);
                tab[t][j][2 * k + 1] = __builtin_amdgcn_perm(raw[t][3 + j][k], raw[t][j][k], 0x07060302);
            }
            *reinterpret_cast<u32x4*>(s_part + (wave ^ 4) * (NI * DH) + ((t * 3 + j) * 64 + lane) * 4) = tab[t][j];
        }
        PIN();
    };
    const auto rk = __builtin_amdgcn_make_buffer_rsrc((void*)qk, 0, KVB, 0x00020000);
    const auto rv = __builtin_amdgcn_make_buffer_rsrc((void*)qv, 0, KVB, 0x00020000);
    const unsigned voff = r * ROWB + c * 16;
    const unsigned woff = hp * HEADB;
    auto toff = [&](int t) { return (unsigned)(row0 ^ srow(t ^ (HX * hp))) * ROWB; };
    u32x4 ring[4][3];
    auto ld = [&](int slot, int j, __amdgpu_buffer_rsrc_t rr, unsigned off) {
        ring[slot][j] = __builtin_amdgcn_raw_buffer_load_b128(rr, voff + j * 256, off, 2);
        PIN();
    };
    auto ld_tile = [&](int slot, __amdgpu_buffer_rsrc_t rr, unsigned off) { ld(slot, 0, rr, off); ld(slot, 1, rr, off); ld(slot, 2, rr, off); };
    unsigned post = 0;
    auto enter = [&](int i) {
        if (WIDE && (i == 9 || i == 10)) {
            const unsigned hs = pace.step >> 1, z = (unsigned)pace.offset >> 16;
            off += i == 10 ? post : hp * 3 * hs;
            step = i == 10 ? pace.step : hs + hp * z;
            post = __builtin_amdgcn_readfirstlane(i == 10 ? 0 : hp ? hs : 4 * (hs + z));
            if (lane == 0) *(vlds_u32x2*)(s_act + 2 * wave) = u32x2{kept, off};
            PIN();
        }
    };
    auto refill = [&](int slot, int j, __amdgpu_buffer_rsrc_t rr, unsigned goff) {
        if (j == 0) pace_wait();
        if (!WIDE) VMCNT(2);
        else if (j == 0) asm volatile("s_cmp_lg_u32 %0, 0\n\ts_waitcnt vmcnt(3)\n\ts_cbranch_scc1 1\n\ts_waitcnt vmcnt(2)" :: "s"(post) : "scc", "memory");
        else asm volatile("s_cmp_lg_u32 %0, 0\n\ts_cbranch_scc1 1\n\ts_waitcnt vmcnt(2)" :: "s"(post) : "scc", "memory");
        ld(slot, j, rr, goff);
        if (j == 0) { const unsigned t = (now32() << 8) - 1024 - (off - step); if ((int)(t - kept) > 0) kept = t; }
        if (j == 2) report();
    };
    auto k0 = [&](int t) { pace_wait(); if (t < 3) gather(3 + t); ld_tile(t, rk, woff + toff(t)); };
    load_cs(0); gather(0);
    pace_wait(); gather(1); load_cs(1); STAMP(1);
    pack_cs(0);
    pace_wait(); pre_sc(); gather(2); off += (pace.offset & 0xffff) - step;
    if (PRO == 0) { k0(0); pack_cs(1); } else { pack_cs(1); k0(0); }
    k0(1);
    VMCNT(3); k0(2);
    STAMP(2);
    VMCNT(3);
    if (BM && hp) { if (lane == 0) *(vlds_u32x2*)(s_act + 2 * wave) = u32x2{kept, off}; BARRIER(); }
    k0(3);
    if (lane == 0) *(vlds_u32x2*)(s_act + 2 * wave) = u32x2{kept, off};
    if (!(BM && hp)) BARRIER();
    origin = *(vlds_u32*)(s_sync + 2);
    STAMP(3);

    u32x4 qp[3];
    auto load_q = [&](int h, int j) { qp[j] = *reinterpret_cast<const u32x4*>(s_q + h * DH + 64 * j + 4 * c); };
    load_q(hp, 0); load_q(hp, 1); load_q(hp, 2);
#pragma unroll
    for (int t = 0; t < 2; ++t)
#pragma unroll
        for (int j = 0; j < 3; ++j) tab[2 + t][j] = *reinterpret_cast<const u32x4*>(s_part + wave * (NI * DH) + ((t * 3 + j) * 64 + lane) * 4);
    if (HX == 0 && hp) {
#pragma unroll
        for (int t = 0; t < 2; ++t)
#pragma unroll
            for (int j = 0; j < 3; ++j)
#pragma unroll
                for (int k = 0; k < 4; ++k) asm volatile("v_swap_b32 %0, %1" : "+v"(tab[t][j][k]), "+v"(tab[2 + t][j][k]));
    }

    for (int i = 0; i < NI; ++i) {
        const bool last = i + 1 == NI;
        enter(i);
        const int h = 2 * i + hp;
        const unsigned hnext = (i + 1) * (2 * HEADB) + woff;
        const auto rn = __builtin_amdgcn_make_buffer_rsrc((void*)(last ? qv : qk), 0, KVB, 0x00020000);
        float* scp = s_w + (h * 4 + rw) * 20;
        f32x4 sc[4];
#pragma unroll
        for (int t = 0; t < 4; ++t)
#pragma unroll
            for (int a = 0; a < 4; ++a) sc[t][a] = bflo(s_sc[h * 256 + (t ^ (HX * hp)) * 64 + a * 16 + rw * 4 + r]);
        float s[4];
#pragma unroll
        for (int t = 0; t < 4; ++t) {
            f32x2 dot, wsum;
#pragma unroll
            for (int j = 0; j < 3; ++j) {
                f32x2 w01, w23, k01, k23;
                dot_cvt(qp[j], tab[t][j], ring[t][j], w01, w23, k01, k23);
                if (j == 0) { wsum = w01 + w23; dot = k01 * w01; } else { wsum += w01; wsum += w23; dot += k01 * w01; }
                dot += k23 * w23;
                asm volatile("" : "+v"(dot), "+v"(wsum));
                PIN();
                if (t == 3) load_q(last ? h : h + 2, j);
                refill(t, j, rn, (last ? woff : hnext) + toff(t));
                if (t == 0 && j == 0) TRACE(i);
            }
            const float a = (dot[0] + dot[1]) - sc[t][1] * (wsum[0] + wsum[1]);
            s[t] = row16_allsum(a) * (sc[t][0] * SCALE_LOG2);
        }
        const float mx = xrow_max(fmaxf(fmaxf(s[0], s[1]), fmaxf(s[2], s[3])));
        float l = 0.f, z = 0.f;
#pragma unroll
        for (int t = 0; t < 4; ++t) {
            const float p = __builtin_amdgcn_exp2f(s[t] - mx);
            l += p;
            s[t] = p * sc[t][2];
            z += s[t] * sc[t][3];
        }
        l = xrow_sum(l);
        z = xrow_sum(z);
        if (c == 0) {
            *reinterpret_cast<f32x4*>(scp + r * 4) = f32x4{s[0], s[1], s[2], s[3]};
            *reinterpret_cast<f32x4*>(scp + 16) = f32x4{mx, l, z, 0.f};
        }
        PIN();
    }
    STAMP(4);

    const float* pwp = s_w + (hp * 4 + rw) * 20 + r * 4;
    f32x4 pw = *reinterpret_cast<const f32x4*>(pwp);
    unsigned tag, lw, lo;
    f32x4 lf;
    f32x2 lz;
    unsigned lp = (hp * 4 * NI + NI - 1) * DH + (lane & 31) * 6;
    f32x2 acc[3][2];
    auto v_acc = [&](int t, int j) {
        const f32x2 p2 = {pw[t], pw[t]};
        const u32x4 vv = ring[t][j];
        const f32x2 v01 = {(float)(int)vv[0], (float)(int)vv[1]}, v23 = {(float)(int)vv[2], (float)(int)vv[3]};
        if (t == 0) { acc[j][0] = p2 * v01; acc[j][1] = p2 * v23; }
        else { acc[j][0] += p2 * v01; acc[j][1] += p2 * v23; }
        asm volatile("" : "+v"(acc[j][0]), "+v"(acc[j][1]));
        PIN();
    };
    auto v_fold = [&](int i, int j) {
        const float x = swap32_add(acc[j][0][0], acc[j][1][0]);
        const float y = swap32_add(acc[j][0][1], acc[j][1][1]);
        s_part[(wave * NI + i) * DH + 64 * j + 4 * c + r] = swap16_add(x, y);
    };
    auto v_done = [&](int i) {
        PIN();
        if (lane == 0) *(vlds_u32*)(s_done + wave) = i + 1;
        PIN();
    };
#pragma unroll
    for (int t = 0; t < LS; ++t)
#pragma unroll
        for (int j = 0; j < 3; ++j) v_acc(t, j);
    for (int i = 0; i < NI - 1; ++i) {
        const unsigned hnext = (i + 1) * (2 * HEADB) + woff;
        enter(i);
#pragma unroll
        for (int k = 0; k < 4; ++k) {
            const int t = (k + LS) & 3;
#pragma unroll
            for (int j = 0; j < 3; ++j) {
                v_acc(t, j);
                if (t == 3 && j == 2) pw = *reinterpret_cast<const f32x4*>(pwp + (i + 1) * 160);
                refill(k, j, rv, hnext + toff(k));
                if (k == 0 && j == 0) TRACE(16 + i);
            }
            if (t == 3) { v_fold(i, 0); v_fold(i, 1); v_fold(i, 2); v_done(i); }
        }
    }
    if (PAD) asm volatile("s_branch .Lkv_tail_%=\n\t.p2align 12\n.Lkv_tail_%=:" ::: "memory");
    STAMP(5);
    TRACE(31);
    while (__builtin_amdgcn_readfirstlane(*(vlds_u32*)(s_sync + 3)) <= NI) __builtin_amdgcn_s_sleep(1);
    PIN();
    tag = s_sync[4 + (lane & 3)];
    lf = *reinterpret_cast<const f32x4*>(s_f + (2 * (NI - 1) + hp) * 8);
    lz = *reinterpret_cast<const f32x2*>(s_f + (2 * (NI - 1) + hp) * 8 + 4);
    origin = *(vlds_u32*)(s_sync + 2);
    lw = last_word(tag, lz[1], lane & 31);
    lo = lane < 32 ? piece_off(hp, lane & 31) : OOB;
    asm volatile("" : "+v"(tag), "+v"(lf), "+v"(lz), "+v"(lw), "+v"(lo), "+v"(lp), "+v"(origin));
    PIN();
#pragma unroll
    for (int t = LS; t < 4; ++t)
#pragma unroll
        for (int j = 0; j < 3; ++j) { v_acc(t, j); if (t == 3) { v_fold(NI - 1, j); PIN(); } }
    v_done(NI - 1);
    STAMP(6);
    {
        const u32x4 dn = *(vlds_u32x4*)(s_done + 4 * hp);
        PIN();
        const u32x4 v = merge(s_part + lp, lf, lz[0], lw);
        if (__builtin_amdgcn_readfirstlane(dn[0] & dn[1] & dn[2] & dn[3]) == NI) publish(v, NI - 1, lo);
        PIN();
    }
    STAMP(7);
    {
        unsigned inbox = WS_PART + (bid >> 4) * GSTR + (bid & 15) * UNITB, obase = bid * 48;
        unsigned xo = tid * 16, yo = tid * 16 + 8192, sel = lane & 1 ? 0x01000504u : 0x05040100u;
        unsigned bo = lane < 16 ? wave * 1024 + (lane & 7) * 128 + (lane >> 3) * 8192 : OOB;
        typedef __attribute__((address_space(3))) float lds_f;
        lds_f* wo = (lds_f*)s_red + wave * 28 + (r < 3 ? c * 6 + r * 2 : 24);
        lds_f* fo = (lds_f*)s_red + (lane & 7) * 28 + 24;
        lds_f* go = (lds_f*)s_red + (lane & 7) * 28 + min(lane >> 3, 5) * 4;
        vlds_u32* dw = (vlds_u32*)(s_done + wave);
        vlds_u32* dr = (vlds_u32*)(s_done + (lane & 7));
        unsigned oo = (lane & 7) == 0 && lane < 48 ? lane : OOB, eo = bid == 0 && lane < 4 ? lane * 2 : OOB;
        asm volatile("" : "+s"(inbox), "+s"(obase), "+v"(xo), "+v"(yo), "+v"(sel), "+v"(bo), "+v"(wo), "+v"(fo), "+v"(go), "+v"(oo), "+v"(eo), "+v"(dw), "+v"(dr));
        const unsigned o = __builtin_amdgcn_readfirstlane(origin) - (15 - (bid >> 4)) * RST;
        while ((int)((now32() << 8) - o) < pace.rd) __builtin_amdgcn_s_sleep(1);
        PIN();
        STAMP(8);
        u32x4 x, y;
        do {
            x = __builtin_amdgcn_raw_buffer_load_b128(rw_, xo, inbox, 16);
            y = __builtin_amdgcn_raw_buffer_load_b128(rw_, yo, inbox, 16);
            const unsigned age = now32() - tstart;
            PIN();
            if (age > SPIN_LIMIT) break;
        } while (__builtin_amdgcn_ballot_w64(((x[3] ^ tag) | (y[3] ^ tag)) > 0xffffu) != 0);
        if (DRY) { __builtin_amdgcn_raw_buffer_store_b32(0u, rw_, bo, inbox, 16); PIN(); }
        STAMP(9);
        const float lx = __uint_as_float(__builtin_amdgcn_perm(__builtin_amdgcn_mov_dpp(x[3], 0xb1, 0xf, 0xf, true), x[3], sel));
        const float ly = __uint_as_float(__builtin_amdgcn_perm(__builtin_amdgcn_mov_dpp(y[3], 0xb1, 0xf, 0xf, true), y[3], sel));
        float M = fmaxf(lx, ly);
        M = fmaxf(M, dpp<0x128>(M));
        M = fmaxf(M, dpp<0x124>(M));
        M = xrow_max(M);
        const float w0 = __builtin_amdgcn_exp2f(lx - M), w1 = __builtin_amdgcn_exp2f(ly - M);
        float a[8];
#pragma unroll
        for (int k = 0; k < 3; ++k) {
            a[2 * k] = w0 * bflo(x[k]) + w1 * bflo(y[k]);
            a[2 * k + 1] = w0 * bfhi(x[k]) + w1 * bfhi(y[k]);
        }
        a[6] = a[7] = w0 + w1;
        float b[4], d[2];
#pragma unroll
        for (int k = 0; k < 4; ++k) b[k] = swap32_add(a[k], a[4 + k]);
#pragma unroll
        for (int k = 0; k < 2; ++k) d[k] = swap16_add(b[k], b[2 + k]);
#pragma unroll
        for (int k = 0; k < 2; ++k) { d[k] += dpp<0x128>(d[k]); d[k] += dpp<0x124>(d[k]); }
        if (c < 4) {
            *(__attribute__((address_space(3))) f32x2*)wo = f32x2{d[0], r < 3 ? d[1] : M};
            PIN();
            *dw = 3 * NI;
        }
        PIN();
        STAMP(10);
        const unsigned fin = *dr;
        PIN();
        f32x2 sm = *(__attribute__((address_space(3))) f32x2*)fo;
        f32x4 ov = *(__attribute__((address_space(3))) f32x4*)go;
        asm volatile("" : "+v"(sm), "+v"(ov));
        if (__builtin_amdgcn_ballot_w64(fin != 3 * NI) == 0) {
            float MM = fmaxf(sm[1], dpp<0xb1>(sm[1]));
            MM = fmaxf(MM, dpp<0x4e>(MM));
            MM = fmaxf(MM, dpp<0x141>(MM));
            const float ff = __builtin_amdgcn_exp2f(sm[1] - MM);
            float e[5] = {ff * ov[0], ff * ov[1], ff * ov[2], ff * ov[3], ff * sm[0]};
#pragma unroll
            for (int k = 0; k < 5; ++k) e[k] += dpp<0xb1>(e[k]);
#pragma unroll
            for (int k = 0; k < 5; ++k) e[k] += dpp<0x4e>(e[k]);
#pragma unroll
            for (int k = 0; k < 5; ++k) e[k] += dpp<0x141>(e[k]);
            const float inv = __builtin_amdgcn_rcpf(e[4]);
            __builtin_amdgcn_raw_buffer_store_b64(u32x2{pk_bf16(f32x2{e[0] * inv, e[1] * inv}), pk_bf16(f32x2{e[2] * inv, e[3] * inv})}, ro, oo, obase, OST);
            if (bid == 0) __builtin_amdgcn_raw_buffer_store_b16((unsigned short)(tag >> 16), rw_, eo, WS_EPOCH, 16);
        }
    }
    if (wave == PWAVE) {
        if (PIDX == 90) probe = __builtin_amdgcn_readfirstlane(s_sync[0]);
        if (PIDX == 91 || PIDX == 92) probe = tstart + ((int)(__builtin_amdgcn_readfirstlane(s_sync[PIDX - 90]) - (tstart << 8)) >> 8);
        if (PIDX == 100) { asm volatile("s_waitcnt vmcnt(0)" ::: "memory"); probe = now32(); }
        const unsigned ptag = __builtin_amdgcn_readlane(tag, 0) | __builtin_amdgcn_readlane(tag, 1) >> 16;
        __builtin_amdgcn_raw_buffer_store_b128(u32x4{tstart, probe, ptag, 0}, rw_, lane == 0 ? bid * 16 : OOB, WS_PROBE, 16);
        asm volatile("s_nop 1" ::: "memory");
        if (bid == 100) {
            while ((int)(now32() - tstart) < 12500) __builtin_amdgcn_s_sleep(1);
            u32x4 p[4];
            for (;;) {
                bool ok = true;
                for (int k = 0; k < 4; ++k) { p[k] = __builtin_amdgcn_raw_buffer_load_b128(rw_, lane * 16 + k * 1024, WS_PROBE, 16); ok = ok && p[k][2] == ptag; }
                if (__builtin_amdgcn_ballot_w64(!ok) == 0 || now32() - tstart > SPIN_LIMIT) break;
                PIN();
            }
            float mn = 1e9f, mx = -1e9f, av = 0.f;
            for (int k = 0; k < 4; ++k) { mn = fminf(mn, (float)(int)(p[k][0] - tstart)); const float v = (float)(int)(p[k][1] - tstart); mx = fmaxf(mx, v); av += v; }
            mn = -xrow_max(row16_allmax(-mn)); mx = xrow_max(row16_allmax(mx)); av = xrow_sum(row16_allsum(av)) / 256.f;
            float val = (PMAX & 1 ? mx : av) - (PMAX & 2 ? (float)((int)(s_sync[2] - (tstart << 8)) >> 8) : mn) - PSUB;
            if (PIDX == 99) val = 0.f;
            val = fminf(fmaxf(val, 0.f), 3000.f);
            const unsigned endt = tstart + __builtin_amdgcn_readfirstlane((int)(mn + 14000.f + 5.f * val));
            while ((int)(endt - now32()) > 0) __builtin_amdgcn_s_sleep(1);
        }
    }
}

#define VAR_XS(p) ((p) / 1000 % 2)
template <int X, int PRO, int GATE, int STAG>
static void launch_cfg(void* const* d_in, void* d_out, void* d_ws, hipStream_t stream, float t_us, float start_us, float off_us, float debt_us, float adj_pct, float pub_us) {
    Pace pace;
    pace.step = (unsigned)(t_us * 25600.f / 128.f);
    pace.start = (int)(start_us * 25600.f);
    const float zone = PRO / 100 % 10 ? (int)off_us * 0.01f : 0.f; off_us -= (int)off_us;
    pace.offset = (int)(off_us * 25600.f) | (int)(zone * 6400.f) << 16; off_us += 2 * zone;
    pace.bias = (int)(debt_us * 25600.f);
    pace.rd = (int)((start_us + off_us + t_us * ((129.f + VAR_XS(PRO)) / 128.f) + adj_pct) * 25600.f);
    pace.pub = (int)((start_us + off_us + t_us * ((129.f + VAR_XS(PRO)) / 128.f) + pub_us) * 25600.f);
    kv_sweep_kernel<X, PRO, GATE, STAG, 1><<<NWG, 576, 0, stream>>>(d_in[0], d_in[1], d_in[2], d_in[3], d_in[4], d_in[5], d_in[6], d_in[7],
        d_in[8], d_in[9], d_in[10], d_ws, d_out, pace EXTVAL);
}

#define CFG 0, 123, 5002501, 112
#define PRM 100.75f, 0.5f, 10.8f, 0.055f, 1.6f, 0.6f
extern "C" void kernel_launch(void* const* d_in, const int* in_sizes, int n_in,
                              void* d_out, int out_size, void* d_ws, size_t ws_size,
                              hipStream_t stream) {
    launch_cfg<CFG>(d_in, d_out, d_ws, stream, PRM);
}
```

```cpp
#include <hip/hip_runtime.h>
#include <hip/hip_bf16.h>
#include <cstdint>
#include <type_traits>

using bf16 = __hip_bfloat16;
using u32x4 = __attribute__((ext_vector_type(4))) unsigned;
using u32x3 = __attribute__((ext_vector_type(3))) unsigned;
using u32x2 = __attribute__((ext_vector_type(2))) unsigned;
using u64x8 = __attribute__((ext_vector_type(8))) unsigned long long;
using f32x2 = __attribute__((ext_vector_type(2))) float;
using f32x4 = __attribute__((ext_vector_type(4))) float;
using bf16x2 = __attribute__((ext_vector_type(2))) __bf16;

constexpr int DH = 192;
constexpr int HALF = 96;
constexpr int NH = 32;
constexpr int SKV = 16384;
constexpr int NWG = 256;
constexpr int NI = 16;
constexpr unsigned ROWB = DH * 4;
constexpr unsigned HEADB = SKV * ROWB;
constexpr unsigned KVB = NH * HEADB;
constexpr unsigned CSB = SKV * DH * 2;
constexpr unsigned OOB = 0x80000000u;
constexpr float SCALE_LOG2 = 1.4426950408889634f * 0.07216878364870322f;
constexpr unsigned SPIN_LIMIT = 20000000u;

constexpr unsigned UNITB = NWG * 64;
constexpr unsigned GSTR = 16 * UNITB + 256;
constexpr unsigned WS_PART = 0;
constexpr unsigned WS_EPOCH = NWG * (UNITB + 1024);
constexpr unsigned WS_REP = WS_EPOCH + 256;
constexpr unsigned WS_PROBE = WS_REP + 256 * 512;
constexpr unsigned WS_BYTES = WS_PROBE + 4096;

__device__ __forceinline__ float bflo(unsigned u) { return __uint_as_float(u << 16); }
__device__ __forceinline__ float bfhi(unsigned u) { return __uint_as_float(u & 0xffff0000u); }
__device__ __forceinline__ float rbf(float x) { return __bfloat162float(__float2bfloat16(x)); }
__device__ __forceinline__ unsigned pk_bf16(f32x2 a) { return __builtin_bit_cast(unsigned, __builtin_convertvector(a, bf16x2)); }
__device__ __forceinline__ void dot_cvt(const u32x4 q, const u32x4 t, const u32x4 ki, f32x2& w01, f32x2& w23, f32x2& k01, f32x2& k23) {
    float w0, w1, w2, w3;
    float k0 = __uint_as_float(ki[0]), k1 = __uint_as_float(ki[1]), k2 = __uint_as_float(ki[2]), k3 = __uint_as_float(ki[3]);
    asm("v_dot2_f32_bf16 %0, %8, %12, 0\n\tv_dot2_f32_bf16 %1, %9, %13, 0\n\tv_dot2_f32_bf16 %2, %10, %14, 0\n\tv_dot2_f32_bf16 %3, %11, %15, 0\n\t"
        "v_cvt_f32_i32 %4, %4\n\tv_cvt_f32_i32 %5, %5\n\tv_cvt_f32_i32 %6, %6\n\tv_cvt_f32_i32 %7, %7"
        : "=&v"(w0), "=&v"(w1), "=&v"(w2), "=&v"(w3), "+v"(k0), "+v"(k1), "+v"(k2), "+v"(k3)
        : "v"(q[0]), "v"(q[1]), "v"(q[2]), "v"(q[3]), "v"(t[0]), "v"(t[1]), "v"(t[2]), "v"(t[3]));
    w01 = f32x2{w0, w1}; w23 = f32x2{w2, w3}; k01 = f32x2{k0, k1}; k23 = f32x2{k2, k3};
}

template <int CTRL> __device__ __forceinline__ float dpp(float x) {
    return __builtin_bit_cast(float, __builtin_amdgcn_mov_dpp(__builtin_bit_cast(int, x), CTRL, 0xf, 0xf, true));
}
__device__ __forceinline__ float row16_allsum(float x) {
    x += dpp<0x128>(x);
    x += dpp<0x124>(x);
    x += dpp<0x122>(x);
    x += dpp<0x121>(x);
    return x;
}
__device__ __forceinline__ float row16_allmax(float x) {
    x = fmaxf(x, dpp<0x128>(x));
    x = fmaxf(x, dpp<0x124>(x));
    x = fmaxf(x, dpp<0x122>(x));
    x = fmaxf(x, dpp<0x121>(x));
    return x;
}
__device__ __forceinline__ float xrow_max(float x) {
    auto s = __builtin_amdgcn_permlane16_swap(__float_as_uint(x), __float_as_uint(x), false, false);
    x = fmaxf(__uint_as_float(s[0]), __uint_as_float(s[1]));
    auto t = __builtin_amdgcn_permlane32_swap(__float_as_uint(x), __float_as_uint(x), false, false);
    return fmaxf(__uint_as_float(t[0]), __uint_as_float(t[1]));
}
__device__ __forceinline__ float xrow_sum(float x) {
    auto s = __builtin_amdgcn_permlane16_swap(__float_as_uint(x), __float_as_uint(x), false, false);
    x = __uint_as_float(s[0]) + __uint_as_float(s[1]);
    auto t = __builtin_amdgcn_permlane32_swap(__float_as_uint(x), __float_as_uint(x), false, false);
    return __uint_as_float(t[0]) + __uint_as_float(t[1]);
}
__device__ __forceinline__ float swap32_add(float a, float b) {
    auto t = __builtin_amdgcn_permlane32_swap(__float_as_uint(a), __float_as_uint(b), false, false);
    return __uint_as_float(t[0]) + __uint_as_float(t[1]);
}
__device__ __forceinline__ float swap16_add(float a, float b) {
    auto t = __builtin_amdgcn_permlane16_swap(__float_as_uint(a), __float_as_uint(b), false, false);
    return __uint_as_float(t[0]) + __uint_as_float(t[1]);
}
__device__ __forceinline__ int row16_sum(int x) {
    x += __builtin_amdgcn_mov_dpp(x, 0x128, 0xf, 0xf, true);
    x += __builtin_amdgcn_mov_dpp(x, 0x124, 0xf, 0xf, true);
    x += __builtin_amdgcn_mov_dpp(x, 0x122, 0xf, 0xf, true);
    x += __builtin_amdgcn_mov_dpp(x, 0x121, 0xf, 0xf, true);
    return x;
}
__device__ __forceinline__ unsigned now32() { return (unsigned)__builtin_amdgcn_s_memrealtime(); }

#define PLAN_EXTRA
#define GNT " nt"
#define GSA 0x22222222
#define GSB 0x55445544
#define CTRL 2, 4, 0, 2560, 768, 13, 9, 200, 984, 2000, 0, 6, 400, -1024
#define NSTAMP 12
#define STAMP(i)
#define TRACE(i)
#define HSTAMP(i)
#define PSTAMP(i)
#define PIN() do { asm volatile("" ::: "memory"); __builtin_amdgcn_sched_barrier(0); } while (0)
#define BARRIER() do { asm volatile("s_waitcnt lgkmcnt(0)" ::: "memory"); __builtin_amdgcn_s_barrier(); PIN(); } while (0)
#define VMCNT(n) asm volatile("s_waitcnt vmcnt(%0)" :: "i"(n) : "memory")

typedef volatile __attribute__((address_space(3))) unsigned vlds_u32;
typedef volatile __attribute__((address_space(3))) u32x2 vlds_u32x2;
typedef volatile __attribute__((address_space(3))) u32x4 vlds_u32x4;

struct Plan { const char* rows; const char* phase; int hx; char sx, tx; };
constexpr Plan PLANS[] = {
    {"1234tcab0s", "vwabc01234h", 2}, {"432cb1ts0a", "1vwacb4320h", 0}, {"123cb0ts4a", "vwabc01234h", 0}, {"103cb2ts4a", "vwabc01234h", 0},
    {"0234catb1s", "vwabc01234h", 2}, {"013cb2ta4s", "vwabc01234h", 0}, {"1234tcab0s", "vwabc01234h", 0}, {"123cb0st4a", "vwabc01234h", 0},
    {"1234tcab0s", "vwabc01234h", 2, 'c'}, {"432cb1ts0a", "1vwacb4320h", 0, 'b', 'c'}, {"123cb0ts4a", "vwabc01234h", 0, 'b', 'c'}, {"0123t4cabs", "vwabc01234h", 2},
    {"1234t0cabs", "vwabc01234h", 2}, {"1234tcab0s", "vwabc01234h", 2, 'c', 'a'},
    PLAN_EXTRA
};
constexpr int sym_pos(const char* s, char c) { for (int i = 0; s[i]; ++i) if (s[i] == c) return i; return -1; }
template <int N> __device__ __forceinline__ int place(const char* dst, const char (&src)[N], int x) {
    int y = 0;
#pragma unroll
    for (int i = 0; i < N - 1; ++i) y |= (x >> i & 1) << sym_pos(dst, src[i]);
    return y;
}
#define EXTARG
#define EXTVAL
struct Pace {
    unsigned step;
    int start;
    int offset;
    int bias;
    int rd;
    int pub;
};

template <int X, int OPT, int GATE, int TP, int SLP>
__global__ __launch_bounds__(576) __attribute__((aligned(4096))) void kv_sweep_kernel(
    const void*, const void*, const void*, const void*, const void*, const void*, const void*, const void*,
    const void*, const void*, const void*, void*, void*, Pace EXTARG)
{
    __shared__ __attribute__((aligned(16))) unsigned s_q[NH * DH];
    __shared__ __attribute__((aligned(16))) unsigned short s_sc[NH * 256];
    __shared__ __attribute__((aligned(16))) float s_w[NH * 4 * 20];
    __shared__ __attribute__((aligned(16))) float s_part[8 * NI * DH];
    __shared__ __attribute__((aligned(16))) float s_red[8 * 28];
    __shared__ __attribute__((aligned(16))) float s_f[NH * 8];
    __shared__ __attribute__((aligned(16))) unsigned s_done[8];
    __shared__ __attribute__((aligned(16))) unsigned s_junk[16];
    __shared__ __attribute__((aligned(16))) unsigned s_sync[8];
    __shared__ __attribute__((aligned(16))) unsigned s_act[16];

    constexpr int HOLD = GATE % 10;
    constexpr int DRY = OPT % 10 & 1, LS = 2, PAD = OPT % 10 / 2 & 1, PF = OPT / 10 % 10, RST = GATE / 1000 % 10 * 256, SLEAD = GATE / 10000 % 10, STA = 16, WIDE = OPT / 100 % 10, OST = 0, PRO = OPT / 1000;
    unsigned long long pc0, t0;
    u64x8 ka, kb;
    asm volatile("s_load_dwordx16 %3, %4, 0x40\n\ts_load_dwordx16 %2, %4, 0x0\n\ts_memrealtime %1\n\ts_getpc_b64 %0\n.Lkv_beg_%5_%6_%7_%8:"
                 : "=&s"(pc0), "=&s"(t0), "=&s"(ka), "=&s"(kb) : "s"(__builtin_amdgcn_kernarg_segment_ptr()), "i"(X), "i"(OPT), "i"(GATE), "i"(TP));
    __builtin_amdgcn_sched_barrier(0);
    const int tid = threadIdx.x;
    const int lane = tid & 63;
    int wave = __builtin_amdgcn_readfirstlane(tid >> 6);
    const int r = lane >> 4;
    const int c = lane & 15;
    const int bid = blockIdx.x;
    const int m = (bid & 7) * 32 + (bid >> 3);
    const int rw = wave & 3, hp = wave >> 2;
    constexpr Plan PL = PLANS[GATE / 1000000];
    constexpr unsigned SA = GSA, SB = GSB, QD = TP >> 6 ? 50 + (TP >> 6) * 50u : 0, PM = GATE / 100 % 10 & 3, BM = GATE / 100 % 10 >> 2;
    constexpr int HX = PL.hx, RS = 16 << sym_pos(PL.rows, 's') | (PL.sx ? 16 << sym_pos(PL.rows, PL.sx) : 0), RT = 16 << sym_pos(PL.rows, 't') | (PL.tx ? 16 << sym_pos(PL.rows, PL.tx) : 0);
    const int grow = place(PL.rows, "abc01234", bid) * 16;
    auto srow = [&](int t) { return (t & 1) * RS ^ (t >> 1) * RT; };
    const int row0 = grow + rw * 4;
    unsigned co = r * 384 + c * 8, so1 = r * 384 + (c < 8 ? c * 8 + 320 : c * 8 - 64), rb0 = (row0 ^ RT * hp) * 384u;
    asm volatile("s_waitcnt lgkmcnt(0)" : "+s"(t0), "+s"(ka), "+s"(kb), "+v"(co), "+v"(so1), "+s"(rb0), "+s"(wave));
    typedef const __attribute__((address_space(1))) char* gptr;
    void* const q = (void*)ka[0]; void* const qk = (void*)ka[1]; void* const qv = (void*)ka[4]; void* const q_cos = (void*)ka[7];
    const gptr ksc = (gptr)ka[2], kzp = (gptr)ka[3], vsc = (gptr)ka[5], vzp = (gptr)ka[6];
    void* const q_sin = (void*)kb[0]; void* const k_cos = (void*)kb[1]; void* const k_sin = (void*)kb[2]; void* const out = (void*)kb[4];
    unsigned char* const ws = (unsigned char*)(__attribute__((address_space(1))) unsigned char*)kb[3];
    const Pace pace = {(unsigned)kb[5], (int)(kb[5] >> 32), (int)kb[6], (int)(kb[6] >> 32), (int)kb[7], (int)(kb[7] >> 32)};
    const unsigned tstart = (unsigned)t0;
    const unsigned tsched = TP & 16 ? now32() : tstart;
    PSTAMP(69);
    const auto rw_ = __builtin_amdgcn_make_buffer_rsrc((void*)ws, 0, WS_BYTES, 0x00020000);
    const auto ro = __builtin_amdgcn_make_buffer_rsrc((void*)out, 0, NH * DH * 2, 0x00020000);

    auto merge = [&](const float* p, f32x4 f, float zs, unsigned w3) {
        f32x2 u[4][3], a[3];
#pragma unroll
        for (int w = 0; w < 4; ++w)
#pragma unroll
            for (int k = 0; k < 3; ++k) u[w][k] = *reinterpret_cast<const f32x2*>(p + w * NI * DH + 2 * k);
        asm volatile("" : "+v"(u[0][0]), "+v"(u[0][1]), "+v"(u[0][2]), "+v"(u[1][0]), "+v"(u[1][1]), "+v"(u[1][2]),
                          "+v"(u[2][0]), "+v"(u[2][1]), "+v"(u[2][2]), "+v"(u[3][0]), "+v"(u[3][1]), "+v"(u[3][2]));
#pragma unroll
        for (int k = 0; k < 3; ++k) a[k] = (f[0] * u[0][k] - zs + f[1] * u[1][k]) + (f[2] * u[2][k] + f[3] * u[3][k]);
        return u32x4{pk_bf16(a[0]), pk_bf16(a[1]), pk_bf16(a[2]), w3};
    };
    auto last_word = [&](unsigned tag, float ls, int c6) { return tag | ((__float_as_uint(ls) >> (16 * (c6 & 1))) & 0xffffu); };
    auto piece_off = [&](int hp, int c6) { return (hp * 8 + (c6 >> 2)) * UNITB + (c6 & 3) * 16; };
    auto publish = [&](u32x4 v, int i, unsigned vo) {
        __builtin_amdgcn_raw_buffer_store_b128(v, rw_, vo, WS_PART + i * GSTR + m * 64, STA);
        asm volatile("s_nop 1" :: "v"(v) : "memory");
    };

    if (__builtin_expect_with_probability(wave == 8, 1, 0.45)) {
        if (TP & 32) __builtin_amdgcn_s_setprio(3);
        HSTAMP(11);
        unsigned clen;
        asm volatile("s_mov_b32 %0, .Lkv_end_%1_%2_%3_%4-.Lkv_beg_%1_%2_%3_%4" : "=s"(clen) : "i"(X), "i"(OPT), "i"(GATE), "i"(TP));
        const auto rcode = __builtin_amdgcn_make_buffer_rsrc((void*)(pc0 & ~127ull), 0, clen, 0x00020000);
        const int jj = bid >> 3;
        auto warm_ld = [&](int n) {
            return __builtin_amdgcn_raw_buffer_load_b32(rcode, lane < n ? (jj * 9 + lane) * 64 : OOB, 0, 0);
        };
        auto warm_code = [&](int n) { if (n) { const unsigned x = warm_ld(n); asm volatile("" :: "v"(x)); } };
        constexpr int WN[10][4] = {{0, 0, 0, 0}, {64, 0, 0, 0}, {9, 0, 0, 0}, {64, 9, 9, 0}, {9, 9, 9, 0}, {18, 0, 0, 0}, {18, 18, 18, 0}, {9, 9, 0, 9}, {36, 0, 0, 0}, {9, 0, 9, 0}};
        constexpr int FREE = 0x3fff & ~(16 << sym_pos(PL.rows, 'a') | 16 << sym_pos(PL.rows, 'b') | 16 << sym_pos(PL.rows, 'c'));
        constexpr int F1 = 1 << (31 - __builtin_clz(FREE)), F2 = 1 << (31 - __builtin_clz(FREE - F1));
        const unsigned trow = (grow & ~FREE) + (jj & 1 ? FREE - F1 - F2 : 0) + (jj & 2 ? F2 : 0) + (jj & 4 ? F1 : 0) + (jj & 8 ? SKV : 0);
        int tp = jj >> 4;
        auto touch = [&]() {
            const unsigned long long a = (unsigned long long)(tp < NI ? qk : qv) + ((tp & 15) * 2 * SKV + trow) * ROWB;
            if (lane == 0)
                asm volatile("s_mov_b32 m0, %1\n\ts_nop 0\n\tglobal_load_lds_dword %0, off nt" :: "v"(a), "s"((unsigned)(unsigned long long)(__attribute__((address_space(3))) unsigned*)s_junk) : "memory");
            tp += 2;
            PIN();
        };
        const unsigned x0 = warm_ld(WN[PF][0]);
        if (TP & 4) touch(); else tp += 2;
        PIN();
        if (QD) while (now32() - tsched < QD) __builtin_amdgcn_s_sleep(1);
        u32x3 lc[2], ls[2];
        u32x4 qraw[12];
        {
            const auto rq = __builtin_amdgcn_make_buffer_rsrc((void*)q, 0, NH * DH * 2, 0x00020000);
            const auto rqc = __builtin_amdgcn_make_buffer_rsrc((void*)q_cos, 0, DH * 2, 0x00020000);
            const auto rqs = __builtin_amdgcn_make_buffer_rsrc((void*)q_sin, 0, DH * 2, 0x00020000);
#pragma unroll
            for (int k = 0; k < 12; ++k) qraw[k] = __builtin_amdgcn_raw_buffer_load_b128(rq, lane * 16 + (k & 3) * 1024, (k >> 2) * 4096, 0);
#pragma unroll
            for (int k = 0; k < 2; ++k) {
                lc[k] = __builtin_amdgcn_raw_buffer_load_b96(rqc, c * 12 + k * 192, 0, 0);
                ls[k] = __builtin_amdgcn_raw_buffer_load_b96(rqs, c * 12 + k * 192, 0, 0);
            }
            PIN();
        }
        const unsigned tag = ((unsigned)__builtin_amdgcn_raw_buffer_load_b16(rw_, (lane & 3) * 2, WS_EPOCH, 16) + 1u) << 16;
        PIN();
        HSTAMP(2);
        unsigned* const s_raw = reinterpret_cast<unsigned*>(s_part) + NI * DH / 2;
#pragma unroll
        for (int k = 0; k < 12; ++k) *reinterpret_cast<u32x4*>(s_raw + k / 6 * (NI * DH) + k % 6 * 256 + lane * 4) = qraw[k];
        f32x2 fc[2][3], fs[2][3];
#pragma unroll
        for (int k = 0; k < 2; ++k)
#pragma unroll
            for (int u = 0; u < 3; ++u) { fc[k][u] = f32x2{bflo(lc[k][u]), bfhi(lc[k][u])}; fs[k][u] = f32x2{bflo(ls[k][u]), bfhi(ls[k][u])}; }
        PIN();
        HSTAMP(3);
#pragma nounroll
        for (int it = 0; it < 4; ++it) {
            unsigned qa[2][3], qb[2][3];
#pragma unroll
            for (int g = 0; g < 2; ++g)
#pragma unroll
                for (int u = 0; u < 3; ++u) {
                    const unsigned* src = s_raw + g * (NI * DH) + (it * 4 + r) * HALF + 3 * c;
                    qa[g][u] = src[u]; qb[g][u] = src[HALF / 2 + u];
                }
#pragma unroll
            for (int g = 0; g < 2; ++g)
#pragma unroll
                for (int u = 0; u < 3; ++u) {
                    const f32x2 a = {bflo(qa[g][u]), bfhi(qa[g][u])}, b = {bflo(qb[g][u]), bfhi(qb[g][u])};
                    const f32x2 o0 = a * fc[0][u] - b * fs[0][u], o1 = b * fc[1][u] + a * fs[1][u];
                    const unsigned l0 = pk_bf16(f32x2{o0[0], o1[0]}), l1 = pk_bf16(f32x2{o0[1], o1[1]});
                    unsigned* dst = s_q + (16 * g + 4 * it + r) * DH + 6 * c + 2 * u;
                    *reinterpret_cast<u32x2*>(dst) = u32x2{l0, l1};
                    *reinterpret_cast<u32x2*>(dst + HALF) = u32x2{__builtin_amdgcn_alignbit(l0, l0, 16) ^ 0x80000000u, __builtin_amdgcn_alignbit(l1, l1, 16) ^ 0x80000000u};
                }
        }
        HSTAMP(1);
        asm volatile("" :: "v"(x0));
        if (TP & 32) __builtin_amdgcn_s_setprio(0);
        unsigned origin = tsched << 8;
        if (lane < 8) { s_done[lane] = 0; s_sync[lane] = lane < 3 ? origin : lane < 4 ? 0 : tag; }
        PIN();
        BARRIER();
        const int mh = lane >> 5, mc = lane & 31;
        const unsigned rtag = __builtin_amdgcn_readlane(tag, 0) | __builtin_amdgcn_readlane(tag, 1) >> 16;
        constexpr int RPX = 2;
        constexpr int gp[] = {CTRL};
        constexpr int KS = gp[0], SR = gp[1], IB2 = gp[2], EHI = gp[3], IB = gp[4], FR = gp[5], RSH = gp[6], UP = gp[8], DN = gp[9], SLIP = gp[10], FF = gp[11], DMAX = gp[12], ELO = gp[13];
        constexpr unsigned RDLY = gp[7];
        const int rj = ((bid >> 3) + 5 * (bid & 7)) & 31;
        const int rid = rj % (32 / RPX) == 0 ? rj / (32 / RPX) * 8 + (bid & 7) : -1;
        unsigned wround = now32() >> RSH, rround = wround, tick = 0, target = origin;
        bool warmed = false, synced = false;
        int drift = 0;
        const unsigned round0 = rround;
        int i = 0;
        auto kept_now = [&](unsigned now) {
            const u32x2 a = *(vlds_u32x2*)(s_act + 2 * (lane & 7));
            const int late = max((int)(a[0] - origin), (int)((now << 8) - a[1] - origin));
            return origin + (__builtin_amdgcn_readfirstlane(row16_sum(late)) >> 4);
        };
        auto steer = [&](unsigned now) {
            if ((now >> 5) != tick) {
                tick = now >> 5;
                target += drift;
                origin += max(-DN, min(UP + drift, (int)(target - origin)));
                if (SLIP) { const int l2 = (int)(kept_now(now) - origin) - SLIP; if (l2 > 0) origin += l2; }
                if (HOLD && !synced) target = origin - (HOLD - 1) * 82;
                if (lane == 0) *(vlds_u32*)(s_sync + 2) = origin;
                PIN();
            }
        };
        auto finished = [&]() {
            const u32x4 a = *(vlds_u32x4*)s_done, b = *(vlds_u32x4*)(s_done + 4);
            return __builtin_amdgcn_readfirstlane(min(min(min(a[0], a[1]), min(a[2], a[3])), min(min(b[0], b[1]), min(b[2], b[3]))));
        };
        while (i < NI - 2) {
            const unsigned now = now32();
            if ((now >> RSH) != wround) {
                wround = now >> RSH;
                if (rid >= 0 && (now & ((1u << RSH) - 1)) < 48)
                    __builtin_amdgcn_raw_buffer_store_b64(u32x2{kept_now(now), rtag + wround}, rw_, lane == 0 ? rid * 8 : OOB, WS_REP + (wround & 255) * 512, 16);
                PIN();
                continue;
            }
            if ((int)(((now - RDLY) >> RSH) - rround) > 0) {
                rround = (now - RDLY) >> RSH;
                const u32x2 x = __builtin_amdgcn_raw_buffer_load_b64(rw_, c * 8, WS_REP + (rround & 255) * 512, 16);
                const int d = (int)(x[0] - origin);
                const bool ok = x[1] == rtag + rround && d < (32 << 16) && d > -(32 << 16);
                const int n = __builtin_popcountll(__builtin_amdgcn_ballot_w64(ok));
                if (n >= 48) {
                    const unsigned nt = origin + __builtin_amdgcn_readfirstlane((int)(row16_allsum(ok ? (float)d : 0.f) * 4.f / (float)n)) - pace.bias;
                    if (KS && synced && rround - round0 > SR) drift = max(0, min(drift + ((max(ELO, min((int)(nt - target) + FF * drift, EHI)) - (rround - round0 < FR ? IB : IB2)) >> (3 + KS)), DMAX));
                    target = nt + FF * drift;
                    synced = true;
                    const int lost = min((int)(kept_now(now32()) - origin) - pace.bias, (int)(target - origin));
                    if (lost > 0) origin += lost;
                }
                PIN();
                continue;
            }
            steer(now);
            if ((TP & 1) && tp < 2 * NI && 4 * (int)((now << 8) - origin) > (16 * tp + (jj & 15) - (TP & 8 ? 40 : 24)) * (int)pace.step) { touch(); continue; }
            if (WN[PF][1] && !warmed && (int)((now << 8) - origin) > 60 * (int)pace.step) { warm_code(WN[PF][1]); warmed = true; continue; }
            if (finished() <= (unsigned)i) { __builtin_amdgcn_s_sleep(2); continue; }
            PIN();
            if (i == 0) {
                f32x4 ml[4], f;
#pragma unroll
                for (int w = 0; w < 4; ++w) ml[w] = *reinterpret_cast<const f32x4*>(s_w + (mc * 4 + w) * 20 + 16);
                const float M = fmaxf(fmaxf(ml[0][0], ml[1][0]), fmaxf(ml[2][0], ml[3][0]));
                float L = 0.f, zs = 0.f;
#pragma unroll
                for (int w = 0; w < 4; ++w) { f[w] = __builtin_amdgcn_exp2f(ml[w][0] - M); L += f[w] * ml[w][1]; zs += f[w] * ml[w][2]; }
                const float inv = 1.f / L;
                *reinterpret_cast<f32x4*>(s_f + mc * 8) = f * inv;
                *reinterpret_cast<f32x2*>(s_f + mc * 8 + 4) = f32x2{zs * inv, M + __builtin_amdgcn_logf(L)};
                PIN();
                if (lane == 0) *(vlds_u32*)(s_sync + 3) = NI + 1;
                PIN();
            }
            if (i == NI - 3) warm_code(WN[PF][2]);
            const f32x2 zl = *reinterpret_cast<const f32x2*>(s_f + (2 * i + mh) * 8 + 4);
            const u32x4 v = merge(s_part + (mh * 4 * NI + i) * DH + mc * 6, *reinterpret_cast<const f32x4*>(s_f + (2 * i + mh) * 8), zl[0], last_word(tag, zl[1], mc));
            PIN();
            *reinterpret_cast<u32x4*>(s_part + (mh * 4 * NI + i) * DH + mc * 4) = v;
            PIN();
            ++i;
        }
        u32x4 v[NI - 1];
#pragma unroll
        for (int i = 0; i < NI - 2; ++i) v[i] = *reinterpret_cast<const u32x4*>(s_part + (mh * 4 * NI + i) * DH + mc * 4);
        f32x4 f = *reinterpret_cast<const f32x4*>(s_f + (2 * (NI - 2) + mh) * 8);
        f32x2 zl = *reinterpret_cast<const f32x2*>(s_f + (2 * (NI - 2) + mh) * 8 + 4);
        PIN();
#pragma unroll
        for (int i = 0; i < NI - 2; ++i) asm volatile("" : "+v"(v[i]));
        asm volatile("" : "+v"(f), "+v"(zl));
        v[NI - 2] = v[0];
        bool have = false, sent = false;
        while (!(have && sent)) {
            const unsigned now = now32();
            if (!have) {
                steer(now);
                if (finished() > NI - 2) {
                    PIN();
                    v[NI - 2] = merge(s_part + (mh * 4 * NI + NI - 2) * DH + mc * 6, f, zl[0], last_word(tag, zl[1], mc));
                    have = true;
                    warm_code(WN[PF][3]);
                    HSTAMP(5);
                }
            }
            if (!sent && (int)((now << 8) - origin) >= pace.pub) {
                HSTAMP(6);
#pragma unroll
                for (int i = 0; i < NI - 2; ++i) publish(v[i], i, piece_off(mh, mc));
                sent = true;
            }
            if (!(have && sent)) __builtin_amdgcn_s_sleep(1);
            PIN();
        }
        publish(v[NI - 2], NI - 2, piece_off(mh, mc));
        HSTAMP(7);
        asm volatile(".Lkv_end_%0_%1_%2_%3:" :: "i"(X), "i"(OPT), "i"(GATE), "i"(TP));
        return;
    }

    const int ph = place(PL.phase, "abc01234", bid) + place(PL.phase, "vw", rw) + place(PL.phase, "h", hp);
    unsigned off = pace.start + ((pace.step * ph) >> 11);
    unsigned origin = tsched << 8, kept = 0;
    unsigned step = pace.step;
    auto pace_wait = [&]() {
        const unsigned tgt = __builtin_amdgcn_readfirstlane(origin) + off;
        unsigned now = now32() << 8;
        kept = ((int)(now - tgt) > 0 ? now : tgt) - off;
        while ((int)(tgt - now) > 0) { if (SLP) __builtin_amdgcn_s_sleep(SLP); now = now32() << 8; }
        off += step;
    };
    auto report = [&]() {
        if (lane == 0) *(vlds_u32x2*)(s_act + 2 * wave) = u32x2{kept, off};
        origin = *(vlds_u32*)(s_sync + 2);
        PIN();
    };
#define TRV (kept - (tstart << 8))

    auto pick = [](int n, gptr p0, gptr p1, gptr p2, gptr p3) { return (n & 2) ? ((n & 1) ? p3 : p2) : ((n & 1) ? p1 : p0); };
    auto dma = [](gptr a, unsigned dst) { asm volatile("s_mov_b32 m0, %1\n\ts_nop 0\n\tglobal_load_lds_dwordx4 %0, off nt" :: "v"(a), "s"(__builtin_amdgcn_readfirstlane(dst)) : "memory"); };
    const int w8 = rw + 4 * hp;
    const unsigned sdst = (unsigned)(unsigned long long)(__attribute__((address_space(3))) unsigned short*)s_sc + w8 * 1024;
    auto gather = [&](unsigned slot) {
        const gptr a = pick(lane >> 1, ksc, kzp, vsc, vzp) + ((w8 * 2 + (lane >> 5)) * SKV + (grow ^ srow(lane >> 3 & 3)) + (lane & 1) * 8) * 2;
        if ((SA >> 4 * w8 & 15) == slot) dma(a, sdst);
        if ((SB >> 4 * w8 & 15) == slot) dma(a + 32 * SKV, sdst + 8192);
        PIN();
    };
    auto pre_sc = [&]() {
        const gptr a = pick(grow >> 4, ksc, kzp, vsc, vzp) + ((w8 * 2 + (lane >> 5)) * SKV + (grow & ~63 ^ srow(lane >> 3 & 3))) * 2 + (lane & 7) * 16;
        const unsigned junk = (unsigned)(unsigned long long)(__attribute__((address_space(3))) float*)s_part + (3 * NI * DH - 256) * 4;
        if (PM >= 2) dma(a, junk);
        if (PM >= 1) dma(a + 32 * SKV, junk);
        PIN();
    };
    const auto rc = __builtin_amdgcn_make_buffer_rsrc((void*)k_cos, 0, CSB, 0x00020000);
    const auto rs = __builtin_amdgcn_make_buffer_rsrc((void*)k_sin, 0, CSB, 0x00020000);
    u32x2 raw[2][6];
    auto load_cs = [&](int t) {
        const unsigned rb = t == 0 ? rb0 : (row0 ^ RT * hp ^ RS) * 384u;
        raw[t][0] = __builtin_amdgcn_raw_buffer_load_b64(rc, co, rb, 2);
        raw[t][1] = __builtin_amdgcn_raw_buffer_load_b64(rc, co + 128, rb, 2);
        raw[t][2] = __builtin_amdgcn_raw_buffer_load_b64(rc, co + 256, rb, 2);
        raw[t][3] = __builtin_amdgcn_raw_buffer_load_b64(rs, co + 192, rb, 2);
        raw[t][4] = __builtin_amdgcn_raw_buffer_load_b64(rs, so1, rb, 2);
        raw[t][5] = __builtin_amdgcn_raw_buffer_load_b64(rs, co + 64, rb, 2);
        PIN();
    };
    u32x4 tab[4][3];
    auto pack_cs = [&](int t) {
#pragma unroll
        for (int j = 0; j < 3; ++j) {
#pragma unroll
            for (int k = 0; k < 2; ++k) {
                tab[t][j][2 * k] = __builtin_amdgcn_perm(raw[t][3 + j][k], raw[t][j][k], 0x05040100);
                tab[t][j][2 * k + 1] = __builtin_amdgcn_perm(raw[t][3 + j][k], raw[t][j][k], 0x07060302);
            }
            *reinterpret_cast<u32x4*>(s_part + (wave ^ 4) * (NI * DH) + ((t * 3 + j) * 64 + lane) * 4) = tab[t][j];
        }
        PIN();
    };
    const auto rk = __builtin_amdgcn_make_buffer_rsrc((void*)qk, 0, KVB, 0x00020000);
    const auto rv = __builtin_amdgcn_make_buffer_rsrc((void*)qv, 0, KVB, 0x00020000);
    const unsigned voff = r * ROWB + c * 16;
    const unsigned woff = hp * HEADB;
    auto toff = [&](int t) { return (unsigned)(row0 ^ srow(t ^ (HX * hp))) * ROWB; };
    u32x4 ring[4][3];
    auto ld = [&](int slot, int j, __amdgpu_buffer_rsrc_t rr, unsigned off) {
        ring[slot][j] = __builtin_amdgcn_raw_buffer_load_b128(rr, voff + j * 256, off, 2);
        PIN();
    };
    auto ld_tile = [&](int slot, __amdgpu_buffer_rsrc_t rr, unsigned off) { ld(slot, 0, rr, off); ld(slot, 1, rr, off); ld(slot, 2, rr, off); };
    unsigned post = 0;
    auto enter = [&](int i) {
        if (WIDE && (i == 9 || i == 10)) {
            const unsigned hs = pace.step >> 1, z = (unsigned)pace.offset >> 16;
            off += i == 10 ? post : hp * 3 * hs;
            step = i == 10 ? pace.step : hs + hp * z;
            post = __builtin_amdgcn_readfirstlane(i == 10 ? 0 : hp ? hs : 4 * (hs + z));
            if (lane == 0) *(vlds_u32x2*)(s_act + 2 * wave) = u32x2{kept, off};
            PIN();
        }
    };
    auto refill = [&](int slot, int j, __amdgpu_buffer_rsrc_t rr, unsigned goff) {
        if (j == 0) pace_wait();
        if (!WIDE) VMCNT(2);
        else if (j == 0) asm volatile("s_cmp_lg_u32 %0, 0\n\ts_waitcnt vmcnt(3)\n\ts_cbranch_scc1 1\n\ts_waitcnt vmcnt(2)" :: "s"(post) : "scc", "memory");
        else asm volatile("s_cmp_lg_u32 %0, 0\n\ts_cbranch_scc1 1\n\ts_waitcnt vmcnt(2)" :: "s"(post) : "scc", "memory");
        ld(slot, j, rr, goff);
        if (j == 0) { const unsigned t = (now32() << 8) - 1024 - (off - step); if ((int)(t - kept) > 0) kept = t; }
        if (j == 2) report();
    };
    auto k0 = [&](int t) { pace_wait(); if (t < 3) gather(3 + t); ld_tile(t, rk, woff + toff(t)); };
    load_cs(0); gather(0);
    pace_wait(); gather(1); load_cs(1); STAMP(1);
    pack_cs(0);
    pace_wait(); pre_sc(); gather(2); off += (pace.offset & 0xffff) - step;
    if (PRO == 0) { k0(0); pack_cs(1); } else { pack_cs(1); k0(0); }
    k0(1);
    VMCNT(3); k0(2);
    STAMP(2);
    VMCNT(3);
    if (BM && hp) { if (lane == 0) *(vlds_u32x2*)(s_act + 2 * wave) = u32x2{kept, off}; BARRIER(); }
    k0(3);
    if (lane == 0) *(vlds_u32x2*)(s_act + 2 * wave) = u32x2{kept, off};
    if (!(BM && hp)) BARRIER();
    origin = *(vlds_u32*)(s_sync + 2);
    STAMP(3);

    u32x4 qp[3];
    auto load_q = [&](int h, int j) { qp[j] = *reinterpret_cast<const u32x4*>(s_q + h * DH + 64 * j + 4 * c); };
    load_q(hp, 0); load_q(hp, 1); load_q(hp, 2);
#pragma unroll
    for (int t = 0; t < 2; ++t)
#pragma unroll
        for (int j = 0; j < 3; ++j) tab[2 + t][j] = *reinterpret_cast<const u32x4*>(s_part + wave * (NI * DH) + ((t * 3 + j) * 64 + lane) * 4);
    if (HX == 0 && hp) {
#pragma unroll
        for (int t = 0; t < 2; ++t)
#pragma unroll
            for (int j = 0; j < 3; ++j)
#pragma unroll
                for (int k = 0; k < 4; ++k) asm volatile("v_swap_b32 %0, %1" : "+v"(tab[t][j][k]), "+v"(tab[2 + t][j][k]));
    }

    for (int i = 0; i < NI; ++i) {
        const bool last = i + 1 == NI;
        enter(i);
        const int h = 2 * i + hp;
        const unsigned hnext = (i + 1) * (2 * HEADB) + woff;
        const auto rn = __builtin_amdgcn_make_buffer_rsrc((void*)(last ? qv : qk), 0, KVB, 0x00020000);
        float* scp = s_w + (h * 4 + rw) * 20;
        f32x4 sc[4];
#pragma unroll
        for (int t = 0; t < 4; ++t)
#pragma unroll
            for (int a = 0; a < 4; ++a) sc[t][a] = bflo(s_sc[h * 256 + (t ^ (HX * hp)) * 64 + a * 16 + rw * 4 + r]);
        float s[4];
#pragma unroll
        for (int t = 0; t < 4; ++t) {
            f32x2 dot, wsum;
#pragma unroll
            for (int j = 0; j < 3; ++j) {
                f32x2 w01, w23, k01, k23;
                dot_cvt(qp[j], tab[t][j], ring[t][j], w01, w23, k01, k23);
                if (j == 0) { wsum = w01 + w23; dot = k01 * w01; } else { wsum += w01; wsum += w23; dot += k01 * w01; }
                dot += k23 * w23;
                asm volatile("" : "+v"(dot), "+v"(wsum));
                PIN();
                if (t == 3) load_q(last ? h : h + 2, j);
                refill(t, j, rn, (last ? woff : hnext) + toff(t));
                if (t == 0 && j == 0) TRACE(i);
            }
            const float a = (dot[0] + dot[1]) - sc[t][1] * (wsum[0] + wsum[1]);
            s[t] = row16_allsum(a) * (sc[t][0] * SCALE_LOG2);
        }
        const float mx = xrow_max(fmaxf(fmaxf(s[0], s[1]), fmaxf(s[2], s[3])));
        float l = 0.f, z = 0.f;
#pragma unroll
        for (int t = 0; t < 4; ++t) {
            const float p = __builtin_amdgcn_exp2f(s[t] - mx);
            l += p;
            s[t] = p * sc[t][2];
            z += s[t] * sc[t][3];
        }
        l = xrow_sum(l);
        z = xrow_sum(z);
        if (c == 0) {
            *reinterpret_cast<f32x4*>(scp + r * 4) = f32x4{s[0], s[1], s[2], s[3]};
            *reinterpret_cast<f32x4*>(scp + 16) = f32x4{mx, l, z, 0.f};
        }
        PIN();
    }
    STAMP(4);

    const float* pwp = s_w + (hp * 4 + rw) * 20 + r * 4;
    f32x4 pw = *reinterpret_cast<const f32x4*>(pwp);
    unsigned tag, lw, lo;
    f32x4 lf;
    f32x2 lz;
    unsigned lp = (hp * 4 * NI + NI - 1) * DH + (lane & 31) * 6;
    f32x2 acc[3][2];
    auto v_acc = [&](int t, int j) {
        const f32x2 p2 = {pw[t], pw[t]};
        const u32x4 vv = ring[t][j];
        const f32x2 v01 = {(float)(int)vv[0], (float)(int)vv[1]}, v23 = {(float)(int)vv[2], (float)(int)vv[3]};
        if (t == 0) { acc[j][0] = p2 * v01; acc[j][1] = p2 * v23; }
        else { acc[j][0] += p2 * v01; acc[j][1] += p2 * v23; }
        asm volatile("" : "+v"(acc[j][0]), "+v"(acc[j][1]));
        PIN();
    };
    auto v_fold = [&](int i, int j) {
        const float x = swap32_add(acc[j][0][0], acc[j][1][0]);
        const float y = swap32_add(acc[j][0][1], acc[j][1][1]);
        s_part[(wave * NI + i) * DH + 64 * j + 4 * c + r] = swap16_add(x, y);
    };
    auto v_done = [&](int i) {
        PIN();
        if (lane == 0) *(vlds_u32*)(s_done + wave) = i + 1;
        PIN();
    };
#pragma unroll
    for (int t = 0; t < LS; ++t)
#pragma unroll
        for (int j = 0; j < 3; ++j) v_acc(t, j);
    for (int i = 0; i < NI - 1; ++i) {
        const unsigned hnext = (i + 1) * (2 * HEADB) + woff;
        enter(i);
#pragma unroll
        for (int k = 0; k < 4; ++k) {
            const int t = (k + LS) & 3;
#pragma unroll
            for (int j = 0; j < 3; ++j) {
                v_acc(t, j);
                if (t == 3 && j == 2) pw = *reinterpret_cast<const f32x4*>(pwp + (i + 1) * 160);
                refill(k, j, rv, hnext + toff(k));
                if (k == 0 && j == 0) TRACE(16 + i);
            }
            if (t == 3) { v_fold(i, 0); v_fold(i, 1); v_fold(i, 2); v_done(i); }
        }
    }
    if (PAD) asm volatile("s_branch .Lkv_tail_%=\n\t.p2align 12\n.Lkv_tail_%=:" ::: "memory");
    STAMP(5);
    TRACE(31);
    while (__builtin_amdgcn_readfirstlane(*(vlds_u32*)(s_sync + 3)) <= NI) __builtin_amdgcn_s_sleep(1);
    PIN();
    tag = s_sync[4 + (lane & 3)];
    lf = *reinterpret_cast<const f32x4*>(s_f + (2 * (NI - 1) + hp) * 8);
    lz = *reinterpret_cast<const f32x2*>(s_f + (2 * (NI - 1) + hp) * 8 + 4);
    origin = *(vlds_u32*)(s_sync + 2);
    lw = last_word(tag, lz[1], lane & 31);
    lo = lane < 32 ? piece_off(hp, lane & 31) : OOB;
    asm volatile("" : "+v"(tag), "+v"(lf), "+v"(lz), "+v"(lw), "+v"(lo), "+v"(lp), "+v"(origin));
    PIN();
#pragma unroll
    for (int t = LS; t < 4; ++t)
#pragma unroll
        for (int j = 0; j < 3; ++j) { v_acc(t, j); if (t == 3) { v_fold(NI - 1, j); PIN(); } }
    v_done(NI - 1);
    STAMP(6);
    {
        const u32x4 dn = *(vlds_u32x4*)(s_done + 4 * hp);
        PIN();
        const u32x4 v = merge(s_part + lp, lf, lz[0], lw);
        if (__builtin_amdgcn_readfirstlane(dn[0] & dn[1] & dn[2] & dn[3]) == NI) publish(v, NI - 1, lo);
        PIN();
    }
    STAMP(7);
    {
        unsigned inbox = WS_PART + (bid >> 4) * GSTR + (bid & 15) * UNITB, obase = bid * 48;
        unsigned xo = tid * 16, yo = tid * 16 + 8192, sel = lane & 1 ? 0x01000504u : 0x05040100u;
        unsigned bo = lane < 16 ? wave * 1024 + (lane & 7) * 128 + (lane >> 3) * 8192 : OOB;
        typedef __attribute__((address_space(3))) float lds_f;
        lds_f* wo = (lds_f*)s_red + wave * 28 + (r < 3 ? c * 6 + r * 2 : 24);
        lds_f* fo = (lds_f*)s_red + (lane & 7) * 28 + 24;
        lds_f* go = (lds_f*)s_red + (lane & 7) * 28 + min(lane >> 3, 5) * 4;
        vlds_u32* dw = (vlds_u32*)(s_done + wave);
        vlds_u32* dr = (vlds_u32*)(s_done + (lane & 7));
        unsigned oo = (lane & 7) == 0 && lane < 48 ? lane : OOB, eo = bid == 0 && lane < 4 ? lane * 2 : OOB;
        asm volatile("" : "+s"(inbox), "+s"(obase), "+v"(xo), "+v"(yo), "+v"(sel), "+v"(bo), "+v"(wo), "+v"(fo), "+v"(go), "+v"(oo), "+v"(eo), "+v"(dw), "+v"(dr));
        const unsigned o = __builtin_amdgcn_readfirstlane(origin) - (15 - (bid >> 4)) * RST;
        while ((int)((now32() << 8) - o) < pace.rd) __builtin_amdgcn_s_sleep(1);
        PIN();
        STAMP(8);
        u32x4 x, y;
        do {
            x = __builtin_amdgcn_raw_buffer_load_b128(rw_, xo, inbox, 16);
            y = __builtin_amdgcn_raw_buffer_load_b128(rw_, yo, inbox, 16);
            const unsigned age = now32() - tstart;
            PIN();
            if (age > SPIN_LIMIT) break;
        } while (__builtin_amdgcn_ballot_w64(((x[3] ^ tag) | (y[3] ^ tag)) > 0xffffu) != 0);
        if (DRY) { __builtin_amdgcn_raw_buffer_store_b32(0u, rw_, bo, inbox, 16); PIN(); }
        STAMP(9);
        const float lx = __uint_as_float(__builtin_amdgcn_perm(__builtin_amdgcn_mov_dpp(x[3], 0xb1, 0xf, 0xf, true), x[3], sel));
        const float ly = __uint_as_float(__builtin_amdgcn_perm(__builtin_amdgcn_mov_dpp(y[3], 0xb1, 0xf, 0xf, true), y[3], sel));
        float M = fmaxf(lx, ly);
        M = fmaxf(M, dpp<0x128>(M));
        M = fmaxf(M, dpp<0x124>(M));
        M = xrow_max(M);
        const float w0 = __builtin_amdgcn_exp2f(lx - M), w1 = __builtin_amdgcn_exp2f(ly - M);
        float a[8];
#pragma unroll
        for (int k = 0; k < 3; ++k) {
            a[2 * k] = w0 * bflo(x[k]) + w1 * bflo(y[k]);
            a[2 * k + 1] = w0 * bfhi(x[k]) + w1 * bfhi(y[k]);
        }
        a[6] = a[7] = w0 + w1;
        float b[4], d[2];
#pragma unroll
        for (int k = 0; k < 4; ++k) b[k] = swap32_add(a[k], a[4 + k]);
#pragma unroll
        for (int k = 0; k < 2; ++k) d[k] = swap16_add(b[k], b[2 + k]);
#pragma unroll
        for (int k = 0; k < 2; ++k) { d[k] += dpp<0x128>(d[k]); d[k] += dpp<0x124>(d[k]); }
        if (c < 4) {
            *(__attribute__((address_space(3))) f32x2*)wo = f32x2{d[0], r < 3 ? d[1] : M};
            PIN();
            *dw = 3 * NI;
        }
        PIN();
        STAMP(10);
        const unsigned fin = *dr;
        PIN();
        f32x2 sm = *(__attribute__((address_space(3))) f32x2*)fo;
        f32x4 ov = *(__attribute__((address_space(3))) f32x4*)go;
        asm volatile("" : "+v"(sm), "+v"(ov));
        if (__builtin_amdgcn_ballot_w64(fin != 3 * NI) == 0) {
            float MM = fmaxf(sm[1], dpp<0xb1>(sm[1]));
            MM = fmaxf(MM, dpp<0x4e>(MM));
            MM = fmaxf(MM, dpp<0x141>(MM));
            const float ff = __builtin_amdgcn_exp2f(sm[1] - MM);
            float e[5] = {ff * ov[0], ff * ov[1], ff * ov[2], ff * ov[3], ff * sm[0]};
#pragma unroll
            for (int k = 0; k < 5; ++k) e[k] += dpp<0xb1>(e[k]);
#pragma unroll
            for (int k = 0; k < 5; ++k) e[k] += dpp<0x4e>(e[k]);
#pragma unroll
            for (int k = 0; k < 5; ++k) e[k] += dpp<0x141>(e[k]);
            const float inv = __builtin_amdgcn_rcpf(e[4]);
            __builtin_amdgcn_raw_buffer_store_b64(u32x2{pk_bf16(f32x2{e[0] * inv, e[1] * inv}), pk_bf16(f32x2{e[2] * inv, e[3] * inv})}, ro, oo, obase, OST);
            if (bid == 0) __builtin_amdgcn_raw_buffer_store_b16((unsigned short)(tag >> 16), rw_, eo, WS_EPOCH, 16);
        }
    }
    __builtin_amdgcn_endpgm();
}

#define VAR_XS(p) ((p) / 1000 % 2)
template <int X, int PRO, int GATE, int STAG>
static void launch_cfg(void* const* d_in, void* d_out, void* d_ws, hipStream_t stream, float t_us, float start_us, float off_us, float debt_us, float adj_pct, float pub_us) {
    Pace pace;
    pace.step = (unsigned)(t_us * 25600.f / 128.f);
    pace.start = (int)(start_us * 25600.f);
    const float zone = PRO / 100 % 10 ? (int)off_us * 0.01f : 0.f; off_us -= (int)off_us;
    pace.offset = (int)(off_us * 25600.f) | (int)(zone * 6400.f) << 16; off_us += 2 * zone;
    pace.bias = (int)(debt_us * 25600.f);
    pace.rd = (int)((start_us + off_us + t_us * ((129.f + VAR_XS(PRO)) / 128.f) + adj_pct) * 25600.f);
    pace.pub = (int)((start_us + off_us + t_us * ((129.f + VAR_XS(PRO)) / 128.f) + pub_us) * 25600.f);
    kv_sweep_kernel<X, PRO, GATE, STAG, 1><<<NWG, 576, 0, stream>>>(d_in[0], d_in[1], d_in[2], d_in[3], d_in[4], d_in[5], d_in[6], d_in[7],
        d_in[8], d_in[9], d_in[10], d_ws, d_out, pace EXTVAL);
}

#define CFG 0, 123, 5002501, 112
#define PRM 100.75f, 0.5f, 10.8f, 0.055f, 1.6f, 0.6f
extern "C" void kernel_launch(void* const* d_in, const int* in_sizes, int n_in,
                              void* d_out, int out_size, void* d_ws, size_t ws_size,
                              hipStream_t stream) {
    launch_cfg<CFG>(d_in, d_out, d_ws, stream, PRM);
}
```

```cpp
#include <hip/hip_runtime.h>
#include <cstdint>


using u32x4 = __attribute__((ext_vector_type(4))) unsigned;
using u32x3 = __attribute__((ext_vector_type(3))) unsigned;
using u32x2 = __attribute__((ext_vector_type(2))) unsigned;
using u64x8 = __attribute__((ext_vector_type(8))) unsigned long long;
using f32x2 = __attribute__((ext_vector_type(2))) float;
using f32x4 = __attribute__((ext_vector_type(4))) float;
using bf16x2 = __attribute__((ext_vector_type(2))) __bf16;

constexpr int DH = 192;
constexpr int HALF = 96;
constexpr int NH = 32;
constexpr int SKV = 16384;
constexpr int NWG = 256;
constexpr int NI = 16;
constexpr unsigned ROWB = DH * 4;
constexpr unsigned HEADB = SKV * ROWB;
constexpr unsigned KVB = NH * HEADB;
constexpr unsigned CSB = SKV * DH * 2;
constexpr unsigned OOB = 0x80000000u;
constexpr int DEV = 16, NT = 2;
constexpr float SCALE_LOG2 = 1.4426950408889634f * 0.07216878364870322f;
constexpr unsigned SPIN_LIMIT = 20000000u;

constexpr unsigned UNITB = NWG * 64;
constexpr unsigned GSTR = 16 * UNITB + 256;
constexpr unsigned WS_PART = 0;
constexpr unsigned WS_EPOCH = NWG * (UNITB + 1024);
constexpr unsigned WS_REP = WS_EPOCH + 256;
constexpr unsigned WS_BYTES = WS_REP + 256 * 512;

__device__ __forceinline__ float bflo(unsigned u) { return __uint_as_float(u << 16); }
__device__ __forceinline__ float bfhi(unsigned u) { return __uint_as_float(u & 0xffff0000u); }
__device__ __forceinline__ unsigned pk_bf16(f32x2 a) { return __builtin_bit_cast(unsigned, __builtin_convertvector(a, bf16x2)); }
__device__ __forceinline__ void dot_cvt(const u32x4 q, const u32x4 t, const u32x4 ki, f32x2& w01, f32x2& w23, f32x2& k01, f32x2& k23) {
    float w0, w1, w2, w3;
    float k0 = __uint_as_float(ki[0]), k1 = __uint_as_float(ki[1]), k2 = __uint_as_float(ki[2]), k3 = __uint_as_float(ki[3]);
    asm("v_dot2_f32_bf16 %0, %8, %12, 0\n\tv_dot2_f32_bf16 %1, %9, %13, 0\n\tv_dot2_f32_bf16 %2, %10, %14, 0\n\tv_dot2_f32_bf16 %3, %11, %15, 0\n\t"
        "v_cvt_f32_i32 %4, %4\n\tv_cvt_f32_i32 %5, %5\n\tv_cvt_f32_i32 %6, %6\n\tv_cvt_f32_i32 %7, %7"
        : "=&v"(w0), "=&v"(w1), "=&v"(w2), "=&v"(w3), "+v"(k0), "+v"(k1), "+v"(k2), "+v"(k3)
        : "v"(q[0]), "v"(q[1]), "v"(q[2]), "v"(q[3]), "v"(t[0]), "v"(t[1]), "v"(t[2]), "v"(t[3]));
    w01 = f32x2{w0, w1}; w23 = f32x2{w2, w3}; k01 = f32x2{k0, k1}; k23 = f32x2{k2, k3};
}

template <int CTRL> __device__ __forceinline__ float dpp(float x) {
    return __builtin_bit_cast(float, __builtin_amdgcn_mov_dpp(__builtin_bit_cast(int, x), CTRL, 0xf, 0xf, true));
}
__device__ __forceinline__ float row16_allsum(float x) {
    x += dpp<0x128>(x);
    x += dpp<0x124>(x);
    x += dpp<0x122>(x);
    x += dpp<0x121>(x);
    return x;
}
__device__ __forceinline__ float xrow_max(float x) {
    auto s = __builtin_amdgcn_permlane16_swap(__float_as_uint(x), __float_as_uint(x), false, false);
    x = fmaxf(__uint_as_float(s[0]), __uint_as_float(s[1]));
    auto t = __builtin_amdgcn_permlane32_swap(__float_as_uint(x), __float_as_uint(x), false, false);
    return fmaxf(__uint_as_float(t[0]), __uint_as_float(t[1]));
}
__device__ __forceinline__ float xrow_sum(float x) {
    auto s = __builtin_amdgcn_permlane16_swap(__float_as_uint(x), __float_as_uint(x), false, false);
    x = __uint_as_float(s[0]) + __uint_as_float(s[1]);
    auto t = __builtin_amdgcn_permlane32_swap(__float_as_uint(x), __float_as_uint(x), false, false);
    return __uint_as_float(t[0]) + __uint_as_float(t[1]);
}
__device__ __forceinline__ float swap32_add(float a, float b) {
    auto t = __builtin_amdgcn_permlane32_swap(__float_as_uint(a), __float_as_uint(b), false, false);
    return __uint_as_float(t[0]) + __uint_as_float(t[1]);
}
__device__ __forceinline__ float swap16_add(float a, float b) {
    auto t = __builtin_amdgcn_permlane16_swap(__float_as_uint(a), __float_as_uint(b), false, false);
    return __uint_as_float(t[0]) + __uint_as_float(t[1]);
}
__device__ __forceinline__ int row16_sum(int x) {
    x += __builtin_amdgcn_mov_dpp(x, 0x128, 0xf, 0xf, true);
    x += __builtin_amdgcn_mov_dpp(x, 0x124, 0xf, 0xf, true);
    x += __builtin_amdgcn_mov_dpp(x, 0x122, 0xf, 0xf, true);
    x += __builtin_amdgcn_mov_dpp(x, 0x121, 0xf, 0xf, true);
    return x;
}
__device__ __forceinline__ unsigned now32() { return (unsigned)__builtin_amdgcn_s_memrealtime(); }

#define PIN() do { asm volatile("" ::: "memory"); __builtin_amdgcn_sched_barrier(0); } while (0)
#define BARRIER() do { asm volatile("s_waitcnt lgkmcnt(0)" ::: "memory"); __builtin_amdgcn_s_barrier(); PIN(); } while (0)
#define VMCNT(n) asm volatile("s_waitcnt vmcnt(%0)" :: "i"(n) : "memory")

typedef volatile __attribute__((address_space(3))) unsigned vlds_u32;
typedef volatile __attribute__((address_space(3))) u32x2 vlds_u32x2;
typedef volatile __attribute__((address_space(3))) u32x4 vlds_u32x4;

struct Pace {
    unsigned step;
    int start;
    int offset;
    int bias;
    int rd;
    int pub;
};

__global__ __launch_bounds__(576, 3) __attribute__((aligned(4096))) void kv_sweep_kernel(
    const void*, const void*, const void*, const void*, const void*, const void*, const void*, const void*,
    const void*, const void*, const void*, void*, void*, Pace)
{
    __shared__ __attribute__((aligned(16))) unsigned s_q[NH * DH];
    __shared__ __attribute__((aligned(16))) unsigned short s_sc[NH * 256];
    __shared__ __attribute__((aligned(16))) float s_w[NH * 4 * 20];
    __shared__ __attribute__((aligned(16))) float s_part[8 * NI * DH];
    __shared__ __attribute__((aligned(16))) float s_red[8 * 28];
    __shared__ __attribute__((aligned(16))) float s_f[NH * 8];
    __shared__ __attribute__((aligned(16))) unsigned s_done[8];
    __shared__ __attribute__((aligned(16))) unsigned s_sync[8];
    __shared__ __attribute__((aligned(16))) unsigned s_act[16];

    unsigned long long pc0, t0;
    u64x8 ka, kb;
    asm volatile("s_load_dwordx16 %3, %4, 0x40\n\ts_load_dwordx16 %2, %4, 0x0\n\ts_memrealtime %1\n\ts_getpc_b64 %0\n.Lkv_beg:"
                 : "=&s"(pc0), "=&s"(t0), "=&s"(ka), "=&s"(kb) : "s"(__builtin_amdgcn_kernarg_segment_ptr()));
    __builtin_amdgcn_sched_barrier(0);
    const int tid = threadIdx.x;
    const int lane = tid & 63;
    int wave = __builtin_amdgcn_readfirstlane(tid >> 6);
    const int r = lane >> 4;
    const int c = lane & 15;
    const int bid = blockIdx.x;
    const int m = (bid & 7) * 32 + (bid >> 3);
    const int rw = wave & 3, hp = wave >> 2;
    constexpr int RS = 8192, RT = 1024;
    const int grow = ((bid >> 3 & 3) | (bid >> 4 & 4) | (bid & 0x84) << 1 | (bid & 2) << 3 | (bid & 32) | (bid & 1) << 7) * 16;
    auto srow = [&](int t) { return (t & 1) * RS ^ (t >> 1) * RT; };
    const int row0 = grow + rw * 4;
    unsigned co = r * 384 + c * 8, so1 = r * 384 + (c < 8 ? c * 8 + 320 : c * 8 - 64), rb0 = (row0 ^ RT * hp) * 384u;
    asm volatile("s_waitcnt lgkmcnt(0)" : "+s"(t0), "+s"(ka), "+s"(kb), "+v"(co), "+v"(so1), "+s"(rb0), "+s"(wave));
    typedef const __attribute__((address_space(1))) char* gptr;
    void* const q = (void*)ka[0]; void* const qk = (void*)ka[1]; void* const qv = (void*)ka[4]; void* const q_cos = (void*)ka[7];
    const gptr ksc = (gptr)ka[2], kzp = (gptr)ka[3], vsc = (gptr)ka[5], vzp = (gptr)ka[6];
    void* const q_sin = (void*)kb[0]; void* const k_cos = (void*)kb[1]; void* const k_sin = (void*)kb[2]; void* const out = (void*)kb[4];
    unsigned char* const ws = (unsigned char*)(__attribute__((address_space(1))) unsigned char*)kb[3];
    const Pace pace = {(unsigned)kb[5], (int)(kb[5] >> 32), (int)kb[6], (int)(kb[6] >> 32), (int)kb[7], (int)(kb[7] >> 32)};
    const unsigned tstart = (unsigned)t0;
    const unsigned tsched = now32();
    const auto rw_ = __builtin_amdgcn_make_buffer_rsrc((void*)ws, 0, WS_BYTES, 0x00020000);
    const auto ro = __builtin_amdgcn_make_buffer_rsrc((void*)out, 0, NH * DH * 2, 0x00020000);

    auto merge = [&](const float* p, f32x4 f, float zs, unsigned w3) {
        f32x2 u[4][3], a[3];
#pragma unroll
        for (int w = 0; w < 4; ++w)
#pragma unroll
            for (int k = 0; k < 3; ++k) u[w][k] = *reinterpret_cast<const f32x2*>(p + w * NI * DH + 2 * k);
        asm volatile("" : "+v"(u[0][0]), "+v"(u[0][1]), "+v"(u[0][2]), "+v"(u[1][0]), "+v"(u[1][1]), "+v"(u[1][2]),
                          "+v"(u[2][0]), "+v"(u[2][1]), "+v"(u[2][2]), "+v"(u[3][0]), "+v"(u[3][1]), "+v"(u[3][2]));
#pragma unroll
        for (int k = 0; k < 3; ++k) a[k] = (f[0] * u[0][k] - zs + f[1] * u[1][k]) + (f[2] * u[2][k] + f[3] * u[3][k]);
        return u32x4{pk_bf16(a[0]), pk_bf16(a[1]), pk_bf16(a[2]), w3};
    };
    auto last_word = [&](unsigned tag, float ls, int c6) { return tag | ((__float_as_uint(ls) >> (16 * (c6 & 1))) & 0xffffu); };
    auto piece_off = [&](int hp, int c6) { return (hp * 8 + (c6 >> 2)) * UNITB + (c6 & 3) * 16; };
    auto publish = [&](u32x4 v, int i, unsigned vo) {
        __builtin_amdgcn_raw_buffer_store_b128(v, rw_, vo, WS_PART + i * GSTR + m * 64, DEV);
        asm volatile("s_nop 1" :: "v"(v) : "memory");
    };

    if (__builtin_expect_with_probability(wave == 8, 1, 0.45)) {
        __builtin_amdgcn_s_setprio(3);
        unsigned clen;
        asm volatile("s_mov_b32 %0, .Lkv_end-.Lkv_beg" : "=s"(clen));
        const auto rcode = __builtin_amdgcn_make_buffer_rsrc((void*)(pc0 & ~127ull), 0, clen, 0x00020000);
        const unsigned x0 = __builtin_amdgcn_raw_buffer_load_b32(rcode, lane < 9 ? ((bid >> 3) * 9 + lane) * 64 : OOB, 0, 0);
        PIN();
        while (now32() - tsched < 100) __builtin_amdgcn_s_sleep(1);
        u32x3 lc[2], ls[2];
        u32x4 qraw[12];
        {
            const auto rq = __builtin_amdgcn_make_buffer_rsrc((void*)q, 0, NH * DH * 2, 0x00020000);
            const auto rqc = __builtin_amdgcn_make_buffer_rsrc((void*)q_cos, 0, DH * 2, 0x00020000);
            const auto rqs = __builtin_amdgcn_make_buffer_rsrc((void*)q_sin, 0, DH * 2, 0x00020000);
#pragma unroll
            for (int k = 0; k < 12; ++k) qraw[k] = __builtin_amdgcn_raw_buffer_load_b128(rq, lane * 16 + (k & 3) * 1024, (k >> 2) * 4096, 0);
#pragma unroll
            for (int k = 0; k < 2; ++k) {
                lc[k] = __builtin_amdgcn_raw_buffer_load_b96(rqc, c * 12 + k * 192, 0, 0);
                ls[k] = __builtin_amdgcn_raw_buffer_load_b96(rqs, c * 12 + k * 192, 0, 0);
            }
            PIN();
        }
        const unsigned tag = ((unsigned)__builtin_amdgcn_raw_buffer_load_b16(rw_, (lane & 3) * 2, WS_EPOCH, DEV) + 1u) << 16;
        PIN();
        unsigned* const s_raw = reinterpret_cast<unsigned*>(s_part) + NI * DH / 2;
#pragma unroll
        for (int k = 0; k < 12; ++k) *reinterpret_cast<u32x4*>(s_raw + k / 6 * (NI * DH) + k % 6 * 256 + lane * 4) = qraw[k];
        f32x2 fc[2][3], fs[2][3];
#pragma unroll
        for (int k = 0; k < 2; ++k)
#pragma unroll
            for (int u = 0; u < 3; ++u) { fc[k][u] = f32x2{bflo(lc[k][u]), bfhi(lc[k][u])}; fs[k][u] = f32x2{bflo(ls[k][u]), bfhi(ls[k][u])}; }
        PIN();
#pragma nounroll
        for (int it = 0; it < 4; ++it) {
            unsigned qa[2][3], qb[2][3];
#pragma unroll
            for (int g = 0; g < 2; ++g)
#pragma unroll
                for (int u = 0; u < 3; ++u) {
                    const unsigned* src = s_raw + g * (NI * DH) + (it * 4 + r) * HALF + 3 * c;
                    qa[g][u] = src[u]; qb[g][u] = src[HALF / 2 + u];
                }
#pragma unroll
            for (int g = 0; g < 2; ++g)
#pragma unroll
                for (int u = 0; u < 3; ++u) {
                    const f32x2 a = {bflo(qa[g][u]), bfhi(qa[g][u])}, b = {bflo(qb[g][u]), bfhi(qb[g][u])};
                    const f32x2 o0 = a * fc[0][u] - b * fs[0][u], o1 = b * fc[1][u] + a * fs[1][u];
                    const unsigned l0 = pk_bf16(f32x2{o0[0], o1[0]}), l1 = pk_bf16(f32x2{o0[1], o1[1]});
                    unsigned* dst = s_q + (16 * g + 4 * it + r) * DH + 6 * c + 2 * u;
                    *reinterpret_cast<u32x2*>(dst) = u32x2{l0, l1};
                    *reinterpret_cast<u32x2*>(dst + HALF) = u32x2{__builtin_amdgcn_alignbit(l0, l0, 16) ^ 0x80000000u, __builtin_amdgcn_alignbit(l1, l1, 16) ^ 0x80000000u};
                }
        }
        asm volatile("" :: "v"(x0));
        __builtin_amdgcn_s_setprio(0);
        unsigned origin = tsched << 8;
        if (lane < 8) { s_done[lane] = 0; s_sync[lane] = lane < 3 ? origin : lane < 4 ? 0 : tag; }
        PIN();
        BARRIER();
        const int mh = lane >> 5, mc = lane & 31;
        const unsigned rtag = __builtin_amdgcn_readlane(tag, 0) | __builtin_amdgcn_readlane(tag, 1) >> 16;
        constexpr int RPX = 2;
        constexpr int RSH = 9;
        constexpr unsigned RDLY = 200;
        constexpr int UP = 984, DN = 2000;
        const int rj = ((bid >> 3) + 5 * (bid & 7)) & 31;
        const int rid = rj % (32 / RPX) == 0 ? rj / (32 / RPX) * 8 + (bid & 7) : -1;
        unsigned wround = now32() >> RSH, rround = wround, tick = 0, target = origin;
        bool synced = false;
        int drift = 0;
        const unsigned round0 = rround;
        int i = 0;
        auto kept_now = [&](unsigned now) {
            const u32x2 a = *(vlds_u32x2*)(s_act + 2 * (lane & 7));
            const int late = max((int)(a[0] - origin), (int)((now << 8) - a[1] - origin));
            return origin + (__builtin_amdgcn_readfirstlane(row16_sum(late)) >> 4);
        };
        auto steer = [&](unsigned now) {
            if ((now >> 5) != tick) {
                tick = now >> 5;
                target += drift;
                origin += max(-DN, min(UP + drift, (int)(target - origin)));
                if (!synced) target = origin;
                if (lane == 0) *(vlds_u32*)(s_sync + 2) = origin;
                PIN();
            }
        };
        auto finished = [&]() {
            const u32x4 a = *(vlds_u32x4*)s_done, b = *(vlds_u32x4*)(s_done + 4);
            return __builtin_amdgcn_readfirstlane(min(min(min(a[0], a[1]), min(a[2], a[3])), min(min(b[0], b[1]), min(b[2], b[3]))));
        };
        while (i < NI - 2) {
            const unsigned now = now32();
            if ((now >> RSH) != wround) {
                wround = now >> RSH;
                if (rid >= 0 && (now & ((1u << RSH) - 1)) < 48)
                    __builtin_amdgcn_raw_buffer_store_b64(u32x2{kept_now(now), rtag + wround}, rw_, lane == 0 ? rid * 8 : OOB, WS_REP + (wround & 255) * 512, DEV);
                PIN();
                continue;
            }
            if ((int)(((now - RDLY) >> RSH) - rround) > 0) {
                rround = (now - RDLY) >> RSH;
                const u32x2 x = __builtin_amdgcn_raw_buffer_load_b64(rw_, c * 8, WS_REP + (rround & 255) * 512, DEV);
                const int d = (int)(x[0] - origin);
                const bool ok = x[1] == rtag + rround && d < (32 << 16) && d > -(32 << 16);
                const int n = __builtin_popcountll(__builtin_amdgcn_ballot_w64(ok));
                if (n >= 48) {
                    const unsigned nt = origin + __builtin_amdgcn_readfirstlane((int)(row16_allsum(ok ? (float)d : 0.f) * 4.f / (float)n)) - pace.bias;
                    if (synced && rround - round0 > 4) drift = max(0, min(drift + ((max(-1024, min((int)(nt - target) + 6 * drift, 2560)) - (rround - round0 < 13 ? 768 : 0)) >> 5), 400));
                    target = nt + 6 * drift;
                    synced = true;
                    const int lost = min((int)(kept_now(now32()) - origin) - pace.bias, (int)(target - origin));
                    if (lost > 0) origin += lost;
                }
                PIN();
                continue;
            }
            steer(now);
            if (finished() <= (unsigned)i) { __builtin_amdgcn_s_sleep(2); continue; }
            PIN();
            if (i == 0) {
                f32x4 ml[4], f;
#pragma unroll
                for (int w = 0; w < 4; ++w) ml[w] = *reinterpret_cast<const f32x4*>(s_w + (mc * 4 + w) * 20 + 16);
                const float M = fmaxf(fmaxf(ml[0][0], ml[1][0]), fmaxf(ml[2][0], ml[3][0]));
                float L = 0.f, zs = 0.f;
#pragma unroll
                for (int w = 0; w < 4; ++w) { f[w] = __builtin_amdgcn_exp2f(ml[w][0] - M); L += f[w] * ml[w][1]; zs += f[w] * ml[w][2]; }
                const float inv = 1.f / L;
                *reinterpret_cast<f32x4*>(s_f + mc * 8) = f * inv;
                *reinterpret_cast<f32x2*>(s_f + mc * 8 + 4) = f32x2{zs * inv, M + __builtin_amdgcn_logf(L)};
                PIN();
                if (lane == 0) *(vlds_u32*)(s_sync + 3) = NI + 1;
                PIN();
            }
            const f32x2 zl = *reinterpret_cast<const f32x2*>(s_f + (2 * i + mh) * 8 + 4);
            const u32x4 v = merge(s_part + (mh * 4 * NI + i) * DH + mc * 6, *reinterpret_cast<const f32x4*>(s_f + (2 * i + mh) * 8), zl[0], last_word(tag, zl[1], mc));
            PIN();
            *reinterpret_cast<u32x4*>(s_part + (mh * 4 * NI + i) * DH + mc * 4) = v;
            PIN();
            ++i;
        }
        u32x4 v[NI - 1];
#pragma unroll
        for (int i = 0; i < NI - 2; ++i) v[i] = *reinterpret_cast<const u32x4*>(s_part + (mh * 4 * NI + i) * DH + mc * 4);
        f32x4 f = *reinterpret_cast<const f32x4*>(s_f + (2 * (NI - 2) + mh) * 8);
        f32x2 zl = *reinterpret_cast<const f32x2*>(s_f + (2 * (NI - 2) + mh) * 8 + 4);
        PIN();
#pragma unroll
        for (int i = 0; i < NI - 2; ++i) asm volatile("" : "+v"(v[i]));
        asm volatile("" : "+v"(f), "+v"(zl));
        v[NI - 2] = v[0];
        bool have = false, sent = false;
        while (!(have && sent)) {
            const unsigned now = now32();
            if (!have) {
                steer(now);
                if (finished() > NI - 2) {
                    PIN();
                    v[NI - 2] = merge(s_part + (mh * 4 * NI + NI - 2) * DH + mc * 6, f, zl[0], last_word(tag, zl[1], mc));
                    have = true;
                }
            }
            if (!sent && (int)((now << 8) - origin) >= pace.pub) {
#pragma unroll
                for (int i = 0; i < NI - 2; ++i) publish(v[i], i, piece_off(mh, mc));
                sent = true;
            }
            if (!(have && sent)) __builtin_amdgcn_s_sleep(1);
            PIN();
        }
        publish(v[NI - 2], NI - 2, piece_off(mh, mc));
        asm volatile(".Lkv_end:");
        return;
    }

    const int ph = rw + 4 * bid + 1024 * hp;
    unsigned off = pace.start + ((pace.step * ph) >> 11);
    unsigned origin = tsched << 8, kept = 0;
    unsigned step = pace.step;
    auto pace_wait = [&]() {
        const unsigned tgt = __builtin_amdgcn_readfirstlane(origin) + off;
        unsigned now = now32() << 8;
        kept = ((int)(now - tgt) > 0 ? now : tgt) - off;
        while ((int)(tgt - now) > 0) { __builtin_amdgcn_s_sleep(1); now = now32() << 8; }
        off += step;
    };
    auto report = [&]() {
        if (lane == 0) *(vlds_u32x2*)(s_act + 2 * wave) = u32x2{kept, off};
        origin = *(vlds_u32*)(s_sync + 2);
        PIN();
    };

    auto pick = [](int n, gptr p0, gptr p1, gptr p2, gptr p3) { return (n & 2) ? ((n & 1) ? p3 : p2) : ((n & 1) ? p1 : p0); };
    auto dma = [](gptr a, unsigned dst) { asm volatile("s_mov_b32 m0, %1\n\ts_nop 0\n\tglobal_load_lds_dwordx4 %0, off nt" :: "v"(a), "s"(__builtin_amdgcn_readfirstlane(dst)) : "memory"); };
    const int w8 = rw + 4 * hp;
    const unsigned sdst = (unsigned)(unsigned long long)(__attribute__((address_space(3))) unsigned short*)s_sc + w8 * 1024;
    auto gather = [&](int pair) {
        dma(pick(lane >> 1, ksc, kzp, vsc, vzp) + ((w8 * 2 + (lane >> 5)) * SKV + (grow ^ srow(lane >> 3 & 3)) + (lane & 1) * 8) * 2 + pair * 32 * SKV, sdst + pair * 8192);
        PIN();
    };
    auto pre_sc = [&]() {
        const unsigned junk = (unsigned)(unsigned long long)(__attribute__((address_space(3))) float*)s_part + (3 * NI * DH - 256) * 4;
        dma(pick(grow >> 4, ksc, kzp, vsc, vzp) + ((w8 * 2 + (lane >> 5)) * SKV + (grow & ~63 ^ srow(lane >> 3 & 3))) * 2 + (lane & 7) * 16 + 32 * SKV, junk);
        PIN();
    };
    const auto rc = __builtin_amdgcn_make_buffer_rsrc((void*)k_cos, 0, CSB, 0x00020000);
    const auto rs = __builtin_amdgcn_make_buffer_rsrc((void*)k_sin, 0, CSB, 0x00020000);
    u32x2 raw[2][6];
    auto load_cs = [&](int t) {
        const unsigned rb = t == 0 ? rb0 : (row0 ^ RT * hp ^ RS) * 384u;
        raw[t][0] = __builtin_amdgcn_raw_buffer_load_b64(rc, co, rb, NT);
        raw[t][1] = __builtin_amdgcn_raw_buffer_load_b64(rc, co + 128, rb, NT);
        raw[t][2] = __builtin_amdgcn_raw_buffer_load_b64(rc, co + 256, rb, NT);
        raw[t][3] = __builtin_amdgcn_raw_buffer_load_b64(rs, co + 192, rb, NT);
        raw[t][4] = __builtin_amdgcn_raw_buffer_load_b64(rs, so1, rb, NT);
        raw[t][5] = __builtin_amdgcn_raw_buffer_load_b64(rs, co + 64, rb, NT);
        PIN();
    };
    u32x4 tab[4][3];
    auto pack_cs = [&](int t) {
#pragma unroll
        for (int j = 0; j < 3; ++j) {
#pragma unroll
            for (int k = 0; k < 2; ++k) {
                tab[t][j][2 * k] = __builtin_amdgcn_perm(raw[t][3 + j][k], raw[t][j][k], 0x05040100);
                tab[t][j][2 * k + 1] = __builtin_amdgcn_perm(raw[t][3 + j][k], raw[t][j][k], 0x07060302);
            }
            *reinterpret_cast<u32x4*>(s_part + (wave ^ 4) * (NI * DH) + ((t * 3 + j) * 64 + lane) * 4) = tab[t][j];
        }
        PIN();
    };
    const auto rk = __builtin_amdgcn_make_buffer_rsrc((void*)qk, 0, KVB, 0x00020000);
    const auto rv = __builtin_amdgcn_make_buffer_rsrc((void*)qv, 0, KVB, 0x00020000);
    const unsigned voff = r * ROWB + c * 16;
    const unsigned woff = hp * HEADB;
    auto toff = [&](int t) { return (unsigned)(row0 ^ srow(t)) * ROWB; };
    u32x4 ring[4][3];
    auto ld = [&](int slot, int j, __amdgpu_buffer_rsrc_t rr, unsigned off) {
        ring[slot][j] = __builtin_amdgcn_raw_buffer_load_b128(rr, voff + j * 256, off, NT);
        PIN();
    };
    auto ld_tile = [&](int slot, __amdgpu_buffer_rsrc_t rr, unsigned off) { ld(slot, 0, rr, off); ld(slot, 1, rr, off); ld(slot, 2, rr, off); };
    unsigned post = 0;
    auto enter = [&](int i) {
        if (i == 9 || i == 10) {
            const unsigned hs = pace.step >> 1, z = (unsigned)pace.offset >> 16;
            off += i == 10 ? post : hp * 3 * hs;
            step = i == 10 ? pace.step : hs + hp * z;
            post = __builtin_amdgcn_readfirstlane(i == 10 ? 0 : hp ? hs : 4 * (hs + z));
            if (lane == 0) *(vlds_u32x2*)(s_act + 2 * wave) = u32x2{kept, off};
            PIN();
        }
    };
    auto refill = [&](int slot, int j, __amdgpu_buffer_rsrc_t rr, unsigned goff) {
        if (j == 0) pace_wait();
        if (j == 0) asm volatile("s_cmp_lg_u32 %0, 0\n\ts_waitcnt vmcnt(3)\n\ts_cbranch_scc1 1\n\ts_waitcnt vmcnt(2)" :: "s"(post) : "scc", "memory");
        else asm volatile("s_cmp_lg_u32 %0, 0\n\ts_cbranch_scc1 1\n\ts_waitcnt vmcnt(2)" :: "s"(post) : "scc", "memory");
        ld(slot, j, rr, goff);
        if (j == 0) { const unsigned t = (now32() << 8) - 1024 - (off - step); if ((int)(t - kept) > 0) kept = t; }
        if (j == 2) report();
    };
    auto k0 = [&](int t) { pace_wait(); if (t == (rw >> 1) + 1) gather(1); ld_tile(t, rk, woff + toff(t)); };
    load_cs(0);
    pace_wait(); load_cs(1);
    pack_cs(0);
    pace_wait(); pre_sc(); gather(0); off += (pace.offset & 0xffff) - step;
    k0(0); pack_cs(1);
    k0(1);
    VMCNT(3); k0(2);
    VMCNT(3);
    if (hp) { if (lane == 0) *(vlds_u32x2*)(s_act + 2 * wave) = u32x2{kept, off}; BARRIER(); }
    k0(3);
    if (lane == 0) *(vlds_u32x2*)(s_act + 2 * wave) = u32x2{kept, off};
    if (!hp) BARRIER();
    origin = *(vlds_u32*)(s_sync + 2);

    u32x4 qp[3];
    auto load_q = [&](int h, int j) { qp[j] = *reinterpret_cast<const u32x4*>(s_q + h * DH + 64 * j + 4 * c); };
    load_q(hp, 0); load_q(hp, 1); load_q(hp, 2);
#pragma unroll
    for (int t = 0; t < 2; ++t)
#pragma unroll
        for (int j = 0; j < 3; ++j) tab[2 + t][j] = *reinterpret_cast<const u32x4*>(s_part + wave * (NI * DH) + ((t * 3 + j) * 64 + lane) * 4);
    if (hp) {
#pragma unroll
        for (int t = 0; t < 2; ++t)
#pragma unroll
            for (int j = 0; j < 3; ++j)
#pragma unroll
                for (int k = 0; k < 4; ++k) asm volatile("v_swap_b32 %0, %1" : "+v"(tab[t][j][k]), "+v"(tab[2 + t][j][k]));
    }

    for (int i = 0; i < NI; ++i) {
        const bool last = i + 1 == NI;
        enter(i);
        const int h = 2 * i + hp;
        const unsigned hnext = (i + 1) * (2 * HEADB) + woff;
        const auto rn = __builtin_amdgcn_make_buffer_rsrc((void*)(last ? qv : qk), 0, KVB, 0x00020000);
        float* scp = s_w + (h * 4 + rw) * 20;
        f32x4 sc[4];
#pragma unroll
        for (int t = 0; t < 4; ++t)
#pragma unroll
            for (int a = 0; a < 4; ++a) sc[t][a] = bflo(s_sc[h * 256 + t * 64 + a * 16 + rw * 4 + r]);
        float s[4];
#pragma unroll
        for (int t = 0; t < 4; ++t) {
            f32x2 dot, wsum;
#pragma unroll
            for (int j = 0; j < 3; ++j) {
                f32x2 w01, w23, k01, k23;
                dot_cvt(qp[j], tab[t][j], ring[t][j], w01, w23, k01, k23);
                if (j == 0) { wsum = w01 + w23; dot = k01 * w01; } else { wsum += w01; wsum += w23; dot += k01 * w01; }
                dot += k23 * w23;
                asm volatile("" : "+v"(dot), "+v"(wsum));
                PIN();
                if (t == 3) load_q(last ? h : h + 2, j);
                refill(t, j, rn, (last ? woff : hnext) + toff(t));
            }
            const float a = (dot[0] + dot[1]) - sc[t][1] * (wsum[0] + wsum[1]);
            s[t] = row16_allsum(a) * (sc[t][0] * SCALE_LOG2);
        }
        const float mx = xrow_max(fmaxf(fmaxf(s[0], s[1]), fmaxf(s[2], s[3])));
        float l = 0.f, z = 0.f;
#pragma unroll
        for (int t = 0; t < 4; ++t) {
            const float p = __builtin_amdgcn_exp2f(s[t] - mx);
            l += p;
            s[t] = p * sc[t][2];
            z += s[t] * sc[t][3];
        }
        l = xrow_sum(l);
        z = xrow_sum(z);
        if (c == 0) {
            *reinterpret_cast<f32x4*>(scp + r * 4) = f32x4{s[0], s[1], s[2], s[3]};
            *reinterpret_cast<f32x4*>(scp + 16) = f32x4{mx, l, z, 0.f};
        }
        PIN();
    }

    const float* pwp = s_w + (hp * 4 + rw) * 20 + r * 4;
    f32x4 pw = *reinterpret_cast<const f32x4*>(pwp);
    unsigned tag, lw, lo;
    f32x4 lf;
    f32x2 lz;
    unsigned lp = (hp * 4 * NI + NI - 1) * DH + (lane & 31) * 6;
    f32x2 acc[3][2];
    auto v_acc = [&](int t, int j) {
        const f32x2 p2 = {pw[t], pw[t]};
        const u32x4 vv = ring[t][j];
        const f32x2 v01 = {(float)(int)vv[0], (float)(int)vv[1]}, v23 = {(float)(int)vv[2], (float)(int)vv[3]};
        if (t == 0) { acc[j][0] = p2 * v01; acc[j][1] = p2 * v23; }
        else { acc[j][0] += p2 * v01; acc[j][1] += p2 * v23; }
        asm volatile("" : "+v"(acc[j][0]), "+v"(acc[j][1]));
        PIN();
    };
    auto v_fold = [&](int i, int j) {
        const float x = swap32_add(acc[j][0][0], acc[j][1][0]);
        const float y = swap32_add(acc[j][0][1], acc[j][1][1]);
        s_part[(wave * NI + i) * DH + 64 * j + 4 * c + r] = swap16_add(x, y);
    };
    auto v_done = [&](int i) {
        PIN();
        if (lane == 0) *(vlds_u32*)(s_done + wave) = i + 1;
        PIN();
    };
    constexpr int LS = 2;
#pragma unroll
    for (int t = 0; t < LS; ++t)
#pragma unroll
        for (int j = 0; j < 3; ++j) v_acc(t, j);
    for (int i = 0; i < NI - 1; ++i) {
        const unsigned hnext = (i + 1) * (2 * HEADB) + woff;
        enter(i);
#pragma unroll
        for (int k = 0; k < 4; ++k) {
            const int t = (k + LS) & 3;
#pragma unroll
            for (int j = 0; j < 3; ++j) {
                v_acc(t, j);
                if (t == 3 && j == 2) pw = *reinterpret_cast<const f32x4*>(pwp + (i + 1) * 160);
                refill(k, j, rv, hnext + toff(k));
            }
            if (t == 3) { v_fold(i, 0); v_fold(i, 1); v_fold(i, 2); v_done(i); }
        }
    }
    asm volatile("s_branch .Lkv_tail_%=\n\t.p2align 12\n.Lkv_tail_%=:" ::: "memory");
    while (__builtin_amdgcn_readfirstlane(*(vlds_u32*)(s_sync + 3)) <= NI) __builtin_amdgcn_s_sleep(1);
    PIN();
    tag = s_sync[4 + (lane & 3)];
    lf = *reinterpret_cast<const f32x4*>(s_f + (2 * (NI - 1) + hp) * 8);
    lz = *reinterpret_cast<const f32x2*>(s_f + (2 * (NI - 1) + hp) * 8 + 4);
    origin = *(vlds_u32*)(s_sync + 2);
    lw = last_word(tag, lz[1], lane & 31);
    lo = lane < 32 ? piece_off(hp, lane & 31) : OOB;
    asm volatile("" : "+v"(tag), "+v"(lf), "+v"(lz), "+v"(lw), "+v"(lo), "+v"(lp), "+v"(origin));
    PIN();
#pragma unroll
    for (int t = LS; t < 4; ++t)
#pragma unroll
        for (int j = 0; j < 3; ++j) { v_acc(t, j); if (t == 3) { v_fold(NI - 1, j); PIN(); } }
    v_done(NI - 1);
    {
        const u32x4 dn = *(vlds_u32x4*)(s_done + 4 * hp);
        PIN();
        const u32x4 v = merge(s_part + lp, lf, lz[0], lw);
        if (__builtin_amdgcn_readfirstlane(dn[0] & dn[1] & dn[2] & dn[3]) == NI) publish(v, NI - 1, lo);
        PIN();
    }
    {
        unsigned inbox = WS_PART + (bid >> 4) * GSTR + (bid & 15) * UNITB, obase = bid * 48;
        unsigned xo = tid * 16, yo = tid * 16 + 8192, sel = lane & 1 ? 0x01000504u : 0x05040100u;
        unsigned bo = lane < 16 ? wave * 1024 + (lane & 7) * 128 + (lane >> 3) * 8192 : OOB;
        typedef __attribute__((address_space(3))) float lds_f;
        lds_f* wo = (lds_f*)s_red + wave * 28 + (r < 3 ? c * 6 + r * 2 : 24);
        lds_f* fo = (lds_f*)s_red + (lane & 7) * 28 + 24;
        lds_f* go = (lds_f*)s_red + (lane & 7) * 28 + min(lane >> 3, 5) * 4;
        vlds_u32* dw = (vlds_u32*)(s_done + wave);
        vlds_u32* dr = (vlds_u32*)(s_done + (lane & 7));
        unsigned oo = (lane & 7) == 0 && lane < 48 ? lane : OOB, eo = bid == 0 && lane < 4 ? lane * 2 : OOB;
        asm volatile("" : "+s"(inbox), "+s"(obase), "+v"(xo), "+v"(yo), "+v"(sel), "+v"(bo), "+v"(wo), "+v"(fo), "+v"(go), "+v"(oo), "+v"(eo), "+v"(dw), "+v"(dr));
        const unsigned o = __builtin_amdgcn_readfirstlane(origin) - (15 - (bid >> 4)) * 512;
        while ((int)((now32() << 8) - o) < pace.rd) __builtin_amdgcn_s_sleep(1);
        PIN();
        u32x4 x, y;
        do {
            x = __builtin_amdgcn_raw_buffer_load_b128(rw_, xo, inbox, DEV);
            y = __builtin_amdgcn_raw_buffer_load_b128(rw_, yo, inbox, DEV);
            const unsigned age = now32() - tstart;
            PIN();
            if (age > SPIN_LIMIT) break;
        } while (__builtin_amdgcn_ballot_w64(((x[3] ^ tag) | (y[3] ^ tag)) > 0xffffu) != 0);
        __builtin_amdgcn_raw_buffer_store_b32(0u, rw_, bo, inbox, DEV);
        PIN();
        const float lx = __uint_as_float(__builtin_amdgcn_perm(__builtin_amdgcn_mov_dpp(x[3], 0xb1, 0xf, 0xf, true), x[3], sel));
        const float ly = __uint_as_float(__builtin_amdgcn_perm(__builtin_amdgcn_mov_dpp(y[3], 0xb1, 0xf, 0xf, true), y[3], sel));
        float M = fmaxf(lx, ly);
        M = fmaxf(M, dpp<0x128>(M));
        M = fmaxf(M, dpp<0x124>(M));
        M = xrow_max(M);
        const float w0 = __builtin_amdgcn_exp2f(lx - M), w1 = __builtin_amdgcn_exp2f(ly - M);
        float a[8];
#pragma unroll
        for (int k = 0; k < 3; ++k) {
            a[2 * k] = w0 * bflo(x[k]) + w1 * bflo(y[k]);
            a[2 * k + 1] = w0 * bfhi(x[k]) + w1 * bfhi(y[k]);
        }
        a[6] = a[7] = w0 + w1;
        float b[4], d[2];
#pragma unroll
        for (int k = 0; k < 4; ++k) b[k] = swap32_add(a[k], a[4 + k]);
#pragma unroll
        for (int k = 0; k < 2; ++k) d[k] = swap16_add(b[k], b[2 + k]);
#pragma unroll
        for (int k = 0; k < 2; ++k) { d[k] += dpp<0x128>(d[k]); d[k] += dpp<0x124>(d[k]); }
        if (c < 4) {
            *(__attribute__((address_space(3))) f32x2*)wo = f32x2{d[0], r < 3 ? d[1] : M};
            PIN();
            *dw = 3 * NI;
        }
        PIN();
        const unsigned fin = *dr;
        PIN();
        f32x2 sm = *(__attribute__((address_space(3))) f32x2*)fo;
        f32x4 ov = *(__attribute__((address_space(3))) f32x4*)go;
        asm volatile("" : "+v"(sm), "+v"(ov));
        if (__builtin_amdgcn_ballot_w64(fin != 3 * NI) == 0) {
            float MM = fmaxf(sm[1], dpp<0xb1>(sm[1]));
            MM = fmaxf(MM, dpp<0x4e>(MM));
            MM = fmaxf(MM, dpp<0x141>(MM));
            const float ff = __builtin_amdgcn_exp2f(sm[1] - MM);
            float e[5] = {ff * ov[0], ff * ov[1], ff * ov[2], ff * ov[3], ff * sm[0]};
#pragma unroll
            for (int k = 0; k < 5; ++k) e[k] += dpp<0xb1>(e[k]);
#pragma unroll
            for (int k = 0; k < 5; ++k) e[k] += dpp<0x4e>(e[k]);
#pragma unroll
            for (int k = 0; k < 5; ++k) e[k] += dpp<0x141>(e[k]);
            const float inv = __builtin_amdgcn_rcpf(e[4]);
            __builtin_amdgcn_raw_buffer_store_b64(u32x2{pk_bf16(f32x2{e[0] * inv, e[1] * inv}), pk_bf16(f32x2{e[2] * inv, e[3] * inv})}, ro, oo, obase, 0);
            if (bid == 0) __builtin_amdgcn_raw_buffer_store_b16((unsigned short)(tag >> 16), rw_, eo, WS_EPOCH, DEV);
        }
    }
    __builtin_amdgcn_endpgm();
}

extern "C" void kernel_launch(void* const* d_in, const int* in_sizes, int n_in,
                              void* d_out, int out_size, void* d_ws, size_t ws_size,
                              hipStream_t stream) {
    const float t_us = 100.75f, start_us = 0.5f, seam_us = 0.1f, scales_us = 0.8f;
    const float end_us = start_us + scales_us + 2 * seam_us + t_us * (129.f / 128.f);
    Pace pace;
    pace.step = (unsigned)(t_us * 25600.f / 128.f);
    pace.start = (int)(start_us * 25600.f);
    pace.offset = (int)(scales_us * 25600.f) | (int)(seam_us * 6400.f) << 16;
    pace.bias = (int)(0.055f * 25600.f);
    pace.rd = (int)((end_us + 1.6f) * 25600.f);
    pace.pub = (int)((end_us + 0.6f) * 25600.f);
    kv_sweep_kernel<<<NWG, 576, 0, stream>>>(d_in[0], d_in[1], d_in[2], d_in[3], d_in[4], d_in[5], d_in[6], d_in[7],
        d_in[8], d_in[9], d_in[10], d_ws, d_out, pace);
}
```

```cpp
#define PIDX 99
#define PMAX 0
#define PSUB 0
#define PWAVE 0
#include <hip/hip_runtime.h>
#include <hip/hip_bf16.h>
#include <cstdint>
#include <type_traits>

using bf16 = __hip_bfloat16;
using u32x4 = __attribute__((ext_vector_type(4))) unsigned;
using u32x3 = __attribute__((ext_vector_type(3))) unsigned;
using u32x2 = __attribute__((ext_vector_type(2))) unsigned;
using u64x8 = __attribute__((ext_vector_type(8))) unsigned long long;
using f32x2 = __attribute__((ext_vector_type(2))) float;
using f32x4 = __attribute__((ext_vector_type(4))) float;
using bf16x2 = __attribute__((ext_vector_type(2))) __bf16;

constexpr int DH = 192;
constexpr int HALF = 96;
constexpr int NH = 32;
constexpr int SKV = 16384;
constexpr int NWG = 256;
constexpr int NI = 16;
constexpr unsigned ROWB = DH * 4;
constexpr unsigned HEADB = SKV * ROWB;
constexpr unsigned KVB = NH * HEADB;
constexpr unsigned CSB = SKV * DH * 2;
constexpr unsigned OOB = 0x80000000u;
constexpr float SCALE_LOG2 = 1.4426950408889634f * 0.07216878364870322f;
constexpr unsigned SPIN_LIMIT = 20000000u;

constexpr unsigned UNITB = NWG * 64;
constexpr unsigned GSTR = 16 * UNITB + 256;
constexpr unsigned WS_PART = 0;
constexpr unsigned WS_EPOCH = NWG * (UNITB + 1024);
constexpr unsigned WS_REP = WS_EPOCH + 256;
constexpr unsigned WS_PROBE = WS_REP + 256 * 512;
constexpr unsigned WS_BYTES = WS_PROBE + 4096;

__device__ __forceinline__ float bflo(unsigned u) { return __uint_as_float(u << 16); }
__device__ __forceinline__ float bfhi(unsigned u) { return __uint_as_float(u & 0xffff0000u); }
__device__ __forceinline__ float rbf(float x) { return __bfloat162float(__float2bfloat16(x)); }
__device__ __forceinline__ unsigned pk_bf16(f32x2 a) { return __builtin_bit_cast(unsigned, __builtin_convertvector(a, bf16x2)); }
__device__ __forceinline__ void dot_cvt(const u32x4 q, const u32x4 t, const u32x4 ki, f32x2& w01, f32x2& w23, f32x2& k01, f32x2& k23) {
    float w0, w1, w2, w3;
    float k0 = __uint_as_float(ki[0]), k1 = __uint_as_float(ki[1]), k2 = __uint_as_float(ki[2]), k3 = __uint_as_float(ki[3]);
    asm("v_dot2_f32_bf16 %0, %8, %12, 0\n\tv_dot2_f32_bf16 %1, %9, %13, 0\n\tv_dot2_f32_bf16 %2, %10, %14, 0\n\tv_dot2_f32_bf16 %3, %11, %15, 0\n\t"
        "v_cvt_f32_i32 %4, %4\n\tv_cvt_f32_i32 %5, %5\n\tv_cvt_f32_i32 %6, %6\n\tv_cvt_f32_i32 %7, %7"
        : "=&v"(w0), "=&v"(w1), "=&v"(w2), "=&v"(w3), "+v"(k0), "+v"(k1), "+v"(k2), "+v"(k3)
        : "v"(q[0]), "v"(q[1]), "v"(q[2]), "v"(q[3]), "v"(t[0]), "v"(t[1]), "v"(t[2]), "v"(t[3]));
    w01 = f32x2{w0, w1}; w23 = f32x2{w2, w3}; k01 = f32x2{k0, k1}; k23 = f32x2{k2, k3};
}

template <int CTRL> __device__ __forceinline__ float dpp(float x) {
    return __builtin_bit_cast(float, __builtin_amdgcn_mov_dpp(__builtin_bit_cast(int, x), CTRL, 0xf, 0xf, true));
}
__device__ __forceinline__ float row16_allsum(float x) {
    x += dpp<0x128>(x);
    x += dpp<0x124>(x);
    x += dpp<0x122>(x);
    x += dpp<0x121>(x);
    return x;
}
__device__ __forceinline__ float row16_allmax(float x) {
    x = fmaxf(x, dpp<0x128>(x));
    x = fmaxf(x, dpp<0x124>(x));
    x = fmaxf(x, dpp<0x122>(x));
    x = fmaxf(x, dpp<0x121>(x));
    return x;
}
__device__ __forceinline__ float xrow_max(float x) {
    auto s = __builtin_amdgcn_permlane16_swap(__float_as_uint(x), __float_as_uint(x), false, false);
    x = fmaxf(__uint_as_float(s[0]), __uint_as_float(s[1]));
    auto t = __builtin_amdgcn_permlane32_swap(__float_as_uint(x), __float_as_uint(x), false, false);
    return fmaxf(__uint_as_float(t[0]), __uint_as_float(t[1]));
}
__device__ __forceinline__ float xrow_sum(float x) {
    auto s = __builtin_amdgcn_permlane16_swap(__float_as_uint(x), __float_as_uint(x), false, false);
    x = __uint_as_float(s[0]) + __uint_as_float(s[1]);
    auto t = __builtin_amdgcn_permlane32_swap(__float_as_uint(x), __float_as_uint(x), false, false);
    return __uint_as_float(t[0]) + __uint_as_float(t[1]);
}
__device__ __forceinline__ float swap32_add(float a, float b) {
    auto t = __builtin_amdgcn_permlane32_swap(__float_as_uint(a), __float_as_uint(b), false, false);
    return __uint_as_float(t[0]) + __uint_as_float(t[1]);
}
__device__ __forceinline__ float swap16_add(float a, float b) {
    auto t = __builtin_amdgcn_permlane16_swap(__float_as_uint(a), __float_as_uint(b), false, false);
    return __uint_as_float(t[0]) + __uint_as_float(t[1]);
}
__device__ __forceinline__ int row16_sum(int x) {
    x += __builtin_amdgcn_mov_dpp(x, 0x128, 0xf, 0xf, true);
    x += __builtin_amdgcn_mov_dpp(x, 0x124, 0xf, 0xf, true);
    x += __builtin_amdgcn_mov_dpp(x, 0x122, 0xf, 0xf, true);
    x += __builtin_amdgcn_mov_dpp(x, 0x121, 0xf, 0xf, true);
    return x;
}
__device__ __forceinline__ unsigned now32() { return (unsigned)__builtin_amdgcn_s_memrealtime(); }

#define PLAN_EXTRA
#define GNT " nt"
#define GSA 0x22222222
#define GSB 0x55445544
#define CTRL 2, 4, 0, 2560, 768, 13, 9, 200, 984, 2000, 0, 6, 400, -1024
#define NSTAMP 12
#define STAMP(i) do { if ((i) == PIDX - 50) { __builtin_amdgcn_sched_barrier(0); probe = now32(); __builtin_amdgcn_sched_barrier(0); } } while (0)
#define PSTAMP(i) do { if ((i) == PIDX) { __builtin_amdgcn_sched_barrier(0); probe = now32(); asm volatile("" : "+s"(probe) :: "memory"); __builtin_amdgcn_sched_barrier(0); } } while (0)
#define TRACE(i) do { if ((i) == PIDX) probe = tstart + ((int)(kept - (tstart << 8)) >> 8); } while (0)
#define HSTAMP(i)
#define PIN() do { asm volatile("" ::: "memory"); __builtin_amdgcn_sched_barrier(0); } while (0)
#define BARRIER() do { asm volatile("s_waitcnt lgkmcnt(0)" ::: "memory"); __builtin_amdgcn_s_barrier(); PIN(); } while (0)
#define VMCNT(n) asm volatile("s_waitcnt vmcnt(%0)" :: "i"(n) : "memory")

typedef volatile __attribute__((address_space(3))) unsigned vlds_u32;
typedef volatile __attribute__((address_space(3))) u32x2 vlds_u32x2;
typedef volatile __attribute__((address_space(3))) u32x4 vlds_u32x4;

struct Plan { const char* rows; const char* phase; int hx; char sx, tx; };
constexpr Plan PLANS[] = {
    {"1234tcab0s", "vwabc01234h", 2}, {"432cb1ts0a", "1vwacb4320h", 0}, {"123cb0ts4a", "vwabc01234h", 0}, {"103cb2ts4a", "vwabc01234h", 0},
    {"0234catb1s", "vwabc01234h", 2}, {"013cb2ta4s", "vwabc01234h", 0}, {"1234tcab0s", "vwabc01234h", 0}, {"123cb0st4a", "vwabc01234h", 0},
    {"1234tcab0s", "vwabc01234h", 2, 'c'}, {"432cb1ts0a", "1vwacb4320h", 0, 'b', 'c'}, {"123cb0ts4a", "vwabc01234h", 0, 'b', 'c'}, {"0123t4cabs", "vwabc01234h", 2},
    {"1234t0cabs", "vwabc01234h", 2}, {"1234tcab0s", "vwabc01234h", 2, 'c', 'a'},
    PLAN_EXTRA
};
constexpr int sym_pos(const char* s, char c) { for (int i = 0; s[i]; ++i) if (s[i] == c) return i; return -1; }
template <int N> __device__ __forceinline__ int place(const char* dst, const char (&src)[N], int x) {
    int y = 0;
#pragma unroll
    for (int i = 0; i < N - 1; ++i) y |= (x >> i & 1) << sym_pos(dst, src[i]);
    return y;
}
#define EXTARG
#define EXTVAL
struct Pace {
    unsigned step;
    int start;
    int offset;
    int bias;
    int rd;
    int pub;
};

template <int X, int OPT, int GATE, int TP, int SLP>
__global__ __launch_bounds__(576) __attribute__((aligned(4096))) void kv_sweep_kernel(
    const void*, const void*, const void*, const void*, const void*, const void*, const void*, const void*,
    const void*, const void*, const void*, void*, void*, Pace EXTARG)
{
    __shared__ __attribute__((aligned(16))) unsigned s_q[NH * DH];
    __shared__ __attribute__((aligned(16))) unsigned short s_sc[NH * 256];
    __shared__ __attribute__((aligned(16))) float s_w[NH * 4 * 20];
    __shared__ __attribute__((aligned(16))) float s_part[8 * NI * DH];
    __shared__ __attribute__((aligned(16))) float s_red[8 * 28];
    __shared__ __attribute__((aligned(16))) float s_f[NH * 8];
    __shared__ __attribute__((aligned(16))) unsigned s_done[8];
    __shared__ __attribute__((aligned(16))) unsigned s_junk[16];
    __shared__ __attribute__((aligned(16))) unsigned s_sync[8];
    __shared__ __attribute__((aligned(16))) unsigned s_act[16];

    unsigned probe = 0;
    constexpr int HOLD = GATE % 10;
    constexpr int DRY = OPT % 10 & 1, LS = 2, PAD = OPT % 10 / 2 & 1, PF = OPT / 10 % 10, RST = GATE / 1000 % 10 * 256, SLEAD = GATE / 10000 % 10, STA = 16, WIDE = OPT / 100 % 10, OST = 0, PRO = OPT / 1000;
    unsigned long long pc0, t0;
    u64x8 ka, kb;
    asm volatile("s_load_dwordx16 %3, %4, 0x40\n\ts_load_dwordx16 %2, %4, 0x0\n\ts_memrealtime %1\n\ts_getpc_b64 %0\n.Lkv_beg_%5_%6_%7_%8:"
                 : "=&s"(pc0), "=&s"(t0), "=&s"(ka), "=&s"(kb) : "s"(__builtin_amdgcn_kernarg_segment_ptr()), "i"(X), "i"(OPT), "i"(GATE), "i"(TP));
    __builtin_amdgcn_sched_barrier(0);
    const int tid = threadIdx.x;
    const int lane = tid & 63;
    int wave = __builtin_amdgcn_readfirstlane(tid >> 6);
    const int r = lane >> 4;
    const int c = lane & 15;
    const int bid = blockIdx.x;
    const int m = (bid & 7) * 32 + (bid >> 3);
    const int rw = wave & 3, hp = wave >> 2;
    constexpr Plan PL = PLANS[GATE / 1000000];
    constexpr unsigned SA = GSA, SB = GSB, QD = TP >> 6 ? 50 + (TP >> 6) * 50u : 0, PM = GATE / 100 % 10 & 3, BM = GATE / 100 % 10 >> 2;
    constexpr int HX = PL.hx, RS = 16 << sym_pos(PL.rows, 's') | (PL.sx ? 16 << sym_pos(PL.rows, PL.sx) : 0), RT = 16 << sym_pos(PL.rows, 't') | (PL.tx ? 16 << sym_pos(PL.rows, PL.tx) : 0);
    const int grow = place(PL.rows, "abc01234", bid) * 16;
    auto srow = [&](int t) { return (t & 1) * RS ^ (t >> 1) * RT; };
    const int row0 = grow + rw * 4;
    unsigned co = r * 384 + c * 8, so1 = r * 384 + (c < 8 ? c * 8 + 320 : c * 8 - 64), rb0 = (row0 ^ RT * hp) * 384u;
    asm volatile("s_waitcnt lgkmcnt(0)" : "+s"(t0), "+s"(ka), "+s"(kb), "+v"(co), "+v"(so1), "+s"(rb0), "+s"(wave));
    typedef const __attribute__((address_space(1))) char* gptr;
    void* const q = (void*)ka[0]; void* const qk = (void*)ka[1]; void* const qv = (void*)ka[4]; void* const q_cos = (void*)ka[7];
    const gptr ksc = (gptr)ka[2], kzp = (gptr)ka[3], vsc = (gptr)ka[5], vzp = (gptr)ka[6];
    void* const q_sin = (void*)kb[0]; void* const k_cos = (void*)kb[1]; void* const k_sin = (void*)kb[2]; void* const out = (void*)kb[4];
    unsigned char* const ws = (unsigned char*)(__attribute__((address_space(1))) unsigned char*)kb[3];
    const Pace pace = {(unsigned)kb[5], (int)(kb[5] >> 32), (int)kb[6], (int)(kb[6] >> 32), (int)kb[7], (int)(kb[7] >> 32)};
    const unsigned tstart = (unsigned)t0;
    const unsigned tsched = TP & 16 ? now32() : tstart;
    PSTAMP(69);
    const auto rw_ = __builtin_amdgcn_make_buffer_rsrc((void*)ws, 0, WS_BYTES, 0x00020000);
    const auto ro = __builtin_amdgcn_make_buffer_rsrc((void*)out, 0, NH * DH * 2, 0x00020000);

    auto merge = [&](const float* p, f32x4 f, float zs, unsigned w3) {
        f32x2 u[4][3], a[3];
#pragma unroll
        for (int w = 0; w < 4; ++w)
#pragma unroll
            for (int k = 0; k < 3; ++k) u[w][k] = *reinterpret_cast<const f32x2*>(p + w * NI * DH + 2 * k);
        asm volatile("" : "+v"(u[0][0]), "+v"(u[0][1]), "+v"(u[0][2]), "+v"(u[1][0]), "+v"(u[1][1]), "+v"(u[1][2]),
                          "+v"(u[2][0]), "+v"(u[2][1]), "+v"(u[2][2]), "+v"(u[3][0]), "+v"(u[3][1]), "+v"(u[3][2]));
#pragma unroll
        for (int k = 0; k < 3; ++k) a[k] = (f[0] * u[0][k] - zs + f[1] * u[1][k]) + (f[2] * u[2][k] + f[3] * u[3][k]);
        return u32x4{pk_bf16(a[0]), pk_bf16(a[1]), pk_bf16(a[2]), w3};
    };
    auto last_word = [&](unsigned tag, float ls, int c6) { return tag | ((__float_as_uint(ls) >> (16 * (c6 & 1))) & 0xffffu); };
    auto piece_off = [&](int hp, int c6) { return (hp * 8 + (c6 >> 2)) * UNITB + (c6 & 3) * 16; };
    auto publish = [&](u32x4 v, int i, unsigned vo) {
        __builtin_amdgcn_raw_buffer_store_b128(v, rw_, vo, WS_PART + i * GSTR + m * 64, STA);
        asm volatile("s_nop 1" :: "v"(v) : "memory");
    };

    if (__builtin_expect_with_probability(wave == 8, 1, 0.45)) {
        if (TP & 32) __builtin_amdgcn_s_setprio(3);
        HSTAMP(11);
        unsigned clen;
        asm volatile("s_mov_b32 %0, .Lkv_end_%1_%2_%3_%4-.Lkv_beg_%1_%2_%3_%4" : "=s"(clen) : "i"(X), "i"(OPT), "i"(GATE), "i"(TP));
        const auto rcode = __builtin_amdgcn_make_buffer_rsrc((void*)(pc0 & ~127ull), 0, clen, 0x00020000);
        const int jj = bid >> 3;
        auto warm_ld = [&](int n) {
            return __builtin_amdgcn_raw_buffer_load_b32(rcode, lane < n ? (jj * 9 + lane) * 64 : OOB, 0, 0);
        };
        auto warm_code = [&](int n) { if (n) { const unsigned x = warm_ld(n); asm volatile("" :: "v"(x)); } };
        constexpr int WN[10][4] = {{0, 0, 0, 0}, {64, 0, 0, 0}, {9, 0, 0, 0}, {64, 9, 9, 0}, {9, 9, 9, 0}, {18, 0, 0, 0}, {18, 18, 18, 0}, {9, 9, 0, 9}, {36, 0, 0, 0}, {9, 0, 9, 0}};
        constexpr int FREE = 0x3fff & ~(16 << sym_pos(PL.rows, 'a') | 16 << sym_pos(PL.rows, 'b') | 16 << sym_pos(PL.rows, 'c'));
        constexpr int F1 = 1 << (31 - __builtin_clz(FREE)), F2 = 1 << (31 - __builtin_clz(FREE - F1));
        const unsigned trow = (grow & ~FREE) + (jj & 1 ? FREE - F1 - F2 : 0) + (jj & 2 ? F2 : 0) + (jj & 4 ? F1 : 0) + (jj & 8 ? SKV : 0);
        int tp = jj >> 4;
        auto touch = [&]() {
            const unsigned long long a = (unsigned long long)(tp < NI ? qk : qv) + ((tp & 15) * 2 * SKV + trow) * ROWB;
            if (lane == 0)
                asm volatile("s_mov_b32 m0, %1\n\ts_nop 0\n\tglobal_load_lds_dword %0, off nt" :: "v"(a), "s"((unsigned)(unsigned long long)(__attribute__((address_space(3))) unsigned*)s_junk) : "memory");
            tp += 2;
            PIN();
        };
        const unsigned x0 = warm_ld(WN[PF][0]);
        if (TP & 4) touch(); else tp += 2;
        PIN();
        if (QD) while (now32() - tsched < QD) __builtin_amdgcn_s_sleep(1);
        u32x3 lc[2], ls[2];
        u32x4 qraw[12];
        {
            const auto rq = __builtin_amdgcn_make_buffer_rsrc((void*)q, 0, NH * DH * 2, 0x00020000);
            const auto rqc = __builtin_amdgcn_make_buffer_rsrc((void*)q_cos, 0, DH * 2, 0x00020000);
            const auto rqs = __builtin_amdgcn_make_buffer_rsrc((void*)q_sin, 0, DH * 2, 0x00020000);
#pragma unroll
            for (int k = 0; k < 12; ++k) qraw[k] = __builtin_amdgcn_raw_buffer_load_b128(rq, lane * 16 + (k & 3) * 1024, (k >> 2) * 4096, 0);
#pragma unroll
            for (int k = 0; k < 2; ++k) {
                lc[k] = __builtin_amdgcn_raw_buffer_load_b96(rqc, c * 12 + k * 192, 0, 0);
                ls[k] = __builtin_amdgcn_raw_buffer_load_b96(rqs, c * 12 + k * 192, 0, 0);
            }
            PIN();
        }
        const unsigned tag = ((unsigned)__builtin_amdgcn_raw_buffer_load_b16(rw_, (lane & 3) * 2, WS_EPOCH, 16) + 1u) << 16;
        PIN();
        HSTAMP(2);
        unsigned* const s_raw = reinterpret_cast<unsigned*>(s_part) + NI * DH / 2;
#pragma unroll
        for (int k = 0; k < 12; ++k) *reinterpret_cast<u32x4*>(s_raw + k / 6 * (NI * DH) + k % 6 * 256 + lane * 4) = qraw[k];
        f32x2 fc[2][3], fs[2][3];
#pragma unroll
        for (int k = 0; k < 2; ++k)
#pragma unroll
            for (int u = 0; u < 3; ++u) { fc[k][u] = f32x2{bflo(lc[k][u]), bfhi(lc[k][u])}; fs[k][u] = f32x2{bflo(ls[k][u]), bfhi(ls[k][u])}; }
        PIN();
        HSTAMP(3);
#pragma nounroll
        for (int it = 0; it < 4; ++it) {
            unsigned qa[2][3], qb[2][3];
#pragma unroll
            for (int g = 0; g < 2; ++g)
#pragma unroll
                for (int u = 0; u < 3; ++u) {
                    const unsigned* src = s_raw + g * (NI * DH) + (it * 4 + r) * HALF + 3 * c;
                    qa[g][u] = src[u]; qb[g][u] = src[HALF / 2 + u];
                }
#pragma unroll
            for (int g = 0; g < 2; ++g)
#pragma unroll
                for (int u = 0; u < 3; ++u) {
                    const f32x2 a = {bflo(qa[g][u]), bfhi(qa[g][u])}, b = {bflo(qb[g][u]), bfhi(qb[g][u])};
                    const f32x2 o0 = a * fc[0][u] - b * fs[0][u], o1 = b * fc[1][u] + a * fs[1][u];
                    const unsigned l0 = pk_bf16(f32x2{o0[0], o1[0]}), l1 = pk_bf16(f32x2{o0[1], o1[1]});
                    unsigned* dst = s_q + (16 * g + 4 * it + r) * DH + 6 * c + 2 * u;
                    *reinterpret_cast<u32x2*>(dst) = u32x2{l0, l1};
                    *reinterpret_cast<u32x2*>(dst + HALF) = u32x2{__builtin_amdgcn_alignbit(l0, l0, 16) ^ 0x80000000u, __builtin_amdgcn_alignbit(l1, l1, 16) ^ 0x80000000u};
                }
        }
        HSTAMP(1);
        asm volatile("" :: "v"(x0));
        if (TP & 32) __builtin_amdgcn_s_setprio(0);
        unsigned origin = tsched << 8;
        if (lane < 8) { s_done[lane] = 0; s_sync[lane] = lane < 3 ? origin : lane < 4 ? 0 : tag; }
        PIN();
        BARRIER();
        const int mh = lane >> 5, mc = lane & 31;
        const unsigned rtag = __builtin_amdgcn_readlane(tag, 0) | __builtin_amdgcn_readlane(tag, 1) >> 16;
        constexpr int RPX = 2;
        constexpr int gp[] = {CTRL};
        constexpr int KS = gp[0], SR = gp[1], IB2 = gp[2], EHI = gp[3], IB = gp[4], FR = gp[5], RSH = gp[6], UP = gp[8], DN = gp[9], SLIP = gp[10], FF = gp[11], DMAX = gp[12], ELO = gp[13];
        constexpr unsigned RDLY = gp[7];
        const int rj = ((bid >> 3) + 5 * (bid & 7)) & 31;
        const int rid = rj % (32 / RPX) == 0 ? rj / (32 / RPX) * 8 + (bid & 7) : -1;
        unsigned wround = now32() >> RSH, rround = wround, tick = 0, target = origin;
        bool warmed = false, synced = false;
        int drift = 0;
        const unsigned round0 = rround;
        int i = 0;
        auto kept_now = [&](unsigned now) {
            const u32x2 a = *(vlds_u32x2*)(s_act + 2 * (lane & 7));
            const int late = max((int)(a[0] - origin), (int)((now << 8) - a[1] - origin));
            return origin + (__builtin_amdgcn_readfirstlane(row16_sum(late)) >> 4);
        };
        auto steer = [&](unsigned now) {
            if ((now >> 5) != tick) {
                tick = now >> 5;
                target += drift;
                origin += max(-DN, min(UP + drift, (int)(target - origin)));
                if (SLIP) { const int l2 = (int)(kept_now(now) - origin) - SLIP; if (l2 > 0) origin += l2; }
                if (HOLD && !synced) target = origin - (HOLD - 1) * 82;
                if (lane == 0) *(vlds_u32*)(s_sync + 2) = origin;
                PIN();
            }
        };
        auto finished = [&]() {
            const u32x4 a = *(vlds_u32x4*)s_done, b = *(vlds_u32x4*)(s_done + 4);
            return __builtin_amdgcn_readfirstlane(min(min(min(a[0], a[1]), min(a[2], a[3])), min(min(b[0], b[1]), min(b[2], b[3]))));
        };
        while (i < NI - 2) {
            const unsigned now = now32();
            if ((now >> RSH) != wround) {
                wround = now >> RSH;
                if (rid >= 0 && (now & ((1u << RSH) - 1)) < 48)
                    __builtin_amdgcn_raw_buffer_store_b64(u32x2{kept_now(now), rtag + wround}, rw_, lane == 0 ? rid * 8 : OOB, WS_REP + (wround & 255) * 512, 16);
                PIN();
                continue;
            }
            if ((int)(((now - RDLY) >> RSH) - rround) > 0) {
                rround = (now - RDLY) >> RSH;
                const u32x2 x = __builtin_amdgcn_raw_buffer_load_b64(rw_, c * 8, WS_REP + (rround & 255) * 512, 16);
                const int d = (int)(x[0] - origin);
                const bool ok = x[1] == rtag + rround && d < (32 << 16) && d > -(32 << 16);
                const int n = __builtin_popcountll(__builtin_amdgcn_ballot_w64(ok));
                if (n >= 48) {
                    const unsigned nt = origin + __builtin_amdgcn_readfirstlane((int)(row16_allsum(ok ? (float)d : 0.f) * 4.f / (float)n)) - pace.bias;
                    if (KS && synced && rround - round0 > SR) drift = max(0, min(drift + ((max(ELO, min((int)(nt - target) + FF * drift, EHI)) - (rround - round0 < FR ? IB : IB2)) >> (3 + KS)), DMAX));
                    target = nt + FF * drift;
                    synced = true;
                    const int lost = min((int)(kept_now(now32()) - origin) - pace.bias, (int)(target - origin));
                    if (lost > 0) origin += lost;
                }
                PIN();
                continue;
            }
            steer(now);
            if ((TP & 1) && tp < 2 * NI && 4 * (int)((now << 8) - origin) > (16 * tp + (jj & 15) - (TP & 8 ? 40 : 24)) * (int)pace.step) { touch(); continue; }
            if (WN[PF][1] && !warmed && (int)((now << 8) - origin) > 60 * (int)pace.step) { warm_code(WN[PF][1]); warmed = true; continue; }
            if (finished() <= (unsigned)i) { __builtin_amdgcn_s_sleep(2); continue; }
            PIN();
            if (i == 0) {
                f32x4 ml[4], f;
#pragma unroll
                for (int w = 0; w < 4; ++w) ml[w] = *reinterpret_cast<const f32x4*>(s_w + (mc * 4 + w) * 20 + 16);
                const float M = fmaxf(fmaxf(ml[0][0], ml[1][0]), fmaxf(ml[2][0], ml[3][0]));
                float L = 0.f, zs = 0.f;
#pragma unroll
                for (int w = 0; w < 4; ++w) { f[w] = __builtin_amdgcn_exp2f(ml[w][0] - M); L += f[w] * ml[w][1]; zs += f[w] * ml[w][2]; }
                const float inv = 1.f / L;
                *reinterpret_cast<f32x4*>(s_f + mc * 8) = f * inv;
                *reinterpret_cast<f32x2*>(s_f + mc * 8 + 4) = f32x2{zs * inv, M + __builtin_amdgcn_logf(L)};
                PIN();
                if (lane == 0) *(vlds_u32*)(s_sync + 3) = NI + 1;
                PIN();
            }
            if (i == NI - 3) warm_code(WN[PF][2]);
            const f32x2 zl = *reinterpret_cast<const f32x2*>(s_f + (2 * i + mh) * 8 + 4);
            const u32x4 v = merge(s_part + (mh * 4 * NI + i) * DH + mc * 6, *reinterpret_cast<const f32x4*>(s_f + (2 * i + mh) * 8), zl[0], last_word(tag, zl[1], mc));
            PIN();
            *reinterpret_cast<u32x4*>(s_part + (mh * 4 * NI + i) * DH + mc * 4) = v;
            PIN();
            ++i;
        }
        u32x4 v[NI - 1];
#pragma unroll
        for (int i = 0; i < NI - 2; ++i) v[i] = *reinterpret_cast<const u32x4*>(s_part + (mh * 4 * NI + i) * DH + mc * 4);
        f32x4 f = *reinterpret_cast<const f32x4*>(s_f + (2 * (NI - 2) + mh) * 8);
        f32x2 zl = *reinterpret_cast<const f32x2*>(s_f + (2 * (NI - 2) + mh) * 8 + 4);
        PIN();
#pragma unroll
        for (int i = 0; i < NI - 2; ++i) asm volatile("" : "+v"(v[i]));
        asm volatile("" : "+v"(f), "+v"(zl));
        v[NI - 2] = v[0];
        bool have = false, sent = false;
        while (!(have && sent)) {
            const unsigned now = now32();
            if (!have) {
                steer(now);
                if (finished() > NI - 2) {
                    PIN();
                    v[NI - 2] = merge(s_part + (mh * 4 * NI + NI - 2) * DH + mc * 6, f, zl[0], last_word(tag, zl[1], mc));
                    have = true;
                    warm_code(WN[PF][3]);
                    HSTAMP(5);
                }
            }
            if (!sent && (int)((now << 8) - origin) >= pace.pub) {
                HSTAMP(6);
#pragma unroll
                for (int i = 0; i < NI - 2; ++i) publish(v[i], i, piece_off(mh, mc));
                sent = true;
            }
            if (!(have && sent)) __builtin_amdgcn_s_sleep(1);
            PIN();
        }
        publish(v[NI - 2], NI - 2, piece_off(mh, mc));
        HSTAMP(7);
        asm volatile(".Lkv_end_%0_%1_%2_%3:" :: "i"(X), "i"(OPT), "i"(GATE), "i"(TP));
        return;
    }

    const int ph = place(PL.phase, "abc01234", bid) + place(PL.phase, "vw", rw) + place(PL.phase, "h", hp);
    unsigned off = pace.start + ((pace.step * ph) >> 11);
    unsigned origin = tsched << 8, kept = 0;
    unsigned step = pace.step;
    auto pace_wait = [&]() {
        const unsigned tgt = __builtin_amdgcn_readfirstlane(origin) + off;
        unsigned now = now32() << 8;
        kept = ((int)(now - tgt) > 0 ? now : tgt) - off;
        while ((int)(tgt - now) > 0) { if (SLP) __builtin_amdgcn_s_sleep(SLP); now = now32() << 8; }
        off += step;
    };
    auto report = [&]() {
        if (lane == 0) *(vlds_u32x2*)(s_act + 2 * wave) = u32x2{kept, off};
        origin = *(vlds_u32*)(s_sync + 2);
        PIN();
    };
#define TRV (kept - (tstart << 8))

    auto pick = [](int n, gptr p0, gptr p1, gptr p2, gptr p3) { return (n & 2) ? ((n & 1) ? p3 : p2) : ((n & 1) ? p1 : p0); };
    auto dma = [](gptr a, unsigned dst) { asm volatile("s_mov_b32 m0, %1\n\ts_nop 0\n\tglobal_load_lds_dwordx4 %0, off nt" :: "v"(a), "s"(__builtin_amdgcn_readfirstlane(dst)) : "memory"); };
    const int w8 = rw + 4 * hp;
    const unsigned sdst = (unsigned)(unsigned long long)(__attribute__((address_space(3))) unsigned short*)s_sc + w8 * 1024;
    auto gather = [&](unsigned slot) {
        const gptr a = pick(lane >> 1, ksc, kzp, vsc, vzp) + ((w8 * 2 + (lane >> 5)) * SKV + (grow ^ srow(lane >> 3 & 3)) + (lane & 1) * 8) * 2;
        if ((SA >> 4 * w8 & 15) == slot) dma(a, sdst);
        if ((SB >> 4 * w8 & 15) == slot) dma(a + 32 * SKV, sdst + 8192);
        PIN();
    };
    auto pre_sc = [&]() {
        const gptr a = pick(grow >> 4, ksc, kzp, vsc, vzp) + ((w8 * 2 + (lane >> 5)) * SKV + (grow & ~63 ^ srow(lane >> 3 & 3))) * 2 + (lane & 7) * 16;
        const unsigned junk = (unsigned)(unsigned long long)(__attribute__((address_space(3))) float*)s_part + (3 * NI * DH - 256) * 4;
        if (PM >= 2) dma(a, junk);
        if (PM >= 1) dma(a + 32 * SKV, junk);
        PIN();
    };
    const auto rc = __builtin_amdgcn_make_buffer_rsrc((void*)k_cos, 0, CSB, 0x00020000);
    const auto rs = __builtin_amdgcn_make_buffer_rsrc((void*)k_sin, 0, CSB, 0x00020000);
    u32x2 raw[2][6];
    auto load_cs = [&](int t) {
        const unsigned rb = t == 0 ? rb0 : (row0 ^ RT * hp ^ RS) * 384u;
        raw[t][0] = __builtin_amdgcn_raw_buffer_load_b64(rc, co, rb, 2);
        raw[t][1] = __builtin_amdgcn_raw_buffer_load_b64(rc, co + 128, rb, 2);
        raw[t][2] = __builtin_amdgcn_raw_buffer_load_b64(rc, co + 256, rb, 2);
        raw[t][3] = __builtin_amdgcn_raw_buffer_load_b64(rs, co + 192, rb, 2);
        raw[t][4] = __builtin_amdgcn_raw_buffer_load_b64(rs, so1, rb, 2);
        raw[t][5] = __builtin_amdgcn_raw_buffer_load_b64(rs, co + 64, rb, 2);
        PIN();
    };
    u32x4 tab[4][3];
    auto pack_cs = [&](int t) {
#pragma unroll
        for (int j = 0; j < 3; ++j) {
#pragma unroll
            for (int k = 0; k < 2; ++k) {
                tab[t][j][2 * k] = __builtin_amdgcn_perm(raw[t][3 + j][k], raw[t][j][k], 0x05040100);
                tab[t][j][2 * k + 1] = __builtin_amdgcn_perm(raw[t][3 + j][k], raw[t][j][k], 0x07060302);
            }
            *reinterpret_cast<u32x4*>(s_part + (wave ^ 4) * (NI * DH) + ((t * 3 + j) * 64 + lane) * 4) = tab[t][j];
        }
        PIN();
    };
    const auto rk = __builtin_amdgcn_make_buffer_rsrc((void*)qk, 0, KVB, 0x00020000);
    const auto rv = __builtin_amdgcn_make_buffer_rsrc((void*)qv, 0, KVB, 0x00020000);
    const unsigned voff = r * ROWB + c * 16;
    const unsigned woff = hp * HEADB;
    auto toff = [&](int t) { return (unsigned)(row0 ^ srow(t ^ (HX * hp))) * ROWB; };
    u32x4 ring[4][3];
    auto ld = [&](int slot, int j, __amdgpu_buffer_rsrc_t rr, unsigned off) {
        ring[slot][j] = __builtin_amdgcn_raw_buffer_load_b128(rr, voff + j * 256, off, 2);
        PIN();
    };
    auto ld_tile = [&](int slot, __amdgpu_buffer_rsrc_t rr, unsigned off) { ld(slot, 0, rr, off); ld(slot, 1, rr, off); ld(slot, 2, rr, off); };
    unsigned post = 0;
    auto enter = [&](int i) {
        if (WIDE && (i == 9 || i == 10)) {
            const unsigned hs = pace.step >> 1, z = (unsigned)pace.offset >> 16;
            off += i == 10 ? post : hp * 3 * hs;
            step = i == 10 ? pace.step : hs + hp * z;
            post = __builtin_amdgcn_readfirstlane(i == 10 ? 0 : hp ? hs : 4 * (hs + z));
            if (lane == 0) *(vlds_u32x2*)(s_act + 2 * wave) = u32x2{kept, off};
            PIN();
        }
    };
    auto refill = [&](int slot, int j, __amdgpu_buffer_rsrc_t rr, unsigned goff) {
        if (j == 0) pace_wait();
        if (!WIDE) VMCNT(2);
        else if (j == 0) asm volatile("s_cmp_lg_u32 %0, 0\n\ts_waitcnt vmcnt(3)\n\ts_cbranch_scc1 1\n\ts_waitcnt vmcnt(2)" :: "s"(post) : "scc", "memory");
        else asm volatile("s_cmp_lg_u32 %0, 0\n\ts_cbranch_scc1 1\n\ts_waitcnt vmcnt(2)" :: "s"(post) : "scc", "memory");
        ld(slot, j, rr, goff);
        if (j == 0) { const unsigned t = (now32() << 8) - 1024 - (off - step); if ((int)(t - kept) > 0) kept = t; }
        if (j == 2) report();
    };
    auto k0 = [&](int t) { pace_wait(); if (t < 3) gather(3 + t); ld_tile(t, rk, woff + toff(t)); };
    load_cs(0); gather(0);
    pace_wait(); gather(1); load_cs(1); STAMP(1);
    pack_cs(0);
    pace_wait(); pre_sc(); gather(2); off += (pace.offset & 0xffff) - step;
    if (PRO == 0) { k0(0); pack_cs(1); } else { pack_cs(1); k0(0); }
    k0(1);
    VMCNT(3); k0(2);
    STAMP(2);
    VMCNT(3);
    if (BM && hp) { if (lane == 0) *(vlds_u32x2*)(s_act + 2 * wave) = u32x2{kept, off}; BARRIER(); }
    k0(3);
    if (lane == 0) *(vlds_u32x2*)(s_act + 2 * wave) = u32x2{kept, off};
    if (!(BM && hp)) BARRIER();
    origin = *(vlds_u32*)(s_sync + 2);
    STAMP(3);

    u32x4 qp[3];
    auto load_q = [&](int h, int j) { qp[j] = *reinterpret_cast<const u32x4*>(s_q + h * DH + 64 * j + 4 * c); };
    load_q(hp, 0); load_q(hp, 1); load_q(hp, 2);
#pragma unroll
    for (int t = 0; t < 2; ++t)
#pragma unroll
        for (int j = 0; j < 3; ++j) tab[2 + t][j] = *reinterpret_cast<const u32x4*>(s_part + wave * (NI * DH) + ((t * 3 + j) * 64 + lane) * 4);
    if (HX == 0 && hp) {
#pragma unroll
        for (int t = 0; t < 2; ++t)
#pragma unroll
            for (int j = 0; j < 3; ++j)
#pragma unroll
                for (int k = 0; k < 4; ++k) asm volatile("v_swap_b32 %0, %1" : "+v"(tab[t][j][k]), "+v"(tab[2 + t][j][k]));
    }

    for (int i = 0; i < NI; ++i) {
        const bool last = i + 1 == NI;
        enter(i);
        const int h = 2 * i + hp;
        const unsigned hnext = (i + 1) * (2 * HEADB) + woff;
        const auto rn = __builtin_amdgcn_make_buffer_rsrc((void*)(last ? qv : qk), 0, KVB, 0x00020000);
        float* scp = s_w + (h * 4 + rw) * 20;
        f32x4 sc[4];
#pragma unroll
        for (int t = 0; t < 4; ++t)
#pragma unroll
            for (int a = 0; a < 4; ++a) sc[t][a] = bflo(s_sc[h * 256 + (t ^ (HX * hp)) * 64 + a * 16 + rw * 4 + r]);
        float s[4];
#pragma unroll
        for (int t = 0; t < 4; ++t) {
            f32x2 dot, wsum;
#pragma unroll
            for (int j = 0; j < 3; ++j) {
                f32x2 w01, w23, k01, k23;
                dot_cvt(qp[j], tab[t][j], ring[t][j], w01, w23, k01, k23);
                if (j == 0) { wsum = w01 + w23; dot = k01 * w01; } else { wsum += w01; wsum += w23; dot += k01 * w01; }
                dot += k23 * w23;
                asm volatile("" : "+v"(dot), "+v"(wsum));
                PIN();
                if (t == 3) load_q(last ? h : h + 2, j);
                refill(t, j, rn, (last ? woff : hnext) + toff(t));
                if (t == 0 && j == 0) TRACE(i);
            }
            const float a = (dot[0] + dot[1]) - sc[t][1] * (wsum[0] + wsum[1]);
            s[t] = row16_allsum(a) * (sc[t][0] * SCALE_LOG2);
        }
        const float mx = xrow_max(fmaxf(fmaxf(s[0], s[1]), fmaxf(s[2], s[3])));
        float l = 0.f, z = 0.f;
#pragma unroll
        for (int t = 0; t < 4; ++t) {
            const float p = __builtin_amdgcn_exp2f(s[t] - mx);
            l += p;
            s[t] = p * sc[t][2];
            z += s[t] * sc[t][3];
        }
        l = xrow_sum(l);
        z = xrow_sum(z);
        if (c == 0) {
            *reinterpret_cast<f32x4*>(scp + r * 4) = f32x4{s[0], s[1], s[2], s[3]};
            *reinterpret_cast<f32x4*>(scp + 16) = f32x4{mx, l, z, 0.f};
        }
        PIN();
    }
    STAMP(4);

    const float* pwp = s_w + (hp * 4 + rw) * 20 + r * 4;
    f32x4 pw = *reinterpret_cast<const f32x4*>(pwp);
    unsigned tag, lw, lo;
    f32x4 lf;
    f32x2 lz;
    unsigned lp = (hp * 4 * NI + NI - 1) * DH + (lane & 31) * 6;
    f32x2 acc[3][2];
    auto v_acc = [&](int t, int j) {
        const f32x2 p2 = {pw[t], pw[t]};
        const u32x4 vv = ring[t][j];
        const f32x2 v01 = {(float)(int)vv[0], (float)(int)vv[1]}, v23 = {(float)(int)vv[2], (float)(int)vv[3]};
        if (t == 0) { acc[j][0] = p2 * v01; acc[j][1] = p2 * v23; }
        else { acc[j][0] += p2 * v01; acc[j][1] += p2 * v23; }
        asm volatile("" : "+v"(acc[j][0]), "+v"(acc[j][1]));
        PIN();
    };
    auto v_fold = [&](int i, int j) {
        const float x = swap32_add(acc[j][0][0], acc[j][1][0]);
        const float y = swap32_add(acc[j][0][1], acc[j][1][1]);
        s_part[(wave * NI + i) * DH + 64 * j + 4 * c + r] = swap16_add(x, y);
    };
    auto v_done = [&](int i) {
        PIN();
        if (lane == 0) *(vlds_u32*)(s_done + wave) = i + 1;
        PIN();
    };
#pragma unroll
    for (int t = 0; t < LS; ++t)
#pragma unroll
        for (int j = 0; j < 3; ++j) v_acc(t, j);
    for (int i = 0; i < NI - 1; ++i) {
        const unsigned hnext = (i + 1) * (2 * HEADB) + woff;
        enter(i);
#pragma unroll
        for (int k = 0; k < 4; ++k) {
            const int t = (k + LS) & 3;
#pragma unroll
            for (int j = 0; j < 3; ++j) {
                v_acc(t, j);
                if (t == 3 && j == 2) pw = *reinterpret_cast<const f32x4*>(pwp + (i + 1) * 160);
                refill(k, j, rv, hnext + toff(k));
                if (k == 0 && j == 0) TRACE(16 + i);
            }
            if (t == 3) { v_fold(i, 0); v_fold(i, 1); v_fold(i, 2); v_done(i); }
        }
    }
    if (PAD) asm volatile("s_branch .Lkv_tail_%=\n\t.p2align 12\n.Lkv_tail_%=:" ::: "memory");
    STAMP(5);
    TRACE(31);
    while (__builtin_amdgcn_readfirstlane(*(vlds_u32*)(s_sync + 3)) <= NI) __builtin_amdgcn_s_sleep(1);
    PIN();
    tag = s_sync[4 + (lane & 3)];
    lf = *reinterpret_cast<const f32x4*>(s_f + (2 * (NI - 1) + hp) * 8);
    lz = *reinterpret_cast<const f32x2*>(s_f + (2 * (NI - 1) + hp) * 8 + 4);
    origin = *(vlds_u32*)(s_sync + 2);
    lw = last_word(tag, lz[1], lane & 31);
    lo = lane < 32 ? piece_off(hp, lane & 31) : OOB;
    asm volatile("" : "+v"(tag), "+v"(lf), "+v"(lz), "+v"(lw), "+v"(lo), "+v"(lp), "+v"(origin));
    PIN();
#pragma unroll
    for (int t = LS; t < 4; ++t)
#pragma unroll
        for (int j = 0; j < 3; ++j) { v_acc(t, j); if (t == 3) { v_fold(NI - 1, j); PIN(); } }
    v_done(NI - 1);
    STAMP(6);
    {
        const u32x4 dn = *(vlds_u32x4*)(s_done + 4 * hp);
        PIN();
        const u32x4 v = merge(s_part + lp, lf, lz[0], lw);
        if (__builtin_amdgcn_readfirstlane(dn[0] & dn[1] & dn[2] & dn[3]) == NI) publish(v, NI - 1, lo);
        PIN();
    }
    STAMP(7);
    {
        unsigned inbox = WS_PART + (bid >> 4) * GSTR + (bid & 15) * UNITB, obase = bid * 48;
        unsigned xo = tid * 16, yo = tid * 16 + 8192, sel = lane & 1 ? 0x01000504u : 0x05040100u;
        unsigned bo = lane < 16 ? wave * 1024 + (lane & 7) * 128 + (lane >> 3) * 8192 : OOB;
        typedef __attribute__((address_space(3))) float lds_f;
        lds_f* wo = (lds_f*)s_red + wave * 28 + (r < 3 ? c * 6 + r * 2 : 24);
        lds_f* fo = (lds_f*)s_red + (lane & 7) * 28 + 24;
        lds_f* go = (lds_f*)s_red + (lane & 7) * 28 + min(lane >> 3, 5) * 4;
        vlds_u32* dw = (vlds_u32*)(s_done + wave);
        vlds_u32* dr = (vlds_u32*)(s_done + (lane & 7));
        unsigned oo = (lane & 7) == 0 && lane < 48 ? lane : OOB, eo = bid == 0 && lane < 4 ? lane * 2 : OOB;
        asm volatile("" : "+s"(inbox), "+s"(obase), "+v"(xo), "+v"(yo), "+v"(sel), "+v"(bo), "+v"(wo), "+v"(fo), "+v"(go), "+v"(oo), "+v"(eo), "+v"(dw), "+v"(dr));
        const unsigned o = __builtin_amdgcn_readfirstlane(origin) - (15 - (bid >> 4)) * RST;
        while ((int)((now32() << 8) - o) < pace.rd) __builtin_amdgcn_s_sleep(1);
        PIN();
        STAMP(8);
        u32x4 x, y;
        do {
            x = __builtin_amdgcn_raw_buffer_load_b128(rw_, xo, inbox, 16);
            y = __builtin_amdgcn_raw_buffer_load_b128(rw_, yo, inbox, 16);
            const unsigned age = now32() - tstart;
            PIN();
            if (age > SPIN_LIMIT) break;
        } while (__builtin_amdgcn_ballot_w64(((x[3] ^ tag) | (y[3] ^ tag)) > 0xffffu) != 0);
        if (DRY) { __builtin_amdgcn_raw_buffer_store_b32(0u, rw_, bo, inbox, 16); PIN(); }
        STAMP(9);
        const float lx = __uint_as_float(__builtin_amdgcn_perm(__builtin_amdgcn_mov_dpp(x[3], 0xb1, 0xf, 0xf, true), x[3], sel));
        const float ly = __uint_as_float(__builtin_amdgcn_perm(__builtin_amdgcn_mov_dpp(y[3], 0xb1, 0xf, 0xf, true), y[3], sel));
        float M = fmaxf(lx, ly);
        M = fmaxf(M, dpp<0x128>(M));
        M = fmaxf(M, dpp<0x124>(M));
        M = xrow_max(M);
        const float w0 = __builtin_amdgcn_exp2f(lx - M), w1 = __builtin_amdgcn_exp2f(ly - M);
        float a[8];
#pragma unroll
        for (int k = 0; k < 3; ++k) {
            a[2 * k] = w0 * bflo(x[k]) + w1 * bflo(y[k]);
            a[2 * k + 1] = w0 * bfhi(x[k]) + w1 * bfhi(y[k]);
        }
        a[6] = a[7] = w0 + w1;
        float b[4], d[2];
#pragma unroll
        for (int k = 0; k < 4; ++k) b[k] = swap32_add(a[k], a[4 + k]);
#pragma unroll
        for (int k = 0; k < 2; ++k) d[k] = swap16_add(b[k], b[2 + k]);
#pragma unroll
        for (int k = 0; k < 2; ++k) { d[k] += dpp<0x128>(d[k]); d[k] += dpp<0x124>(d[k]); }
        if (c < 4) {
            *(__attribute__((address_space(3))) f32x2*)wo = f32x2{d[0], r < 3 ? d[1] : M};
            PIN();
            *dw = 3 * NI;
        }
        PIN();
        STAMP(10);
        const unsigned fin = *dr;
        PIN();
        f32x2 sm = *(__attribute__((address_space(3))) f32x2*)fo;
        f32x4 ov = *(__attribute__((address_space(3))) f32x4*)go;
        asm volatile("" : "+v"(sm), "+v"(ov));
        if (__builtin_amdgcn_ballot_w64(fin != 3 * NI) == 0) {
            float MM = fmaxf(sm[1], dpp<0xb1>(sm[1]));
            MM = fmaxf(MM, dpp<0x4e>(MM));
            MM = fmaxf(MM, dpp<0x141>(MM));
            const float ff = __builtin_amdgcn_exp2f(sm[1] - MM);
            float e[5] = {ff * ov[0], ff * ov[1], ff * ov[2], ff * ov[3], ff * sm[0]};
#pragma unroll
            for (int k = 0; k < 5; ++k) e[k] += dpp<0xb1>(e[k]);
#pragma unroll
            for (int k = 0; k < 5; ++k) e[k] += dpp<0x4e>(e[k]);
#pragma unroll
            for (int k = 0; k < 5; ++k) e[k] += dpp<0x141>(e[k]);
            const float inv = __builtin_amdgcn_rcpf(e[4]);
            __builtin_amdgcn_raw_buffer_store_b64(u32x2{pk_bf16(f32x2{e[0] * inv, e[1] * inv}), pk_bf16(f32x2{e[2] * inv, e[3] * inv})}, ro, oo, obase, OST);
            if (bid == 0) __builtin_amdgcn_raw_buffer_store_b16((unsigned short)(tag >> 16), rw_, eo, WS_EPOCH, 16);
        }
    }
    if (wave == PWAVE) {
        if (PIDX == 90) probe = __builtin_amdgcn_readfirstlane(s_sync[0]);
        if (PIDX == 91 || PIDX == 92) probe = tstart + ((int)(__builtin_amdgcn_readfirstlane(s_sync[PIDX - 90]) - (tstart << 8)) >> 8);
        if (PIDX == 100) { asm volatile("s_waitcnt vmcnt(0)" ::: "memory"); probe = now32(); }
        const unsigned ptag = __builtin_amdgcn_readlane(tag, 0) | __builtin_amdgcn_readlane(tag, 1) >> 16;
        __builtin_amdgcn_raw_buffer_store_b128(u32x4{tstart, probe, ptag, 0}, rw_, lane == 0 ? bid * 16 : OOB, WS_PROBE, 16);
        asm volatile("s_nop 1" ::: "memory");
        if (bid == 100) {
            while ((int)(now32() - tstart) < 12500) __builtin_amdgcn_s_sleep(1);
            u32x4 p[4];
            for (;;) {
                bool ok = true;
                for (int k = 0; k < 4; ++k) { p[k] = __builtin_amdgcn_raw_buffer_load_b128(rw_, lane * 16 + k * 1024, WS_PROBE, 16); ok = ok && p[k][2] == ptag; }
                if (__builtin_amdgcn_ballot_w64(!ok) == 0 || now32() - tstart > SPIN_LIMIT) break;
                PIN();
            }
            float mn = 1e9f, mx = -1e9f, av = 0.f;
            for (int k = 0; k < 4; ++k) { mn = fminf(mn, (float)(int)(p[k][0] - tstart)); const float v = (float)(int)(p[k][1] - tstart); mx = fmaxf(mx, v); av += v; }
            mn = -xrow_max(row16_allmax(-mn)); mx = xrow_max(row16_allmax(mx)); av = xrow_sum(row16_allsum(av)) / 256.f;
            float val = (PMAX & 1 ? mx : av) - (PMAX & 2 ? (float)((int)(s_sync[2] - (tstart << 8)) >> 8) : mn) - PSUB;
            if (PIDX == 99) val = 0.f;
            val = fminf(fmaxf(val, 0.f), 3000.f);
            const unsigned endt = tstart + __builtin_amdgcn_readfirstlane((int)(mn + 14000.f + 5.f * val));
            while ((int)(endt - now32()) > 0) __builtin_amdgcn_s_sleep(1);
        }
    }
}

#define VAR_XS(p) ((p) / 1000 % 2)
template <int X, int PRO, int GATE, int STAG>
static void launch_cfg(void* const* d_in, void* d_out, void* d_ws, hipStream_t stream, float t_us, float start_us, float off_us, float debt_us, float adj_pct, float pub_us) {
    Pace pace;
    pace.step = (unsigned)(t_us * 25600.f / 128.f);
    pace.start = (int)(start_us * 25600.f);
    const float zone = PRO / 100 % 10 ? (int)off_us * 0.01f : 0.f; off_us -= (int)off_us;
    pace.offset = (int)(off_us * 25600.f) | (int)(zone * 6400.f) << 16; off_us += 2 * zone;
    pace.bias = (int)(debt_us * 25600.f);
    pace.rd = (int)((start_us + off_us + t_us * ((129.f + VAR_XS(PRO)) / 128.f) + adj_pct) * 25600.f);
    pace.pub = (int)((start_us + off_us + t_us * ((129.f + VAR_XS(PRO)) / 128.f) + pub_us) * 25600.f);
    kv_sweep_kernel<X, PRO, GATE, STAG, 1><<<NWG, 576, 0, stream>>>(d_in[0], d_in[1], d_in[2], d_in[3], d_in[4], d_in[5], d_in[6], d_in[7],
        d_in[8], d_in[9], d_in[10], d_ws, d_out, pace EXTVAL);
}

#define CFG 0, 123, 5002501, 112
#define PRM 100.75f, 0.5f, 10.8f, 0.055f, 1.6f, 0.6f
extern "C" void kernel_launch(void* const* d_in, const int* in_sizes, int n_in,
                              void* d_out, int out_size, void* d_ws, size_t ws_size,
                              hipStream_t stream) {
    launch_cfg<CFG>(d_in, d_out, d_ws, stream, PRM);
}
```

```cpp
#include <hip/hip_runtime.h>
#include <cstdint>


using u32x4 = __attribute__((ext_vector_type(4))) unsigned;
using u32x3 = __attribute__((ext_vector_type(3))) unsigned;
using u32x2 = __attribute__((ext_vector_type(2))) unsigned;
using u64x8 = __attribute__((ext_vector_type(8))) unsigned long long;
using f32x2 = __attribute__((ext_vector_type(2))) float;
using f32x4 = __attribute__((ext_vector_type(4))) float;
using bf16x2 = __attribute__((ext_vector_type(2))) __bf16;

constexpr int DH = 192;
constexpr int HALF = 96;
constexpr int NH = 32;
constexpr int SKV = 16384;
constexpr int NWG = 256;
constexpr int NI = 16;
constexpr int NB = 2;
constexpr unsigned ROWB = DH * 4;
constexpr unsigned HEADB = SKV * ROWB;
constexpr unsigned KVB = NH * HEADB;
constexpr unsigned CSB = SKV * DH * 2;
constexpr unsigned OOB = 0x80000000u;
constexpr int DEV = 16, NT = 2;
constexpr float SCALE_LOG2 = 1.4426950408889634f * 0.07216878364870322f;
constexpr unsigned SPIN_LIMIT = 20000000u;

constexpr unsigned UNITB = NWG * 64;
constexpr unsigned GSTR = 16 * UNITB + 256;
constexpr unsigned WS_PART = 0;
constexpr unsigned WS_EPOCH = NWG * (UNITB + 1024);
constexpr unsigned WS_REP = WS_EPOCH + 256;
constexpr unsigned WS_BYTES = WS_REP + 256 * 512;

__device__ __forceinline__ float bflo(unsigned u) { return __uint_as_float(u << 16); }
__device__ __forceinline__ float bfhi(unsigned u) { return __uint_as_float(u & 0xffff0000u); }
__device__ __forceinline__ unsigned pk_bf16(f32x2 a) { return __builtin_bit_cast(unsigned, __builtin_convertvector(a, bf16x2)); }
__device__ __forceinline__ void dot_cvt(const u32x4 q, const u32x4 t, const u32x4 ki, f32x2& w01, f32x2& w23, f32x2& k01, f32x2& k23) {
    float w0, w1, w2, w3;
    float k0 = __uint_as_float(ki[0]), k1 = __uint_as_float(ki[1]), k2 = __uint_as_float(ki[2]), k3 = __uint_as_float(ki[3]);
    asm("v_dot2_f32_bf16 %0, %8, %12, 0\n\tv_dot2_f32_bf16 %1, %9, %13, 0\n\tv_dot2_f32_bf16 %2, %10, %14, 0\n\tv_dot2_f32_bf16 %3, %11, %15, 0\n\t"
        "v_cvt_f32_i32 %4, %4\n\tv_cvt_f32_i32 %5, %5\n\tv_cvt_f32_i32 %6, %6\n\tv_cvt_f32_i32 %7, %7"
        : "=&v"(w0), "=&v"(w1), "=&v"(w2), "=&v"(w3), "+v"(k0), "+v"(k1), "+v"(k2), "+v"(k3)
        : "v"(q[0]), "v"(q[1]), "v"(q[2]), "v"(q[3]), "v"(t[0]), "v"(t[1]), "v"(t[2]), "v"(t[3]));
    w01 = f32x2{w0, w1}; w23 = f32x2{w2, w3}; k01 = f32x2{k0, k1}; k23 = f32x2{k2, k3};
}

template <int CTRL> __device__ __forceinline__ float dpp(float x) {
    return __builtin_bit_cast(float, __builtin_amdgcn_mov_dpp(__builtin_bit_cast(int, x), CTRL, 0xf, 0xf, true));
}
__device__ __forceinline__ float row16_allsum(float x) {
    x += dpp<0x128>(x);
    x += dpp<0x124>(x);
    x += dpp<0x122>(x);
    x += dpp<0x121>(x);
    return x;
}
__device__ __forceinline__ float xrow_max(float x) {
    auto s = __builtin_amdgcn_permlane16_swap(__float_as_uint(x), __float_as_uint(x), false, false);
    x = fmaxf(__uint_as_float(s[0]), __uint_as_float(s[1]));
    auto t = __builtin_amdgcn_permlane32_swap(__float_as_uint(x), __float_as_uint(x), false, false);
    return fmaxf(__uint_as_float(t[0]), __uint_as_float(t[1]));
}
__device__ __forceinline__ float xrow_sum(float x) {
    auto s = __builtin_amdgcn_permlane16_swap(__float_as_uint(x), __float_as_uint(x), false, false);
    x = __uint_as_float(s[0]) + __uint_as_float(s[1]);
    auto t = __builtin_amdgcn_permlane32_swap(__float_as_uint(x), __float_as_uint(x), false, false);
    return __uint_as_float(t[0]) + __uint_as_float(t[1]);
}
__device__ __forceinline__ float swap32_add(float a, float b) {
    auto t = __builtin_amdgcn_permlane32_swap(__float_as_uint(a), __float_as_uint(b), false, false);
    return __uint_as_float(t[0]) + __uint_as_float(t[1]);
}
__device__ __forceinline__ float swap16_add(float a, float b) {
    auto t = __builtin_amdgcn_permlane16_swap(__float_as_uint(a), __float_as_uint(b), false, false);
    return __uint_as_float(t[0]) + __uint_as_float(t[1]);
}
__device__ __forceinline__ int row16_sum(int x) {
    x += __builtin_amdgcn_mov_dpp(x, 0x128, 0xf, 0xf, true);
    x += __builtin_amdgcn_mov_dpp(x, 0x124, 0xf, 0xf, true);
    x += __builtin_amdgcn_mov_dpp(x, 0x122, 0xf, 0xf, true);
    x += __builtin_amdgcn_mov_dpp(x, 0x121, 0xf, 0xf, true);
    return x;
}
__device__ __forceinline__ unsigned now32() { return (unsigned)__builtin_amdgcn_s_memrealtime(); }

#define PIN() do { asm volatile("" ::: "memory"); __builtin_amdgcn_sched_barrier(0); } while (0)
#define BARRIER() do { asm volatile("s_waitcnt lgkmcnt(0)" ::: "memory"); __builtin_amdgcn_s_barrier(); PIN(); } while (0)
#define VMCNT(n) asm volatile("s_waitcnt vmcnt(%0)" :: "i"(n) : "memory")

typedef volatile __attribute__((address_space(3))) unsigned vlds_u32;
typedef volatile __attribute__((address_space(3))) u32x2 vlds_u32x2;
typedef volatile __attribute__((address_space(3))) u32x4 vlds_u32x4;

struct Pace {
    unsigned step;
    int start;
    int offset;
    int bias;
    int rd;
    int pub;
};

__global__ __launch_bounds__(576, 3) __attribute__((aligned(4096))) void kv_sweep_kernel(
    const void*, const void*, const void*, const void*, const void*, const void*, const void*, const void*,
    const void*, const void*, const void*, void*, void*, Pace)
{
    __shared__ __attribute__((aligned(16))) unsigned s_q[NH * DH];
    __shared__ __attribute__((aligned(16))) unsigned short s_sc[NH * 256];
    __shared__ __attribute__((aligned(16))) float s_w[NH * 4 * 20];
    __shared__ __attribute__((aligned(16))) float s_part[8 * NI * DH];
    __shared__ __attribute__((aligned(16))) float s_red[8 * 28];
    __shared__ __attribute__((aligned(16))) float s_f[NH * 8];
    __shared__ __attribute__((aligned(16))) unsigned s_done[8];
    __shared__ __attribute__((aligned(16))) unsigned s_sync[8];
    __shared__ __attribute__((aligned(16))) unsigned s_act[16];

    unsigned long long pc0, t0;
    u64x8 ka, kb;
    asm volatile("s_load_dwordx16 %3, %4, 0x40\n\ts_load_dwordx16 %2, %4, 0x0\n\ts_memrealtime %1\n\ts_getpc_b64 %0\n.Lkv_beg:"
                 : "=&s"(pc0), "=&s"(t0), "=&s"(ka), "=&s"(kb) : "s"(__builtin_amdgcn_kernarg_segment_ptr()));
    unsigned clen, xw;
    asm volatile("s_mov_b32 %0, .Lkv_end-.Lkv_beg" : "=s"(clen));
    asm volatile("global_load_dword %0, %1, %2" : "=&v"(xw) : "v"(min((blockIdx.x >> 3) * 64 + (threadIdx.x >> 6) * 2048, clen)), "s"(pc0 & ~63ull) : "memory");
    __builtin_amdgcn_sched_barrier(0);
    const int tid = threadIdx.x;
    const int lane = tid & 63;
    int wave = __builtin_amdgcn_readfirstlane(tid >> 6);
    const int r = lane >> 4;
    const int c = lane & 15;
    const int bid = blockIdx.x;
    const int m = (bid & 7) * 32 + (bid >> 3);
    const int rw = wave & 3, hp = wave >> 2;
    constexpr int RS = 8192 + 128, RT = 1024 + 256;
    const int grow = ((bid >> 3 & 3) | (bid >> 4 & 4) | (bid & 0x84) << 1 | (bid & 2) << 3 | (bid & 32) | (bid & 1) << 7) * 16;
    auto srow = [&](int t) { return (t & 1) * RS ^ (t >> 1) * RT; };
    const int row0 = grow + rw * 4;
    unsigned co = r * 384 + c * 8, so1 = r * 384 + (c < 8 ? c * 8 + 320 : c * 8 - 64), rb0 = (row0 ^ RT * hp) * 384u;
    asm volatile("s_waitcnt lgkmcnt(0)" : "+s"(t0), "+s"(ka), "+s"(kb), "+v"(co), "+v"(so1), "+s"(rb0), "+s"(wave));
    typedef const __attribute__((address_space(1))) char* gptr;
    void* const q = (void*)ka[0]; void* const qk = (void*)ka[1]; void* const qv = (void*)ka[4]; void* const q_cos = (void*)ka[7];
    const gptr ksc = (gptr)ka[2], kzp = (gptr)ka[3], vsc = (gptr)ka[5], vzp = (gptr)ka[6];
    void* const q_sin = (void*)kb[0]; void* const k_cos = (void*)kb[1]; void* const k_sin = (void*)kb[2]; void* const out = (void*)kb[4];
    unsigned char* const ws = (unsigned char*)(__attribute__((address_space(1))) unsigned char*)kb[3];
    const Pace pace = {(unsigned)kb[5], (int)(kb[5] >> 32), (int)kb[6], (int)(kb[6] >> 32), (int)kb[7], (int)(kb[7] >> 32)};
    const unsigned tstart = (unsigned)t0;
    const unsigned tsched = now32();
    const auto rw_ = __builtin_amdgcn_make_buffer_rsrc((void*)ws, 0, WS_BYTES, 0x00020000);
    const auto ro = __builtin_amdgcn_make_buffer_rsrc((void*)out, 0, NH * DH * 2, 0x00020000);

    auto pick = [](int n, gptr p0, gptr p1, gptr p2, gptr p3) { return (n & 2) ? ((n & 1) ? p3 : p2) : ((n & 1) ? p1 : p0); };
    auto merge = [&](const float* p, f32x4 f, float zs, unsigned w3) {
        f32x2 u[4][3], a[3];
#pragma unroll
        for (int w = 0; w < 4; ++w)
#pragma unroll
            for (int k = 0; k < 3; ++k) u[w][k] = *reinterpret_cast<const f32x2*>(p + w * NI * DH + 2 * k);
        asm volatile("" : "+v"(u[0][0]), "+v"(u[0][1]), "+v"(u[0][2]), "+v"(u[1][0]), "+v"(u[1][1]), "+v"(u[1][2]),
                          "+v"(u[2][0]), "+v"(u[2][1]), "+v"(u[2][2]), "+v"(u[3][0]), "+v"(u[3][1]), "+v"(u[3][2]));
#pragma unroll
        for (int k = 0; k < 3; ++k) a[k] = (f[0] * u[0][k] - zs + f[1] * u[1][k]) + (f[2] * u[2][k] + f[3] * u[3][k]);
        return u32x4{pk_bf16(a[0]), pk_bf16(a[1]), pk_bf16(a[2]), w3};
    };
    auto last_word = [&](unsigned tag, float ls, int c6) { return tag | ((__float_as_uint(ls) >> (16 * (c6 & 1))) & 0xffffu); };
    auto piece_off = [&](int hp, int c6) { return (hp * 8 + (c6 >> 2)) * UNITB + (c6 & 3) * 16; };
    auto publish = [&](u32x4 v, int i, unsigned vo) {
        __builtin_amdgcn_raw_buffer_store_b128(v, rw_, vo, WS_PART + i * GSTR + m * 64, DEV);
        asm volatile("s_nop 1" :: "v"(v) : "memory");
    };

    if (__builtin_expect_with_probability(wave == 8, 1, 0.45)) {
        __builtin_amdgcn_s_setprio(3);
        unsigned sci = 0;
        gptr scp = pick(lane >> 1, ksc, kzp, vsc, vzp) + ((lane >> 5) * SKV + (grow ^ srow(lane >> 3 & 3)) + (lane & 1) * 8) * 2;
        auto gather = [&]() {
            asm volatile("s_mov_b32 m0, %1\n\ts_nop 0\n\tglobal_load_lds_dwordx4 %0, off nt"
                         :: "v"(scp), "s"((unsigned)(unsigned long long)(__attribute__((address_space(3))) unsigned short*)s_sc + sci * 1024) : "memory");
            scp += 4 * SKV;
        };
        while (now32() - tsched < 100) __builtin_amdgcn_s_sleep(1);
        u32x3 lc[2], ls[2];
        u32x4 qraw[12];
        {
            const auto rq = __builtin_amdgcn_make_buffer_rsrc((void*)q, 0, NH * DH * 2, 0x00020000);
            const auto rqc = __builtin_amdgcn_make_buffer_rsrc((void*)q_cos, 0, DH * 2, 0x00020000);
            const auto rqs = __builtin_amdgcn_make_buffer_rsrc((void*)q_sin, 0, DH * 2, 0x00020000);
#pragma unroll
            for (int k = 0; k < 12; ++k) qraw[k] = __builtin_amdgcn_raw_buffer_load_b128(rq, lane * 16 + (k & 3) * 1024, (k >> 2) * 4096, 0);
#pragma unroll
            for (int k = 0; k < 2; ++k) {
                lc[k] = __builtin_amdgcn_raw_buffer_load_b96(rqc, c * 12 + k * 192, 0, 0);
                ls[k] = __builtin_amdgcn_raw_buffer_load_b96(rqs, c * 12 + k * 192, 0, 0);
            }
            PIN();
        }
        const unsigned tag = ((unsigned)__builtin_amdgcn_raw_buffer_load_b16(rw_, (lane & 3) * 2, WS_EPOCH, DEV) + 1u) << 16;
        PIN();
        unsigned* const s_raw = reinterpret_cast<unsigned*>(s_part) + NI * DH / 2;
#pragma unroll
        for (int k = 0; k < 12; ++k) *reinterpret_cast<u32x4*>(s_raw + k / 6 * (NI * DH) + k % 6 * 256 + lane * 4) = qraw[k];
        f32x2 fc[2][3], fs[2][3];
#pragma unroll
        for (int k = 0; k < 2; ++k)
#pragma unroll
            for (int u = 0; u < 3; ++u) { fc[k][u] = f32x2{bflo(lc[k][u]), bfhi(lc[k][u])}; fs[k][u] = f32x2{bflo(ls[k][u]), bfhi(ls[k][u])}; }
        PIN();
#pragma nounroll
        for (int it = 0; it < 4; ++it) {
            if (sci < NB) { gather(); ++sci; }
            unsigned qa[2][3], qb[2][3];
#pragma unroll
            for (int g = 0; g < 2; ++g)
#pragma unroll
                for (int u = 0; u < 3; ++u) {
                    const unsigned* src = s_raw + g * (NI * DH) + (it * 4 + r) * HALF + 3 * c;
                    qa[g][u] = src[u]; qb[g][u] = src[HALF / 2 + u];
                }
#pragma unroll
            for (int g = 0; g < 2; ++g)
#pragma unroll
                for (int u = 0; u < 3; ++u) {
                    const f32x2 a = {bflo(qa[g][u]), bfhi(qa[g][u])}, b = {bflo(qb[g][u]), bfhi(qb[g][u])};
                    const f32x2 o0 = a * fc[0][u] - b * fs[0][u], o1 = b * fc[1][u] + a * fs[1][u];
                    const unsigned l0 = pk_bf16(f32x2{o0[0], o1[0]}), l1 = pk_bf16(f32x2{o0[1], o1[1]});
                    unsigned* dst = s_q + (16 * g + 4 * it + r) * DH + 6 * c + 2 * u;
                    *reinterpret_cast<u32x2*>(dst) = u32x2{l0, l1};
                    *reinterpret_cast<u32x2*>(dst + HALF) = u32x2{__builtin_amdgcn_alignbit(l0, l0, 16) ^ 0x80000000u, __builtin_amdgcn_alignbit(l1, l1, 16) ^ 0x80000000u};
                }
        }
        asm volatile("" :: "v"(xw));
        __builtin_amdgcn_s_setprio(0);
        unsigned origin = tsched << 8;
        if (lane < 8) { s_done[lane] = 0; s_sync[lane] = lane < 3 ? origin : lane < 4 ? NB : tag; }
        PIN();
        VMCNT(0);
        BARRIER();
        const int mh = lane >> 5, mc = lane & 31;
        const unsigned rtag = __builtin_amdgcn_readlane(tag, 0) | __builtin_amdgcn_readlane(tag, 1) >> 16;
        constexpr int RPX = 2;
        constexpr int RSH = 9;
        constexpr unsigned RDLY = 200;
        constexpr int UP = 984, DN = 2000;
        const int rj = ((bid >> 3) + 5 * (bid & 7)) & 31;
        const int rid = rj % (32 / RPX) == 0 ? rj / (32 / RPX) * 8 + (bid & 7) : -1;
        unsigned wround = now32() >> RSH, rround = wround, tick = 0, target = origin;
        bool synced = false;
        int drift = 0;
        const unsigned round0 = rround;
        int i = 0;
        unsigned sct = tstart;
        auto kept_now = [&](unsigned now) {
            const u32x2 a = *(vlds_u32x2*)(s_act + 2 * (lane & 7));
            const int late = max((int)(a[0] - origin), (int)((now << 8) - a[1] - origin));
            return origin + (__builtin_amdgcn_readfirstlane(row16_sum(late)) >> 4);
        };
        auto steer = [&](unsigned now) {
            if ((now >> 5) != tick) {
                tick = now >> 5;
                target += drift;
                origin += max(-DN, min(UP + drift, (int)(target - origin)));
                if (!synced) {
                    origin += max(0, (int)(kept_now(now) - origin) - 2000);
                    target = origin;
                }
                if (lane == 0) *(vlds_u32*)(s_sync + 2) = origin;
                PIN();
            }
        };
        auto finished = [&]() {
            const u32x4 a = *(vlds_u32x4*)s_done, b = *(vlds_u32x4*)(s_done + 4);
            return __builtin_amdgcn_readfirstlane(min(min(min(a[0], a[1]), min(a[2], a[3])), min(min(b[0], b[1]), min(b[2], b[3]))));
        };
        while (i < NI - 2) {
            const unsigned now = now32();
            if ((now >> RSH) != wround) {
                wround = now >> RSH;
                if (rid >= 0 && (now & ((1u << RSH) - 1)) < 48)
                    __builtin_amdgcn_raw_buffer_store_b64(u32x2{kept_now(now), rtag + wround}, rw_, lane == 0 ? rid * 8 : OOB, WS_REP + (wround & 255) * 512, DEV);
                PIN();
                continue;
            }
            if ((int)(((now - RDLY) >> RSH) - rround) > 0) {
                rround = (now - RDLY) >> RSH;
                const u32x2 x = __builtin_amdgcn_raw_buffer_load_b64(rw_, c * 8, WS_REP + (rround & 255) * 512, DEV);
                const int d = (int)(x[0] - origin);
                const bool ok = x[1] == rtag + rround && d < (32 << 16) && d > -(32 << 16);
                const int n = __builtin_popcountll(__builtin_amdgcn_ballot_w64(ok));
                if (n >= 48) {
                    const unsigned nt = origin + __builtin_amdgcn_readfirstlane((int)(row16_allsum(ok ? (float)d : 0.f) * 4.f / (float)n)) - pace.bias;
                    if (synced && rround - round0 > 4) drift = max(0, min(drift + ((max(-1024, min((int)(nt - target) + 6 * drift, 2560)) - (rround - round0 < 13 ? 768 : 0)) >> 5), 400));
                    target = nt + 6 * drift;
                    synced = true;
                    const int lost = min((int)(kept_now(now32()) - origin) - pace.bias, (int)(target - origin));
                    if (lost > 0) origin += lost;
                }
                PIN();
                continue;
            }
            steer(now);
            if (sci <= NI && now - sct >= 100) {
                VMCNT(0);
                if (lane == 0) *(vlds_u32*)(s_sync + 3) = sci;
                if (sci < NI) gather();
                ++sci; sct = now;
                PIN();
                continue;
            }
            if (finished() <= (unsigned)i) { __builtin_amdgcn_s_sleep(2); continue; }
            PIN();
            if (i == 0) {
                f32x4 ml[4], f;
#pragma unroll
                for (int w = 0; w < 4; ++w) ml[w] = *reinterpret_cast<const f32x4*>(s_w + (mc * 4 + w) * 20 + 16);
                const float M = fmaxf(fmaxf(ml[0][0], ml[1][0]), fmaxf(ml[2][0], ml[3][0]));
                float L = 0.f, zs = 0.f;
#pragma unroll
                for (int w = 0; w < 4; ++w) { f[w] = __builtin_amdgcn_exp2f(ml[w][0] - M); L += f[w] * ml[w][1]; zs += f[w] * ml[w][2]; }
                const float inv = 1.f / L;
                *reinterpret_cast<f32x4*>(s_f + mc * 8) = f * inv;
                *reinterpret_cast<f32x2*>(s_f + mc * 8 + 4) = f32x2{zs * inv, M + __builtin_amdgcn_logf(L)};
                PIN();
                if (lane == 0) *(vlds_u32*)(s_sync + 3) = NI + 1;
                PIN();
            }
            const f32x2 zl = *reinterpret_cast<const f32x2*>(s_f + (2 * i + mh) * 8 + 4);
            const u32x4 v = merge(s_part + (mh * 4 * NI + i) * DH + mc * 6, *reinterpret_cast<const f32x4*>(s_f + (2 * i + mh) * 8), zl[0], last_word(tag, zl[1], mc));
            PIN();
            *reinterpret_cast<u32x4*>(s_part + (mh * 4 * NI + i) * DH + mc * 4) = v;
            PIN();
            ++i;
        }
        u32x4 v[NI - 1];
#pragma unroll
        for (int i = 0; i < NI - 2; ++i) v[i] = *reinterpret_cast<const u32x4*>(s_part + (mh * 4 * NI + i) * DH + mc * 4);
        f32x4 f = *reinterpret_cast<const f32x4*>(s_f + (2 * (NI - 2) + mh) * 8);
        f32x2 zl = *reinterpret_cast<const f32x2*>(s_f + (2 * (NI - 2) + mh) * 8 + 4);
        PIN();
#pragma unroll
        for (int i = 0; i < NI - 2; ++i) asm volatile("" : "+v"(v[i]));
        asm volatile("" : "+v"(f), "+v"(zl));
        v[NI - 2] = v[0];
        bool have = false, sent = false;
        while (!(have && sent)) {
            const unsigned now = now32();
            if (!have) {
                steer(now);
                if (finished() > NI - 2) {
                    PIN();
                    v[NI - 2] = merge(s_part + (mh * 4 * NI + NI - 2) * DH + mc * 6, f, zl[0], last_word(tag, zl[1], mc));
                    have = true;
                }
            }
            if (!sent && (int)((now << 8) - origin) >= pace.pub) {
#pragma unroll
                for (int i = 0; i < NI - 2; ++i) publish(v[i], i, piece_off(mh, mc));
                sent = true;
            }
            if (!(have && sent)) __builtin_amdgcn_s_sleep(1);
            PIN();
        }
        publish(v[NI - 2], NI - 2, piece_off(mh, mc));
        asm volatile(".Lkv_end:");
        return;
    }

    const int ph = rw + 4 * bid + 1024 * hp;
    unsigned off = pace.start + ((pace.step * ph) >> 11);
    unsigned origin = tsched << 8, kept = 0;
    unsigned step = pace.step;
    auto pace_wait = [&]() {
        const unsigned tgt = __builtin_amdgcn_readfirstlane(origin) + off;
        unsigned now = now32() << 8;
        kept = ((int)(now - tgt) > 0 ? now : tgt) - off;
        while ((int)(tgt - now) > 0) { __builtin_amdgcn_s_sleep(1); now = now32() << 8; }
        off += step;
    };
    auto report = [&]() {
        if (lane == 0) *(vlds_u32x2*)(s_act + 2 * wave) = u32x2{kept, off};
        origin = *(vlds_u32*)(s_sync + 2);
        PIN();
    };

    auto pre_sc = [&]() {
        const unsigned junk = (unsigned)(unsigned long long)(__attribute__((address_space(3))) float*)s_part + (3 * NI * DH - 256) * 4;
        const gptr a = pick(grow >> 4, ksc, kzp, vsc, vzp) + ((tid >> 5) * SKV + (grow & ~63 ^ srow(lane >> 3 & 3))) * 2 + (lane & 7) * 16;
        asm volatile("s_mov_b32 m0, %2\n\ts_nop 0\n\tglobal_load_lds_dwordx4 %0, off\n\tglobal_load_lds_dwordx4 %1, off" :: "v"(a), "v"(a + 32 * SKV), "s"(junk) : "memory");
        PIN();
    };
    const auto rc = __builtin_amdgcn_make_buffer_rsrc((void*)k_cos, 0, CSB, 0x00020000);
    const auto rs = __builtin_amdgcn_make_buffer_rsrc((void*)k_sin, 0, CSB, 0x00020000);
    u32x2 raw[2][6];
    auto load_cs = [&](int t) {
        const unsigned rb = t == 0 ? rb0 : (row0 ^ RT * hp ^ RS) * 384u;
        raw[t][0] = __builtin_amdgcn_raw_buffer_load_b64(rc, co, rb, NT);
        raw[t][1] = __builtin_amdgcn_raw_buffer_load_b64(rc, co + 128, rb, NT);
        raw[t][2] = __builtin_amdgcn_raw_buffer_load_b64(rc, co + 256, rb, NT);
        raw[t][3] = __builtin_amdgcn_raw_buffer_load_b64(rs, co + 192, rb, NT);
        raw[t][4] = __builtin_amdgcn_raw_buffer_load_b64(rs, so1, rb, NT);
        raw[t][5] = __builtin_amdgcn_raw_buffer_load_b64(rs, co + 64, rb, NT);
        PIN();
    };
    u32x4 tab[4][3];
    auto pack_cs = [&](int t) {
#pragma unroll
        for (int j = 0; j < 3; ++j) {
#pragma unroll
            for (int k = 0; k < 2; ++k) {
                tab[t][j][2 * k] = __builtin_amdgcn_perm(raw[t][3 + j][k], raw[t][j][k], 0x05040100);
                tab[t][j][2 * k + 1] = __builtin_amdgcn_perm(raw[t][3 + j][k], raw[t][j][k], 0x07060302);
            }
            *reinterpret_cast<u32x4*>(s_part + (wave ^ 4) * (NI * DH) + ((t * 3 + j) * 64 + lane) * 4) = tab[t][j];
        }
        PIN();
    };
    const auto rk = __builtin_amdgcn_make_buffer_rsrc((void*)qk, 0, KVB, 0x00020000);
    const auto rv = __builtin_amdgcn_make_buffer_rsrc((void*)qv, 0, KVB, 0x00020000);
    const unsigned voff = r * ROWB + c * 16;
    const unsigned woff = hp * HEADB;
    auto toff = [&](int t) { return (unsigned)(row0 ^ srow(t)) * ROWB; };
    u32x4 ring[4][3];
    auto ld = [&](int slot, int j, __amdgpu_buffer_rsrc_t rr, unsigned off) {
        ring[slot][j] = __builtin_amdgcn_raw_buffer_load_b128(rr, voff + j * 256, off, NT);
        PIN();
    };
    auto ld_tile = [&](int slot, __amdgpu_buffer_rsrc_t rr, unsigned off) { ld(slot, 0, rr, off); ld(slot, 1, rr, off); ld(slot, 2, rr, off); };
    unsigned post = 0;
    auto enter = [&](int i) {
        if (i == 9 || i == 10) {
            const unsigned hs = pace.step >> 1, z = (unsigned)pace.offset >> 16;
            off += i == 10 ? post : hp * 3 * hs;
            step = i == 10 ? pace.step : hs + hp * z;
            post = __builtin_amdgcn_readfirstlane(i == 10 ? 0 : hp ? hs : 4 * (hs + z));
            if (lane == 0) *(vlds_u32x2*)(s_act + 2 * wave) = u32x2{kept, off};
            PIN();
        }
    };
    auto refill = [&](int slot, int j, __amdgpu_buffer_rsrc_t rr, unsigned goff) {
        if (j == 0) pace_wait();
        if (j == 0) asm volatile("s_cmp_lg_u32 %0, 0\n\ts_waitcnt vmcnt(3)\n\ts_cbranch_scc1 1\n\ts_waitcnt vmcnt(2)" :: "s"(post) : "scc", "memory");
        else asm volatile("s_cmp_lg_u32 %0, 0\n\ts_cbranch_scc1 1\n\ts_waitcnt vmcnt(2)" :: "s"(post) : "scc", "memory");
        ld(slot, j, rr, goff);
        if (j == 0) { const unsigned t = (now32() << 8) - 1024 - (off - step); if ((int)(t - kept) > 0) kept = t; }
        if (j == 2) report();
    };
    auto k0 = [&](int t) { pace_wait(); ld_tile(t, rk, woff + toff(t)); };
    load_cs(0);
    pace_wait(); load_cs(1);
    pack_cs(0);
    pace_wait(); pre_sc(); off += (pace.offset & 0xffff) - step;
    k0(0); pack_cs(1);
    asm volatile("" :: "v"(xw));
    k0(1);
    VMCNT(3); k0(2);
    VMCNT(3);
    if (hp) { if (lane == 0) *(vlds_u32x2*)(s_act + 2 * wave) = u32x2{kept, off}; BARRIER(); }
    k0(3);
    if (lane == 0) *(vlds_u32x2*)(s_act + 2 * wave) = u32x2{kept, off};
    if (!hp) BARRIER();
    origin = *(vlds_u32*)(s_sync + 2);

    u32x4 qp[3];
    auto load_q = [&](int h, int j) { qp[j] = *reinterpret_cast<const u32x4*>(s_q + h * DH + 64 * j + 4 * c); };
    load_q(hp, 0); load_q(hp, 1); load_q(hp, 2);
#pragma unroll
    for (int t = 0; t < 2; ++t)
#pragma unroll
        for (int j = 0; j < 3; ++j) tab[2 + t][j] = *reinterpret_cast<const u32x4*>(s_part + wave * (NI * DH) + ((t * 3 + j) * 64 + lane) * 4);
    if (hp) {
#pragma unroll
        for (int t = 0; t < 2; ++t)
#pragma unroll
            for (int j = 0; j < 3; ++j)
#pragma unroll
                for (int k = 0; k < 4; ++k) asm volatile("v_swap_b32 %0, %1" : "+v"(tab[t][j][k]), "+v"(tab[2 + t][j][k]));
    }

    unsigned ready = NB;
    for (int i = 0; i < NI; ++i) {
        const bool last = i + 1 == NI;
        enter(i);
        const int h = 2 * i + hp;
        const unsigned hnext = (i + 1) * (2 * HEADB) + woff;
        const auto rn = __builtin_amdgcn_make_buffer_rsrc((void*)(last ? qv : qk), 0, KVB, 0x00020000);
        float* scp = s_w + (h * 4 + rw) * 20;
        f32x4 sc[4];
        while (__builtin_amdgcn_readfirstlane(ready) <= i) { __builtin_amdgcn_s_sleep(1); ready = *(vlds_u32*)(s_sync + 3); }
        PIN();
#pragma unroll
        for (int t = 0; t < 4; ++t)
#pragma unroll
            for (int a = 0; a < 4; ++a) sc[t][a] = bflo(s_sc[h * 256 + t * 64 + a * 16 + rw * 4 + r]);
        float s[4];
#pragma unroll
        for (int t = 0; t < 4; ++t) {
            f32x2 dot, wsum;
#pragma unroll
            for (int j = 0; j < 3; ++j) {
                f32x2 w01, w23, k01, k23;
                dot_cvt(qp[j], tab[t][j], ring[t][j], w01, w23, k01, k23);
                if (j == 0) { wsum = w01 + w23; dot = k01 * w01; } else { wsum += w01; wsum += w23; dot += k01 * w01; }
                dot += k23 * w23;
                asm volatile("" : "+v"(dot), "+v"(wsum));
                PIN();
                if (t == 3) load_q(last ? h : h + 2, j);
                if (t == 3 && j == 0) ready = *(vlds_u32*)(s_sync + 3);
                refill(t, j, rn, (last ? woff : hnext) + toff(t));
            }
            const float a = (dot[0] + dot[1]) - sc[t][1] * (wsum[0] + wsum[1]);
            s[t] = row16_allsum(a) * (sc[t][0] * SCALE_LOG2);
        }
        const float mx = xrow_max(fmaxf(fmaxf(s[0], s[1]), fmaxf(s[2], s[3])));
        float l = 0.f, z = 0.f;
#pragma unroll
        for (int t = 0; t < 4; ++t) {
            const float p = __builtin_amdgcn_exp2f(s[t] - mx);
            l += p;
            s[t] = p * sc[t][2];
            z += s[t] * sc[t][3];
        }
        l = xrow_sum(l);
        z = xrow_sum(z);
        if (c == 0) {
            *reinterpret_cast<f32x4*>(scp + r * 4) = f32x4{s[0], s[1], s[2], s[3]};
            *reinterpret_cast<f32x4*>(scp + 16) = f32x4{mx, l, z, 0.f};
        }
        PIN();
    }

    const float* pwp = s_w + (hp * 4 + rw) * 20 + r * 4;
    f32x4 pw = *reinterpret_cast<const f32x4*>(pwp);
    unsigned tag, lw, lo;
    f32x4 lf;
    f32x2 lz;
    unsigned lp = (hp * 4 * NI + NI - 1) * DH + (lane & 31) * 6;
    f32x2 acc[3][2];
    auto v_acc = [&](int t, int j) {
        const f32x2 p2 = {pw[t], pw[t]};
        const u32x4 vv = ring[t][j];
        const f32x2 v01 = {(float)(int)vv[0], (float)(int)vv[1]}, v23 = {(float)(int)vv[2], (float)(int)vv[3]};
        if (t == 0) { acc[j][0] = p2 * v01; acc[j][1] = p2 * v23; }
        else { acc[j][0] += p2 * v01; acc[j][1] += p2 * v23; }
        asm volatile("" : "+v"(acc[j][0]), "+v"(acc[j][1]));
        PIN();
    };
    auto v_fold = [&](int i, int j) {
        const float x = swap32_add(acc[j][0][0], acc[j][1][0]);
        const float y = swap32_add(acc[j][0][1], acc[j][1][1]);
        s_part[(wave * NI + i) * DH + 64 * j + 4 * c + r] = swap16_add(x, y);
    };
    auto v_done = [&](int i) {
        PIN();
        if (lane == 0) *(vlds_u32*)(s_done + wave) = i + 1;
        PIN();
    };
    constexpr int LS = 2;
#pragma unroll
    for (int t = 0; t < LS; ++t)
#pragma unroll
        for (int j = 0; j < 3; ++j) v_acc(t, j);
    for (int i = 0; i < NI - 1; ++i) {
        const unsigned hnext = (i + 1) * (2 * HEADB) + woff;
        enter(i);
#pragma unroll
        for (int k = 0; k < 4; ++k) {
            const int t = (k + LS) & 3;
#pragma unroll
            for (int j = 0; j < 3; ++j) {
                v_acc(t, j);
                if (t == 3 && j == 2) pw = *reinterpret_cast<const f32x4*>(pwp + (i + 1) * 160);
                refill(k, j, rv, hnext + toff(k));
            }
            if (t == 3) { v_fold(i, 0); v_fold(i, 1); v_fold(i, 2); v_done(i); }
        }
    }
    asm volatile("s_branch .Lkv_tail_%=\n\t.p2align 12\n.Lkv_tail_%=:" ::: "memory");
    while (__builtin_amdgcn_readfirstlane(*(vlds_u32*)(s_sync + 3)) <= NI) __builtin_amdgcn_s_sleep(1);
    PIN();
    tag = s_sync[4 + (lane & 3)];
    lf = *reinterpret_cast<const f32x4*>(s_f + (2 * (NI - 1) + hp) * 8);
    lz = *reinterpret_cast<const f32x2*>(s_f + (2 * (NI - 1) + hp) * 8 + 4);
    origin = *(vlds_u32*)(s_sync + 2);
    lw = last_word(tag, lz[1], lane & 31);
    lo = lane < 32 ? piece_off(hp, lane & 31) : OOB;
    asm volatile("" : "+v"(tag), "+v"(lf), "+v"(lz), "+v"(lw), "+v"(lo), "+v"(lp), "+v"(origin));
    PIN();
#pragma unroll
    for (int t = LS; t < 4; ++t)
#pragma unroll
        for (int j = 0; j < 3; ++j) { v_acc(t, j); if (t == 3) { v_fold(NI - 1, j); PIN(); } }
    v_done(NI - 1);
    {
        const u32x4 dn = *(vlds_u32x4*)(s_done + 4 * hp);
        PIN();
        const u32x4 v = merge(s_part + lp, lf, lz[0], lw);
        if (__builtin_amdgcn_readfirstlane(dn[0] & dn[1] & dn[2] & dn[3]) == NI) publish(v, NI - 1, lo);
        PIN();
    }
    {
        unsigned inbox = WS_PART + (bid >> 4) * GSTR + (bid & 15) * UNITB, obase = bid * 48;
        unsigned xo = tid * 16, yo = tid * 16 + 8192, sel = lane & 1 ? 0x01000504u : 0x05040100u;
        unsigned bo = lane < 16 ? wave * 1024 + (lane & 7) * 128 + (lane >> 3) * 8192 : OOB;
        typedef __attribute__((address_space(3))) float lds_f;
        lds_f* wo = (lds_f*)s_red + wave * 28 + (r < 3 ? c * 6 + r * 2 : 24);
        lds_f* fo = (lds_f*)s_red + (lane & 7) * 28 + 24;
        lds_f* go = (lds_f*)s_red + (lane & 7) * 28 + min(lane >> 3, 5) * 4;
        vlds_u32* dw = (vlds_u32*)(s_done + wave);
        vlds_u32* dr = (vlds_u32*)(s_done + (lane & 7));
        unsigned oo = (lane & 7) == 0 && lane < 48 ? lane : OOB, eo = bid == 0 && lane < 4 ? lane * 2 : OOB;
        asm volatile("" : "+s"(inbox), "+s"(obase), "+v"(xo), "+v"(yo), "+v"(sel), "+v"(bo), "+v"(wo), "+v"(fo), "+v"(go), "+v"(oo), "+v"(eo), "+v"(dw), "+v"(dr));
        const unsigned o = __builtin_amdgcn_readfirstlane(origin) - (15 - (bid >> 4)) * 512;
        while ((int)((now32() << 8) - o) < pace.rd) __builtin_amdgcn_s_sleep(1);
        PIN();
        u32x4 x, y;
        do {
            x = __builtin_amdgcn_raw_buffer_load_b128(rw_, xo, inbox, DEV);
            y = __builtin_amdgcn_raw_buffer_load_b128(rw_, yo, inbox, DEV);
            const unsigned age = now32() - tstart;
            PIN();
            if (age > SPIN_LIMIT) break;
        } while (__builtin_amdgcn_ballot_w64(((x[3] ^ tag) | (y[3] ^ tag)) > 0xffffu) != 0);
        __builtin_amdgcn_raw_buffer_store_b32(0u, rw_, bo, inbox, DEV);
        PIN();
        const float lx = __uint_as_float(__builtin_amdgcn_perm(__builtin_amdgcn_mov_dpp(x[3], 0xb1, 0xf, 0xf, true), x[3], sel));
        const float ly = __uint_as_float(__builtin_amdgcn_perm(__builtin_amdgcn_mov_dpp(y[3], 0xb1, 0xf, 0xf, true), y[3], sel));
        float M = fmaxf(lx, ly);
        M = fmaxf(M, dpp<0x128>(M));
        M = fmaxf(M, dpp<0x124>(M));
        M = xrow_max(M);
        const float w0 = __builtin_amdgcn_exp2f(lx - M), w1 = __builtin_amdgcn_exp2f(ly - M);
        float a[8];
#pragma unroll
        for (int k = 0; k < 3; ++k) {
            a[2 * k] = w0 * bflo(x[k]) + w1 * bflo(y[k]);
            a[2 * k + 1] = w0 * bfhi(x[k]) + w1 * bfhi(y[k]);
        }
        a[6] = a[7] = w0 + w1;
        float b[4], d[2];
#pragma unroll
        for (int k = 0; k < 4; ++k) b[k] = swap32_add(a[k], a[4 + k]);
#pragma unroll
        for (int k = 0; k < 2; ++k) d[k] = swap16_add(b[k], b[2 + k]);
#pragma unroll
        for (int k = 0; k < 2; ++k) { d[k] += dpp<0x128>(d[k]); d[k] += dpp<0x124>(d[k]); }
        if (c < 4) {
            *(__attribute__((address_space(3))) f32x2*)wo = f32x2{d[0], r < 3 ? d[1] : M};
            PIN();
            *dw = 3 * NI;
        }
        PIN();
        const unsigned fin = *dr;
        PIN();
        f32x2 sm = *(__attribute__((address_space(3))) f32x2*)fo;
        f32x4 ov = *(__attribute__((address_space(3))) f32x4*)go;
        asm volatile("" : "+v"(sm), "+v"(ov));
        if (__builtin_amdgcn_ballot_w64(fin != 3 * NI) == 0) {
            float MM = fmaxf(sm[1], dpp<0xb1>(sm[1]));
            MM = fmaxf(MM, dpp<0x4e>(MM));
            MM = fmaxf(MM, dpp<0x141>(MM));
            const float ff = __builtin_amdgcn_exp2f(sm[1] - MM);
            float e[5] = {ff * ov[0], ff * ov[1], ff * ov[2], ff * ov[3], ff * sm[0]};
#pragma unroll
            for (int k = 0; k < 5; ++k) e[k] += dpp<0xb1>(e[k]);
#pragma unroll
            for (int k = 0; k < 5; ++k) e[k] += dpp<0x4e>(e[k]);
#pragma unroll
            for (int k = 0; k < 5; ++k) e[k] += dpp<0x141>(e[k]);
            const float inv = __builtin_amdgcn_rcpf(e[4]);
            __builtin_amdgcn_raw_buffer_store_b64(u32x2{pk_bf16(f32x2{e[0] * inv, e[1] * inv}), pk_bf16(f32x2{e[2] * inv, e[3] * inv})}, ro, oo, obase, 0);
            if (bid == 0) __builtin_amdgcn_raw_buffer_store_b16((unsigned short)(tag >> 16), rw_, eo, WS_EPOCH, DEV);
        }
    }
    __builtin_amdgcn_endpgm();
}

extern "C" void kernel_launch(void* const* d_in, const int* in_sizes, int n_in,
                              void* d_out, int out_size, void* d_ws, size_t ws_size,
                              hipStream_t stream) {
    const float t_us = 100.6f, start_us = 0.2f, seam_us = 0.1f, scales_us = 1.15f;
    const float end_us = start_us + scales_us + 2 * seam_us + t_us * (129.f / 128.f);
    Pace pace;
    pace.step = (unsigned)(t_us * 25600.f / 128.f);
    pace.start = (int)(start_us * 25600.f);
    pace.offset = (int)(scales_us * 25600.f) | (int)(seam_us * 6400.f) << 16;
    pace.bias = (int)(0.052f * 25600.f);
    pace.rd = (int)((end_us + 1.53f) * 25600.f);
    pace.pub = (int)((end_us + 0.45f) * 25600.f);
    kv_sweep_kernel<<<NWG, 576, 0, stream>>>(d_in[0], d_in[1], d_in[2], d_in[3], d_in[4], d_in[5], d_in[6], d_in[7],
        d_in[8], d_in[9], d_in[10], d_ws, d_out, pace);
}
```

```cpp
#include <hip/hip_runtime.h>
#include <cstdint>


using u32x4 = __attribute__((ext_vector_type(4))) unsigned;
using u32x3 = __attribute__((ext_vector_type(3))) unsigned;
using u32x2 = __attribute__((ext_vector_type(2))) unsigned;
using u64x8 = __attribute__((ext_vector_type(8))) unsigned long long;
using f32x2 = __attribute__((ext_vector_type(2))) float;
using f32x4 = __attribute__((ext_vector_type(4))) float;
using bf16x2 = __attribute__((ext_vector_type(2))) __bf16;

constexpr int DH = 192;
constexpr int HALF = 96;
constexpr int NH = 32;
constexpr int SKV = 16384;
constexpr int NWG = 256;
constexpr int NI = 16;
constexpr int NB = 2;
constexpr unsigned ROWB = DH * 4;
constexpr unsigned HEADB = SKV * ROWB;
constexpr unsigned KVB = NH * HEADB;
constexpr unsigned CSB = SKV * DH * 2;
constexpr unsigned OOB = 0x80000000u;
constexpr int DEV = 16, NT = 2;
constexpr float SCALE_LOG2 = 1.4426950408889634f * 0.07216878364870322f;
constexpr unsigned SPIN_LIMIT = 20000000u;

constexpr unsigned UNITB = NWG * 64;
constexpr unsigned GSTR = 16 * UNITB + 256;
constexpr unsigned WS_PART = 0;
constexpr unsigned WS_EPOCH = NWG * (UNITB + 1024);
constexpr unsigned WS_REP = WS_EPOCH + 256;
constexpr unsigned WS_BYTES = WS_REP + 256 * 512;

__device__ __forceinline__ float bflo(unsigned u) { return __uint_as_float(u << 16); }
__device__ __forceinline__ float bfhi(unsigned u) { return __uint_as_float(u & 0xffff0000u); }
__device__ __forceinline__ unsigned pk_bf16(f32x2 a) { return __builtin_bit_cast(unsigned, __builtin_convertvector(a, bf16x2)); }
__device__ __forceinline__ void dot_cvt(const u32x4 q, const u32x4 t, const u32x4 ki, f32x2& w01, f32x2& w23, f32x2& k01, f32x2& k23) {
    float w0, w1, w2, w3;
    float k0 = __uint_as_float(ki[0]), k1 = __uint_as_float(ki[1]), k2 = __uint_as_float(ki[2]), k3 = __uint_as_float(ki[3]);
    asm("v_dot2_f32_bf16 %0, %8, %12, 0\n\tv_dot2_f32_bf16 %1, %9, %13, 0\n\tv_dot2_f32_bf16 %2, %10, %14, 0\n\tv_dot2_f32_bf16 %3, %11, %15, 0\n\t"
        "v_cvt_f32_i32 %4, %4\n\tv_cvt_f32_i32 %5, %5\n\tv_cvt_f32_i32 %6, %6\n\tv_cvt_f32_i32 %7, %7"
        : "=&v"(w0), "=&v"(w1), "=&v"(w2), "=&v"(w3), "+v"(k0), "+v"(k1), "+v"(k2), "+v"(k3)
        : "v"(q[0]), "v"(q[1]), "v"(q[2]), "v"(q[3]), "v"(t[0]), "v"(t[1]), "v"(t[2]), "v"(t[3]));
    w01 = f32x2{w0, w1}; w23 = f32x2{w2, w3}; k01 = f32x2{k0, k1}; k23 = f32x2{k2, k3};
}

template <int CTRL> __device__ __forceinline__ float dpp(float x) {
    return __builtin_bit_cast(float, __builtin_amdgcn_mov_dpp(__builtin_bit_cast(int, x), CTRL, 0xf, 0xf, true));
}
__device__ __forceinline__ float row16_allsum(float x) {
    x += dpp<0x128>(x);
    x += dpp<0x124>(x);
    x += dpp<0x122>(x);
    x += dpp<0x121>(x);
    return x;
}
__device__ __forceinline__ float xrow_max(float x) {
    auto s = __builtin_amdgcn_permlane16_swap(__float_as_uint(x), __float_as_uint(x), false, false);
    x = fmaxf(__uint_as_float(s[0]), __uint_as_float(s[1]));
    auto t = __builtin_amdgcn_permlane32_swap(__float_as_uint(x), __float_as_uint(x), false, false);
    return fmaxf(__uint_as_float(t[0]), __uint_as_float(t[1]));
}
__device__ __forceinline__ float xrow_sum(float x) {
    auto s = __builtin_amdgcn_permlane16_swap(__float_as_uint(x), __float_as_uint(x), false, false);
    x = __uint_as_float(s[0]) + __uint_as_float(s[1]);
    auto t = __builtin_amdgcn_permlane32_swap(__float_as_uint(x), __float_as_uint(x), false, false);
    return __uint_as_float(t[0]) + __uint_as_float(t[1]);
}
__device__ __forceinline__ float swap32_add(float a, float b) {
    auto t = __builtin_amdgcn_permlane32_swap(__float_as_uint(a), __float_as_uint(b), false, false);
    return __uint_as_float(t[0]) + __uint_as_float(t[1]);
}
__device__ __forceinline__ float swap16_add(float a, float b) {
    auto t = __builtin_amdgcn_permlane16_swap(__float_as_uint(a), __float_as_uint(b), false, false);
    return __uint_as_float(t[0]) + __uint_as_float(t[1]);
}
__device__ __forceinline__ int row16_sum(int x) {
    x += __builtin_amdgcn_mov_dpp(x, 0x128, 0xf, 0xf, true);
    x += __builtin_amdgcn_mov_dpp(x, 0x124, 0xf, 0xf, true);
    x += __builtin_amdgcn_mov_dpp(x, 0x122, 0xf, 0xf, true);
    x += __builtin_amdgcn_mov_dpp(x, 0x121, 0xf, 0xf, true);
    return x;
}
__device__ __forceinline__ unsigned now32() { return (unsigned)__builtin_amdgcn_s_memrealtime(); }

#define PIN() do { asm volatile("" ::: "memory"); __builtin_amdgcn_sched_barrier(0); } while (0)
#define BARRIER() do { asm volatile("s_waitcnt lgkmcnt(0)" ::: "memory"); __builtin_amdgcn_s_barrier(); PIN(); } while (0)
#define LDSA(p) ((unsigned)(unsigned long long)(__attribute__((address_space(3))) void*)(p))
#define VMCNT(n) asm volatile("s_waitcnt vmcnt(%0)" :: "i"(n) : "memory")

typedef volatile __attribute__((address_space(3))) unsigned vlds_u32;
typedef volatile __attribute__((address_space(3))) u32x2 vlds_u32x2;
typedef volatile __attribute__((address_space(3))) u32x4 vlds_u32x4;

struct Pace {
    unsigned step;
    int start;
    int offset;
    int bias;
    int rd;
    int pub;
};

__global__ __launch_bounds__(576, 3) __attribute__((aligned(4096))) void kv_sweep_kernel(
    const void*, const void*, const void*, const void*, const void*, const void*, const void*, const void*,
    const void*, const void*, const void*, void*, void*, Pace)
{
    __shared__ __attribute__((aligned(16))) unsigned s_q[NH * DH];
    __shared__ __attribute__((aligned(16))) unsigned short s_sc[NH * 256];
    __shared__ __attribute__((aligned(16))) float s_w[NH * 4 * 20];
    __shared__ __attribute__((aligned(16))) float s_part[8 * NI * DH];
    __shared__ __attribute__((aligned(16))) float s_red[8 * 28];
    __shared__ __attribute__((aligned(16))) float s_f[NH * 8];
    __shared__ __attribute__((aligned(16))) unsigned s_done[8];
    __shared__ __attribute__((aligned(16))) unsigned s_sync[8];
    __shared__ __attribute__((aligned(16))) unsigned s_act[16];

    unsigned long long pc0, t0;
    u64x8 ka, kb;
    asm volatile("s_load_dwordx16 %3, %4, 0x40\n\ts_load_dwordx16 %2, %4, 0x0\n\ts_memrealtime %1\n\ts_getpc_b64 %0\n.Lkv_beg:"
                 : "=&s"(pc0), "=&s"(t0), "=&s"(ka), "=&s"(kb) : "s"(__builtin_amdgcn_kernarg_segment_ptr()));
    unsigned clen, xw;
    asm volatile("s_mov_b32 %0, .Lkv_end-.Lkv_beg" : "=s"(clen));
    asm volatile("global_load_dword %0, %1, %2" : "=&v"(xw) : "v"(min((blockIdx.x >> 3) * 64 + (threadIdx.x >> 6) * 2048, clen)), "s"(pc0 & ~63ull) : "memory");
    __builtin_amdgcn_sched_barrier(0);
    const int tid = threadIdx.x;
    const int lane = tid & 63;
    int wave = __builtin_amdgcn_readfirstlane(tid >> 6);
    const int r = lane >> 4;
    const int c = lane & 15;
    const int bid = blockIdx.x;
    const int m = (bid & 7) * 32 + (bid >> 3);
    const int rw = wave & 3, hp = wave >> 2;
    constexpr int RS = 8192 + 128, RT = 1024 + 256;
    const int grow = ((bid >> 3 & 3) | (bid >> 4 & 4) | (bid & 0x84) << 1 | (bid & 2) << 3 | (bid & 32) | (bid & 1) << 7) * 16;
    auto srow = [&](int t) { return (t & 1) * RS ^ (t >> 1) * RT; };
    const int row0 = grow + rw * 4;
    unsigned co = r * 384 + c * 8, so1 = r * 384 + (c < 8 ? c * 8 + 320 : c * 8 - 64), rb0 = (row0 ^ RT * hp) * 384u;
    asm volatile("s_waitcnt lgkmcnt(0)" : "+s"(t0), "+s"(ka), "+s"(kb), "+v"(co), "+v"(so1), "+s"(rb0), "+s"(wave));
    typedef const __attribute__((address_space(1))) char* gptr;
    void* const q = (void*)ka[0]; void* const qk = (void*)ka[1]; void* const qv = (void*)ka[4]; void* const q_cos = (void*)ka[7];
    const gptr ksc = (gptr)ka[2], kzp = (gptr)ka[3], vsc = (gptr)ka[5], vzp = (gptr)ka[6];
    void* const q_sin = (void*)kb[0]; void* const k_cos = (void*)kb[1]; void* const k_sin = (void*)kb[2];
    unsigned char* const ws = (unsigned char*)(__attribute__((address_space(1))) unsigned char*)kb[3];
    const Pace pace = {(unsigned)kb[5], (int)(kb[5] >> 32), (int)kb[6], (int)(kb[6] >> 32), (int)kb[7], (int)(kb[7] >> 32)};
    const unsigned tstart = (unsigned)t0;
    const unsigned tsched = now32();
    const auto rw_ = __builtin_amdgcn_make_buffer_rsrc((void*)ws, 0, WS_BYTES, 0x00020000);

    auto pick = [](int n, gptr p0, gptr p1, gptr p2, gptr p3) { return (n & 2) ? ((n & 1) ? p3 : p2) : ((n & 1) ? p1 : p0); };
    auto merge = [&](const float* p, f32x4 f, float zs, unsigned w3) {
        f32x2 u[4][3], a[3];
#pragma unroll
        for (int w = 0; w < 4; ++w)
#pragma unroll
            for (int k = 0; k < 3; ++k) u[w][k] = *reinterpret_cast<const f32x2*>(p + w * NI * DH + 2 * k);
        asm volatile("" : "+v"(u[0][0]), "+v"(u[0][1]), "+v"(u[0][2]), "+v"(u[1][0]), "+v"(u[1][1]), "+v"(u[1][2]),
                          "+v"(u[2][0]), "+v"(u[2][1]), "+v"(u[2][2]), "+v"(u[3][0]), "+v"(u[3][1]), "+v"(u[3][2]));
#pragma unroll
        for (int k = 0; k < 3; ++k) a[k] = (f[0] * u[0][k] - zs + f[1] * u[1][k]) + (f[2] * u[2][k] + f[3] * u[3][k]);
        return u32x4{pk_bf16(a[0]), pk_bf16(a[1]), pk_bf16(a[2]), w3};
    };
    auto last_word = [&](unsigned tag, float ls, int c6) { return tag | ((__float_as_uint(ls) >> (16 * (c6 & 1))) & 0xffffu); };
    auto piece_off = [&](int hp, int c6) { return (hp * 8 + (c6 >> 2)) * UNITB + (c6 & 3) * 16; };
    auto publish = [&](u32x4 v, int i, unsigned vo) {
        __builtin_amdgcn_raw_buffer_store_b128(v, rw_, vo, WS_PART + i * GSTR + m * 64, DEV);
        asm volatile("s_nop 1" :: "v"(v) : "memory");
    };

    if (__builtin_expect_with_probability(wave == 8, 1, 0.45)) {
        __builtin_amdgcn_s_setprio(3);
        unsigned sci = 0;
        gptr scp = pick(lane >> 1, ksc, kzp, vsc, vzp) + ((lane >> 5) * SKV + (grow ^ srow(lane >> 3 & 3)) + (lane & 1) * 8) * 2;
        auto gather = [&]() {
            asm volatile("s_mov_b32 m0, %1\n\ts_nop 0\n\tglobal_load_lds_dwordx4 %0, off nt"
                         :: "v"(scp), "s"(LDSA(s_sc) + sci * 1024) : "memory");
            scp += 4 * SKV;
        };
        while (now32() - tsched < 100) __builtin_amdgcn_s_sleep(1);
        u32x3 lc[2], ls[2];
        u32x4 qraw[12];
        {
            const auto rq = __builtin_amdgcn_make_buffer_rsrc((void*)q, 0, NH * DH * 2, 0x00020000);
            const auto rqc = __builtin_amdgcn_make_buffer_rsrc((void*)q_cos, 0, DH * 2, 0x00020000);
            const auto rqs = __builtin_amdgcn_make_buffer_rsrc((void*)q_sin, 0, DH * 2, 0x00020000);
#pragma unroll
            for (int k = 0; k < 12; ++k) qraw[k] = __builtin_amdgcn_raw_buffer_load_b128(rq, lane * 16 + (k & 3) * 1024, (k >> 2) * 4096, 0);
#pragma unroll
            for (int k = 0; k < 2; ++k) {
                lc[k] = __builtin_amdgcn_raw_buffer_load_b96(rqc, c * 12 + k * 192, 0, 0);
                ls[k] = __builtin_amdgcn_raw_buffer_load_b96(rqs, c * 12 + k * 192, 0, 0);
            }
            PIN();
        }
        const unsigned tag = ((unsigned)__builtin_amdgcn_raw_buffer_load_b16(rw_, (lane & 3) * 2, WS_EPOCH, DEV) + 1u) << 16;
        PIN();
        unsigned* const s_raw = reinterpret_cast<unsigned*>(s_part) + NI * DH / 2;
#pragma unroll
        for (int k = 0; k < 12; ++k) *reinterpret_cast<u32x4*>(s_raw + k / 6 * (NI * DH) + k % 6 * 256 + lane * 4) = qraw[k];
        f32x2 fc[2][3], fs[2][3];
#pragma unroll
        for (int k = 0; k < 2; ++k)
#pragma unroll
            for (int u = 0; u < 3; ++u) { fc[k][u] = f32x2{bflo(lc[k][u]), bfhi(lc[k][u])}; fs[k][u] = f32x2{bflo(ls[k][u]), bfhi(ls[k][u])}; }
        PIN();
#pragma nounroll
        for (int it = 0; it < 4; ++it) {
            if (sci < NB) { gather(); ++sci; }
            unsigned qa[2][3], qb[2][3];
#pragma unroll
            for (int g = 0; g < 2; ++g)
#pragma unroll
                for (int u = 0; u < 3; ++u) {
                    const unsigned* src = s_raw + g * (NI * DH) + (it * 4 + r) * HALF + 3 * c;
                    qa[g][u] = src[u]; qb[g][u] = src[HALF / 2 + u];
                }
#pragma unroll
            for (int g = 0; g < 2; ++g)
#pragma unroll
                for (int u = 0; u < 3; ++u) {
                    const f32x2 a = {bflo(qa[g][u]), bfhi(qa[g][u])}, b = {bflo(qb[g][u]), bfhi(qb[g][u])};
                    const f32x2 o0 = a * fc[0][u] - b * fs[0][u], o1 = b * fc[1][u] + a * fs[1][u];
                    const unsigned l0 = pk_bf16(f32x2{o0[0], o1[0]}), l1 = pk_bf16(f32x2{o0[1], o1[1]});
                    unsigned* dst = s_q + (16 * g + 4 * it + r) * DH + 6 * c + 2 * u;
                    *reinterpret_cast<u32x2*>(dst) = u32x2{l0, l1};
                    *reinterpret_cast<u32x2*>(dst + HALF) = u32x2{__builtin_amdgcn_alignbit(l0, l0, 16) ^ 0x80000000u, __builtin_amdgcn_alignbit(l1, l1, 16) ^ 0x80000000u};
                }
        }
        asm volatile("" :: "v"(xw));
        __builtin_amdgcn_s_setprio(0);
        unsigned origin = tsched << 8;
        if (lane < 8) { s_done[lane] = 0; s_sync[lane] = lane < 3 ? origin : lane < 4 ? NB : tag; }
        PIN();
        VMCNT(0);
        BARRIER();
        const int mh = lane >> 5, mc = lane & 31;
        const unsigned rtag = __builtin_amdgcn_readlane(tag, 0) | __builtin_amdgcn_readlane(tag, 1) >> 16;
        constexpr int RPX = 2;
        constexpr int RSH = 9;
        constexpr unsigned RDLY = 200;
        constexpr int UP = 984, DN = 2000;
        const int rj = ((bid >> 3) + 5 * (bid & 7)) & 31;
        const int rid = rj % (32 / RPX) == 0 ? rj / (32 / RPX) * 8 + (bid & 7) : -1;
        unsigned wround = now32() >> RSH, rround = wround, tick = 0, target = origin;
        bool synced = false;
        int drift = 0;
        const unsigned round0 = rround;
        int i = 0;
        unsigned sct = tstart;
        auto kept_now = [&](unsigned now) {
            const u32x2 a = *(vlds_u32x2*)(s_act + 2 * (lane & 7));
            const int late = max((int)(a[0] - origin), (int)((now << 8) - a[1] - origin));
            return origin + (__builtin_amdgcn_readfirstlane(row16_sum(late)) >> 4);
        };
        auto steer = [&](unsigned now) {
            if ((now >> 5) != tick) {
                tick = now >> 5;
                target += drift;
                origin += max(-DN, min(UP + drift, (int)(target - origin)));
                if (!synced) {
                    origin += max(0, (int)(kept_now(now) - origin) - 2000);
                    target = origin;
                }
                if (lane == 0) *(vlds_u32*)(s_sync + 2) = origin;
                PIN();
            }
        };
        auto finished = [&]() {
            const u32x4 a = *(vlds_u32x4*)s_done, b = *(vlds_u32x4*)(s_done + 4);
            return __builtin_amdgcn_readfirstlane(min(min(min(a[0], a[1]), min(a[2], a[3])), min(min(b[0], b[1]), min(b[2], b[3]))));
        };
        while (i < NI - 2) {
            const unsigned now = now32();
            if ((now >> RSH) != wround) {
                wround = now >> RSH;
                if (rid >= 0 && (now & ((1u << RSH) - 1)) < 48)
                    __builtin_amdgcn_raw_buffer_store_b64(u32x2{kept_now(now), rtag + wround}, rw_, lane == 0 ? rid * 8 : OOB, WS_REP + (wround & 255) * 512, DEV);
                PIN();
                continue;
            }
            if ((int)(((now - RDLY) >> RSH) - rround) > 0) {
                rround = (now - RDLY) >> RSH;
                const u32x2 x = __builtin_amdgcn_raw_buffer_load_b64(rw_, c * 8, WS_REP + (rround & 255) * 512, DEV);
                const int d = (int)(x[0] - origin);
                const bool ok = x[1] == rtag + rround && d < (32 << 16) && d > -(32 << 16);
                const int n = __builtin_popcountll(__builtin_amdgcn_ballot_w64(ok));
                if (n >= 48) {
                    const unsigned nt = origin + __builtin_amdgcn_readfirstlane((int)(row16_allsum(ok ? (float)d : 0.f) * 4.f / (float)n)) - pace.bias;
                    if (synced && rround - round0 > 4) drift = max(0, min(drift + ((max(-1024, min((int)(nt - target) + 6 * drift, 5120)) - (rround - round0 < 13 ? 768 : 0)) >> 5), 800));
                    target = nt + 6 * drift;
                    synced = true;
                    const int lost = min((int)(kept_now(now32()) - origin) - pace.bias, (int)(target - origin));
                    if (lost > 0) origin += lost;
                }
                PIN();
                continue;
            }
            steer(now);
            if (sci <= NI && now - sct >= 100) {
                VMCNT(0);
                if (lane == 0) *(vlds_u32*)(s_sync + 3) = sci;
                if (sci < NI) gather();
                ++sci; sct = now;
                PIN();
                continue;
            }
            if (finished() <= (unsigned)i) { __builtin_amdgcn_s_sleep(2); continue; }
            PIN();
            if (i == 0) {
                f32x4 ml[4], f;
#pragma unroll
                for (int w = 0; w < 4; ++w) ml[w] = *reinterpret_cast<const f32x4*>(s_w + (mc * 4 + w) * 20 + 16);
                const float M = fmaxf(fmaxf(ml[0][0], ml[1][0]), fmaxf(ml[2][0], ml[3][0]));
                float L = 0.f, zs = 0.f;
#pragma unroll
                for (int w = 0; w < 4; ++w) { f[w] = __builtin_amdgcn_exp2f(ml[w][0] - M); L += f[w] * ml[w][1]; zs += f[w] * ml[w][2]; }
                const float inv = 1.f / L;
                *reinterpret_cast<f32x4*>(s_f + mc * 8) = f * inv;
                *reinterpret_cast<f32x2*>(s_f + mc * 8 + 4) = f32x2{zs * inv, M + __builtin_amdgcn_logf(L)};
                PIN();
                if (lane == 0) *(vlds_u32*)(s_sync + 3) = NI + 1;
                PIN();
            }
            const f32x2 zl = *reinterpret_cast<const f32x2*>(s_f + (2 * i + mh) * 8 + 4);
            const u32x4 v = merge(s_part + (mh * 4 * NI + i) * DH + mc * 6, *reinterpret_cast<const f32x4*>(s_f + (2 * i + mh) * 8), zl[0], last_word(tag, zl[1], mc));
            PIN();
            *reinterpret_cast<u32x4*>(s_part + (mh * 4 * NI + i) * DH + mc * 4) = v;
            PIN();
            ++i;
        }
        u32x4 v[NI - 1];
#pragma unroll
        for (int i = 0; i < NI - 2; ++i) v[i] = *reinterpret_cast<const u32x4*>(s_part + (mh * 4 * NI + i) * DH + mc * 4);
        f32x4 f = *reinterpret_cast<const f32x4*>(s_f + (2 * (NI - 2) + mh) * 8);
        f32x2 zl = *reinterpret_cast<const f32x2*>(s_f + (2 * (NI - 2) + mh) * 8 + 4);
        PIN();
#pragma unroll
        for (int i = 0; i < NI - 2; ++i) asm volatile("" : "+v"(v[i]));
        asm volatile("" : "+v"(f), "+v"(zl));
        v[NI - 2] = v[0];
        bool have = false, sent = false;
        while (!(have && sent)) {
            const unsigned now = now32();
            if (!have) {
                steer(now);
                if (finished() > NI - 2) {
                    PIN();
                    v[NI - 2] = merge(s_part + (mh * 4 * NI + NI - 2) * DH + mc * 6, f, zl[0], last_word(tag, zl[1], mc));
                    have = true;
                }
            }
            if (!sent && (int)((now << 8) - origin) >= pace.pub) {
#pragma unroll
                for (int i = 0; i < NI - 2; ++i) publish(v[i], i, piece_off(mh, mc));
                sent = true;
            }
            if (!(have && sent)) __builtin_amdgcn_s_sleep(1);
            PIN();
        }
        publish(v[NI - 2], NI - 2, piece_off(mh, mc));
        asm volatile(".Lkv_end:");
        return;
    }

    const int ph = rw + 4 * bid + 1024 * hp;
    unsigned off = pace.start + ((pace.step * ph) >> 11);
    unsigned origin = tsched << 8, kept = 0;
    unsigned step = pace.step;
    auto pace_wait = [&]() {
        const unsigned tgt = __builtin_amdgcn_readfirstlane(origin) + off;
        unsigned now = now32() << 8;
        kept = ((int)(now - tgt) > 0 ? now : tgt) - off;
        while ((int)(tgt - now) > 0) { __builtin_amdgcn_s_sleep(1); now = now32() << 8; }
        off += step;
    };
    auto report = [&]() {
        if (lane == 0) *(vlds_u32x2*)(s_act + 2 * wave) = u32x2{kept, off};
        origin = *(vlds_u32*)(s_sync + 2);
        PIN();
    };

    auto pre_sc = [&]() {
        const unsigned junk = LDSA(s_part) + (3 * NI * DH - 256) * 4;
        const gptr a = pick(grow >> 4, ksc, kzp, vsc, vzp) + ((tid >> 5) * SKV + (grow & ~63 ^ srow(lane >> 3 & 3))) * 2 + (lane & 7) * 16;
        asm volatile("s_mov_b32 m0, %2\n\ts_nop 0\n\tglobal_load_lds_dwordx4 %0, off\n\tglobal_load_lds_dwordx4 %1, off" :: "v"(a), "v"(a + 32 * SKV), "s"(junk) : "memory");
        PIN();
    };
    const auto rc = __builtin_amdgcn_make_buffer_rsrc((void*)k_cos, 0, CSB, 0x00020000);
    const auto rs = __builtin_amdgcn_make_buffer_rsrc((void*)k_sin, 0, CSB, 0x00020000);
    u32x2 raw[2][6];
    auto load_cs = [&](int t) {
        const unsigned rb = t == 0 ? rb0 : (row0 ^ RT * hp ^ RS) * 384u;
        raw[t][0] = __builtin_amdgcn_raw_buffer_load_b64(rc, co, rb, NT);
        raw[t][1] = __builtin_amdgcn_raw_buffer_load_b64(rc, co + 128, rb, NT);
        raw[t][2] = __builtin_amdgcn_raw_buffer_load_b64(rc, co + 256, rb, NT);
        raw[t][3] = __builtin_amdgcn_raw_buffer_load_b64(rs, co + 192, rb, NT);
        raw[t][4] = __builtin_amdgcn_raw_buffer_load_b64(rs, so1, rb, NT);
        raw[t][5] = __builtin_amdgcn_raw_buffer_load_b64(rs, co + 64, rb, NT);
        PIN();
    };
    u32x4 tab[4][3];
    auto pack_cs = [&](int t) {
#pragma unroll
        for (int j = 0; j < 3; ++j) {
#pragma unroll
            for (int k = 0; k < 2; ++k) {
                tab[t][j][2 * k] = __builtin_amdgcn_perm(raw[t][3 + j][k], raw[t][j][k], 0x05040100);
                tab[t][j][2 * k + 1] = __builtin_amdgcn_perm(raw[t][3 + j][k], raw[t][j][k], 0x07060302);
            }
            *reinterpret_cast<u32x4*>(s_part + (wave ^ 4) * (NI * DH) + ((t * 3 + j) * 64 + lane) * 4) = tab[t][j];
        }
        PIN();
    };
    const auto rk = __builtin_amdgcn_make_buffer_rsrc((void*)qk, 0, KVB, 0x00020000);
    const auto rv = __builtin_amdgcn_make_buffer_rsrc((void*)qv, 0, KVB, 0x00020000);
    const unsigned voff = r * ROWB + c * 16;
    const unsigned woff = hp * HEADB;
    auto toff = [&](int t) { return (unsigned)(row0 ^ srow(t)) * ROWB; };
    u32x4 ring[4][3];
    auto ld = [&](int slot, int j, __amdgpu_buffer_rsrc_t rr, unsigned off) {
        ring[slot][j] = __builtin_amdgcn_raw_buffer_load_b128(rr, voff + j * 256, off, NT);
        PIN();
    };
    auto ld_tile = [&](int slot, __amdgpu_buffer_rsrc_t rr, unsigned off) { ld(slot, 0, rr, off); ld(slot, 1, rr, off); ld(slot, 2, rr, off); };
    unsigned post = 0;
    auto enter = [&](int i) {
        if (i == 9 || i == 10) {
            const unsigned hs = pace.step >> 1, z = (unsigned)pace.offset >> 16;
            off += i == 10 ? post : hp * 3 * hs;
            step = i == 10 ? pace.step : hs + hp * z;
            post = __builtin_amdgcn_readfirstlane(i == 10 ? 0 : hp ? hs : 4 * (hs + z));
            if (lane == 0) *(vlds_u32x2*)(s_act + 2 * wave) = u32x2{kept, off};
            PIN();
        }
    };
    auto refill = [&](int slot, int j, __amdgpu_buffer_rsrc_t rr, unsigned goff) {
        if (j == 0) pace_wait();
        if (j == 0) asm volatile("s_cmp_lg_u32 %0, 0\n\ts_waitcnt vmcnt(3)\n\ts_cbranch_scc1 1\n\ts_waitcnt vmcnt(2)" :: "s"(post) : "scc", "memory");
        else asm volatile("s_cmp_lg_u32 %0, 0\n\ts_cbranch_scc1 1\n\ts_waitcnt vmcnt(2)" :: "s"(post) : "scc", "memory");
        ld(slot, j, rr, goff);
        if (j == 0) { const unsigned t = (now32() << 8) - 1024 - (off - step); if ((int)(t - kept) > 0) kept = t; }
        if (j == 2) report();
    };
    auto k0 = [&](int t) { pace_wait(); ld_tile(t, rk, woff + toff(t)); };
    load_cs(0);
    pace_wait(); load_cs(1);
    pack_cs(0);
    pace_wait(); pre_sc(); off += (pace.offset & 0xffff) - step;
    k0(0); pack_cs(1);
    asm volatile("" :: "v"(xw));
    k0(1);
    VMCNT(3); k0(2);
    VMCNT(3);
    if (hp) { if (lane == 0) *(vlds_u32x2*)(s_act + 2 * wave) = u32x2{kept, off}; BARRIER(); }
    k0(3);
    if (lane == 0) *(vlds_u32x2*)(s_act + 2 * wave) = u32x2{kept, off};
    if (!hp) BARRIER();
    origin = *(vlds_u32*)(s_sync + 2);

    u32x4 qp[3];
    auto load_q = [&](int h, int j) { qp[j] = *reinterpret_cast<const u32x4*>(s_q + h * DH + 64 * j + 4 * c); };
    load_q(hp, 0); load_q(hp, 1); load_q(hp, 2);
#pragma unroll
    for (int t = 0; t < 2; ++t)
#pragma unroll
        for (int j = 0; j < 3; ++j) tab[2 + t][j] = *reinterpret_cast<const u32x4*>(s_part + wave * (NI * DH) + ((t * 3 + j) * 64 + lane) * 4);
    if (hp) {
#pragma unroll
        for (int t = 0; t < 2; ++t)
#pragma unroll
            for (int j = 0; j < 3; ++j)
#pragma unroll
                for (int k = 0; k < 4; ++k) asm volatile("v_swap_b32 %0, %1" : "+v"(tab[t][j][k]), "+v"(tab[2 + t][j][k]));
    }

    unsigned ready = NB;
    for (int i = 0; i < NI; ++i) {
        const bool last = i + 1 == NI;
        enter(i);
        const int h = 2 * i + hp;
        const unsigned hnext = (i + 1) * (2 * HEADB) + woff;
        const auto rn = __builtin_amdgcn_make_buffer_rsrc((void*)(last ? qv : qk), 0, KVB, 0x00020000);
        float* scp = s_w + (h * 4 + rw) * 20;
        f32x4 sc[4];
        while (__builtin_amdgcn_readfirstlane(ready) <= i) { __builtin_amdgcn_s_sleep(1); ready = *(vlds_u32*)(s_sync + 3); }
        PIN();
#pragma unroll
        for (int t = 0; t < 4; ++t)
#pragma unroll
            for (int a = 0; a < 4; ++a) sc[t][a] = bflo(s_sc[h * 256 + t * 64 + a * 16 + rw * 4 + r]);
        float s[4];
#pragma unroll
        for (int t = 0; t < 4; ++t) {
            f32x2 dot, wsum;
#pragma unroll
            for (int j = 0; j < 3; ++j) {
                f32x2 w01, w23, k01, k23;
                dot_cvt(qp[j], tab[t][j], ring[t][j], w01, w23, k01, k23);
                if (j == 0) { wsum = w01 + w23; dot = k01 * w01; } else { wsum += w01; wsum += w23; dot += k01 * w01; }
                dot += k23 * w23;
                asm volatile("" : "+v"(dot), "+v"(wsum));
                PIN();
                if (t == 3) load_q(last ? h : h + 2, j);
                if (t == 3 && j == 0) ready = *(vlds_u32*)(s_sync + 3);
                refill(t, j, rn, (last ? woff : hnext) + toff(t));
            }
            const float a = (dot[0] + dot[1]) - sc[t][1] * (wsum[0] + wsum[1]);
            s[t] = row16_allsum(a) * (sc[t][0] * SCALE_LOG2);
        }
        const float mx = xrow_max(fmaxf(fmaxf(s[0], s[1]), fmaxf(s[2], s[3])));
        float l = 0.f, z = 0.f;
#pragma unroll
        for (int t = 0; t < 4; ++t) {
            const float p = __builtin_amdgcn_exp2f(s[t] - mx);
            l += p;
            s[t] = p * sc[t][2];
            z += s[t] * sc[t][3];
        }
        l = xrow_sum(l);
        z = xrow_sum(z);
        if (c == 0) {
            *reinterpret_cast<f32x4*>(scp + r * 4) = f32x4{s[0], s[1], s[2], s[3]};
            *reinterpret_cast<f32x4*>(scp + 16) = f32x4{mx, l, z, 0.f};
        }
        PIN();
    }

    const float* pwp = s_w + (hp * 4 + rw) * 20 + r * 4;
    f32x4 pw = *reinterpret_cast<const f32x4*>(pwp);
    unsigned tag, lw, lo;
    f32x4 lf;
    f32x2 lz;
    unsigned lp = (hp * 4 * NI + NI - 1) * DH + (lane & 31) * 6;
    f32x2 acc[3][2];
    auto v_acc = [&](int t, int j) {
        const f32x2 p2 = {pw[t], pw[t]};
        const u32x4 vv = ring[t][j];
        const f32x2 v01 = {(float)(int)vv[0], (float)(int)vv[1]}, v23 = {(float)(int)vv[2], (float)(int)vv[3]};
        if (t == 0) { acc[j][0] = p2 * v01; acc[j][1] = p2 * v23; }
        else { acc[j][0] += p2 * v01; acc[j][1] += p2 * v23; }
        asm volatile("" : "+v"(acc[j][0]), "+v"(acc[j][1]));
        PIN();
    };
    auto v_fold = [&](int i, int j) {
        const float x = swap32_add(acc[j][0][0], acc[j][1][0]);
        const float y = swap32_add(acc[j][0][1], acc[j][1][1]);
        s_part[(wave * NI + i) * DH + 64 * j + 4 * c + r] = swap16_add(x, y);
    };
    auto v_done = [&](int i) {
        PIN();
        if (lane == 0) *(vlds_u32*)(s_done + wave) = i + 1;
        PIN();
    };
    constexpr int LS = 2;
#pragma unroll
    for (int t = 0; t < LS; ++t)
#pragma unroll
        for (int j = 0; j < 3; ++j) v_acc(t, j);
    for (int i = 0; i < NI - 1; ++i) {
        const unsigned hnext = (i + 1) * (2 * HEADB) + woff;
        enter(i);
#pragma unroll
        for (int k = 0; k < 4; ++k) {
            const int t = (k + LS) & 3;
#pragma unroll
            for (int j = 0; j < 3; ++j) {
                v_acc(t, j);
                if (t == 3 && j == 2) pw = *reinterpret_cast<const f32x4*>(pwp + (i + 1) * 160);
                refill(k, j, rv, hnext + toff(k));
            }
            if (t == 3) { v_fold(i, 0); v_fold(i, 1); v_fold(i, 2); v_done(i); }
        }
    }
    asm volatile("s_branch .Lkv_tail_%=\n\t.p2align 12\n.Lkv_tail_%=:" ::: "memory");
    while (__builtin_amdgcn_readfirstlane(*(vlds_u32*)(s_sync + 3)) <= NI) __builtin_amdgcn_s_sleep(1);
    PIN();
    tag = s_sync[4 + (lane & 3)];
    lf = *reinterpret_cast<const f32x4*>(s_f + (2 * (NI - 1) + hp) * 8);
    lz = *reinterpret_cast<const f32x2*>(s_f + (2 * (NI - 1) + hp) * 8 + 4);
    origin = *(vlds_u32*)(s_sync + 2);
    lw = last_word(tag, lz[1], lane & 31);
    lo = lane < 32 ? piece_off(hp, lane & 31) : OOB;
    unsigned la[5], ld_ = LDSA(s_done) + 16 * hp, lq = LDSA(s_done) + 4 * wave, lv = NI, lso = WS_PART + (NI - 1) * GSTR + m * 64;
#pragma unroll
    for (int w = 0; w < 5; ++w) la[w] = LDSA(s_part) + lp * 4 + (w < 4 ? w * NI * DH * 4 : 16);
    u32x4 rws = {(unsigned)kb[3], (unsigned)(kb[3] >> 32) & 0xffffu, WS_BYTES, 0x00020000};
    f32x2 lf01 = {lf[0], lf[1]}, lf23 = {lf[2], lf[3]};
    asm volatile("" : "+v"(tag), "+v"(lf01), "+v"(lf23), "+v"(lz), "+v"(lw), "+v"(lo), "+v"(origin), "+v"(la[0]), "+v"(la[1]), "+v"(la[2]), "+v"(la[3]), "+v"(la[4]),
                      "+v"(ld_), "+v"(lq), "+v"(lv), "+s"(lso), "+s"(rws));
    PIN();
#pragma unroll
    for (int t = LS; t < 4; ++t)
#pragma unroll
        for (int j = 0; j < 3; ++j) { v_acc(t, j); if (t == 3) { v_fold(NI - 1, j); PIN(); } }
    {
#define FMA0(a) "v_pk_fma_f32 " a ", %7, " a ", %9 op_sel_hi:[0,1,0] neg_lo:[0,0,1] neg_hi:[0,0,1]\n\t"
#define FMA(a, f, b, hi) "v_pk_fma_f32 " a ", " f ", " b ", " a " op_sel:[" hi ",0,0] op_sel_hi:[" hi ",1,1]\n\t"
        asm volatile(
            "ds_write_b32 %5, %6\n\tds_read_b128 v[96:99], %14\n\t"
            "ds_read2_b64 v[64:67], %0 offset1:1\n\tds_read2_b64 v[68:71], %1 offset1:1\n\tds_read2st64_b64 v[80:83], %4 offset1:24\n\t"
            "ds_read2_b64 v[72:75], %2 offset1:1\n\tds_read2_b64 v[76:79], %3 offset1:1\n\tds_read2st64_b64 v[84:87], %4 offset0:48 offset1:72\n\t"
            "v_mov_b32 v91, %10\n\t"
            "s_waitcnt lgkmcnt(6)\n\t"
            "v_and_b32 v96, v96, v97\n\tv_bitop3_b32 v96, v96, v98, v99 bitop3:0x80\n\tv_cmp_ne_u32 vcc, v96, %6\n\t"
            "s_cbranch_vccnz .Lkv_skip_%=\n\t"
            "s_waitcnt lgkmcnt(3)\n\t"
            FMA0("v[64:65]") FMA0("v[66:67]") FMA0("v[80:81]")
            FMA("v[64:65]", "%7", "v[68:69]", "1") FMA("v[66:67]", "%7", "v[70:71]", "1") FMA("v[80:81]", "%7", "v[82:83]", "1")
            "s_waitcnt lgkmcnt(0)\n\t"
            FMA("v[64:65]", "%8", "v[72:73]", "0") FMA("v[66:67]", "%8", "v[74:75]", "0") FMA("v[80:81]", "%8", "v[84:85]", "0")
            FMA("v[64:65]", "%8", "v[76:77]", "1") FMA("v[66:67]", "%8", "v[78:79]", "1") FMA("v[80:81]", "%8", "v[86:87]", "1")
            "v_cvt_pk_bf16_f32 v88, v64, v65\n\tv_cvt_pk_bf16_f32 v89, v66, v67\n\tv_cvt_pk_bf16_f32 v90, v80, v81\n\t"
            "buffer_store_dwordx4 v[88:91], %11, %12, %13 offen sc1\n"
            ".Lkv_skip_%=:\n\ts_waitcnt lgkmcnt(0)"
            :: "v"(la[0]), "v"(la[1]), "v"(la[2]), "v"(la[3]), "v"(la[4]), "v"(lq), "v"(lv), "v"(lf01), "v"(lf23), "v"(lz), "v"(lw), "v"(lo), "s"(rws), "s"(lso), "v"(ld_)
            : "v64", "v65", "v66", "v67", "v68", "v69", "v70", "v71", "v72", "v73", "v74", "v75", "v76", "v77", "v78", "v79", "v80", "v81", "v82", "v83",
              "v84", "v85", "v86", "v87", "v88", "v89", "v90", "v91", "v96", "v97", "v98", "v99", "vcc", "memory");
        asm volatile("s_nop 0" ::: "v88", "v89", "v90", "v91");
        PIN();
    }
    {
        unsigned inbox = WS_PART + (bid >> 4) * GSTR + (bid & 15) * UNITB, obase = bid * 48;
        unsigned xo = tid * 16, yo = tid * 16 + 8192, sel = lane & 1 ? 0x01000504u : 0x05040100u;
        unsigned fl = 3 * NI;
        u32x4 rout = {(unsigned)kb[4], (unsigned)(kb[4] >> 32) & 0xffffu, NH * DH * 2, 0x00020000};
        typedef __attribute__((address_space(3))) float lds_f;
        lds_f* wo = (lds_f*)s_red + wave * 28 + (r < 3 ? (c & 3) * 6 + r * 2 : 24);
        lds_f* fo = (lds_f*)s_red + (lane & 7) * 28 + 24;
        lds_f* go = (lds_f*)s_red + (lane & 7) * 28 + min(lane >> 3, 5) * 4;
        vlds_u32* dw = (vlds_u32*)(s_done + wave);
        vlds_u32* dr = (vlds_u32*)(s_done + (lane & 7));
        unsigned oo = (lane & 7) == 0 && lane < 48 ? lane : OOB, eo = bid == 0 && lane < 4 ? lane * 2 : OOB;
        asm volatile("" : "+s"(inbox), "+s"(obase), "+v"(xo), "+v"(yo), "+v"(sel), "+v"(fl), "+s"(rout), "+v"(wo), "+v"(fo), "+v"(go), "+v"(oo), "+v"(eo), "+v"(dw), "+v"(dr));
        const unsigned o = __builtin_amdgcn_readfirstlane(origin) - (15 - (bid >> 4)) * 850;
        while ((int)((now32() << 8) - o) < pace.rd) __builtin_amdgcn_s_sleep(1);
        PIN();
        u32x4 x, y;
        unsigned same;
        do {
            x = __builtin_amdgcn_raw_buffer_load_b128(rw_, xo, inbox, DEV | NT);
            y = __builtin_amdgcn_raw_buffer_load_b128(rw_, yo, inbox, DEV | NT);
            same = now32() - tstart > SPIN_LIMIT ? ~0u : 0xffffu;
            asm volatile("" : "+s"(same));
            PIN();
        } while (__builtin_amdgcn_ballot_w64(((x[3] ^ tag) | (y[3] ^ tag)) > same) != 0);
#define DPP(c) " " c " row_mask:0xf bank_mask:0xf\n\t"
        asm volatile(
            "v_mov_b32_dpp v54, %3" DPP("quad_perm:[1,0,3,2]") "v_mov_b32_dpp v55, %7" DPP("quad_perm:[1,0,3,2]")
            "v_perm_b32 v54, v54, %3, %8\n\tv_perm_b32 v55, v55, %7, %8\n\t"
            "v_max_f32 v56, v54, v55\n\t"
            "v_lshlrev_b32 v40, 16, %0\n\tv_and_b32 v41, 0xffff0000, %0\n\t"
            "v_max_f32_dpp v56, v56, v56" DPP("row_ror:8")
            "v_lshlrev_b32 v42, 16, %1\n\tv_and_b32 v43, 0xffff0000, %1\n\t"
            "v_max_f32_dpp v56, v56, v56" DPP("row_ror:4")
            "v_lshlrev_b32 v44, 16, %2\n\tv_and_b32 v45, 0xffff0000, %2\n\t"
            "v_max_f32_dpp v56, v56, v56 row_bcast:15 row_mask:0xa bank_mask:0xf\n\t"
            "v_lshlrev_b32 v48, 16, %4\n\tv_and_b32 v49, 0xffff0000, %4\n\t"
            "v_max_f32_dpp v56, v56, v56 row_bcast:31 row_mask:0xc bank_mask:0xf\n\t"
            "v_lshlrev_b32 v50, 16, %5\n\t"
            "v_readlane_b32 vcc_lo, v56, 63\n\t"
            "v_and_b32 v51, 0xffff0000, %5\n\tv_lshlrev_b32 v52, 16, %6\n\t"
            "v_subrev_f32 v54, vcc_lo, v54\n\tv_subrev_f32 v55, vcc_lo, v55\n\t"
            "v_exp_f32 v54, v54\n\tv_exp_f32 v55, v55\n\t"
            "v_and_b32 v53, 0xffff0000, %6\n\t"
            "v_pk_mul_f32 v[40:41], v[40:41], v[54:55] op_sel_hi:[1,0]\n\t"
            "v_pk_mul_f32 v[44:45], v[44:45], v[54:55] op_sel_hi:[1,0]\n\t"
            "v_pk_mul_f32 v[42:43], v[42:43], v[54:55] op_sel_hi:[1,0]\n\t"
            "v_pk_fma_f32 v[40:41], v[48:49], v[54:55], v[40:41] op_sel:[0,1,0]\n\t"
            "v_pk_fma_f32 v[44:45], v[52:53], v[54:55], v[44:45] op_sel:[0,1,0]\n\t"
            "v_pk_fma_f32 v[42:43], v[50:51], v[54:55], v[42:43] op_sel:[0,1,0]\n\t"
            "v_add_f32 v46, v54, v55\n\t"
            "v_ldexp_f32 v47, vcc_lo, -4\n\t"
            "v_permlane32_swap_b32 v40, v44\n\tv_permlane32_swap_b32 v41, v45\n\tv_permlane32_swap_b32 v42, v46\n\tv_permlane32_swap_b32 v43, v47\n\t"
            "v_pk_add_f32 v[40:41], v[40:41], v[44:45]\n\tv_pk_add_f32 v[42:43], v[42:43], v[46:47]\n\t"
            "s_nop 1\n\t"
            "v_permlane16_swap_b32 v40, v42\n\tv_permlane16_swap_b32 v41, v43\n\t"
            "v_add_f32 v40, v40, v42\n\tv_add_f32 v41, v41, v43\n\t"
            "s_nop 0\n\t"
            "v_add_f32_dpp v40, v40, v40" DPP("row_ror:8") "v_add_f32_dpp v41, v41, v41" DPP("row_ror:8")
            "s_nop 0\n\t"
            "v_add_f32_dpp v40, v40, v40" DPP("row_ror:4") "v_add_f32_dpp v41, v41, v41" DPP("row_ror:4")
            "ds_write_b64 %9, v[40:41]\n\tds_write_b32 %10, %11"
            :: "v"(x[0]), "v"(x[1]), "v"(x[2]), "v"(x[3]), "v"(y[0]), "v"(y[1]), "v"(y[2]), "v"(y[3]), "v"(sel), "v"(wo), "v"(dw), "v"(fl)
            : "v40", "v41", "v42", "v43", "v44", "v45", "v46", "v47", "v48", "v49", "v50", "v51", "v52", "v53", "v54", "v55", "v56", "vcc", "memory");
        PIN();
        const unsigned fin = *dr;
        PIN();
        f32x2 sm = *(__attribute__((address_space(3))) f32x2*)fo;
        const f32x4 ov = *(__attribute__((address_space(3))) f32x4*)go;
        f32x2 o01 = {ov[0], ov[1]}, o23 = {ov[2], ov[3]};
        asm volatile("" : "+v"(sm), "+v"(o01), "+v"(o23));
        if (__builtin_amdgcn_ballot_w64(fin != 3 * NI) == 0) {
#define ADD4(c) "v_add_f32_dpp v40, v40, v40" DPP(c) "v_add_f32_dpp v41, v41, v41" DPP(c) "v_add_f32_dpp v42, v42, v42" DPP(c) "v_add_f32_dpp v43, v43, v43" DPP(c)
            asm volatile(
                "v_max_f32_dpp v46, %3, %3" DPP("quad_perm:[1,0,3,2]") "s_nop 1\n\t"
                "v_max_f32_dpp v46, v46, v46" DPP("quad_perm:[2,3,0,1]") "s_nop 1\n\t"
                "v_max_f32_dpp v46, v46, v46" DPP("row_half_mirror")
                "v_sub_f32 v46, %3, v46\n\tv_exp_f32 v46, v46\n\ts_nop 0\n\t"
                "v_mul_f32 v44, %2, v46\n\t"
                "v_pk_mul_f32 v[40:41], %4, v[46:47] op_sel_hi:[1,0]\n\tv_pk_mul_f32 v[42:43], %5, v[46:47] op_sel_hi:[1,0]\n\t"
                "v_add_f32_dpp v44, v44, v44" DPP("quad_perm:[1,0,3,2]") ADD4("quad_perm:[1,0,3,2]")
                "v_add_f32_dpp v44, v44, v44" DPP("quad_perm:[2,3,0,1]") ADD4("quad_perm:[2,3,0,1]")
                "v_add_f32_dpp v44, v44, v44" DPP("row_half_mirror") "v_rcp_f32 v44, v44\n\t" ADD4("row_half_mirror")
                "v_pk_mul_f32 v[40:41], v[40:41], v[44:45] op_sel_hi:[1,0]\n\tv_pk_mul_f32 v[42:43], v[42:43], v[44:45] op_sel_hi:[1,0]\n\t"
                "v_cvt_pk_bf16_f32 v40, v40, v41\n\tv_cvt_pk_bf16_f32 v41, v42, v43\n\t"
                "buffer_store_dwordx2 v[40:41], %0, %1, %6 offen"
                :: "v"(oo), "s"(rout), "v"(sm[0]), "v"(sm[1]), "v"(o01), "v"(o23), "s"(obase)
                : "v40", "v41", "v42", "v43", "v44", "v45", "v46", "v47", "memory");
            if (bid == 0) __builtin_amdgcn_raw_buffer_store_b16((unsigned short)(tag >> 16), rw_, eo, WS_EPOCH, DEV);
        }
    }
    __builtin_amdgcn_endpgm();
}

extern "C" void kernel_launch(void* const* d_in, const int* in_sizes, int n_in,
                              void* d_out, int out_size, void* d_ws, size_t ws_size,
                              hipStream_t stream) {
    const float t_us = 100.6f, start_us = 0.2f, seam_us = 0.1f, scales_us = 1.15f;
    const float end_us = start_us + scales_us + 2 * seam_us + t_us * (129.f / 128.f);
    Pace pace;
    pace.step = (unsigned)(t_us * 25600.f / 128.f);
    pace.start = (int)(start_us * 25600.f);
    pace.offset = (int)(scales_us * 25600.f) | (int)(seam_us * 6400.f) << 16;
    pace.bias = (int)(0.052f * 25600.f);
    pace.rd = (int)((end_us + 1.58f) * 25600.f);
    pace.pub = (int)((end_us + 0.4f) * 25600.f);
    kv_sweep_kernel<<<NWG, 576, 0, stream>>>(d_in[0], d_in[1], d_in[2], d_in[3], d_in[4], d_in[5], d_in[6], d_in[7],
        d_in[8], d_in[9], d_in[10], d_ws, d_out, pace);
}
```

```cpp
#include <hip/hip_runtime.h>
#include <cstdint>


using u32x4 = __attribute__((ext_vector_type(4))) unsigned;
using u32x3 = __attribute__((ext_vector_type(3))) unsigned;
using u32x2 = __attribute__((ext_vector_type(2))) unsigned;
using u64x8 = __attribute__((ext_vector_type(8))) unsigned long long;
using f32x2 = __attribute__((ext_vector_type(2))) float;
using f32x4 = __attribute__((ext_vector_type(4))) float;
using bf16x2 = __attribute__((ext_vector_type(2))) __bf16;

constexpr int DH = 192;
constexpr int HALF = 96;
constexpr int NH = 32;
constexpr int SKV = 16384;
constexpr int NWG = 256;
constexpr int NI = 16;
constexpr int NB = 2;
constexpr unsigned ROWB = DH * 4;
constexpr unsigned HEADB = SKV * ROWB;
constexpr unsigned KVB = NH * HEADB;
constexpr unsigned OOB = 0x80000000u;
constexpr int DEV = 16, NT = 2;
constexpr float SCALE_LOG2 = 1.4426950408889634f * 0.07216878364870322f;
constexpr unsigned SPIN_LIMIT = 20000000u;

constexpr unsigned BLK = 8192;
constexpr int NW = 4;
constexpr unsigned WS_EPOCH = 1u << 20;
constexpr unsigned WS_SIDE = WS_EPOCH + 128;
constexpr unsigned WS_REP = WS_EPOCH + 256;
constexpr unsigned WS_PROBE = 3u << 19;
constexpr unsigned WS_BYTES = 8u << 20;
__device__ __forceinline__ unsigned pair_off(int i) { return (i >> 2 << 21) + (i & 3) * (32 * BLK); }

__device__ __forceinline__ float bflo(unsigned u) { return __uint_as_float(u << 16); }
__device__ __forceinline__ float bfhi(unsigned u) { return __uint_as_float(u & 0xffff0000u); }
__device__ __forceinline__ unsigned pk_bf16(f32x2 a) { return __builtin_bit_cast(unsigned, __builtin_convertvector(a, bf16x2)); }
__device__ __forceinline__ void dot_cvt(const u32x4 q, const u32x4 t, const u32x4 ki, f32x2& w01, f32x2& w23, f32x2& k01, f32x2& k23) {
    float w0, w1, w2, w3;
    float k0 = __uint_as_float(ki[0]), k1 = __uint_as_float(ki[1]), k2 = __uint_as_float(ki[2]), k3 = __uint_as_float(ki[3]);
    asm("v_dot2_f32_bf16 %0, %8, %12, 0\n\tv_dot2_f32_bf16 %1, %9, %13, 0\n\tv_dot2_f32_bf16 %2, %10, %14, 0\n\tv_dot2_f32_bf16 %3, %11, %15, 0\n\t"
        "v_cvt_f32_i32 %4, %4\n\tv_cvt_f32_i32 %5, %5\n\tv_cvt_f32_i32 %6, %6\n\tv_cvt_f32_i32 %7, %7"
        : "=&v"(w0), "=&v"(w1), "=&v"(w2), "=&v"(w3), "+v"(k0), "+v"(k1), "+v"(k2), "+v"(k3)
        : "v"(q[0]), "v"(q[1]), "v"(q[2]), "v"(q[3]), "v"(t[0]), "v"(t[1]), "v"(t[2]), "v"(t[3]));
    w01 = f32x2{w0, w1}; w23 = f32x2{w2, w3}; k01 = f32x2{k0, k1}; k23 = f32x2{k2, k3};
}

template <int CTRL> __device__ __forceinline__ float dpp(float x) {
    return __builtin_bit_cast(float, __builtin_amdgcn_mov_dpp(__builtin_bit_cast(int, x), CTRL, 0xf, 0xf, true));
}
__device__ __forceinline__ float row16_allsum(float x) {
    x += dpp<0x128>(x);
    x += dpp<0x124>(x);
    x += dpp<0x122>(x);
    x += dpp<0x121>(x);
    return x;
}
__device__ __forceinline__ float xrow_max(float x) {
    auto s = __builtin_amdgcn_permlane16_swap(__float_as_uint(x), __float_as_uint(x), false, false);
    x = fmaxf(__uint_as_float(s[0]), __uint_as_float(s[1]));
    auto t = __builtin_amdgcn_permlane32_swap(__float_as_uint(x), __float_as_uint(x), false, false);
    return fmaxf(__uint_as_float(t[0]), __uint_as_float(t[1]));
}
__device__ __forceinline__ float xrow_sum(float x) {
    auto s = __builtin_amdgcn_permlane16_swap(__float_as_uint(x), __float_as_uint(x), false, false);
    x = __uint_as_float(s[0]) + __uint_as_float(s[1]);
    auto t = __builtin_amdgcn_permlane32_swap(__float_as_uint(x), __float_as_uint(x), false, false);
    return __uint_as_float(t[0]) + __uint_as_float(t[1]);
}
__device__ __forceinline__ float swap32_add(float a, float b) {
    auto t = __builtin_amdgcn_permlane32_swap(__float_as_uint(a), __float_as_uint(b), false, false);
    return __uint_as_float(t[0]) + __uint_as_float(t[1]);
}
__device__ __forceinline__ float swap16_add(float a, float b) {
    auto t = __builtin_amdgcn_permlane16_swap(__float_as_uint(a), __float_as_uint(b), false, false);
    return __uint_as_float(t[0]) + __uint_as_float(t[1]);
}
__device__ __forceinline__ int row16_sum(int x) {
    x += __builtin_amdgcn_mov_dpp(x, 0x128, 0xf, 0xf, true);
    x += __builtin_amdgcn_mov_dpp(x, 0x124, 0xf, 0xf, true);
    x += __builtin_amdgcn_mov_dpp(x, 0x122, 0xf, 0xf, true);
    x += __builtin_amdgcn_mov_dpp(x, 0x121, 0xf, 0xf, true);
    return x;
}
__device__ __forceinline__ unsigned now32() { return (unsigned)__builtin_amdgcn_s_memrealtime(); }

#define PIN() do { asm volatile("" ::: "memory"); __builtin_amdgcn_sched_barrier(0); } while (0)
#define BARRIER() do { asm volatile("s_waitcnt lgkmcnt(0)" ::: "memory"); __builtin_amdgcn_s_barrier(); PIN(); } while (0)
#define LDSA(p) ((unsigned)(unsigned long long)(__attribute__((address_space(3))) void*)(p))
#define VMCNT(n) asm volatile("s_waitcnt vmcnt(%0)" :: "i"(n) : "memory")

typedef volatile __attribute__((address_space(3))) unsigned vlds_u32;
typedef volatile __attribute__((address_space(3))) u32x2 vlds_u32x2;
typedef volatile __attribute__((address_space(3))) u32x4 vlds_u32x4;

struct Pace {
    unsigned step;
    int start;
    int offset;
    int bias;
    int rd;
    int pub;
};

__global__ __launch_bounds__(576, 3) __attribute__((aligned(4096))) void kv_sweep_kernel(
    const void*, const void*, const void*, const void*, const void*, const void*, const void*, const void*,
    const void*, const void*, const void*, void*, void*, Pace)
{
    __shared__ __attribute__((aligned(16))) unsigned s_q[NH * DH];
    __shared__ __attribute__((aligned(16))) unsigned short s_sc[NH * 256];
    __shared__ __attribute__((aligned(16))) float s_w[NH * 4 * 20];
    __shared__ __attribute__((aligned(16))) float s_part[8 * NI * DH];
    __shared__ __attribute__((aligned(16))) float s_red[8 * 28];
    __shared__ __attribute__((aligned(16))) float s_f[NH * 8];
    __shared__ __attribute__((aligned(16))) unsigned s_done[8];
    __shared__ __attribute__((aligned(16))) unsigned s_sync[8];
    __shared__ __attribute__((aligned(16))) unsigned s_act[16];

    unsigned long long pc0, t0;
    u64x8 ka, kb;
    asm volatile("s_load_dwordx16 %3, %4, 0x40\n\ts_load_dwordx16 %2, %4, 0x0\n\ts_memrealtime %1\n\ts_getpc_b64 %0\n.Lkv_beg:"
                 : "=&s"(pc0), "=&s"(t0), "=&s"(ka), "=&{s[8:23]}"(kb) : "s"(__builtin_amdgcn_kernarg_segment_ptr()));
    unsigned clen, xw;
    asm volatile("s_mov_b32 %0, .Lkv_end-.Lkv_beg" : "=s"(clen));
    asm volatile("global_load_dword %0, %1, %2" : "=&v"(xw) : "v"(min((blockIdx.x >> 3) * 64 + (threadIdx.x >> 6) * 2048, clen)), "s"(pc0 & ~63ull) : "memory");
    __builtin_amdgcn_sched_barrier(0);
    const int tid = threadIdx.x;
    const int lane = tid & 63;
    int wave = __builtin_amdgcn_readfirstlane(tid >> 6);
    const int r = lane >> 4;
    const int c = lane & 15;
    const int bid = blockIdx.x;
    const int rw = wave & 3, hp = wave >> 2;
    constexpr int RS = 8192 + 128, RT = 1024 + 256;
    const int grow = ((bid >> 3 & 3) | (bid >> 4 & 4) | (bid & 0x84) << 1 | (bid & 2) << 3 | (bid & 32) | (bid & 1) << 7) * 16;
    auto srow = [&](int t) { return (t & 1) * RS ^ (t >> 1) * RT; };
    const int row0 = grow + rw * 4;
    const unsigned rb0 = (row0 ^ RT * hp) * 384u;
    unsigned co = r * 384 + c * 8 + rb0;
#define LOAD_CS(a) "global_load_dwordx2 %0, " a ", s[10:11] nt\n\tglobal_load_dwordx2 %3, " a ", s[12:13] nt\n\t" \
                   "global_load_dwordx2 %1, " a ", s[10:11] offset:128 nt\n\tglobal_load_dwordx2 %4, " a ", s[12:13] offset:128 nt\n\t" \
                   "global_load_dwordx2 %2, " a ", s[10:11] offset:256 nt\n\tglobal_load_dwordx2 %5, " a ", s[12:13] offset:256 nt\n"
    u32x2 raw[2][6];
    unsigned long long t1;
    asm volatile("s_cmp_eq_u32 %11, 8\n\t.p2align 6\n\ts_waitcnt lgkmcnt(0)\n\ts_memrealtime %6\n\ts_cbranch_scc1 .Lkv_arg\n\t"
                 LOAD_CS("%10") ".Lkv_arg:\n\ts_waitcnt lgkmcnt(0)"
                 : "=&v"(raw[0][0]), "=&v"(raw[0][1]), "=&v"(raw[0][2]), "=&v"(raw[0][3]), "=&v"(raw[0][4]), "=&v"(raw[0][5]), "=&s"(t1),
                   "+s"(t0), "+s"(ka), "+{s[8:23]}"(kb), "+v"(co), "+s"(wave) :: "scc", "memory");
    __builtin_amdgcn_sched_barrier(0);
    typedef const __attribute__((address_space(1))) char* gptr;
    void* const q = (void*)ka[0]; void* const qk = (void*)ka[1]; void* const qv = (void*)ka[4]; void* const q_cos = (void*)ka[7];
    const gptr ksc = (gptr)ka[2], kzp = (gptr)ka[3], vsc = (gptr)ka[5], vzp = (gptr)ka[6];
    void* const q_sin = (void*)kb[0];
    const unsigned long long wsa = kb[3] + 0x1fffff & ~0x1fffffull;
    unsigned char* const ws = (unsigned char*)(__attribute__((address_space(1))) unsigned char*)wsa;
    const Pace pace = {(unsigned)kb[5], (int)(kb[5] >> 32), (int)kb[6], (int)(kb[6] >> 32), (int)kb[7], (int)(kb[7] >> 32)};
    const int m = (bid >> 5) * 16 + (bid & 3) * 4 + (bid >> 3 & 3);
    const unsigned tstart = (unsigned)t0;
    const unsigned tsched = (unsigned)t1;
    const auto rw_ = __builtin_amdgcn_make_buffer_rsrc((void*)ws, 0, WS_BYTES, 0x00020000);

    auto pick = [](int n, gptr p0, gptr p1, gptr p2, gptr p3) { return (n & 2) ? ((n & 1) ? p3 : p2) : ((n & 1) ? p1 : p0); };
    auto merge = [&](const float* p, f32x4 f, float zs, unsigned w3) {
        f32x2 u[4][3], a[3];
#pragma unroll
        for (int w = 0; w < 4; ++w)
#pragma unroll
            for (int k = 0; k < 3; ++k) u[w][k] = *reinterpret_cast<const f32x2*>(p + w * NI * DH + 2 * k);
        asm volatile("" : "+v"(u[0][0]), "+v"(u[0][1]), "+v"(u[0][2]), "+v"(u[1][0]), "+v"(u[1][1]), "+v"(u[1][2]),
                          "+v"(u[2][0]), "+v"(u[2][1]), "+v"(u[2][2]), "+v"(u[3][0]), "+v"(u[3][1]), "+v"(u[3][2]));
#pragma unroll
        for (int k = 0; k < 3; ++k) a[k] = (f[0] * u[0][k] - zs + f[1] * u[1][k]) + (f[2] * u[2][k] + f[3] * u[3][k]);
        return u32x4{pk_bf16(a[0]), pk_bf16(a[1]), pk_bf16(a[2]), w3};
    };
    auto last_word = [&](unsigned tag, float ls, int c6) { return tag | ((__float_as_uint(ls) >> (16 * (c6 & 1))) & 0xffffu); };
    auto piece_off = [&](int hp, int c6) { return (hp * 16 + (c6 & 12) + ((c6 >> 4 ^ bid >> 2) & 1) * 2 + (c6 >> 4)) * BLK + (c6 & 3) * 16; };
    auto publish = [&](u32x4 v, int i, unsigned vo, unsigned sides) {
        __builtin_amdgcn_raw_buffer_store_b128(v, rw_, vo ^ (sides >> (i >> 2) & 1) * BLK ^ (i < NW ? vo >> 1 & BLK : 0), pair_off(i) + m * 64, DEV);
        asm volatile("s_nop 1" :: "v"(v) : "memory");
    };

    if (__builtin_expect_with_probability(wave == 8, 1, 0.45)) {
        __builtin_amdgcn_s_setprio(3);
        unsigned sci = 0;
        gptr scp = pick(lane >> 1, ksc, kzp, vsc, vzp) + ((lane >> 5) * SKV + (grow ^ srow(lane >> 3 & 3)) + (lane & 1) * 8) * 2;
        auto gather = [&]() {
            asm volatile("s_mov_b32 m0, %1\n\ts_nop 0\n\tglobal_load_lds_dwordx4 %0, off nt"
                         :: "v"(scp), "s"(LDSA(s_sc) + sci * 1024) : "memory");
            scp += 4 * SKV;
        };
        while (now32() - tsched < 100) __builtin_amdgcn_s_sleep(1);
        u32x3 lc[2], ls[2];
        u32x4 qraw[12];
        {
            const auto rq = __builtin_amdgcn_make_buffer_rsrc((void*)q, 0, NH * DH * 2, 0x00020000);
            const auto rqc = __builtin_amdgcn_make_buffer_rsrc((void*)q_cos, 0, DH * 2, 0x00020000);
            const auto rqs = __builtin_amdgcn_make_buffer_rsrc((void*)q_sin, 0, DH * 2, 0x00020000);
#pragma unroll
            for (int k = 0; k < 12; ++k) qraw[k] = __builtin_amdgcn_raw_buffer_load_b128(rq, lane * 16 + (k & 3) * 1024, (k >> 2) * 4096, 0);
#pragma unroll
            for (int k = 0; k < 2; ++k) {
                lc[k] = __builtin_amdgcn_raw_buffer_load_b96(rqc, c * 12 + k * 192, 0, 0);
                ls[k] = __builtin_amdgcn_raw_buffer_load_b96(rqs, c * 12 + k * 192, 0, 0);
            }
            PIN();
        }
        const unsigned tag = ((unsigned)__builtin_amdgcn_raw_buffer_load_b16(rw_, (lane & 3) * 2, WS_EPOCH, DEV) + 1u) << 16;
        PIN();
        unsigned* const s_raw = reinterpret_cast<unsigned*>(s_part) + NI * DH / 2;
#pragma unroll
        for (int k = 0; k < 12; ++k) *reinterpret_cast<u32x4*>(s_raw + k / 6 * (NI * DH) + k % 6 * 256 + lane * 4) = qraw[k];
        f32x2 fc[2][3], fs[2][3];
#pragma unroll
        for (int k = 0; k < 2; ++k)
#pragma unroll
            for (int u = 0; u < 3; ++u) { fc[k][u] = f32x2{bflo(lc[k][u]), bfhi(lc[k][u])}; fs[k][u] = f32x2{bflo(ls[k][u]), bfhi(ls[k][u])}; }
        PIN();
#pragma nounroll
        for (int it = 0; it < 4; ++it) {
            if (sci < NB) { gather(); ++sci; }
            unsigned qa[2][3], qb[2][3];
#pragma unroll
            for (int g = 0; g < 2; ++g)
#pragma unroll
                for (int u = 0; u < 3; ++u) {
                    const unsigned* src = s_raw + g * (NI * DH) + (it * 4 + r) * HALF + 3 * c;
                    qa[g][u] = src[u]; qb[g][u] = src[HALF / 2 + u];
                }
#pragma unroll
            for (int g = 0; g < 2; ++g)
#pragma unroll
                for (int u = 0; u < 3; ++u) {
                    const f32x2 a = {bflo(qa[g][u]), bfhi(qa[g][u])}, b = {bflo(qb[g][u]), bfhi(qb[g][u])};
                    const f32x2 o0 = a * fc[0][u] - b * fs[0][u], o1 = b * fc[1][u] + a * fs[1][u];
                    const unsigned l0 = pk_bf16(f32x2{o0[0], o1[0]}), l1 = pk_bf16(f32x2{o0[1], o1[1]});
                    unsigned* dst = s_q + (16 * g + 4 * it + r) * DH + 6 * c + 2 * u;
                    *reinterpret_cast<u32x2*>(dst) = u32x2{l0, l1};
                    *reinterpret_cast<u32x2*>(dst + HALF) = u32x2{__builtin_amdgcn_alignbit(l0, l0, 16) ^ 0x80000000u, __builtin_amdgcn_alignbit(l1, l1, 16) ^ 0x80000000u};
                }
        }
        asm volatile("" :: "v"(xw));
        __builtin_amdgcn_s_setprio(0);
        unsigned origin = tsched << 8;
        if (lane < 8) { s_done[lane] = 0; s_sync[lane] = lane < 3 ? origin : lane < 4 ? NB : tag; }
        PIN();
        VMCNT(0);
        BARRIER();
        const int mh = lane >> 5, mc = lane & 31;
        const unsigned rtag = __builtin_amdgcn_readlane(tag, 0) | __builtin_amdgcn_readlane(tag, 1) >> 16;
        const unsigned probe = WS_PROBE + ((bid >> 3 & 3) << 21);
        constexpr int RPX = 2;
        constexpr int RSH = 9;
        constexpr unsigned RDLY = 200;
        constexpr int UP = 984, DN = 2000;
        const int rj = ((bid >> 3) + 5 * (bid & 7)) & 31;
        const int rid = rj % (32 / RPX) == 0 ? rj / (32 / RPX) * 8 + (bid & 7) : -1;
        unsigned wround = now32() >> RSH, rround = wround, tick = 0, target = origin;
        bool synced = false;
        int drift = 0;
        const unsigned round0 = rround;
        int i = 0;
        unsigned sct = tstart, sides = 0;
        int np = (bid & ~24) == 32 ? 0 : -1, score = 0;
        const unsigned stag = __builtin_amdgcn_readlane(tag, 2) | __builtin_amdgcn_readlane(tag, 3) >> 16;
        auto kept_now = [&](unsigned now) {
            const u32x2 a = *(vlds_u32x2*)(s_act + 2 * (lane & 7));
            const int late = max((int)(a[0] - origin), (int)((now << 8) - a[1] - origin));
            return origin + (__builtin_amdgcn_readfirstlane(row16_sum(late)) >> 4);
        };
        auto steer = [&](unsigned now) {
            if ((now >> 5) != tick) {
                tick = now >> 5;
                target += drift;
                origin += max(-DN, min(UP + drift, (int)(target - origin)));
                if (!synced) {
                    origin += max(0, (int)(kept_now(now) - origin) - 2000);
                    target = origin;
                }
                if (lane == 0) *(vlds_u32*)(s_sync + 2) = origin;
                PIN();
            }
        };
        auto finished = [&]() {
            const u32x4 a = *(vlds_u32x4*)s_done, b = *(vlds_u32x4*)(s_done + 4);
            return __builtin_amdgcn_readfirstlane(min(min(min(a[0], a[1]), min(a[2], a[3])), min(min(b[0], b[1]), min(b[2], b[3]))));
        };
        while (i < NI - 2) {
            const unsigned now = now32();
            if ((now >> RSH) != wround) {
                wround = now >> RSH;
                if (rid >= 0 && (now & ((1u << RSH) - 1)) < 48)
                    __builtin_amdgcn_raw_buffer_store_b64(u32x2{kept_now(now), rtag + wround}, rw_, lane == 0 ? rid * 8 : OOB, WS_REP + (wround & 255) * 512, DEV);
                PIN();
                continue;
            }
            if ((int)(((now - RDLY) >> RSH) - rround) > 0) {
                rround = (now - RDLY) >> RSH;
                const u32x2 x = __builtin_amdgcn_raw_buffer_load_b64(rw_, c * 8, WS_REP + (rround & 255) * 512, DEV);
                const int d = (int)(x[0] - origin);
                const bool ok = x[1] == rtag + rround && d < (32 << 16) && d > -(32 << 16);
                const int n = __builtin_popcountll(__builtin_amdgcn_ballot_w64(ok));
                if (n >= 48) {
                    const unsigned nt = origin + __builtin_amdgcn_readfirstlane((int)(row16_allsum(ok ? (float)d : 0.f) * 4.f / (float)n)) - pace.bias;
                    if (synced && rround - round0 > 4) drift = max(0, min(drift + ((max(-1024, min((int)(nt - target) + 6 * drift, 5120)) - (rround - round0 < 13 ? 768 : 0)) >> 5), 800));
                    target = nt + 6 * drift;
                    synced = true;
                    const int lost = min((int)(kept_now(now32()) - origin) - pace.bias, (int)(target - origin));
                    if (lost > 0) origin += lost;
                }
                PIN();
                continue;
            }
            steer(now);
            if (sci <= NI && now - sct >= 100) {
                VMCNT(0);
                if (lane == 0) *(vlds_u32*)(s_sync + 3) = sci;
                if (sci < NI) gather();
                ++sci; sct = now;
                PIN();
                continue;
            }
            if (np >= 0 && sci > NI) {
                if (np < 8) {
                    const int o = (np ^ np >> 2) & 1;
                    const unsigned a = (np & 3) * 2048 + o * BLK;
                    unsigned long long u0, u1, u2;
                    unsigned x;
                    asm volatile("s_waitcnt lgkmcnt(0)\n\ts_memrealtime %0\n\ts_load_dword %3, %4, %5 glc\n\ts_waitcnt lgkmcnt(0)\n\t"
                                 "s_memrealtime %1\n\ts_load_dword %3, %4, %6 glc\n\ts_waitcnt lgkmcnt(0)\n\ts_memrealtime %2\n\ts_waitcnt lgkmcnt(0)"
                                 : "=&s"(u0), "=&s"(u1), "=&s"(u2), "=&s"(x) : "s"(wsa + probe), "s"(a), "s"(a ^ BLK) : "memory");
                    const int d = max(-16, min((int)(u2 + u0 - 2 * u1), 16));
                    score += o ? d : -d;
                    ++np;
                } else {
                    __builtin_amdgcn_raw_buffer_store_b64(u32x2{stag ^ (score > 0), rtag}, rw_, lane == 0 ? bid & 24 : OOB, WS_SIDE, DEV);
                    np = -1;
                }
                PIN();
                continue;
            }
            if (np >= 0 || finished() <= (unsigned)i) { __builtin_amdgcn_s_sleep(2); continue; }
            PIN();
            if (i == 0) {
                f32x4 ml[4], f;
#pragma unroll
                for (int w = 0; w < 4; ++w) ml[w] = *reinterpret_cast<const f32x4*>(s_w + (mc * 4 + w) * 20 + 16);
                const float M = fmaxf(fmaxf(ml[0][0], ml[1][0]), fmaxf(ml[2][0], ml[3][0]));
                float L = 0.f, zs = 0.f;
#pragma unroll
                for (int w = 0; w < 4; ++w) { f[w] = __builtin_amdgcn_exp2f(ml[w][0] - M); L += f[w] * ml[w][1]; zs += f[w] * ml[w][2]; }
                const float inv = 1.f / L;
                *reinterpret_cast<f32x4*>(s_f + mc * 8) = f * inv;
                *reinterpret_cast<f32x2*>(s_f + mc * 8 + 4) = f32x2{zs * inv, M + __builtin_amdgcn_logf(L)};
                u32x2 sd;
                do { sd = __builtin_amdgcn_raw_buffer_load_b64(rw_, (lane & 3) * 8, WS_SIDE, DEV); PIN(); }
                while (__builtin_amdgcn_ballot_w64(sd[1] != rtag || (sd[0] ^ stag) > 1) != 0 && now32() - tstart < SPIN_LIMIT);
                sides = (unsigned)__builtin_amdgcn_ballot_w64(sd[0] != stag) & 15;
                if (lane == 0) *(vlds_u32*)(s_sync + 1) = sides;
                PIN();
                if (lane == 0) *(vlds_u32*)(s_sync + 3) = NI + 1;
                PIN();
            }
            const f32x2 zl = *reinterpret_cast<const f32x2*>(s_f + (2 * i + mh) * 8 + 4);
            const u32x4 v = merge(s_part + (mh * 4 * NI + i) * DH + mc * 6, *reinterpret_cast<const f32x4*>(s_f + (2 * i + mh) * 8), zl[0], last_word(tag, zl[1], mc));
            PIN();
            *reinterpret_cast<u32x4*>(s_part + (mh * 4 * NI + i) * DH + mc * 4) = v;
            PIN();
            ++i;
        }
        u32x4 v[NI - 1];
#pragma unroll
        for (int i = 0; i < NI - 2; ++i) v[i] = *reinterpret_cast<const u32x4*>(s_part + (mh * 4 * NI + i) * DH + mc * 4);
        f32x4 f = *reinterpret_cast<const f32x4*>(s_f + (2 * (NI - 2) + mh) * 8);
        f32x2 zl = *reinterpret_cast<const f32x2*>(s_f + (2 * (NI - 2) + mh) * 8 + 4);
        PIN();
#pragma unroll
        for (int i = 0; i < NI - 2; ++i) asm volatile("" : "+v"(v[i]));
        asm volatile("" : "+v"(f), "+v"(zl));
        v[NI - 2] = v[0];
        bool have = false, sent = false;
        while (!(have && sent)) {
            const unsigned now = now32();
            if (!have) {
                steer(now);
                if (finished() > NI - 2) {
                    PIN();
                    v[NI - 2] = merge(s_part + (mh * 4 * NI + NI - 2) * DH + mc * 6, f, zl[0], last_word(tag, zl[1], mc));
                    have = true;
                }
            }
            if (!sent && (int)((now << 8) - origin) >= pace.pub) {
#pragma unroll
                for (int i = 0; i < NI - 2; ++i) publish(v[i], i, piece_off(mh, mc), sides);
                sent = true;
            }
            if (!(have && sent)) __builtin_amdgcn_s_sleep(1);
            PIN();
        }
        publish(v[NI - 2], NI - 2, piece_off(mh, mc), sides);
        asm volatile(".Lkv_end:");
        return;
    }

    const int ph = rw + 4 * bid + 1024 * hp;
    unsigned off = pace.start + ((pace.step * ph) >> 11);
    unsigned origin = tsched << 8, kept = 0;
    unsigned step = pace.step;
    auto pace_wait = [&]() {
        const unsigned tgt = __builtin_amdgcn_readfirstlane(origin) + off;
        unsigned now = now32() << 8;
        kept = ((int)(now - tgt) > 0 ? now : tgt) - off;
        while ((int)(tgt - now) > 0) { __builtin_amdgcn_s_sleep(1); now = now32() << 8; }
        off += step;
    };
    auto report = [&]() {
        if (lane == 0) *(vlds_u32x2*)(s_act + 2 * wave) = u32x2{kept, off};
        origin = *(vlds_u32*)(s_sync + 2);
        PIN();
    };

    auto pre_sc = [&]() {
        const unsigned junk = LDSA(s_part) + (3 * NI * DH - 256) * 4;
        const gptr a = pick(grow >> 4, ksc, kzp, vsc, vzp) + ((tid >> 5) * SKV + (grow & ~63 ^ srow(lane >> 3 & 3))) * 2 + (lane & 7) * 16;
        asm volatile("s_mov_b32 m0, %2\n\ts_nop 0\n\tglobal_load_lds_dwordx4 %0, off\n\tglobal_load_lds_dwordx4 %1, off" :: "v"(a), "v"(a + 32 * SKV), "s"(junk) : "memory");
        PIN();
    };
    const unsigned rb1 = (row0 ^ RT * hp ^ RS) * 384u - rb0;
    u32x4 tab[4][3];
    auto pack_cs = [&](int t) {
#pragma unroll
        for (int j = 0; j < 3; ++j) {
#pragma unroll
            for (int k = 0; k < 2; ++k) {
                const unsigned lo = __builtin_amdgcn_mov_dpp(raw[t][3 + (j + 1) % 3][k], 0x128, 0xf, 0x3, false);
                const unsigned sn = __builtin_amdgcn_update_dpp(lo, raw[t][3 + (j + 2) % 3][k], 0x128, 0xf, 0xc, false);
                tab[t][j][2 * k] = __builtin_amdgcn_perm(sn, raw[t][j][k], 0x05040100);
                tab[t][j][2 * k + 1] = __builtin_amdgcn_perm(sn, raw[t][j][k], 0x07060302);
            }
            *reinterpret_cast<u32x4*>(s_part + (wave ^ 4) * (NI * DH) + ((t * 3 + j) * 64 + lane) * 4) = tab[t][j];
        }
        PIN();
    };
    const auto rk = __builtin_amdgcn_make_buffer_rsrc((void*)qk, 0, KVB, 0x00020000);
    const auto rv = __builtin_amdgcn_make_buffer_rsrc((void*)qv, 0, KVB, 0x00020000);
    const unsigned voff = r * ROWB + c * 16;
    const unsigned woff = hp * HEADB;
    auto toff = [&](int t) { return (unsigned)(row0 ^ srow(t)) * ROWB; };
    u32x4 ring[4][3];
    auto ld = [&](int slot, int j, __amdgpu_buffer_rsrc_t rr, unsigned off) {
        ring[slot][j] = __builtin_amdgcn_raw_buffer_load_b128(rr, voff + j * 256, off, NT);
        PIN();
    };
    auto ld_tile = [&](int slot, __amdgpu_buffer_rsrc_t rr, unsigned off) { ld(slot, 0, rr, off); ld(slot, 1, rr, off); ld(slot, 2, rr, off); };
    unsigned post = 0;
    auto enter = [&](int i) {
        if (i == 9 || i == 10) {
            const unsigned hs = pace.step >> 1, z = (unsigned)pace.offset >> 16;
            off += i == 10 ? post : hp * 3 * hs;
            step = i == 10 ? pace.step : hs + hp * z;
            post = __builtin_amdgcn_readfirstlane(i == 10 ? 0 : hp ? hs : 4 * (hs + z));
            if (lane == 0) *(vlds_u32x2*)(s_act + 2 * wave) = u32x2{kept, off};
            PIN();
        }
    };
    auto refill = [&](int slot, int j, __amdgpu_buffer_rsrc_t rr, unsigned goff) {
        if (j == 0) pace_wait();
        if (j == 0) asm volatile("s_cmp_lg_u32 %0, 0\n\ts_waitcnt vmcnt(3)\n\ts_cbranch_scc1 1\n\ts_waitcnt vmcnt(2)" :: "s"(post) : "scc", "memory");
        else asm volatile("s_cmp_lg_u32 %0, 0\n\ts_cbranch_scc1 1\n\ts_waitcnt vmcnt(2)" :: "s"(post) : "scc", "memory");
        ld(slot, j, rr, goff);
        if (j == 0) { const unsigned t = (now32() << 8) - 1024 - (off - step); if ((int)(t - kept) > 0) kept = t; }
        if (j == 2) report();
    };
    auto k0 = [&](int t) { pace_wait(); ld_tile(t, rk, woff + toff(t)); };
    co += rb1;
    pace_wait();
    asm volatile(LOAD_CS("%6") : "=&v"(raw[1][0]), "=&v"(raw[1][1]), "=&v"(raw[1][2]), "=&v"(raw[1][3]), "=&v"(raw[1][4]), "=&v"(raw[1][5])
                 : "v"(co), "{s[8:23]}"(kb) : "memory");
    PIN();
    asm volatile("s_waitcnt vmcnt(6)" : "+v"(raw[0][0]), "+v"(raw[0][1]), "+v"(raw[0][2]), "+v"(raw[0][3]), "+v"(raw[0][4]), "+v"(raw[0][5]) :: "memory");
    pack_cs(0);
    pace_wait(); pre_sc(); off += (pace.offset & 0xffff) - step;
    k0(0);
    asm volatile("s_waitcnt vmcnt(5)" : "+v"(raw[1][0]), "+v"(raw[1][1]), "+v"(raw[1][2]), "+v"(raw[1][3]), "+v"(raw[1][4]), "+v"(raw[1][5]) :: "memory");
    pack_cs(1);
    VMCNT(3);
    asm volatile("" :: "v"(xw));
    k0(1);
    VMCNT(3); k0(2);
    VMCNT(3);
    if (hp) { if (lane == 0) *(vlds_u32x2*)(s_act + 2 * wave) = u32x2{kept, off}; BARRIER(); }
    k0(3);
    if (lane == 0) *(vlds_u32x2*)(s_act + 2 * wave) = u32x2{kept, off};
    if (!hp) BARRIER();
    origin = *(vlds_u32*)(s_sync + 2);

    u32x4 qp[3];
    auto load_q = [&](int h, int j) { qp[j] = *reinterpret_cast<const u32x4*>(s_q + h * DH + 64 * j + 4 * c); };
    load_q(hp, 0); load_q(hp, 1); load_q(hp, 2);
#pragma unroll
    for (int t = 0; t < 2; ++t)
#pragma unroll
        for (int j = 0; j < 3; ++j) tab[2 + t][j] = *reinterpret_cast<const u32x4*>(s_part + wave * (NI * DH) + ((t * 3 + j) * 64 + lane) * 4);
    if (hp) {
#pragma unroll
        for (int t = 0; t < 2; ++t)
#pragma unroll
            for (int j = 0; j < 3; ++j)
#pragma unroll
                for (int k = 0; k < 4; ++k) asm volatile("v_swap_b32 %0, %1" : "+v"(tab[t][j][k]), "+v"(tab[2 + t][j][k]));
    }

    unsigned ready = NB;
    for (int i = 0; i < NI; ++i) {
        const bool last = i + 1 == NI;
        enter(i);
        const int h = 2 * i + hp;
        const unsigned hnext = (i + 1) * (2 * HEADB) + woff;
        const auto rn = __builtin_amdgcn_make_buffer_rsrc((void*)(last ? qv : qk), 0, KVB, 0x00020000);
        float* scp = s_w + (h * 4 + rw) * 20;
        f32x4 sc[4];
        while (__builtin_amdgcn_readfirstlane(ready) <= i) { __builtin_amdgcn_s_sleep(1); ready = *(vlds_u32*)(s_sync + 3); }
        PIN();
#pragma unroll
        for (int t = 0; t < 4; ++t)
#pragma unroll
            for (int a = 0; a < 4; ++a) sc[t][a] = bflo(s_sc[h * 256 + t * 64 + a * 16 + rw * 4 + r]);
        float s[4];
#pragma unroll
        for (int t = 0; t < 4; ++t) {
            f32x2 dot, wsum;
#pragma unroll
            for (int j = 0; j < 3; ++j) {
                f32x2 w01, w23, k01, k23;
                dot_cvt(qp[j], tab[t][j], ring[t][j], w01, w23, k01, k23);
                if (j == 0) { wsum = w01 + w23; dot = k01 * w01; } else { wsum += w01; wsum += w23; dot += k01 * w01; }
                dot += k23 * w23;
                asm volatile("" : "+v"(dot), "+v"(wsum));
                PIN();
                if (t == 3) load_q(last ? h : h + 2, j);
                if (t == 3 && j == 0) ready = *(vlds_u32*)(s_sync + 3);
                refill(t, j, rn, (last ? woff : hnext) + toff(t));
            }
            const float a = (dot[0] + dot[1]) - sc[t][1] * (wsum[0] + wsum[1]);
            s[t] = row16_allsum(a) * (sc[t][0] * SCALE_LOG2);
        }
        const float mx = xrow_max(fmaxf(fmaxf(s[0], s[1]), fmaxf(s[2], s[3])));
        float l = 0.f, z = 0.f;
#pragma unroll
        for (int t = 0; t < 4; ++t) {
            const float p = __builtin_amdgcn_exp2f(s[t] - mx);
            l += p;
            s[t] = p * sc[t][2];
            z += s[t] * sc[t][3];
        }
        l = xrow_sum(l);
        z = xrow_sum(z);
        if (c == 0) {
            *reinterpret_cast<f32x4*>(scp + r * 4) = f32x4{s[0], s[1], s[2], s[3]};
            *reinterpret_cast<f32x4*>(scp + 16) = f32x4{mx, l, z, 0.f};
        }
        PIN();
    }

    const float* pwp = s_w + (hp * 4 + rw) * 20 + r * 4;
    f32x4 pw = *reinterpret_cast<const f32x4*>(pwp);
    unsigned tag, lw, lo;
    f32x4 lf;
    f32x2 lz;
    unsigned lp = (hp * 4 * NI + NI - 1) * DH + (lane & 31) * 6;
    f32x2 acc[3][2];
    auto v_acc = [&](int t, int j) {
        const f32x2 p2 = {pw[t], pw[t]};
        const u32x4 vv = ring[t][j];
        const f32x2 v01 = {(float)(int)vv[0], (float)(int)vv[1]}, v23 = {(float)(int)vv[2], (float)(int)vv[3]};
        if (t == 0) { acc[j][0] = p2 * v01; acc[j][1] = p2 * v23; }
        else { acc[j][0] += p2 * v01; acc[j][1] += p2 * v23; }
        asm volatile("" : "+v"(acc[j][0]), "+v"(acc[j][1]));
        PIN();
    };
    auto v_fold = [&](int i, int j) {
        const float x = swap32_add(acc[j][0][0], acc[j][1][0]);
        const float y = swap32_add(acc[j][0][1], acc[j][1][1]);
        s_part[(wave * NI + i) * DH + 64 * j + 4 * c + r] = swap16_add(x, y);
    };
    auto v_done = [&](int i) {
        PIN();
        if (lane == 0) *(vlds_u32*)(s_done + wave) = i + 1;
        PIN();
    };
    constexpr int LS = 2;
#pragma unroll
    for (int t = 0; t < LS; ++t)
#pragma unroll
        for (int j = 0; j < 3; ++j) v_acc(t, j);
    for (int i = 0; i < NI - 1; ++i) {
        const unsigned hnext = (i + 1) * (2 * HEADB) + woff;
        enter(i);
#pragma unroll
        for (int k = 0; k < 4; ++k) {
            const int t = (k + LS) & 3;
#pragma unroll
            for (int j = 0; j < 3; ++j) {
                v_acc(t, j);
                if (t == 3 && j == 2) pw = *reinterpret_cast<const f32x4*>(pwp + (i + 1) * 160);
                refill(k, j, rv, hnext + toff(k));
            }
            if (t == 3) { v_fold(i, 0); v_fold(i, 1); v_fold(i, 2); v_done(i); }
        }
    }
    asm volatile("s_branch .Lkv_tail_%=\n\t.p2align 12\n.Lkv_tail_%=:" ::: "memory");
    while (__builtin_amdgcn_readfirstlane(*(vlds_u32*)(s_sync + 3)) <= NI) __builtin_amdgcn_s_sleep(1);
    PIN();
    tag = s_sync[4 + (lane & 3)];
    lf = *reinterpret_cast<const f32x4*>(s_f + (2 * (NI - 1) + hp) * 8);
    lz = *reinterpret_cast<const f32x2*>(s_f + (2 * (NI - 1) + hp) * 8 + 4);
    origin = *(vlds_u32*)(s_sync + 2);
    lw = last_word(tag, lz[1], lane & 31);
    const unsigned sides = s_sync[1];
    lo = lane < 32 ? piece_off(hp, lane & 31) ^ (sides >> 3) * BLK : OOB;
    unsigned la[5], ld_ = LDSA(s_done) + 16 * hp, lq = LDSA(s_done) + 4 * wave, lv = NI, lso = pair_off(NI - 1) + m * 64;
#pragma unroll
    for (int w = 0; w < 5; ++w) la[w] = LDSA(s_part) + lp * 4 + (w < 4 ? w * NI * DH * 4 : 16);
    u32x4 rws = {(unsigned)wsa, (unsigned)(wsa >> 32) & 0xffffu, WS_BYTES, 0x00020000};
    f32x2 lf01 = {lf[0], lf[1]}, lf23 = {lf[2], lf[3]};
    asm volatile("" : "+v"(tag), "+v"(lf01), "+v"(lf23), "+v"(lz), "+v"(lw), "+v"(lo), "+v"(origin), "+v"(la[0]), "+v"(la[1]), "+v"(la[2]), "+v"(la[3]), "+v"(la[4]),
                      "+v"(ld_), "+v"(lq), "+v"(lv), "+s"(lso), "+s"(rws));
    PIN();
#pragma unroll
    for (int t = LS; t < 4; ++t)
#pragma unroll
        for (int j = 0; j < 3; ++j) { v_acc(t, j); if (t == 3) { v_fold(NI - 1, j); PIN(); } }
    {
#define FMA0(a) "v_pk_fma_f32 " a ", %7, " a ", %9 op_sel_hi:[0,1,0] neg_lo:[0,0,1] neg_hi:[0,0,1]\n\t"
#define FMA(a, f, b, hi) "v_pk_fma_f32 " a ", " f ", " b ", " a " op_sel:[" hi ",0,0] op_sel_hi:[" hi ",1,1]\n\t"
        asm volatile(
            "ds_write_b32 %5, %6\n\tds_read_b128 v[96:99], %14\n\t"
            "ds_read2_b64 v[64:67], %0 offset1:1\n\tds_read2_b64 v[68:71], %1 offset1:1\n\tds_read2st64_b64 v[80:83], %4 offset1:24\n\t"
            "ds_read2_b64 v[72:75], %2 offset1:1\n\tds_read2_b64 v[76:79], %3 offset1:1\n\tds_read2st64_b64 v[84:87], %4 offset0:48 offset1:72\n\t"
            "v_mov_b32 v91, %10\n\t"
            "s_waitcnt lgkmcnt(6)\n\t"
            "v_and_b32 v96, v96, v97\n\tv_bitop3_b32 v96, v96, v98, v99 bitop3:0x80\n\tv_cmp_ne_u32 vcc, v96, %6\n\t"
            "s_cbranch_vccnz .Lkv_skip_%=\n\t"
            "s_waitcnt lgkmcnt(3)\n\t"
            FMA0("v[64:65]") FMA0("v[66:67]") FMA0("v[80:81]")
            FMA("v[64:65]", "%7", "v[68:69]", "1") FMA("v[66:67]", "%7", "v[70:71]", "1") FMA("v[80:81]", "%7", "v[82:83]", "1")
            "s_waitcnt lgkmcnt(0)\n\t"
            FMA("v[64:65]", "%8", "v[72:73]", "0") FMA("v[66:67]", "%8", "v[74:75]", "0") FMA("v[80:81]", "%8", "v[84:85]", "0")
            FMA("v[64:65]", "%8", "v[76:77]", "1") FMA("v[66:67]", "%8", "v[78:79]", "1") FMA("v[80:81]", "%8", "v[86:87]", "1")
            "v_cvt_pk_bf16_f32 v88, v64, v65\n\tv_cvt_pk_bf16_f32 v89, v66, v67\n\tv_cvt_pk_bf16_f32 v90, v80, v81\n\t"
            "buffer_store_dwordx4 v[88:91], %11, %12, %13 offen sc1\n"
            ".Lkv_skip_%=:\n\ts_waitcnt lgkmcnt(0)"
            :: "v"(la[0]), "v"(la[1]), "v"(la[2]), "v"(la[3]), "v"(la[4]), "v"(lq), "v"(lv), "v"(lf01), "v"(lf23), "v"(lz), "v"(lw), "v"(lo), "s"(rws), "s"(lso), "v"(ld_)
            : "v64", "v65", "v66", "v67", "v68", "v69", "v70", "v71", "v72", "v73", "v74", "v75", "v76", "v77", "v78", "v79", "v80", "v81", "v82", "v83",
              "v84", "v85", "v86", "v87", "v88", "v89", "v90", "v91", "v96", "v97", "v98", "v99", "vcc", "memory");
        asm volatile("s_nop 0" ::: "v88", "v89", "v90", "v91");
        PIN();
    }
    {
        unsigned inbox = pair_off(bid >> 4) + ((bid >> 1 & 4) + (bid & 3)) * 4 * BLK, obase = bid * 48;
        unsigned xo = tid * 16 + ((bid >> 2 ^ sides >> (bid >> 6)) & 1) * BLK, yo = (xo ^ (bid >> 4 < NW ? BLK : 0)) + 2 * BLK, sel = lane & 1 ? 0x01000504u : 0x05040100u;
        unsigned fl = 3 * NI;
        u32x4 rout = {(unsigned)kb[4], (unsigned)(kb[4] >> 32) & 0xffffu, NH * DH * 2, 0x00020000};
        typedef __attribute__((address_space(3))) float lds_f;
        lds_f* wo = (lds_f*)s_red + wave * 28 + (r < 3 ? (c & 3) * 6 + r * 2 : 24);
        lds_f* fo = (lds_f*)s_red + (lane & 7) * 28 + 24;
        lds_f* go = (lds_f*)s_red + (lane & 7) * 28 + min(lane >> 3, 5) * 4;
        vlds_u32* dw = (vlds_u32*)(s_done + wave);
        vlds_u32* dr = (vlds_u32*)(s_done + (lane & 7));
        unsigned oo = (lane & 7) == 0 && lane < 48 ? lane : OOB, eo = bid == 0 && lane < 4 ? lane * 2 : OOB;
        asm volatile("" : "+s"(inbox), "+s"(obase), "+v"(xo), "+v"(yo), "+v"(sel), "+v"(fl), "+s"(rout), "+v"(wo), "+v"(fo), "+v"(go), "+v"(oo), "+v"(eo), "+v"(dw), "+v"(dr));
        const unsigned o = __builtin_amdgcn_readfirstlane(origin) - (bid >= 240 ? (7 - wave) * 900 + (bid < 248 ? 6000 : 0) : 3328 + (14 - (bid >> 4)) * 750);
        while ((int)((now32() << 8) - o) < pace.rd) __builtin_amdgcn_s_sleep(1);
        PIN();
        u32x4 x, y;
        unsigned same;
        do {
            x = __builtin_amdgcn_raw_buffer_load_b128(rw_, xo, inbox, DEV | NT);
            y = __builtin_amdgcn_raw_buffer_load_b128(rw_, yo, inbox, DEV | NT);
            same = now32() - tstart > SPIN_LIMIT ? ~0u : 0xffffu;
            asm volatile("" : "+s"(same));
            PIN();
        } while (__builtin_amdgcn_ballot_w64(((x[3] ^ tag) | (y[3] ^ tag)) > same) != 0);
#define DPP(c) " " c " row_mask:0xf bank_mask:0xf\n\t"
        asm volatile(
            "v_mov_b32_dpp v54, %3" DPP("quad_perm:[1,0,3,2]") "v_mov_b32_dpp v55, %7" DPP("quad_perm:[1,0,3,2]")
            "v_perm_b32 v54, v54, %3, %8\n\tv_perm_b32 v55, v55, %7, %8\n\t"
            "v_max_f32 v56, v54, v55\n\t"
            "v_lshlrev_b32 v40, 16, %0\n\tv_and_b32 v41, 0xffff0000, %0\n\t"
            "v_max_f32_dpp v56, v56, v56" DPP("row_ror:8")
            "v_lshlrev_b32 v42, 16, %1\n\tv_and_b32 v43, 0xffff0000, %1\n\t"
            "v_max_f32_dpp v56, v56, v56" DPP("row_ror:4")
            "v_lshlrev_b32 v44, 16, %2\n\tv_and_b32 v45, 0xffff0000, %2\n\t"
            "v_max_f32_dpp v56, v56, v56 row_bcast:15 row_mask:0xa bank_mask:0xf\n\t"
            "v_lshlrev_b32 v48, 16, %4\n\tv_and_b32 v49, 0xffff0000, %4\n\t"
            "v_max_f32_dpp v56, v56, v56 row_bcast:31 row_mask:0xc bank_mask:0xf\n\t"
            "v_lshlrev_b32 v50, 16, %5\n\t"
            "v_readlane_b32 vcc_lo, v56, 63\n\t"
            "v_and_b32 v51, 0xffff0000, %5\n\tv_lshlrev_b32 v52, 16, %6\n\t"
            "v_subrev_f32 v54, vcc_lo, v54\n\tv_subrev_f32 v55, vcc_lo, v55\n\t"
            "v_exp_f32 v54, v54\n\tv_exp_f32 v55, v55\n\t"
            "v_and_b32 v53, 0xffff0000, %6\n\t"
            "v_pk_mul_f32 v[40:41], v[40:41], v[54:55] op_sel_hi:[1,0]\n\t"
            "v_pk_mul_f32 v[44:45], v[44:45], v[54:55] op_sel_hi:[1,0]\n\t"
            "v_pk_mul_f32 v[42:43], v[42:43], v[54:55] op_sel_hi:[1,0]\n\t"
            "v_pk_fma_f32 v[40:41], v[48:49], v[54:55], v[40:41] op_sel:[0,1,0]\n\t"
            "v_pk_fma_f32 v[44:45], v[52:53], v[54:55], v[44:45] op_sel:[0,1,0]\n\t"
            "v_pk_fma_f32 v[42:43], v[50:51], v[54:55], v[42:43] op_sel:[0,1,0]\n\t"
            "v_add_f32 v46, v54, v55\n\t"
            "v_ldexp_f32 v47, vcc_lo, -4\n\t"
            "v_permlane32_swap_b32 v40, v44\n\tv_permlane32_swap_b32 v41, v45\n\tv_permlane32_swap_b32 v42, v46\n\tv_permlane32_swap_b32 v43, v47\n\t"
            "v_pk_add_f32 v[40:41], v[40:41], v[44:45]\n\tv_pk_add_f32 v[42:43], v[42:43], v[46:47]\n\t"
            "s_nop 1\n\t"
            "v_permlane16_swap_b32 v40, v42\n\tv_permlane16_swap_b32 v41, v43\n\t"
            "v_add_f32 v40, v40, v42\n\tv_add_f32 v41, v41, v43\n\t"
            "s_nop 0\n\t"
            "v_add_f32_dpp v40, v40, v40" DPP("row_ror:8") "v_add_f32_dpp v41, v41, v41" DPP("row_ror:8")
            "s_nop 0\n\t"
            "v_add_f32_dpp v40, v40, v40" DPP("row_ror:4") "v_add_f32_dpp v41, v41, v41" DPP("row_ror:4")
            "ds_write_b64 %9, v[40:41]\n\tds_write_b32 %10, %11"
            :: "v"(x[0]), "v"(x[1]), "v"(x[2]), "v"(x[3]), "v"(y[0]), "v"(y[1]), "v"(y[2]), "v"(y[3]), "v"(sel), "v"(wo), "v"(dw), "v"(fl)
            : "v40", "v41", "v42", "v43", "v44", "v45", "v46", "v47", "v48", "v49", "v50", "v51", "v52", "v53", "v54", "v55", "v56", "vcc", "memory");
        PIN();
        const unsigned fin = *dr;
        PIN();
        f32x2 sm = *(__attribute__((address_space(3))) f32x2*)fo;
        const f32x4 ov = *(__attribute__((address_space(3))) f32x4*)go;
        f32x2 o01 = {ov[0], ov[1]}, o23 = {ov[2], ov[3]};
        asm volatile("" : "+v"(sm), "+v"(o01), "+v"(o23));
        if (__builtin_amdgcn_ballot_w64(fin != 3 * NI) == 0) {
#define ADD4(c) "v_add_f32_dpp v40, v40, v40" DPP(c) "v_add_f32_dpp v41, v41, v41" DPP(c) "v_add_f32_dpp v42, v42, v42" DPP(c) "v_add_f32_dpp v43, v43, v43" DPP(c)
            asm volatile(
                "v_max_f32_dpp v46, %3, %3" DPP("quad_perm:[1,0,3,2]") "s_nop 1\n\t"
                "v_max_f32_dpp v46, v46, v46" DPP("quad_perm:[2,3,0,1]") "s_nop 1\n\t"
                "v_max_f32_dpp v46, v46, v46" DPP("row_half_mirror")
                "v_sub_f32 v46, %3, v46\n\tv_exp_f32 v46, v46\n\ts_nop 0\n\t"
                "v_mul_f32 v44, %2, v46\n\t"
                "v_pk_mul_f32 v[40:41], %4, v[46:47] op_sel_hi:[1,0]\n\tv_pk_mul_f32 v[42:43], %5, v[46:47] op_sel_hi:[1,0]\n\t"
                "v_add_f32_dpp v44, v44, v44" DPP("quad_perm:[1,0,3,2]") ADD4("quad_perm:[1,0,3,2]")
                "v_add_f32_dpp v44, v44, v44" DPP("quad_perm:[2,3,0,1]") ADD4("quad_perm:[2,3,0,1]")
                "v_add_f32_dpp v44, v44, v44" DPP("row_half_mirror") "v_rcp_f32 v44, v44\n\t" ADD4("row_half_mirror")
                "v_pk_mul_f32 v[40:41], v[40:41], v[44:45] op_sel_hi:[1,0]\n\tv_pk_mul_f32 v[42:43], v[42:43], v[44:45] op_sel_hi:[1,0]\n\t"
                "v_cvt_pk_bf16_f32 v40, v40, v41\n\tv_cvt_pk_bf16_f32 v41, v42, v43\n\t"
                "buffer_store_dwordx2 v[40:41], %0, %1, %6 offen"
                :: "v"(oo), "s"(rout), "v"(sm[0]), "v"(sm[1]), "v"(o01), "v"(o23), "s"(obase)
                : "v40", "v41", "v42", "v43", "v44", "v45", "v46", "v47", "memory");
            if (bid == 0) __builtin_amdgcn_raw_buffer_store_b16((unsigned short)(tag >> 16), rw_, eo, WS_EPOCH, DEV);
        }
    }
    __builtin_amdgcn_endpgm();
}

extern "C" void kernel_launch(void* const* d_in, const int* in_sizes, int n_in,
                              void* d_out, int out_size, void* d_ws, size_t ws_size,
                              hipStream_t stream) {
    const float t_us = 100.6f, start_us = 0.15f, seam_us = 0.1f, scales_us = 1.1f;
    const float end_us = start_us + scales_us + 2 * seam_us + t_us * (129.f / 128.f);
    Pace pace;
    pace.step = (unsigned)(t_us * 25600.f / 128.f);
    pace.start = (int)(start_us * 25600.f);
    pace.offset = (int)(scales_us * 25600.f) | (int)(seam_us * 6400.f) << 16;
    pace.bias = (int)(0.052f * 25600.f);
    pace.rd = (int)((end_us + 1.46f) * 25600.f);
    pace.pub = (int)((end_us + 0.46f) * 25600.f);
    kv_sweep_kernel<<<NWG, 576, 0, stream>>>(d_in[0], d_in[1], d_in[2], d_in[3], d_in[4], d_in[5], d_in[6], d_in[7],
        d_in[8], d_in[9], d_in[10], d_ws, d_out, pace);
}
```

```cpp
#include <hip/hip_runtime.h>
#include <cstdint>


using u32x4 = __attribute__((ext_vector_type(4))) unsigned;
using u32x3 = __attribute__((ext_vector_type(3))) unsigned;
using u32x2 = __attribute__((ext_vector_type(2))) unsigned;
using u64x8 = __attribute__((ext_vector_type(8))) unsigned long long;
using f32x2 = __attribute__((ext_vector_type(2))) float;
using f32x4 = __attribute__((ext_vector_type(4))) float;
using bf16x2 = __attribute__((ext_vector_type(2))) __bf16;

constexpr int DH = 192;
constexpr int HALF = 96;
constexpr int NH = 32;
constexpr int SKV = 16384;
constexpr int NWG = 256;
constexpr int NI = 16;
constexpr int NB = 2;
constexpr unsigned ROWB = DH * 4;
constexpr unsigned HEADB = SKV * ROWB;
constexpr unsigned KVB = NH * HEADB;
constexpr unsigned OOB = 0x80000000u;
constexpr int DEV = 16, NT = 2;
constexpr float SCALE_LOG2 = 1.4426950408889634f * 0.07216878364870322f;
constexpr unsigned SPIN_LIMIT = 20000000u;

constexpr unsigned BLK = 8192;
constexpr int NW = 4;
constexpr unsigned WS_EPOCH = 1u << 20;
constexpr unsigned WS_SIDE = WS_EPOCH + 128;
constexpr unsigned WS_REP = WS_EPOCH + 256;
constexpr unsigned WS_PROBE = 3u << 19;
constexpr unsigned WS_BYTES = 8u << 20;
__device__ __forceinline__ unsigned pair_off(int i) { return (i >> 2 << 21) + (i & 3) * (32 * BLK); }

__device__ __forceinline__ float bflo(unsigned u) { return __uint_as_float(u << 16); }
__device__ __forceinline__ float bfhi(unsigned u) { return __uint_as_float(u & 0xffff0000u); }
__device__ __forceinline__ unsigned pk_bf16(f32x2 a) { return __builtin_bit_cast(unsigned, __builtin_convertvector(a, bf16x2)); }
__device__ __forceinline__ void dot_cvt(const u32x4 q, const u32x4 t, const u32x4 ki, f32x2& w01, f32x2& w23, f32x2& k01, f32x2& k23) {
    float w0, w1, w2, w3;
    float k0 = __uint_as_float(ki[0]), k1 = __uint_as_float(ki[1]), k2 = __uint_as_float(ki[2]), k3 = __uint_as_float(ki[3]);
    asm("v_dot2_f32_bf16 %0, %8, %12, 0\n\tv_dot2_f32_bf16 %1, %9, %13, 0\n\tv_dot2_f32_bf16 %2, %10, %14, 0\n\tv_dot2_f32_bf16 %3, %11, %15, 0\n\t"
        "v_cvt_f32_i32 %4, %4\n\tv_cvt_f32_i32 %5, %5\n\tv_cvt_f32_i32 %6, %6\n\tv_cvt_f32_i32 %7, %7"
        : "=&v"(w0), "=&v"(w1), "=&v"(w2), "=&v"(w3), "+v"(k0), "+v"(k1), "+v"(k2), "+v"(k3)
        : "v"(q[0]), "v"(q[1]), "v"(q[2]), "v"(q[3]), "v"(t[0]), "v"(t[1]), "v"(t[2]), "v"(t[3]));
    w01 = f32x2{w0, w1}; w23 = f32x2{w2, w3}; k01 = f32x2{k0, k1}; k23 = f32x2{k2, k3};
}

template <int CTRL> __device__ __forceinline__ float dpp(float x) {
    return __builtin_bit_cast(float, __builtin_amdgcn_mov_dpp(__builtin_bit_cast(int, x), CTRL, 0xf, 0xf, true));
}
__device__ __forceinline__ float row16_allsum(float x) {
    x += dpp<0x128>(x);
    x += dpp<0x124>(x);
    x += dpp<0x122>(x);
    x += dpp<0x121>(x);
    return x;
}
__device__ __forceinline__ float xrow_max(float x) {
    auto s = __builtin_amdgcn_permlane16_swap(__float_as_uint(x), __float_as_uint(x), false, false);
    x = fmaxf(__uint_as_float(s[0]), __uint_as_float(s[1]));
    auto t = __builtin_amdgcn_permlane32_swap(__float_as_uint(x), __float_as_uint(x), false, false);
    return fmaxf(__uint_as_float(t[0]), __uint_as_float(t[1]));
}
__device__ __forceinline__ float xrow_sum(float x) {
    auto s = __builtin_amdgcn_permlane16_swap(__float_as_uint(x), __float_as_uint(x), false, false);
    x = __uint_as_float(s[0]) + __uint_as_float(s[1]);
    auto t = __builtin_amdgcn_permlane32_swap(__float_as_uint(x), __float_as_uint(x), false, false);
    return __uint_as_float(t[0]) + __uint_as_float(t[1]);
}
__device__ __forceinline__ float swap32_add(float a, float b) {
    auto t = __builtin_amdgcn_permlane32_swap(__float_as_uint(a), __float_as_uint(b), false, false);
    return __uint_as_float(t[0]) + __uint_as_float(t[1]);
}
__device__ __forceinline__ float swap16_add(float a, float b) {
    auto t = __builtin_amdgcn_permlane16_swap(__float_as_uint(a), __float_as_uint(b), false, false);
    return __uint_as_float(t[0]) + __uint_as_float(t[1]);
}
__device__ __forceinline__ int row16_sum(int x) {
    x += __builtin_amdgcn_mov_dpp(x, 0x128, 0xf, 0xf, true);
    x += __builtin_amdgcn_mov_dpp(x, 0x124, 0xf, 0xf, true);
    x += __builtin_amdgcn_mov_dpp(x, 0x122, 0xf, 0xf, true);
    x += __builtin_amdgcn_mov_dpp(x, 0x121, 0xf, 0xf, true);
    return x;
}
__device__ __forceinline__ unsigned now32() { return (unsigned)__builtin_amdgcn_s_memrealtime(); }

#define PIN() do { asm volatile("" ::: "memory"); __builtin_amdgcn_sched_barrier(0); } while (0)
#define BARRIER() do { asm volatile("s_waitcnt lgkmcnt(0)" ::: "memory"); __builtin_amdgcn_s_barrier(); PIN(); } while (0)
#define LDSA(p) ((unsigned)(unsigned long long)(__attribute__((address_space(3))) void*)(p))
#define VMCNT(n) asm volatile("s_waitcnt vmcnt(%0)" :: "i"(n) : "memory")

typedef volatile __attribute__((address_space(3))) unsigned vlds_u32;
typedef volatile __attribute__((address_space(3))) u32x2 vlds_u32x2;
typedef volatile __attribute__((address_space(3))) u32x4 vlds_u32x4;

struct Pace {
    unsigned step;
    int start;
    int offset;
    int bias;
    int rd;
    int pub;
};

__global__ __launch_bounds__(576, 3) __attribute__((aligned(4096))) void kv_sweep_kernel(
    const void*, const void*, const void*, const void*, const void*, const void*, const void*, const void*,
    const void*, const void*, const void*, void*, void*, Pace)
{
    __shared__ __attribute__((aligned(16))) unsigned s_q[NH * DH];
    __shared__ __attribute__((aligned(16))) unsigned short s_sc[NH * 256];
    __shared__ __attribute__((aligned(16))) float s_w[NH * 4 * 20];
    __shared__ __attribute__((aligned(16))) float s_part[8 * NI * DH];
    __shared__ __attribute__((aligned(16))) float s_red[8 * 28];
    __shared__ __attribute__((aligned(16))) float s_f[NH * 8];
    __shared__ __attribute__((aligned(16))) unsigned s_done[8];
    __shared__ __attribute__((aligned(16))) unsigned s_sync[8];
    __shared__ __attribute__((aligned(16))) unsigned s_act[16];

    unsigned long long pc0, t0;
    u64x8 ka, kb;
    asm volatile("s_load_dwordx16 %3, %4, 0x40\n\ts_load_dwordx16 %2, %4, 0x0\n\ts_memrealtime %1\n\ts_getpc_b64 %0\n.Lkv_beg:"
                 : "=&s"(pc0), "=&s"(t0), "=&s"(ka), "=&{s[8:23]}"(kb) : "s"(__builtin_amdgcn_kernarg_segment_ptr()));
    unsigned clen, xw;
    asm volatile("s_mov_b32 %0, .Lkv_end-.Lkv_beg" : "=s"(clen));
    asm volatile("global_load_dword %0, %1, %2" : "=&v"(xw) : "v"(min((blockIdx.x >> 3) * 64 + (threadIdx.x >> 6) * 2048, clen)), "s"(pc0 & ~63ull) : "memory");
    __builtin_amdgcn_sched_barrier(0);
    const int tid = threadIdx.x;
    const int lane = tid & 63;
    int wave = __builtin_amdgcn_readfirstlane(tid >> 6);
    const int r = lane >> 4;
    const int c = lane & 15;
    const int bid = blockIdx.x;
    const int rw = wave & 3, hp = wave >> 2;
    constexpr int RS = 8192 + 128, RT = 1024 + 256;
    const int grow = ((bid >> 3 & 3) | (bid >> 4 & 4) | (bid & 0x84) << 1 | (bid & 2) << 3 | (bid & 32) | (bid & 1) << 7) * 16;
    auto srow = [&](int t) { return (t & 1) * RS ^ (t >> 1) * RT; };
    const int row0 = grow + rw * 4;
    const unsigned rb0 = (row0 ^ RT * hp) * 384u;
    unsigned co = r * 384 + c * 8 + rb0;
#define LOAD_CS(a) "global_load_dwordx2 %0, " a ", s[10:11] nt\n\tglobal_load_dwordx2 %3, " a ", s[12:13] nt\n\t" \
                   "global_load_dwordx2 %1, " a ", s[10:11] offset:128 nt\n\tglobal_load_dwordx2 %4, " a ", s[12:13] offset:128 nt\n\t" \
                   "global_load_dwordx2 %2, " a ", s[10:11] offset:256 nt\n\tglobal_load_dwordx2 %5, " a ", s[12:13] offset:256 nt\n"
    u32x2 raw[2][6];
    unsigned long long t1;
    asm volatile("s_cmp_eq_u32 %11, 8\n\t.p2align 6\n\ts_waitcnt lgkmcnt(0)\n\ts_memrealtime %6\n\ts_cbranch_scc1 .Lkv_arg\n\t"
                 LOAD_CS("%10") ".Lkv_arg:\n\ts_waitcnt lgkmcnt(0)"
                 : "=&v"(raw[0][0]), "=&v"(raw[0][1]), "=&v"(raw[0][2]), "=&v"(raw[0][3]), "=&v"(raw[0][4]), "=&v"(raw[0][5]), "=&s"(t1),
                   "+s"(t0), "+s"(ka), "+{s[8:23]}"(kb), "+v"(co), "+s"(wave) :: "scc", "memory");
    __builtin_amdgcn_sched_barrier(0);
    typedef const __attribute__((address_space(1))) char* gptr;
    void* const q = (void*)ka[0]; void* const qk = (void*)ka[1]; void* const qv = (void*)ka[4]; void* const q_cos = (void*)ka[7];
    const gptr ksc = (gptr)ka[2], kzp = (gptr)ka[3], vsc = (gptr)ka[5], vzp = (gptr)ka[6];
    void* const q_sin = (void*)kb[0];
    const unsigned long long wsa = kb[3] + 0x1fffff & ~0x1fffffull;
    unsigned char* const ws = (unsigned char*)(__attribute__((address_space(1))) unsigned char*)wsa;
    const Pace pace = {(unsigned)kb[5], (int)(kb[5] >> 32), (int)kb[6], (int)(kb[6] >> 32), (int)kb[7], (int)(kb[7] >> 32)};
    const int m = (bid >> 5) * 16 + (bid & 3) * 4 + (bid >> 3 & 3);
    const unsigned tstart = (unsigned)t0;
    const unsigned tsched = (unsigned)t1;
    const auto rw_ = __builtin_amdgcn_make_buffer_rsrc((void*)ws, 0, WS_BYTES, 0x00020000);

    auto pick = [](int n, gptr p0, gptr p1, gptr p2, gptr p3) { return (n & 2) ? ((n & 1) ? p3 : p2) : ((n & 1) ? p1 : p0); };
    auto merge = [&](const float* p, f32x4 f, float zs, unsigned w3) {
        f32x2 u[4][3], a[3];
#pragma unroll
        for (int w = 0; w < 4; ++w)
#pragma unroll
            for (int k = 0; k < 3; ++k) u[w][k] = *reinterpret_cast<const f32x2*>(p + w * NI * DH + 2 * k);
        asm volatile("" : "+v"(u[0][0]), "+v"(u[0][1]), "+v"(u[0][2]), "+v"(u[1][0]), "+v"(u[1][1]), "+v"(u[1][2]),
                          "+v"(u[2][0]), "+v"(u[2][1]), "+v"(u[2][2]), "+v"(u[3][0]), "+v"(u[3][1]), "+v"(u[3][2]));
#pragma unroll
        for (int k = 0; k < 3; ++k) a[k] = (f[0] * u[0][k] - zs + f[1] * u[1][k]) + (f[2] * u[2][k] + f[3] * u[3][k]);
        return u32x4{pk_bf16(a[0]), pk_bf16(a[1]), pk_bf16(a[2]), w3};
    };
    auto last_word = [&](unsigned tag, float ls, int c6) { return tag | ((__float_as_uint(ls) >> (16 * (c6 & 1))) & 0xffffu); };
    auto piece_off = [&](int hp, int c6) { return (hp * 16 + (c6 & 12) + ((c6 >> 4 ^ bid >> 2) & 1) * 2 + (c6 >> 4)) * BLK + (c6 & 3) * 16; };
    auto publish = [&](u32x4 v, int i, unsigned vo, unsigned sides) {
        __builtin_amdgcn_raw_buffer_store_b128(v, rw_, vo ^ (sides >> (i >> 2) & 1) * BLK ^ (i < NW ? vo >> 1 & BLK : 0), pair_off(i) + m * 64, DEV);
        asm volatile("s_nop 1" :: "v"(v) : "memory");
    };

    if (__builtin_expect_with_probability(wave == 8, 1, 0.45)) {
        __builtin_amdgcn_s_setprio(3);
        unsigned sci = 0;
        gptr scp = pick(lane >> 1, ksc, kzp, vsc, vzp) + ((lane >> 5) * SKV + (grow ^ srow(lane >> 3 & 3)) + (lane & 1) * 8) * 2;
        auto gather = [&]() {
            asm volatile("s_mov_b32 m0, %1\n\ts_nop 0\n\tglobal_load_lds_dwordx4 %0, off nt"
                         :: "v"(scp), "s"(LDSA(s_sc) + sci * 1024) : "memory");
            scp += 4 * SKV;
        };
        while (now32() - tsched < 100) __builtin_amdgcn_s_sleep(1);
        u32x3 lc[2], ls[2];
        u32x4 qraw[12];
        {
            const auto rq = __builtin_amdgcn_make_buffer_rsrc((void*)q, 0, NH * DH * 2, 0x00020000);
            const auto rqc = __builtin_amdgcn_make_buffer_rsrc((void*)q_cos, 0, DH * 2, 0x00020000);
            const auto rqs = __builtin_amdgcn_make_buffer_rsrc((void*)q_sin, 0, DH * 2, 0x00020000);
#pragma unroll
            for (int k = 0; k < 12; ++k) qraw[k] = __builtin_amdgcn_raw_buffer_load_b128(rq, lane * 16 + (k & 3) * 1024, (k >> 2) * 4096, 0);
#pragma unroll
            for (int k = 0; k < 2; ++k) {
                lc[k] = __builtin_amdgcn_raw_buffer_load_b96(rqc, c * 12 + k * 192, 0, 0);
                ls[k] = __builtin_amdgcn_raw_buffer_load_b96(rqs, c * 12 + k * 192, 0, 0);
            }
            PIN();
        }
        const unsigned tag = ((unsigned)__builtin_amdgcn_raw_buffer_load_b16(rw_, (lane & 3) * 2, WS_EPOCH, DEV) + 1u) << 16;
        PIN();
        unsigned* const s_raw = reinterpret_cast<unsigned*>(s_part) + NI * DH / 2;
#pragma unroll
        for (int k = 0; k < 12; ++k) *reinterpret_cast<u32x4*>(s_raw + k / 6 * (NI * DH) + k % 6 * 256 + lane * 4) = qraw[k];
        f32x2 fc[2][3], fs[2][3];
#pragma unroll
        for (int k = 0; k < 2; ++k)
#pragma unroll
            for (int u = 0; u < 3; ++u) { fc[k][u] = f32x2{bflo(lc[k][u]), bfhi(lc[k][u])}; fs[k][u] = f32x2{bflo(ls[k][u]), bfhi(ls[k][u])}; }
        PIN();
#pragma nounroll
        for (int it = 0; it < 4; ++it) {
            if (sci < NB) { gather(); ++sci; }
            unsigned qa[2][3], qb[2][3];
#pragma unroll
            for (int g = 0; g < 2; ++g)
#pragma unroll
                for (int u = 0; u < 3; ++u) {
                    const unsigned* src = s_raw + g * (NI * DH) + (it * 4 + r) * HALF + 3 * c;
                    qa[g][u] = src[u]; qb[g][u] = src[HALF / 2 + u];
                }
#pragma unroll
            for (int g = 0; g < 2; ++g)
#pragma unroll
                for (int u = 0; u < 3; ++u) {
                    const f32x2 a = {bflo(qa[g][u]), bfhi(qa[g][u])}, b = {bflo(qb[g][u]), bfhi(qb[g][u])};
                    const f32x2 o0 = a * fc[0][u] - b * fs[0][u], o1 = b * fc[1][u] + a * fs[1][u];
                    const unsigned l0 = pk_bf16(f32x2{o0[0], o1[0]}), l1 = pk_bf16(f32x2{o0[1], o1[1]});
                    unsigned* dst = s_q + (16 * g + 4 * it + r) * DH + 6 * c + 2 * u;
                    *reinterpret_cast<u32x2*>(dst) = u32x2{l0, l1};
                    *reinterpret_cast<u32x2*>(dst + HALF) = u32x2{__builtin_amdgcn_alignbit(l0, l0, 16) ^ 0x80000000u, __builtin_amdgcn_alignbit(l1, l1, 16) ^ 0x80000000u};
                }
        }
        asm volatile("" :: "v"(xw));
        __builtin_amdgcn_s_setprio(0);
        unsigned origin = tsched << 8;
        if (lane < 8) { s_done[lane] = 0; s_sync[lane] = lane < 3 ? origin : lane < 4 ? NB : tag; }
        PIN();
        VMCNT(0);
        BARRIER();
        const int mh = lane >> 5, mc = lane & 31;
        const unsigned rtag = __builtin_amdgcn_readlane(tag, 0) | __builtin_amdgcn_readlane(tag, 1) >> 16;
        const unsigned probe = WS_PROBE + ((bid >> 3 & 3) << 21);
        constexpr int RPX = 2;
        constexpr int RSH = 9;
        constexpr unsigned RDLY = 200;
        constexpr int UP = 300, DN = 2000;
        const int rj = ((bid >> 3) + 5 * (bid & 7)) & 31;
        const int rid = rj % (32 / RPX) == 0 ? rj / (32 / RPX) * 8 + (bid & 7) : -1;
        unsigned wround = now32() >> RSH, rround = wround, tick = 0, target = origin;
        bool synced = false;
        int drift = 0;
        const unsigned round0 = rround;
        int i = 0;
        unsigned sct = tstart, sides = 0;
        int np = (bid & ~24) == 32 ? 0 : -1, score = 0;
        const unsigned stag = __builtin_amdgcn_readlane(tag, 2) | __builtin_amdgcn_readlane(tag, 3) >> 16;
        auto kept_now = [&](unsigned now) {
            const u32x2 a = *(vlds_u32x2*)(s_act + 2 * (lane & 7));
            const int late = max((int)(a[0] - origin), (int)((now << 8) - a[1] - origin));
            return origin + (__builtin_amdgcn_readfirstlane(row16_sum(late)) >> 4);
        };
        auto steer = [&](unsigned now) {
            if ((now >> 5) != tick) {
                tick = now >> 5;
                target += drift;
                origin += max(-DN, min(UP + drift, (int)(target - origin)));
                if (!synced) {
                    origin += max(0, (int)(kept_now(now) - origin) - 2600);
                    target = origin;
                }
                if (lane == 0) *(vlds_u32*)(s_sync + 2) = origin;
                PIN();
            }
        };
        auto finished = [&]() {
            const u32x4 a = *(vlds_u32x4*)s_done, b = *(vlds_u32x4*)(s_done + 4);
            return __builtin_amdgcn_readfirstlane(min(min(min(a[0], a[1]), min(a[2], a[3])), min(min(b[0], b[1]), min(b[2], b[3]))));
        };
        while (i < NI - 2) {
            const unsigned now = now32();
            if ((now >> RSH) != wround) {
                wround = now >> RSH;
                if (rid >= 0 && (now & ((1u << RSH) - 1)) < 48)
                    __builtin_amdgcn_raw_buffer_store_b64(u32x2{kept_now(now), rtag + wround}, rw_, lane == 0 ? rid * 8 : OOB, WS_REP + (wround & 255) * 512, DEV);
                PIN();
                continue;
            }
            if ((int)(((now - RDLY) >> RSH) - rround) > 0) {
                rround = (now - RDLY) >> RSH;
                const u32x2 x = __builtin_amdgcn_raw_buffer_load_b64(rw_, c * 8, WS_REP + (rround & 255) * 512, DEV);
                const int d = (int)(x[0] - origin);
                const bool ok = x[1] == rtag + rround && d < (32 << 16) && d > -(32 << 16);
                const int n = __builtin_popcountll(__builtin_amdgcn_ballot_w64(ok));
                if (n >= 48) {
                    const unsigned nt = origin + __builtin_amdgcn_readfirstlane((int)(row16_allsum(ok ? (float)d : 0.f) * 4.f / (float)n)) - pace.bias;
                    if (synced && rround - round0 > 2) drift = max(0, min(drift + ((max(-1024, min((int)(nt - target) + 6 * drift, 5120)) - (rround - round0 < 10 ? 768 : 0)) >> 5), 800));
                    target = nt + 6 * drift;
                    synced = true;
                    const int lost = min((int)(kept_now(now32()) - origin) - pace.bias, (int)(target - origin));
                    if (lost > 0) origin += lost;
                }
                PIN();
                continue;
            }
            steer(now);
            if (sci <= NI && now - sct >= 100) {
                VMCNT(0);
                if (lane == 0) *(vlds_u32*)(s_sync + 3) = sci;
                if (sci < NI) gather();
                ++sci; sct = now;
                PIN();
                continue;
            }
            if (np >= 0 && sci > NI) {
                if (np < 8) {
                    const int o = (np ^ np >> 2) & 1;
                    const unsigned a = (np & 3) * 2048 + o * BLK;
                    unsigned long long u0, u1, u2;
                    unsigned x;
                    asm volatile("s_waitcnt lgkmcnt(0)\n\ts_memrealtime %0\n\ts_load_dword %3, %4, %5 glc\n\ts_waitcnt lgkmcnt(0)\n\t"
                                 "s_memrealtime %1\n\ts_load_dword %3, %4, %6 glc\n\ts_waitcnt lgkmcnt(0)\n\ts_memrealtime %2\n\ts_waitcnt lgkmcnt(0)"
                                 : "=&s"(u0), "=&s"(u1), "=&s"(u2), "=&s"(x) : "s"(wsa + probe), "s"(a), "s"(a ^ BLK) : "memory");
                    const int d = max(-16, min((int)(u2 + u0 - 2 * u1), 16));
                    score += o ? d : -d;
                    ++np;
                } else {
                    __builtin_amdgcn_raw_buffer_store_b64(u32x2{stag ^ (score > 0), rtag}, rw_, lane == 0 ? bid & 24 : OOB, WS_SIDE, DEV);
                    np = -1;
                }
                PIN();
                continue;
            }
            if (np >= 0 || finished() <= (unsigned)i) { __builtin_amdgcn_s_sleep(2); continue; }
            PIN();
            if (i == 0) {
                f32x4 ml[4], f;
#pragma unroll
                for (int w = 0; w < 4; ++w) ml[w] = *reinterpret_cast<const f32x4*>(s_w + (mc * 4 + w) * 20 + 16);
                const float M = fmaxf(fmaxf(ml[0][0], ml[1][0]), fmaxf(ml[2][0], ml[3][0]));
                float L = 0.f, zs = 0.f;
#pragma unroll
                for (int w = 0; w < 4; ++w) { f[w] = __builtin_amdgcn_exp2f(ml[w][0] - M); L += f[w] * ml[w][1]; zs += f[w] * ml[w][2]; }
                const float inv = 1.f / L;
                *reinterpret_cast<f32x4*>(s_f + mc * 8) = f * inv;
                *reinterpret_cast<f32x2*>(s_f + mc * 8 + 4) = f32x2{zs * inv, M + __builtin_amdgcn_logf(L)};
                u32x2 sd;
                do { sd = __builtin_amdgcn_raw_buffer_load_b64(rw_, (lane & 3) * 8, WS_SIDE, DEV); PIN(); }
                while (__builtin_amdgcn_ballot_w64(sd[1] != rtag || (sd[0] ^ stag) > 1) != 0 && now32() - tstart < SPIN_LIMIT);
                sides = (unsigned)__builtin_amdgcn_ballot_w64(sd[0] != stag) & 15;
                if (lane == 0) *(vlds_u32*)(s_sync + 1) = sides;
                PIN();
                if (lane == 0) *(vlds_u32*)(s_sync + 3) = NI + 1;
                PIN();
            }
            const f32x2 zl = *reinterpret_cast<const f32x2*>(s_f + (2 * i + mh) * 8 + 4);
            const u32x4 v = merge(s_part + (mh * 4 * NI + i) * DH + mc * 6, *reinterpret_cast<const f32x4*>(s_f + (2 * i + mh) * 8), zl[0], last_word(tag, zl[1], mc));
            PIN();
            *reinterpret_cast<u32x4*>(s_part + (mh * 4 * NI + i) * DH + mc * 4) = v;
            PIN();
            ++i;
        }
        u32x4 v[NI - 1];
#pragma unroll
        for (int i = 0; i < NI - 2; ++i) v[i] = *reinterpret_cast<const u32x4*>(s_part + (mh * 4 * NI + i) * DH + mc * 4);
        f32x4 f = *reinterpret_cast<const f32x4*>(s_f + (2 * (NI - 2) + mh) * 8);
        f32x2 zl = *reinterpret_cast<const f32x2*>(s_f + (2 * (NI - 2) + mh) * 8 + 4);
        PIN();
#pragma unroll
        for (int i = 0; i < NI - 2; ++i) asm volatile("" : "+v"(v[i]));
        asm volatile("" : "+v"(f), "+v"(zl));
        v[NI - 2] = v[0];
        bool have = false, sent = false;
        while (!(have && sent)) {
            const unsigned now = now32();
            if (!have) {
                steer(now);
                if (finished() > NI - 2) {
                    PIN();
                    v[NI - 2] = merge(s_part + (mh * 4 * NI + NI - 2) * DH + mc * 6, f, zl[0], last_word(tag, zl[1], mc));
                    have = true;
                }
            }
            if (!sent && (int)((now << 8) - origin) >= pace.pub) {
#pragma unroll
                for (int i = 0; i < NI - 2; ++i) publish(v[i], i, piece_off(mh, mc), sides);
                sent = true;
            }
            if (!(have && sent)) __builtin_amdgcn_s_sleep(1);
            PIN();
        }
        publish(v[NI - 2], NI - 2, piece_off(mh, mc), sides);
        asm volatile(".Lkv_end:");
        return;
    }

    const int ph = rw + 4 * bid + 1024 * hp;
    unsigned off = pace.start + ((pace.step * ph) >> 11);
    unsigned origin = tsched << 8, kept = 0;
    unsigned step = pace.step;
    auto pace_wait = [&]() {
        const unsigned tgt = __builtin_amdgcn_readfirstlane(origin) + off;
        unsigned now = now32() << 8;
        kept = ((int)(now - tgt) > 0 ? now : tgt) - off;
        while ((int)(tgt - now) > 0) { __builtin_amdgcn_s_sleep(1); now = now32() << 8; }
        off += step;
    };
    auto report = [&]() {
        if (lane == 0) *(vlds_u32x2*)(s_act + 2 * wave) = u32x2{kept, off};
        origin = *(vlds_u32*)(s_sync + 2);
        PIN();
    };

    auto pre_sc = [&]() {
        const unsigned junk = LDSA(s_part) + (3 * NI * DH - 256) * 4;
        const gptr a = pick(grow >> 4, ksc, kzp, vsc, vzp) + ((tid >> 5) * SKV + (grow & ~63 ^ srow(lane >> 3 & 3))) * 2 + (lane & 7) * 16;
        asm volatile("s_mov_b32 m0, %2\n\ts_nop 0\n\tglobal_load_lds_dwordx4 %0, off\n\tglobal_load_lds_dwordx4 %1, off" :: "v"(a), "v"(a + 32 * SKV), "s"(junk) : "memory");
        PIN();
    };
    const unsigned rb1 = (row0 ^ RT * hp ^ RS) * 384u - rb0;
    u32x4 tab[4][3];
    auto pack_cs = [&](int t) {
#pragma unroll
        for (int j = 0; j < 3; ++j) {
#pragma unroll
            for (int k = 0; k < 2; ++k) {
                const unsigned lo = __builtin_amdgcn_mov_dpp(raw[t][3 + (j + 1) % 3][k], 0x128, 0xf, 0x3, false);
                const unsigned sn = __builtin_amdgcn_update_dpp(lo, raw[t][3 + (j + 2) % 3][k], 0x128, 0xf, 0xc, false);
                tab[t][j][2 * k] = __builtin_amdgcn_perm(sn, raw[t][j][k], 0x05040100);
                tab[t][j][2 * k + 1] = __builtin_amdgcn_perm(sn, raw[t][j][k], 0x07060302);
            }
            *reinterpret_cast<u32x4*>(s_part + (wave ^ 4) * (NI * DH) + ((t * 3 + j) * 64 + lane) * 4) = tab[t][j];
        }
        PIN();
    };
    const auto rk = __builtin_amdgcn_make_buffer_rsrc((void*)qk, 0, KVB, 0x00020000);
    const auto rv = __builtin_amdgcn_make_buffer_rsrc((void*)qv, 0, KVB, 0x00020000);
    const unsigned voff = r * ROWB + c * 16;
    const unsigned woff = hp * HEADB;
    auto toff = [&](int t) { return (unsigned)(row0 ^ srow(t)) * ROWB; };
    u32x4 ring[4][3];
    auto ld = [&](int slot, int j, __amdgpu_buffer_rsrc_t rr, unsigned off) {
        ring[slot][j] = __builtin_amdgcn_raw_buffer_load_b128(rr, voff + j * 256, off, NT);
        PIN();
    };
    auto ld_tile = [&](int slot, __amdgpu_buffer_rsrc_t rr, unsigned off) { ld(slot, 0, rr, off); ld(slot, 1, rr, off); ld(slot, 2, rr, off); };
    unsigned post = 0;
    auto enter = [&](int i) {
        if (i == 9 || i == 10) {
            const unsigned hs = pace.step >> 1, z = (unsigned)pace.offset >> 16;
            off += i == 10 ? post : hp * 3 * hs;
            step = i == 10 ? pace.step : hs + hp * z;
            post = __builtin_amdgcn_readfirstlane(i == 10 ? 0 : hp ? hs : 4 * (hs + z));
            if (lane == 0) *(vlds_u32x2*)(s_act + 2 * wave) = u32x2{kept, off};
            PIN();
        }
    };
    auto refill = [&](int slot, int j, __amdgpu_buffer_rsrc_t rr, unsigned goff) {
        if (j == 0) pace_wait();
        if (j == 0) asm volatile("s_cmp_lg_u32 %0, 0\n\ts_waitcnt vmcnt(3)\n\ts_cbranch_scc1 1\n\ts_waitcnt vmcnt(2)" :: "s"(post) : "scc", "memory");
        else asm volatile("s_cmp_lg_u32 %0, 0\n\ts_cbranch_scc1 1\n\ts_waitcnt vmcnt(2)" :: "s"(post) : "scc", "memory");
        ld(slot, j, rr, goff);
        if (j == 0) { const unsigned t = (now32() << 8) - 1024 - (off - step); if ((int)(t - kept) > 0) kept = t; }
        if (j == 2) report();
    };
    auto k0 = [&](int t) { pace_wait(); ld_tile(t, rk, woff + toff(t)); };
    co += rb1;
    pace_wait();
    asm volatile(LOAD_CS("%6") : "=&v"(raw[1][0]), "=&v"(raw[1][1]), "=&v"(raw[1][2]), "=&v"(raw[1][3]), "=&v"(raw[1][4]), "=&v"(raw[1][5])
                 : "v"(co), "{s[8:23]}"(kb) : "memory");
    PIN();
    asm volatile("s_waitcnt vmcnt(6)" : "+v"(raw[0][0]), "+v"(raw[0][1]), "+v"(raw[0][2]), "+v"(raw[0][3]), "+v"(raw[0][4]), "+v"(raw[0][5]) :: "memory");
    pack_cs(0);
    pace_wait(); pre_sc(); off += (pace.offset & 0xffff) - step;
    k0(0);
    asm volatile("s_waitcnt vmcnt(5)" : "+v"(raw[1][0]), "+v"(raw[1][1]), "+v"(raw[1][2]), "+v"(raw[1][3]), "+v"(raw[1][4]), "+v"(raw[1][5]) :: "memory");
    pack_cs(1);
    VMCNT(3);
    asm volatile("" :: "v"(xw));
    k0(1);
    VMCNT(3); k0(2);
    VMCNT(3);
    if (hp) { if (lane == 0) *(vlds_u32x2*)(s_act + 2 * wave) = u32x2{kept, off}; BARRIER(); }
    k0(3);
    if (lane == 0) *(vlds_u32x2*)(s_act + 2 * wave) = u32x2{kept, off};
    if (!hp) BARRIER();
    origin = *(vlds_u32*)(s_sync + 2);

    u32x4 qp[3];
    auto load_q = [&](int h, int j) { qp[j] = *reinterpret_cast<const u32x4*>(s_q + h * DH + 64 * j + 4 * c); };
    load_q(hp, 0); load_q(hp, 1); load_q(hp, 2);
#pragma unroll
    for (int t = 0; t < 2; ++t)
#pragma unroll
        for (int j = 0; j < 3; ++j) tab[2 + t][j] = *reinterpret_cast<const u32x4*>(s_part + wave * (NI * DH) + ((t * 3 + j) * 64 + lane) * 4);
    if (hp) {
#pragma unroll
        for (int t = 0; t < 2; ++t)
#pragma unroll
            for (int j = 0; j < 3; ++j)
#pragma unroll
                for (int k = 0; k < 4; ++k) asm volatile("v_swap_b32 %0, %1" : "+v"(tab[t][j][k]), "+v"(tab[2 + t][j][k]));
    }

    unsigned ready = NB;
    for (int i = 0; i < NI; ++i) {
        const bool last = i + 1 == NI;
        enter(i);
        const int h = 2 * i + hp;
        const unsigned hnext = (i + 1) * (2 * HEADB) + woff;
        const auto rn = __builtin_amdgcn_make_buffer_rsrc((void*)(last ? qv : qk), 0, KVB, 0x00020000);
        float* scp = s_w + (h * 4 + rw) * 20;
        f32x4 sc[4];
        while (__builtin_amdgcn_readfirstlane(ready) <= i) { __builtin_amdgcn_s_sleep(1); ready = *(vlds_u32*)(s_sync + 3); }
        PIN();
#pragma unroll
        for (int t = 0; t < 4; ++t)
#pragma unroll
            for (int a = 0; a < 4; ++a) sc[t][a] = bflo(s_sc[h * 256 + t * 64 + a * 16 + rw * 4 + r]);
        float s[4];
#pragma unroll
        for (int t = 0; t < 4; ++t) {
            f32x2 dot, wsum;
#pragma unroll
            for (int j = 0; j < 3; ++j) {
                f32x2 w01, w23, k01, k23;
                dot_cvt(qp[j], tab[t][j], ring[t][j], w01, w23, k01, k23);
                if (j == 0) { wsum = w01 + w23; dot = k01 * w01; } else { wsum += w01; wsum += w23; dot += k01 * w01; }
                dot += k23 * w23;
                asm volatile("" : "+v"(dot), "+v"(wsum));
                PIN();
                if (t == 3) load_q(last ? h : h + 2, j);
                if (t == 3 && j == 0) ready = *(vlds_u32*)(s_sync + 3);
                refill(t, j, rn, (last ? woff : hnext) + toff(t));
            }
            const float a = (dot[0] + dot[1]) - sc[t][1] * (wsum[0] + wsum[1]);
            s[t] = row16_allsum(a) * (sc[t][0] * SCALE_LOG2);
        }
        const float mx = xrow_max(fmaxf(fmaxf(s[0], s[1]), fmaxf(s[2], s[3])));
        float l = 0.f, z = 0.f;
#pragma unroll
        for (int t = 0; t < 4; ++t) {
            const float p = __builtin_amdgcn_exp2f(s[t] - mx);
            l += p;
            s[t] = p * sc[t][2];
            z += s[t] * sc[t][3];
        }
        l = xrow_sum(l);
        z = xrow_sum(z);
        if (c == 0) {
            *reinterpret_cast<f32x4*>(scp + r * 4) = f32x4{s[0], s[1], s[2], s[3]};
            *reinterpret_cast<f32x4*>(scp + 16) = f32x4{mx, l, z, 0.f};
        }
        PIN();
    }

    const float* pwp = s_w + (hp * 4 + rw) * 20 + r * 4;
    f32x4 pw = *reinterpret_cast<const f32x4*>(pwp);
    unsigned tag, lw, lo;
    f32x4 lf;
    f32x2 lz;
    unsigned lp = (hp * 4 * NI + NI - 1) * DH + (lane & 31) * 6;
    f32x2 acc[3][2];
    auto v_acc = [&](int t, int j) {
        const f32x2 p2 = {pw[t], pw[t]};
        const u32x4 vv = ring[t][j];
        const f32x2 v01 = {(float)(int)vv[0], (float)(int)vv[1]}, v23 = {(float)(int)vv[2], (float)(int)vv[3]};
        if (t == 0) { acc[j][0] = p2 * v01; acc[j][1] = p2 * v23; }
        else { acc[j][0] += p2 * v01; acc[j][1] += p2 * v23; }
        asm volatile("" : "+v"(acc[j][0]), "+v"(acc[j][1]));
        PIN();
    };
    auto v_fold = [&](int i, int j) {
        const float x = swap32_add(acc[j][0][0], acc[j][1][0]);
        const float y = swap32_add(acc[j][0][1], acc[j][1][1]);
        s_part[(wave * NI + i) * DH + 64 * j + 4 * c + r] = swap16_add(x, y);
    };
    auto v_done = [&](int i) {
        PIN();
        if (lane == 0) *(vlds_u32*)(s_done + wave) = i + 1;
        PIN();
    };
    constexpr int LS = 2;
#pragma unroll
    for (int t = 0; t < LS; ++t)
#pragma unroll
        for (int j = 0; j < 3; ++j) v_acc(t, j);
    for (int i = 0; i < NI - 1; ++i) {
        const unsigned hnext = (i + 1) * (2 * HEADB) + woff;
        enter(i);
#pragma unroll
        for (int k = 0; k < 4; ++k) {
            const int t = (k + LS) & 3;
#pragma unroll
            for (int j = 0; j < 3; ++j) {
                v_acc(t, j);
                if (t == 3 && j == 2) pw = *reinterpret_cast<const f32x4*>(pwp + (i + 1) * 160);
                refill(k, j, rv, hnext + toff(k));
            }
            if (t == 3) { v_fold(i, 0); v_fold(i, 1); v_fold(i, 2); v_done(i); }
        }
    }
    asm volatile("s_branch .Lkv_tail_%=\n\t.p2align 12\n.Lkv_tail_%=:" ::: "memory");
    while (__builtin_amdgcn_readfirstlane(*(vlds_u32*)(s_sync + 3)) <= NI) __builtin_amdgcn_s_sleep(1);
    PIN();
    tag = s_sync[4 + (lane & 3)];
    lf = *reinterpret_cast<const f32x4*>(s_f + (2 * (NI - 1) + hp) * 8);
    lz = *reinterpret_cast<const f32x2*>(s_f + (2 * (NI - 1) + hp) * 8 + 4);
    origin = *(vlds_u32*)(s_sync + 2);
    lw = last_word(tag, lz[1], lane & 31);
    const unsigned sides = s_sync[1];
    lo = lane < 32 ? piece_off(hp, lane & 31) ^ (sides >> 3) * BLK : OOB;
    unsigned la[5], ld_ = LDSA(s_done) + 16 * hp, lq = LDSA(s_done) + 4 * wave, lv = NI, lso = pair_off(NI - 1) + m * 64;
#pragma unroll
    for (int w = 0; w < 5; ++w) la[w] = LDSA(s_part) + lp * 4 + (w < 4 ? w * NI * DH * 4 : 16);
    u32x4 rws = {(unsigned)wsa, (unsigned)(wsa >> 32) & 0xffffu, WS_BYTES, 0x00020000};
    f32x2 lf01 = {lf[0], lf[1]}, lf23 = {lf[2], lf[3]};
    asm volatile("" : "+v"(tag), "+v"(lf01), "+v"(lf23), "+v"(lz), "+v"(lw), "+v"(lo), "+v"(origin), "+v"(la[0]), "+v"(la[1]), "+v"(la[2]), "+v"(la[3]), "+v"(la[4]),
                      "+v"(ld_), "+v"(lq), "+v"(lv), "+s"(lso), "+s"(rws));
    PIN();
#pragma unroll
    for (int t = LS; t < 4; ++t)
#pragma unroll
        for (int j = 0; j < 3; ++j) { v_acc(t, j); if (t == 3) { v_fold(NI - 1, j); PIN(); } }
    {
#define FMA0(a) "v_pk_fma_f32 " a ", %7, " a ", %9 op_sel_hi:[0,1,0] neg_lo:[0,0,1] neg_hi:[0,0,1]\n\t"
#define FMA(a, f, b, hi) "v_pk_fma_f32 " a ", " f ", " b ", " a " op_sel:[" hi ",0,0] op_sel_hi:[" hi ",1,1]\n\t"
        asm volatile(
            "ds_write_b32 %5, %6\n\tds_read_b128 v[96:99], %14\n\t"
            "ds_read2_b64 v[64:67], %0 offset1:1\n\tds_read2_b64 v[68:71], %1 offset1:1\n\tds_read2st64_b64 v[80:83], %4 offset1:24\n\t"
            "ds_read2_b64 v[72:75], %2 offset1:1\n\tds_read2_b64 v[76:79], %3 offset1:1\n\tds_read2st64_b64 v[84:87], %4 offset0:48 offset1:72\n\t"
            "v_mov_b32 v91, %10\n\t"
            "s_waitcnt lgkmcnt(6)\n\t"
            "v_and_b32 v96, v96, v97\n\tv_bitop3_b32 v96, v96, v98, v99 bitop3:0x80\n\tv_cmp_ne_u32 vcc, v96, %6\n\t"
            "s_cbranch_vccnz .Lkv_skip_%=\n\t"
            "s_waitcnt lgkmcnt(3)\n\t"
            FMA0("v[64:65]") FMA0("v[66:67]") FMA0("v[80:81]")
            FMA("v[64:65]", "%7", "v[68:69]", "1") FMA("v[66:67]", "%7", "v[70:71]", "1") FMA("v[80:81]", "%7", "v[82:83]", "1")
            "s_waitcnt lgkmcnt(0)\n\t"
            FMA("v[64:65]", "%8", "v[72:73]", "0") FMA("v[66:67]", "%8", "v[74:75]", "0") FMA("v[80:81]", "%8", "v[84:85]", "0")
            FMA("v[64:65]", "%8", "v[76:77]", "1") FMA("v[66:67]", "%8", "v[78:79]", "1") FMA("v[80:81]", "%8", "v[86:87]", "1")
            "v_cvt_pk_bf16_f32 v88, v64, v65\n\tv_cvt_pk_bf16_f32 v89, v66, v67\n\tv_cvt_pk_bf16_f32 v90, v80, v81\n\t"
            "buffer_store_dwordx4 v[88:91], %11, %12, %13 offen sc1\n"
            ".Lkv_skip_%=:\n\ts_waitcnt lgkmcnt(0)"
            :: "v"(la[0]), "v"(la[1]), "v"(la[2]), "v"(la[3]), "v"(la[4]), "v"(lq), "v"(lv), "v"(lf01), "v"(lf23), "v"(lz), "v"(lw), "v"(lo), "s"(rws), "s"(lso), "v"(ld_)
            : "v64", "v65", "v66", "v67", "v68", "v69", "v70", "v71", "v72", "v73", "v74", "v75", "v76", "v77", "v78", "v79", "v80", "v81", "v82", "v83",
              "v84", "v85", "v86", "v87", "v88", "v89", "v90", "v91", "v96", "v97", "v98", "v99", "vcc", "memory");
        asm volatile("s_nop 0" ::: "v88", "v89", "v90", "v91");
        PIN();
    }
    {
        unsigned inbox = pair_off(bid >> 4) + ((bid >> 1 & 4) + (bid & 3)) * 4 * BLK, obase = bid * 48;
        unsigned xo = tid * 16 + ((bid >> 2 ^ sides >> (bid >> 6)) & 1) * BLK, yo = (xo ^ (bid >> 4 < NW ? BLK : 0)) + 2 * BLK, sel = lane & 1 ? 0x01000504u : 0x05040100u;
        unsigned fl = 3 * NI;
        u32x4 rout = {(unsigned)kb[4], (unsigned)(kb[4] >> 32) & 0xffffu, NH * DH * 2, 0x00020000};
        typedef __attribute__((address_space(3))) float lds_f;
        lds_f* wo = (lds_f*)s_red + wave * 28 + (r < 3 ? (c & 3) * 6 + r * 2 : 24);
        lds_f* fo = (lds_f*)s_red + (lane & 7) * 28 + 24;
        lds_f* go = (lds_f*)s_red + (lane & 7) * 28 + min(lane >> 3, 5) * 4;
        vlds_u32* dw = (vlds_u32*)(s_done + wave);
        vlds_u32* dr = (vlds_u32*)(s_done + (lane & 7));
        unsigned oo = (lane & 7) == 0 && lane < 48 ? lane : OOB, eo = bid == 0 && lane < 4 ? lane * 2 : OOB;
        asm volatile("" : "+s"(inbox), "+s"(obase), "+v"(xo), "+v"(yo), "+v"(sel), "+v"(fl), "+s"(rout), "+v"(wo), "+v"(fo), "+v"(go), "+v"(oo), "+v"(eo), "+v"(dw), "+v"(dr));
        const unsigned o = __builtin_amdgcn_readfirstlane(origin) - (bid >= 240 ? (7 - wave) * 900 + (bid < 248 ? 6000 : 0) : 3328 + (14 - (bid >> 4)) * 750);
        while ((int)((now32() << 8) - o) < pace.rd) __builtin_amdgcn_s_sleep(1);
        PIN();
        u32x4 x, y;
        unsigned same;
        do {
            x = __builtin_amdgcn_raw_buffer_load_b128(rw_, xo, inbox, DEV | NT);
            y = __builtin_amdgcn_raw_buffer_load_b128(rw_, yo, inbox, DEV | NT);
            same = now32() - tstart > SPIN_LIMIT ? ~0u : 0xffffu;
            asm volatile("" : "+s"(same));
            PIN();
        } while (__builtin_amdgcn_ballot_w64(((x[3] ^ tag) | (y[3] ^ tag)) > same) != 0);
#define DPP(c) " " c " row_mask:0xf bank_mask:0xf\n\t"
        asm volatile(
            "v_mov_b32_dpp v54, %3" DPP("quad_perm:[1,0,3,2]") "v_mov_b32_dpp v55, %7" DPP("quad_perm:[1,0,3,2]")
            "v_perm_b32 v54, v54, %3, %8\n\tv_perm_b32 v55, v55, %7, %8\n\t"
            "v_max_f32 v56, v54, v55\n\t"
            "v_lshlrev_b32 v40, 16, %0\n\tv_and_b32 v41, 0xffff0000, %0\n\t"
            "v_max_f32_dpp v56, v56, v56" DPP("row_ror:8")
            "v_lshlrev_b32 v42, 16, %1\n\tv_and_b32 v43, 0xffff0000, %1\n\t"
            "v_max_f32_dpp v56, v56, v56" DPP("row_ror:4")
            "v_lshlrev_b32 v44, 16, %2\n\tv_and_b32 v45, 0xffff0000, %2\n\t"
            "v_max_f32_dpp v56, v56, v56 row_bcast:15 row_mask:0xa bank_mask:0xf\n\t"
            "v_lshlrev_b32 v48, 16, %4\n\tv_and_b32 v49, 0xffff0000, %4\n\t"
            "v_max_f32_dpp v56, v56, v56 row_bcast:31 row_mask:0xc bank_mask:0xf\n\t"
            "v_lshlrev_b32 v50, 16, %5\n\t"
            "v_readlane_b32 vcc_lo, v56, 63\n\t"
            "v_and_b32 v51, 0xffff0000, %5\n\tv_lshlrev_b32 v52, 16, %6\n\t"
            "v_subrev_f32 v54, vcc_lo, v54\n\tv_subrev_f32 v55, vcc_lo, v55\n\t"
            "v_exp_f32 v54, v54\n\tv_exp_f32 v55, v55\n\t"
            "v_and_b32 v53, 0xffff0000, %6\n\t"
            "v_pk_mul_f32 v[40:41], v[40:41], v[54:55] op_sel_hi:[1,0]\n\t"
            "v_pk_mul_f32 v[44:45], v[44:45], v[54:55] op_sel_hi:[1,0]\n\t"
            "v_pk_mul_f32 v[42:43], v[42:43], v[54:55] op_sel_hi:[1,0]\n\t"
            "v_pk_fma_f32 v[40:41], v[48:49], v[54:55], v[40:41] op_sel:[0,1,0]\n\t"
            "v_pk_fma_f32 v[44:45], v[52:53], v[54:55], v[44:45] op_sel:[0,1,0]\n\t"
            "v_pk_fma_f32 v[42:43], v[50:51], v[54:55], v[42:43] op_sel:[0,1,0]\n\t"
            "v_add_f32 v46, v54, v55\n\t"
            "v_ldexp_f32 v47, vcc_lo, -4\n\t"
            "v_permlane32_swap_b32 v40, v44\n\tv_permlane32_swap_b32 v41, v45\n\tv_permlane32_swap_b32 v42, v46\n\tv_permlane32_swap_b32 v43, v47\n\t"
            "v_pk_add_f32 v[40:41], v[40:41], v[44:45]\n\tv_pk_add_f32 v[42:43], v[42:43], v[46:47]\n\t"
            "s_nop 1\n\t"
            "v_permlane16_swap_b32 v40, v42\n\tv_permlane16_swap_b32 v41, v43\n\t"
            "v_add_f32 v40, v40, v42\n\tv_add_f32 v41, v41, v43\n\t"
            "s_nop 0\n\t"
            "v_add_f32_dpp v40, v40, v40" DPP("row_ror:8") "v_add_f32_dpp v41, v41, v41" DPP("row_ror:8")
            "s_nop 0\n\t"
            "v_add_f32_dpp v40, v40, v40" DPP("row_ror:4") "v_add_f32_dpp v41, v41, v41" DPP("row_ror:4")
            "ds_write_b64 %9, v[40:41]\n\tds_write_b32 %10, %11"
            :: "v"(x[0]), "v"(x[1]), "v"(x[2]), "v"(x[3]), "v"(y[0]), "v"(y[1]), "v"(y[2]), "v"(y[3]), "v"(sel), "v"(wo), "v"(dw), "v"(fl)
            : "v40", "v41", "v42", "v43", "v44", "v45", "v46", "v47", "v48", "v49", "v50", "v51", "v52", "v53", "v54", "v55", "v56", "vcc", "memory");
        PIN();
        const unsigned fin = *dr;
        PIN();
        f32x2 sm = *(__attribute__((address_space(3))) f32x2*)fo;
        const f32x4 ov = *(__attribute__((address_space(3))) f32x4*)go;
        f32x2 o01 = {ov[0], ov[1]}, o23 = {ov[2], ov[3]};
        asm volatile("" : "+v"(sm), "+v"(o01), "+v"(o23));
        if (__builtin_amdgcn_ballot_w64(fin != 3 * NI) == 0) {
#define ADD4(c) "v_add_f32_dpp v40, v40, v40" DPP(c) "v_add_f32_dpp v41, v41, v41" DPP(c) "v_add_f32_dpp v42, v42, v42" DPP(c) "v_add_f32_dpp v43, v43, v43" DPP(c)
            asm volatile(
                "v_max_f32_dpp v46, %3, %3" DPP("quad_perm:[1,0,3,2]") "s_nop 1\n\t"
                "v_max_f32_dpp v46, v46, v46" DPP("quad_perm:[2,3,0,1]") "s_nop 1\n\t"
                "v_max_f32_dpp v46, v46, v46" DPP("row_half_mirror")
                "v_sub_f32 v46, %3, v46\n\tv_exp_f32 v46, v46\n\ts_nop 0\n\t"
                "v_mul_f32 v44, %2, v46\n\t"
                "v_pk_mul_f32 v[40:41], %4, v[46:47] op_sel_hi:[1,0]\n\tv_pk_mul_f32 v[42:43], %5, v[46:47] op_sel_hi:[1,0]\n\t"
                "v_add_f32_dpp v44, v44, v44" DPP("quad_perm:[1,0,3,2]") ADD4("quad_perm:[1,0,3,2]")
                "v_add_f32_dpp v44, v44, v44" DPP("quad_perm:[2,3,0,1]") ADD4("quad_perm:[2,3,0,1]")
                "v_add_f32_dpp v44, v44, v44" DPP("row_half_mirror") "v_rcp_f32 v44, v44\n\t" ADD4("row_half_mirror")
                "v_pk_mul_f32 v[40:41], v[40:41], v[44:45] op_sel_hi:[1,0]\n\tv_pk_mul_f32 v[42:43], v[42:43], v[44:45] op_sel_hi:[1,0]\n\t"
                "v_cvt_pk_bf16_f32 v40, v40, v41\n\tv_cvt_pk_bf16_f32 v41, v42, v43\n\t"
                "buffer_store_dwordx2 v[40:41], %0, %1, %6 offen"
                :: "v"(oo), "s"(rout), "v"(sm[0]), "v"(sm[1]), "v"(o01), "v"(o23), "s"(obase)
                : "v40", "v41", "v42", "v43", "v44", "v45", "v46", "v47", "memory");
            if (bid == 0) __builtin_amdgcn_raw_buffer_store_b16((unsigned short)(tag >> 16), rw_, eo, WS_EPOCH, DEV);
        }
    }
    __builtin_amdgcn_endpgm();
}

extern "C" void kernel_launch(void* const* d_in, const int* in_sizes, int n_in,
                              void* d_out, int out_size, void* d_ws, size_t ws_size,
                              hipStream_t stream) {
    const float t_us = 100.6f, start_us = 0.2f, seam_us = 0.1f, scales_us = 1.15f;
    const float end_us = start_us + scales_us + 2 * seam_us + t_us * (129.f / 128.f);
    Pace pace;
    pace.step = (unsigned)(t_us * 25600.f / 128.f);
    pace.start = (int)(start_us * 25600.f);
    pace.offset = (int)(scales_us * 25600.f) | (int)(seam_us * 6400.f) << 16;
    pace.bias = (int)(0.052f * 25600.f);
    pace.rd = (int)((end_us + 1.46f) * 25600.f);
    pace.pub = (int)((end_us + 0.46f) * 25600.f);
    kv_sweep_kernel<<<NWG, 576, 0, stream>>>(d_in[0], d_in[1], d_in[2], d_in[3], d_in[4], d_in[5], d_in[6], d_in[7],
        d_in[8], d_in[9], d_in[10], d_ws, d_out, pace);
}
```

```cpp
#include <hip/hip_runtime.h>
#include <cstdint>


using u32x4 = __attribute__((ext_vector_type(4))) unsigned;
using u32x3 = __attribute__((ext_vector_type(3))) unsigned;
using u32x2 = __attribute__((ext_vector_type(2))) unsigned;
using u64x8 = __attribute__((ext_vector_type(8))) unsigned long long;
using f32x2 = __attribute__((ext_vector_type(2))) float;
using f32x4 = __attribute__((ext_vector_type(4))) float;
using bf16x2 = __attribute__((ext_vector_type(2))) __bf16;

constexpr int DH = 192;
constexpr int HALF = 96;
constexpr int NH = 32;
constexpr int SKV = 16384;
constexpr int NWG = 256;
constexpr int NI = 16;
constexpr int NB = 2;
constexpr unsigned ROWB = DH * 4;
constexpr unsigned HEADB = SKV * ROWB;
constexpr unsigned KVB = NH * HEADB;
constexpr unsigned OOB = 0x80000000u;
constexpr int DEV = 16, NT = 2;
constexpr float SCALE_LOG2 = 1.4426950408889634f * 0.07216878364870322f;
constexpr unsigned SPIN_LIMIT = 20000000u;

constexpr unsigned BLK = 8192;
constexpr int NW = 4;
constexpr unsigned WS_EPOCH = 1u << 20;
constexpr unsigned WS_SIDE = WS_EPOCH + 128;
constexpr unsigned WS_REP = WS_EPOCH + 256;
constexpr unsigned WS_PROBE = 3u << 19;
constexpr unsigned WS_BYTES = 8u << 20;
__device__ __forceinline__ unsigned pair_off(int i) { return (i >> 2 << 21) + (i & 3) * (32 * BLK); }

__device__ __forceinline__ float bflo(unsigned u) { return __uint_as_float(u << 16); }
__device__ __forceinline__ float bfhi(unsigned u) { return __uint_as_float(u & 0xffff0000u); }
__device__ __forceinline__ unsigned pk_bf16(f32x2 a) { return __builtin_bit_cast(unsigned, __builtin_convertvector(a, bf16x2)); }
__device__ __forceinline__ void dot_cvt(const u32x4 q, const u32x4 t, const u32x4 ki, f32x2& w01, f32x2& w23, f32x2& k01, f32x2& k23) {
    float w0, w1, w2, w3;
    float k0 = __uint_as_float(ki[0]), k1 = __uint_as_float(ki[1]), k2 = __uint_as_float(ki[2]), k3 = __uint_as_float(ki[3]);
    asm("v_dot2_f32_bf16 %0, %8, %12, 0\n\tv_dot2_f32_bf16 %1, %9, %13, 0\n\tv_dot2_f32_bf16 %2, %10, %14, 0\n\tv_dot2_f32_bf16 %3, %11, %15, 0\n\t"
        "v_cvt_f32_i32 %4, %4\n\tv_cvt_f32_i32 %5, %5\n\tv_cvt_f32_i32 %6, %6\n\tv_cvt_f32_i32 %7, %7"
        : "=&v"(w0), "=&v"(w1), "=&v"(w2), "=&v"(w3), "+v"(k0), "+v"(k1), "+v"(k2), "+v"(k3)
        : "v"(q[0]), "v"(q[1]), "v"(q[2]), "v"(q[3]), "v"(t[0]), "v"(t[1]), "v"(t[2]), "v"(t[3]));
    w01 = f32x2{w0, w1}; w23 = f32x2{w2, w3}; k01 = f32x2{k0, k1}; k23 = f32x2{k2, k3};
}

template <int CTRL> __device__ __forceinline__ float dpp(float x) {
    return __builtin_bit_cast(float, __builtin_amdgcn_mov_dpp(__builtin_bit_cast(int, x), CTRL, 0xf, 0xf, true));
}
__device__ __forceinline__ float row16_allsum(float x) {
    x += dpp<0x128>(x);
    x += dpp<0x124>(x);
    x += dpp<0x122>(x);
    x += dpp<0x121>(x);
    return x;
}
__device__ __forceinline__ float xrow_max(float x) {
    auto s = __builtin_amdgcn_permlane16_swap(__float_as_uint(x), __float_as_uint(x), false, false);
    x = fmaxf(__uint_as_float(s[0]), __uint_as_float(s[1]));
    auto t = __builtin_amdgcn_permlane32_swap(__float_as_uint(x), __float_as_uint(x), false, false);
    return fmaxf(__uint_as_float(t[0]), __uint_as_float(t[1]));
}
__device__ __forceinline__ float xrow_sum(float x) {
    auto s = __builtin_amdgcn_permlane16_swap(__float_as_uint(x), __float_as_uint(x), false, false);
    x = __uint_as_float(s[0]) + __uint_as_float(s[1]);
    auto t = __builtin_amdgcn_permlane32_swap(__float_as_uint(x), __float_as_uint(x), false, false);
    return __uint_as_float(t[0]) + __uint_as_float(t[1]);
}
__device__ __forceinline__ float swap32_add(float a, float b) {
    auto t = __builtin_amdgcn_permlane32_swap(__float_as_uint(a), __float_as_uint(b), false, false);
    return __uint_as_float(t[0]) + __uint_as_float(t[1]);
}
__device__ __forceinline__ float swap16_add(float a, float b) {
    auto t = __builtin_amdgcn_permlane16_swap(__float_as_uint(a), __float_as_uint(b), false, false);
    return __uint_as_float(t[0]) + __uint_as_float(t[1]);
}
__device__ __forceinline__ int row16_sum(int x) {
    x += __builtin_amdgcn_mov_dpp(x, 0x128, 0xf, 0xf, true);
    x += __builtin_amdgcn_mov_dpp(x, 0x124, 0xf, 0xf, true);
    x += __builtin_amdgcn_mov_dpp(x, 0x122, 0xf, 0xf, true);
    x += __builtin_amdgcn_mov_dpp(x, 0x121, 0xf, 0xf, true);
    return x;
}
__device__ __forceinline__ unsigned now32() { return (unsigned)__builtin_amdgcn_s_memrealtime(); }

#define PIN() do { asm volatile("" ::: "memory"); __builtin_amdgcn_sched_barrier(0); } while (0)
#define BARRIER() do { asm volatile("s_waitcnt lgkmcnt(0)" ::: "memory"); __builtin_amdgcn_s_barrier(); PIN(); } while (0)
#define LDSA(p) ((unsigned)(unsigned long long)(__attribute__((address_space(3))) void*)(p))
#define VMCNT(n) asm volatile("s_waitcnt vmcnt(%0)" :: "i"(n) : "memory")

typedef volatile __attribute__((address_space(3))) unsigned vlds_u32;
typedef volatile __attribute__((address_space(3))) u32x2 vlds_u32x2;
typedef volatile __attribute__((address_space(3))) u32x4 vlds_u32x4;

struct Pace {
    unsigned step;
    int start;
    int offset;
    int bias;
    int rd;
    int pub;
};

__global__ __launch_bounds__(576, 3) __attribute__((aligned(4096))) void kv_sweep_kernel(
    const void*, const void*, const void*, const void*, const void*, const void*, const void*, const void*,
    const void*, const void*, const void*, void*, void*, Pace)
{
    __shared__ __attribute__((aligned(16))) unsigned s_q[NH * DH];
    __shared__ __attribute__((aligned(16))) unsigned short s_sc[NH * 256];
    __shared__ __attribute__((aligned(16))) float s_w[NH * 4 * 20];
    __shared__ __attribute__((aligned(16))) float s_part[8 * NI * DH];
    __shared__ __attribute__((aligned(16))) float s_red[8 * 28];
    __shared__ __attribute__((aligned(16))) float s_f[NH * 8];
    __shared__ __attribute__((aligned(16))) unsigned s_done[8];
    __shared__ __attribute__((aligned(16))) unsigned s_sync[8];
    __shared__ __attribute__((aligned(16))) unsigned s_act[16];

    unsigned long long pc0, t0;
    u64x8 ka, kb;
    asm volatile("s_load_dwordx16 %3, %4, 0x40\n\ts_load_dwordx16 %2, %4, 0x0\n\ts_memrealtime %1\n\ts_getpc_b64 %0\n.Lkv_beg:"
                 : "=&s"(pc0), "=&s"(t0), "=&s"(ka), "=&{s[8:23]}"(kb) : "s"(__builtin_amdgcn_kernarg_segment_ptr()));
    unsigned clen, xw;
    asm volatile("s_mov_b32 %0, .Lkv_end-.Lkv_beg" : "=s"(clen));
    asm volatile("global_load_dword %0, %1, %2" : "=&v"(xw) : "v"(min((blockIdx.x >> 3) * 64 + (threadIdx.x >> 6) * 2048, clen)), "s"(pc0 & ~63ull) : "memory");
    __builtin_amdgcn_sched_barrier(0);
    const int tid = threadIdx.x;
    const int lane = tid & 63;
    int wave = __builtin_amdgcn_readfirstlane(tid >> 6);
    const int r = lane >> 4;
    const int c = lane & 15;
    const int bid = blockIdx.x;
    const int rw = wave & 3, hp = wave >> 2;
    constexpr int RS = 8192 + 128, RT = 1024 + 256;
    const int grow = ((bid >> 3 & 3) | (bid >> 4 & 4) | (bid & 0x84) << 1 | (bid & 2) << 3 | (bid & 32) | (bid & 1) << 7) * 16;
    auto srow = [&](int t) { return (t & 1) * RS ^ (t >> 1) * RT; };
    const int row0 = grow + rw * 4;
    const unsigned rb0 = (row0 ^ RT * hp) * 384u;
    unsigned co = r * 384 + c * 8 + rb0;
#define LOAD_CS(a) "global_load_dwordx2 %0, " a ", s[10:11] nt\n\tglobal_load_dwordx2 %3, " a ", s[12:13] nt\n\t" \
                   "global_load_dwordx2 %1, " a ", s[10:11] offset:128 nt\n\tglobal_load_dwordx2 %4, " a ", s[12:13] offset:128 nt\n\t" \
                   "global_load_dwordx2 %2, " a ", s[10:11] offset:256 nt\n\tglobal_load_dwordx2 %5, " a ", s[12:13] offset:256 nt\n"
    u32x2 raw[2][6];
    unsigned long long t1;
    asm volatile("s_cmp_eq_u32 %11, 8\n\t.p2align 6\n\ts_waitcnt lgkmcnt(0)\n\ts_memrealtime %6\n\ts_cbranch_scc1 .Lkv_arg\n\t"
                 LOAD_CS("%10") ".Lkv_arg:\n\ts_waitcnt lgkmcnt(0)"
                 : "=&v"(raw[0][0]), "=&v"(raw[0][1]), "=&v"(raw[0][2]), "=&v"(raw[0][3]), "=&v"(raw[0][4]), "=&v"(raw[0][5]), "=&s"(t1),
                   "+s"(t0), "+s"(ka), "+{s[8:23]}"(kb), "+v"(co), "+s"(wave) :: "scc", "memory");
    __builtin_amdgcn_sched_barrier(0);
    typedef const __attribute__((address_space(1))) char* gptr;
    void* const q = (void*)ka[0]; void* const qk = (void*)ka[1]; void* const qv = (void*)ka[4]; void* const q_cos = (void*)ka[7];
    const gptr ksc = (gptr)ka[2], kzp = (gptr)ka[3], vsc = (gptr)ka[5], vzp = (gptr)ka[6];
    void* const q_sin = (void*)kb[0];
    const unsigned long long wsa = kb[3] + 0x1fffff & ~0x1fffffull;
    unsigned char* const ws = (unsigned char*)(__attribute__((address_space(1))) unsigned char*)wsa;
    const Pace pace = {(unsigned)kb[5], (int)(kb[5] >> 32), (int)kb[6], (int)(kb[6] >> 32), (int)kb[7], (int)(kb[7] >> 32)};
    const int m = (bid >> 5) * 16 + (bid & 3) * 4 + (bid >> 3 & 3);
    const unsigned tstart = (unsigned)t0;
    const unsigned tsched = (unsigned)t1;
    const auto rw_ = __builtin_amdgcn_make_buffer_rsrc((void*)ws, 0, WS_BYTES, 0x00020000);

    auto pick = [](int n, gptr p0, gptr p1, gptr p2, gptr p3) { return (n & 2) ? ((n & 1) ? p3 : p2) : ((n & 1) ? p1 : p0); };
    auto merge = [&](const float* p, f32x4 f, float zs, unsigned w3) {
        f32x2 u[4][3], a[3];
#pragma unroll
        for (int w = 0; w < 4; ++w)
#pragma unroll
            for (int k = 0; k < 3; ++k) u[w][k] = *reinterpret_cast<const f32x2*>(p + w * NI * DH + 2 * k);
        asm volatile("" : "+v"(u[0][0]), "+v"(u[0][1]), "+v"(u[0][2]), "+v"(u[1][0]), "+v"(u[1][1]), "+v"(u[1][2]),
                          "+v"(u[2][0]), "+v"(u[2][1]), "+v"(u[2][2]), "+v"(u[3][0]), "+v"(u[3][1]), "+v"(u[3][2]));
#pragma unroll
        for (int k = 0; k < 3; ++k) a[k] = (f[0] * u[0][k] - zs + f[1] * u[1][k]) + (f[2] * u[2][k] + f[3] * u[3][k]);
        return u32x4{pk_bf16(a[0]), pk_bf16(a[1]), pk_bf16(a[2]), w3};
    };
    auto last_word = [&](unsigned tag, float ls, int c6) { return tag | ((__float_as_uint(ls) >> (16 * (c6 & 1))) & 0xffffu); };
    auto piece_off = [&](int hp, int c6) { return (hp * 16 + (c6 & 12) + ((c6 >> 4 ^ bid >> 2) & 1) * 2 + (c6 >> 4)) * BLK + (c6 & 3) * 16; };
    auto publish = [&](u32x4 v, int i, unsigned vo, unsigned sides) {
        __builtin_amdgcn_raw_buffer_store_b128(v, rw_, vo ^ (sides >> (i >> 2) & 1) * BLK ^ (i < NW ? vo >> 1 & BLK : 0), pair_off(i) + m * 64, DEV);
        asm volatile("s_nop 1" :: "v"(v) : "memory");
    };

    if (__builtin_expect_with_probability(wave == 8, 1, 0.45)) {
        __builtin_amdgcn_s_setprio(3);
        unsigned sci = 0;
        gptr scp = pick(lane >> 1, ksc, kzp, vsc, vzp) + ((lane >> 5) * SKV + (grow ^ srow(lane >> 3 & 3)) + (lane & 1) * 8) * 2;
        auto gather = [&]() {
            asm volatile("s_mov_b32 m0, %1\n\ts_nop 0\n\tglobal_load_lds_dwordx4 %0, off nt"
                         :: "v"(scp), "s"(LDSA(s_sc) + sci * 1024) : "memory");
            scp += 4 * SKV;
        };
        while (now32() - tsched < 100) __builtin_amdgcn_s_sleep(1);
        u32x3 lc[2], ls[2];
        u32x4 qraw[12];
        {
            const auto rq = __builtin_amdgcn_make_buffer_rsrc((void*)q, 0, NH * DH * 2, 0x00020000);
            const auto rqc = __builtin_amdgcn_make_buffer_rsrc((void*)q_cos, 0, DH * 2, 0x00020000);
            const auto rqs = __builtin_amdgcn_make_buffer_rsrc((void*)q_sin, 0, DH * 2, 0x00020000);
#pragma unroll
            for (int k = 0; k < 12; ++k) qraw[k] = __builtin_amdgcn_raw_buffer_load_b128(rq, lane * 16 + (k & 3) * 1024, (k >> 2) * 4096, 0);
#pragma unroll
            for (int k = 0; k < 2; ++k) {
                lc[k] = __builtin_amdgcn_raw_buffer_load_b96(rqc, c * 12 + k * 192, 0, 0);
                ls[k] = __builtin_amdgcn_raw_buffer_load_b96(rqs, c * 12 + k * 192, 0, 0);
            }
            PIN();
        }
        const unsigned tag = ((unsigned)__builtin_amdgcn_raw_buffer_load_b16(rw_, (lane & 3) * 2, WS_EPOCH, DEV) + 1u) << 16;
        PIN();
        unsigned* const s_raw = reinterpret_cast<unsigned*>(s_part) + NI * DH / 2;
#pragma unroll
        for (int k = 0; k < 12; ++k) *reinterpret_cast<u32x4*>(s_raw + k / 6 * (NI * DH) + k % 6 * 256 + lane * 4) = qraw[k];
        f32x2 fc[2][3], fs[2][3];
#pragma unroll
        for (int k = 0; k < 2; ++k)
#pragma unroll
            for (int u = 0; u < 3; ++u) { fc[k][u] = f32x2{bflo(lc[k][u]), bfhi(lc[k][u])}; fs[k][u] = f32x2{bflo(ls[k][u]), bfhi(ls[k][u])}; }
        PIN();
#pragma nounroll
        for (int it = 0; it < 4; ++it) {
            if (sci < NB) { gather(); ++sci; }
            unsigned qa[2][3], qb[2][3];
#pragma unroll
            for (int g = 0; g < 2; ++g)
#pragma unroll
                for (int u = 0; u < 3; ++u) {
                    const unsigned* src = s_raw + g * (NI * DH) + (it * 4 + r) * HALF + 3 * c;
                    qa[g][u] = src[u]; qb[g][u] = src[HALF / 2 + u];
                }
#pragma unroll
            for (int g = 0; g < 2; ++g)
#pragma unroll
                for (int u = 0; u < 3; ++u) {
                    const f32x2 a = {bflo(qa[g][u]), bfhi(qa[g][u])}, b = {bflo(qb[g][u]), bfhi(qb[g][u])};
                    const f32x2 o0 = a * fc[0][u] - b * fs[0][u], o1 = b * fc[1][u] + a * fs[1][u];
                    const unsigned l0 = pk_bf16(f32x2{o0[0], o1[0]}), l1 = pk_bf16(f32x2{o0[1], o1[1]});
                    unsigned* dst = s_q + (16 * g + 4 * it + r) * DH + 6 * c + 2 * u;
                    *reinterpret_cast<u32x2*>(dst) = u32x2{l0, l1};
                    *reinterpret_cast<u32x2*>(dst + HALF) = u32x2{__builtin_amdgcn_alignbit(l0, l0, 16) ^ 0x80000000u, __builtin_amdgcn_alignbit(l1, l1, 16) ^ 0x80000000u};
                }
        }
        asm volatile("" :: "v"(xw));
        __builtin_amdgcn_s_setprio(0);
        unsigned origin = tsched << 8;
        if (lane < 8) { s_done[lane] = 0; s_sync[lane] = lane < 3 ? origin : lane < 4 ? NB : tag; }
        PIN();
        VMCNT(0);
        BARRIER();
        const int mh = lane >> 5, mc = lane & 31;
        const unsigned rtag = __builtin_amdgcn_readlane(tag, 0) | __builtin_amdgcn_readlane(tag, 1) >> 16;
        const unsigned probe = WS_PROBE + ((bid >> 3 & 3) << 21);
        constexpr int RPX = 2;
        constexpr int RSH = 9;
        constexpr unsigned RDLY = 200;
        constexpr int UP = 960, DN = 2000;
        const int rj = ((bid >> 3) + 5 * (bid & 7)) & 31;
        const int rid = rj % (32 / RPX) == 0 ? rj / (32 / RPX) * 8 + (bid & 7) : -1;
        unsigned wround = now32() >> RSH, rround = wround, tick = 0, target = origin;
        bool synced = false;
        int drift = 0;
        const unsigned round0 = rround;
        int i = 0;
        unsigned sct = tstart, sides = 0;
        int np = (bid & ~24) == 32 ? 0 : -1, score = 0;
        const unsigned stag = __builtin_amdgcn_readlane(tag, 2) | __builtin_amdgcn_readlane(tag, 3) >> 16;
        auto kept_now = [&](unsigned now) {
            const u32x2 a = *(vlds_u32x2*)(s_act + 2 * (lane & 7));
            const int late = max((int)(a[0] - origin), (int)((now << 8) - a[1] - origin));
            return origin + (__builtin_amdgcn_readfirstlane(row16_sum(late)) >> 4);
        };
        auto steer = [&](unsigned now, bool alone) {
            if ((now >> 5) != tick) {
                tick = now >> 5;
                target += drift;
                origin += max(-DN, min(((int)(target - origin) > 6400 ? UP : UP / 3) + drift, (int)(target - origin)));
                if (alone) {
                    origin += max(0, (int)(kept_now(now) - origin) - 2600);
                    target = origin;
                }
                if (lane == 0) *(vlds_u32*)(s_sync + 2) = origin;
                PIN();
            }
        };
        auto finished = [&]() {
            const u32x4 a = *(vlds_u32x4*)s_done, b = *(vlds_u32x4*)(s_done + 4);
            return __builtin_amdgcn_readfirstlane(min(min(min(a[0], a[1]), min(a[2], a[3])), min(min(b[0], b[1]), min(b[2], b[3]))));
        };
        while (i < NI - 2) {
            const unsigned now = now32();
            if ((now >> RSH) != wround) {
                wround = now >> RSH;
                if (rid >= 0 && (now & ((1u << RSH) - 1)) < 48)
                    __builtin_amdgcn_raw_buffer_store_b64(u32x2{kept_now(now), rtag + wround}, rw_, lane == 0 ? rid * 8 : OOB, WS_REP + (wround & 255) * 512, DEV);
                PIN();
                continue;
            }
            if ((int)(((now - RDLY) >> RSH) - rround) > 0) {
                rround = (now - RDLY) >> RSH;
                const u32x2 x = __builtin_amdgcn_raw_buffer_load_b64(rw_, c * 8, WS_REP + (rround & 255) * 512, DEV);
                const int d = (int)(x[0] - origin);
                const bool ok = x[1] == rtag + rround && d < (32 << 16) && d > -(32 << 16);
                const int n = __builtin_popcountll(__builtin_amdgcn_ballot_w64(ok));
                if (n >= 48) {
                    const unsigned nt = origin + __builtin_amdgcn_readfirstlane((int)(row16_allsum(ok ? (float)d : 0.f) * 4.f / (float)n)) - pace.bias;
                    if (synced && rround - round0 > 2) drift = max(0, min(drift + ((max(-1024, min((int)(nt - target) + 6 * drift, 5120)) - (rround - round0 < 10 ? 768 : 0)) >> 5), 800));
                    target = nt + 6 * drift;
                    synced = true;
                    const int lost = min((int)(kept_now(now32()) - origin) - pace.bias, (int)(target - origin));
                    if (lost > 0) origin += lost;
                }
                PIN();
                continue;
            }
            steer(now, !synced);
            if (sci <= NI && now - sct >= 100) {
                VMCNT(0);
                if (lane == 0) *(vlds_u32*)(s_sync + 3) = sci;
                if (sci < NI) gather();
                ++sci; sct = now;
                PIN();
                continue;
            }
            if (np >= 0 && sci > NI) {
                if (np < 8) {
                    const int o = (np ^ np >> 2) & 1;
                    const unsigned a = (np & 3) * 2048 + o * BLK;
                    unsigned long long u0, u1, u2;
                    unsigned x;
                    asm volatile("s_waitcnt lgkmcnt(0)\n\ts_memrealtime %0\n\ts_load_dword %3, %4, %5 glc\n\ts_waitcnt lgkmcnt(0)\n\t"
                                 "s_memrealtime %1\n\ts_load_dword %3, %4, %6 glc\n\ts_waitcnt lgkmcnt(0)\n\ts_memrealtime %2\n\ts_waitcnt lgkmcnt(0)"
                                 : "=&s"(u0), "=&s"(u1), "=&s"(u2), "=&s"(x) : "s"(wsa + probe), "s"(a), "s"(a ^ BLK) : "memory");
                    const int d = max(-16, min((int)(u2 + u0 - 2 * u1), 16));
                    score += o ? d : -d;
                    ++np;
                } else {
                    __builtin_amdgcn_raw_buffer_store_b64(u32x2{stag ^ (score > 0), rtag}, rw_, lane == 0 ? bid & 24 : OOB, WS_SIDE, DEV);
                    np = -1;
                }
                PIN();
                continue;
            }
            if (np >= 0 || finished() <= (unsigned)i) { __builtin_amdgcn_s_sleep(2); continue; }
            PIN();
            if (i == 0) {
                f32x4 ml[4], f;
#pragma unroll
                for (int w = 0; w < 4; ++w) ml[w] = *reinterpret_cast<const f32x4*>(s_w + (mc * 4 + w) * 20 + 16);
                const float M = fmaxf(fmaxf(ml[0][0], ml[1][0]), fmaxf(ml[2][0], ml[3][0]));
                float L = 0.f, zs = 0.f;
#pragma unroll
                for (int w = 0; w < 4; ++w) { f[w] = __builtin_amdgcn_exp2f(ml[w][0] - M); L += f[w] * ml[w][1]; zs += f[w] * ml[w][2]; }
                const float inv = 1.f / L;
                *reinterpret_cast<f32x4*>(s_f + mc * 8) = f * inv;
                *reinterpret_cast<f32x2*>(s_f + mc * 8 + 4) = f32x2{zs * inv, M + __builtin_amdgcn_logf(L)};
                u32x2 sd;
                do { sd = __builtin_amdgcn_raw_buffer_load_b64(rw_, (lane & 3) * 8, WS_SIDE, DEV); PIN(); }
                while (__builtin_amdgcn_ballot_w64(sd[1] != rtag || (sd[0] ^ stag) > 1) != 0 && now32() - tstart < SPIN_LIMIT);
                sides = (unsigned)__builtin_amdgcn_ballot_w64(sd[0] != stag) & 15;
                if (lane == 0) *(vlds_u32*)(s_sync + 1) = sides;
                PIN();
                if (lane == 0) *(vlds_u32*)(s_sync + 3) = NI + 1;
                PIN();
            }
            const f32x2 zl = *reinterpret_cast<const f32x2*>(s_f + (2 * i + mh) * 8 + 4);
            const u32x4 v = merge(s_part + (mh * 4 * NI + i) * DH + mc * 6, *reinterpret_cast<const f32x4*>(s_f + (2 * i + mh) * 8), zl[0], last_word(tag, zl[1], mc));
            PIN();
            *reinterpret_cast<u32x4*>(s_part + (mh * 4 * NI + i) * DH + mc * 4) = v;
            PIN();
            ++i;
        }
        u32x4 v[NI - 1];
#pragma unroll
        for (int i = 0; i < NI - 2; ++i) v[i] = *reinterpret_cast<const u32x4*>(s_part + (mh * 4 * NI + i) * DH + mc * 4);
        f32x4 f = *reinterpret_cast<const f32x4*>(s_f + (2 * (NI - 2) + mh) * 8);
        f32x2 zl = *reinterpret_cast<const f32x2*>(s_f + (2 * (NI - 2) + mh) * 8 + 4);
        PIN();
#pragma unroll
        for (int i = 0; i < NI - 2; ++i) asm volatile("" : "+v"(v[i]));
        asm volatile("" : "+v"(f), "+v"(zl));
        v[NI - 2] = v[0];
        bool have = false, sent = false;
        while (!(have && sent)) {
            const unsigned now = now32();
            if (!have) {
                steer(now, false);
                if (finished() > NI - 2) {
                    PIN();
                    v[NI - 2] = merge(s_part + (mh * 4 * NI + NI - 2) * DH + mc * 6, f, zl[0], last_word(tag, zl[1], mc));
                    have = true;
                }
            }
            if (!sent && (int)((now << 8) - origin) >= pace.pub) {
#pragma unroll
                for (int i = 0; i < NI - 2; ++i) publish(v[i], i, piece_off(mh, mc), sides);
                sent = true;
            }
            if (!(have && sent)) __builtin_amdgcn_s_sleep(1);
            PIN();
        }
        publish(v[NI - 2], NI - 2, piece_off(mh, mc), sides);
        asm volatile(".Lkv_end:");
        return;
    }

    const int ph = rw + 4 * bid + 1024 * hp;
    unsigned off = pace.start + ((pace.step * ph) >> 11);
    unsigned origin = tsched << 8, kept = 0;
    unsigned step = pace.step;
    auto pace_wait = [&]() {
        const unsigned tgt = __builtin_amdgcn_readfirstlane(origin) + off;
        unsigned now = now32() << 8;
        kept = ((int)(now - tgt) > 0 ? now : tgt) - off;
        while ((int)(tgt - now) > 0) { __builtin_amdgcn_s_sleep(1); now = now32() << 8; }
        off += step;
    };
    auto report = [&]() {
        if (lane == 0) *(vlds_u32x2*)(s_act + 2 * wave) = u32x2{kept, off};
        origin = *(vlds_u32*)(s_sync + 2);
        PIN();
    };

    auto pre_sc = [&]() {
        const unsigned junk = LDSA(s_part) + (3 * NI * DH - 256) * 4;
        const gptr a = pick(grow >> 4, ksc, kzp, vsc, vzp) + ((tid >> 5) * SKV + (grow & ~63 ^ srow(lane >> 3 & 3))) * 2 + (lane & 7) * 16;
        asm volatile("s_mov_b32 m0, %2\n\ts_nop 0\n\tglobal_load_lds_dwordx4 %0, off\n\tglobal_load_lds_dwordx4 %1, off" :: "v"(a), "v"(a + 32 * SKV), "s"(junk) : "memory");
        PIN();
    };
    const unsigned rb1 = (row0 ^ RT * hp ^ RS) * 384u - rb0;
    u32x4 tab[4][3];
    auto pack_cs = [&](int t) {
#pragma unroll
        for (int j = 0; j < 3; ++j) {
#pragma unroll
            for (int k = 0; k < 2; ++k) {
                const unsigned lo = __builtin_amdgcn_mov_dpp(raw[t][3 + (j + 1) % 3][k], 0x128, 0xf, 0x3, false);
                const unsigned sn = __builtin_amdgcn_update_dpp(lo, raw[t][3 + (j + 2) % 3][k], 0x128, 0xf, 0xc, false);
                tab[t][j][2 * k] = __builtin_amdgcn_perm(sn, raw[t][j][k], 0x05040100);
                tab[t][j][2 * k + 1] = __builtin_amdgcn_perm(sn, raw[t][j][k], 0x07060302);
            }
            *reinterpret_cast<u32x4*>(s_part + (wave ^ 4) * (NI * DH) + ((t * 3 + j) * 64 + lane) * 4) = tab[t][j];
        }
        PIN();
    };
    const auto rk = __builtin_amdgcn_make_buffer_rsrc((void*)qk, 0, KVB, 0x00020000);
    const auto rv = __builtin_amdgcn_make_buffer_rsrc((void*)qv, 0, KVB, 0x00020000);
    const unsigned voff = r * ROWB + c * 16;
    const unsigned woff = hp * HEADB;
    auto toff = [&](int t) { return (unsigned)(row0 ^ srow(t)) * ROWB; };
    u32x4 ring[4][3];
    auto ld = [&](int slot, int j, __amdgpu_buffer_rsrc_t rr, unsigned off) {
        ring[slot][j] = __builtin_amdgcn_raw_buffer_load_b128(rr, voff + j * 256, off, NT);
        PIN();
    };
    auto ld_tile = [&](int slot, __amdgpu_buffer_rsrc_t rr, unsigned off) { ld(slot, 0, rr, off); ld(slot, 1, rr, off); ld(slot, 2, rr, off); };
    unsigned post = 0;
    auto enter = [&](int i) {
        if (i == 9 || i == 10) {
            const unsigned hs = pace.step >> 1, z = (unsigned)pace.offset >> 16;
            off += i == 10 ? post : hp * 3 * hs;
            step = i == 10 ? pace.step : hs + hp * z;
            post = __builtin_amdgcn_readfirstlane(i == 10 ? 0 : hp ? hs : 4 * (hs + z));
            if (lane == 0) *(vlds_u32x2*)(s_act + 2 * wave) = u32x2{kept, off};
            PIN();
        }
    };
    auto refill = [&](int slot, int j, __amdgpu_buffer_rsrc_t rr, unsigned goff) {
        if (j == 0) pace_wait();
        if (j == 0) asm volatile("s_cmp_lg_u32 %0, 0\n\ts_waitcnt vmcnt(3)\n\ts_cbranch_scc1 1\n\ts_waitcnt vmcnt(2)" :: "s"(post) : "scc", "memory");
        else asm volatile("s_cmp_lg_u32 %0, 0\n\ts_cbranch_scc1 1\n\ts_waitcnt vmcnt(2)" :: "s"(post) : "scc", "memory");
        ld(slot, j, rr, goff);
        if (j == 0) { const unsigned t = (now32() << 8) - 1024 - (off - step); if ((int)(t - kept) > 0) kept = t; }
        if (j == 2) report();
    };
    auto k0 = [&](int t) { pace_wait(); ld_tile(t, rk, woff + toff(t)); };
    co += rb1;
    pace_wait();
    asm volatile(LOAD_CS("%6") : "=&v"(raw[1][0]), "=&v"(raw[1][1]), "=&v"(raw[1][2]), "=&v"(raw[1][3]), "=&v"(raw[1][4]), "=&v"(raw[1][5])
                 : "v"(co), "{s[8:23]}"(kb) : "memory");
    PIN();
    asm volatile("s_waitcnt vmcnt(6)" : "+v"(raw[0][0]), "+v"(raw[0][1]), "+v"(raw[0][2]), "+v"(raw[0][3]), "+v"(raw[0][4]), "+v"(raw[0][5]) :: "memory");
    pack_cs(0);
    pace_wait(); pre_sc(); off += (pace.offset & 0xffff) - step;
    k0(0);
    asm volatile("s_waitcnt vmcnt(5)" : "+v"(raw[1][0]), "+v"(raw[1][1]), "+v"(raw[1][2]), "+v"(raw[1][3]), "+v"(raw[1][4]), "+v"(raw[1][5]) :: "memory");
    pack_cs(1);
    VMCNT(3);
    asm volatile("" :: "v"(xw));
    k0(1);
    VMCNT(3); k0(2);
    VMCNT(3);
    if (hp) { if (lane == 0) *(vlds_u32x2*)(s_act + 2 * wave) = u32x2{kept, off}; BARRIER(); }
    k0(3);
    if (lane == 0) *(vlds_u32x2*)(s_act + 2 * wave) = u32x2{kept, off};
    if (!hp) BARRIER();
    origin = *(vlds_u32*)(s_sync + 2);

    u32x4 qp[3];
    auto load_q = [&](int h, int j) { qp[j] = *reinterpret_cast<const u32x4*>(s_q + h * DH + 64 * j + 4 * c); };
    load_q(hp, 0); load_q(hp, 1); load_q(hp, 2);
#pragma unroll
    for (int t = 0; t < 2; ++t)
#pragma unroll
        for (int j = 0; j < 3; ++j) tab[2 + t][j] = *reinterpret_cast<const u32x4*>(s_part + wave * (NI * DH) + ((t * 3 + j) * 64 + lane) * 4);
    if (hp) {
#pragma unroll
        for (int t = 0; t < 2; ++t)
#pragma unroll
            for (int j = 0; j < 3; ++j)
#pragma unroll
                for (int k = 0; k < 4; ++k) asm volatile("v_swap_b32 %0, %1" : "+v"(tab[t][j][k]), "+v"(tab[2 + t][j][k]));
    }

    unsigned ready = NB;
    for (int i = 0; i < NI; ++i) {
        const bool last = i + 1 == NI;
        enter(i);
        const int h = 2 * i + hp;
        const unsigned hnext = (i + 1) * (2 * HEADB) + woff;
        const auto rn = __builtin_amdgcn_make_buffer_rsrc((void*)(last ? qv : qk), 0, KVB, 0x00020000);
        float* scp = s_w + (h * 4 + rw) * 20;
        f32x4 sc[4];
        while (__builtin_amdgcn_readfirstlane(ready) <= i) { __builtin_amdgcn_s_sleep(1); ready = *(vlds_u32*)(s_sync + 3); }
        PIN();
#pragma unroll
        for (int t = 0; t < 4; ++t)
#pragma unroll
            for (int a = 0; a < 4; ++a) sc[t][a] = bflo(s_sc[h * 256 + t * 64 + a * 16 + rw * 4 + r]);
        float s[4];
#pragma unroll
        for (int t = 0; t < 4; ++t) {
            f32x2 dot, wsum;
#pragma unroll
            for (int j = 0; j < 3; ++j) {
                f32x2 w01, w23, k01, k23;
                dot_cvt(qp[j], tab[t][j], ring[t][j], w01, w23, k01, k23);
                if (j == 0) { wsum = w01 + w23; dot = k01 * w01; } else { wsum += w01; wsum += w23; dot += k01 * w01; }
                dot += k23 * w23;
                asm volatile("" : "+v"(dot), "+v"(wsum));
                PIN();
                if (t == 3) load_q(last ? h : h + 2, j);
                if (t == 3 && j == 0) ready = *(vlds_u32*)(s_sync + 3);
                refill(t, j, rn, (last ? woff : hnext) + toff(t));
            }
            const float a = (dot[0] + dot[1]) - sc[t][1] * (wsum[0] + wsum[1]);
            s[t] = row16_allsum(a) * (sc[t][0] * SCALE_LOG2);
        }
        const float mx = xrow_max(fmaxf(fmaxf(s[0], s[1]), fmaxf(s[2], s[3])));
        float l = 0.f, z = 0.f;
#pragma unroll
        for (int t = 0; t < 4; ++t) {
            const float p = __builtin_amdgcn_exp2f(s[t] - mx);
            l += p;
            s[t] = p * sc[t][2];
            z += s[t] * sc[t][3];
        }
        l = xrow_sum(l);
        z = xrow_sum(z);
        if (c == 0) {
            *reinterpret_cast<f32x4*>(scp + r * 4) = f32x4{s[0], s[1], s[2], s[3]};
            *reinterpret_cast<f32x4*>(scp + 16) = f32x4{mx, l, z, 0.f};
        }
        PIN();
    }

    const float* pwp = s_w + (hp * 4 + rw) * 20 + r * 4;
    f32x4 pw = *reinterpret_cast<const f32x4*>(pwp);
    unsigned tag, lw, lo;
    f32x4 lf;
    f32x2 lz;
    unsigned lp = (hp * 4 * NI + NI - 1) * DH + (lane & 31) * 6;
    f32x2 acc[3][2];
    auto v_acc = [&](int t, int j) {
        const f32x2 p2 = {pw[t], pw[t]};
        const u32x4 vv = ring[t][j];
        const f32x2 v01 = {(float)(int)vv[0], (float)(int)vv[1]}, v23 = {(float)(int)vv[2], (float)(int)vv[3]};
        if (t == 0) { acc[j][0] = p2 * v01; acc[j][1] = p2 * v23; }
        else { acc[j][0] += p2 * v01; acc[j][1] += p2 * v23; }
        asm volatile("" : "+v"(acc[j][0]), "+v"(acc[j][1]));
        PIN();
    };
    auto v_fold = [&](int i, int j) {
        const float x = swap32_add(acc[j][0][0], acc[j][1][0]);
        const float y = swap32_add(acc[j][0][1], acc[j][1][1]);
        s_part[(wave * NI + i) * DH + 64 * j + 4 * c + r] = swap16_add(x, y);
    };
    auto v_done = [&](int i) {
        PIN();
        if (lane == 0) *(vlds_u32*)(s_done + wave) = i + 1;
        PIN();
    };
    constexpr int LS = 2;
#pragma unroll
    for (int t = 0; t < LS; ++t)
#pragma unroll
        for (int j = 0; j < 3; ++j) v_acc(t, j);
    for (int i = 0; i < NI - 1; ++i) {
        const unsigned hnext = (i + 1) * (2 * HEADB) + woff;
        enter(i);
#pragma unroll
        for (int k = 0; k < 4; ++k) {
            const int t = (k + LS) & 3;
#pragma unroll
            for (int j = 0; j < 3; ++j) {
                v_acc(t, j);
                if (t == 3 && j == 2) pw = *reinterpret_cast<const f32x4*>(pwp + (i + 1) * 160);
                refill(k, j, rv, hnext + toff(k));
            }
            if (t == 3) { v_fold(i, 0); v_fold(i, 1); v_fold(i, 2); v_done(i); }
        }
    }
    asm volatile("s_branch .Lkv_tail_%=\n\t.p2align 12\n.Lkv_tail_%=:" ::: "memory");
    while (__builtin_amdgcn_readfirstlane(*(vlds_u32*)(s_sync + 3)) <= NI) __builtin_amdgcn_s_sleep(1);
    PIN();
    tag = s_sync[4 + (lane & 3)];
    lf = *reinterpret_cast<const f32x4*>(s_f + (2 * (NI - 1) + hp) * 8);
    lz = *reinterpret_cast<const f32x2*>(s_f + (2 * (NI - 1) + hp) * 8 + 4);
    origin = *(vlds_u32*)(s_sync + 2);
    lw = last_word(tag, lz[1], lane & 31);
    const unsigned sides = s_sync[1];
    lo = lane < 32 ? piece_off(hp, lane & 31) ^ (sides >> 3) * BLK : OOB;
    unsigned la[5], ld_ = LDSA(s_done) + 16 * hp, lq = LDSA(s_done) + 4 * wave, lv = NI, lso = pair_off(NI - 1) + m * 64;
#pragma unroll
    for (int w = 0; w < 5; ++w) la[w] = LDSA(s_part) + lp * 4 + (w < 4 ? w * NI * DH * 4 : 16);
    u32x4 rws = {(unsigned)wsa, (unsigned)(wsa >> 32) & 0xffffu, WS_BYTES, 0x00020000};
    f32x2 lf01 = {lf[0], lf[1]}, lf23 = {lf[2], lf[3]};
    asm volatile("" : "+v"(tag), "+v"(lf01), "+v"(lf23), "+v"(lz), "+v"(lw), "+v"(lo), "+v"(origin), "+v"(la[0]), "+v"(la[1]), "+v"(la[2]), "+v"(la[3]), "+v"(la[4]),
                      "+v"(ld_), "+v"(lq), "+v"(lv), "+s"(lso), "+s"(rws));
    PIN();
#pragma unroll
    for (int t = LS; t < 4; ++t)
#pragma unroll
        for (int j = 0; j < 3; ++j) { v_acc(t, j); if (t == 3) { v_fold(NI - 1, j); PIN(); } }
    {
#define FMA0(a) "v_pk_fma_f32 " a ", %7, " a ", %9 op_sel_hi:[0,1,0] neg_lo:[0,0,1] neg_hi:[0,0,1]\n\t"
#define FMA(a, f, b, hi) "v_pk_fma_f32 " a ", " f ", " b ", " a " op_sel:[" hi ",0,0] op_sel_hi:[" hi ",1,1]\n\t"
        asm volatile(
            "ds_write_b32 %5, %6\n\tds_read_b128 v[96:99], %14\n\t"
            "ds_read2_b64 v[64:67], %0 offset1:1\n\tds_read2_b64 v[68:71], %1 offset1:1\n\tds_read2st64_b64 v[80:83], %4 offset1:24\n\t"
            "ds_read2_b64 v[72:75], %2 offset1:1\n\tds_read2_b64 v[76:79], %3 offset1:1\n\tds_read2st64_b64 v[84:87], %4 offset0:48 offset1:72\n\t"
            "v_mov_b32 v91, %10\n\t"
            "s_waitcnt lgkmcnt(6)\n\t"
            "v_and_b32 v96, v96, v97\n\tv_bitop3_b32 v96, v96, v98, v99 bitop3:0x80\n\tv_cmp_ne_u32 vcc, v96, %6\n\t"
            "s_cbranch_vccnz .Lkv_skip_%=\n\t"
            "s_waitcnt lgkmcnt(3)\n\t"
            FMA0("v[64:65]") FMA0("v[66:67]") FMA0("v[80:81]")
            FMA("v[64:65]", "%7", "v[68:69]", "1") FMA("v[66:67]", "%7", "v[70:71]", "1") FMA("v[80:81]", "%7", "v[82:83]", "1")
            "s_waitcnt lgkmcnt(0)\n\t"
            FMA("v[64:65]", "%8", "v[72:73]", "0") FMA("v[66:67]", "%8", "v[74:75]", "0") FMA("v[80:81]", "%8", "v[84:85]", "0")
            FMA("v[64:65]", "%8", "v[76:77]", "1") FMA("v[66:67]", "%8", "v[78:79]", "1") FMA("v[80:81]", "%8", "v[86:87]", "1")
            "v_cvt_pk_bf16_f32 v88, v64, v65\n\tv_cvt_pk_bf16_f32 v89, v66, v67\n\tv_cvt_pk_bf16_f32 v90, v80, v81\n\t"
            "buffer_store_dwordx4 v[88:91], %11, %12, %13 offen sc1\n"
            ".Lkv_skip_%=:\n\ts_waitcnt lgkmcnt(0)"
            :: "v"(la[0]), "v"(la[1]), "v"(la[2]), "v"(la[3]), "v"(la[4]), "v"(lq), "v"(lv), "v"(lf01), "v"(lf23), "v"(lz), "v"(lw), "v"(lo), "s"(rws), "s"(lso), "v"(ld_)
            : "v64", "v65", "v66", "v67", "v68", "v69", "v70", "v71", "v72", "v73", "v74", "v75", "v76", "v77", "v78", "v79", "v80", "v81", "v82", "v83",
              "v84", "v85", "v86", "v87", "v88", "v89", "v90", "v91", "v96", "v97", "v98", "v99", "vcc", "memory");
        asm volatile("s_nop 0" ::: "v88", "v89", "v90", "v91");
        PIN();
    }
    {
        unsigned inbox = pair_off(bid >> 4) + ((bid >> 1 & 4) + (bid & 3)) * 4 * BLK, obase = bid * 48;
        unsigned xo = tid * 16 + ((bid >> 2 ^ sides >> (bid >> 6)) & 1) * BLK, yo = (xo ^ (bid >> 4 < NW ? BLK : 0)) + 2 * BLK, sel = lane & 1 ? 0x01000504u : 0x05040100u;
        unsigned fl = 3 * NI;
        u32x4 rout = {(unsigned)kb[4], (unsigned)(kb[4] >> 32) & 0xffffu, NH * DH * 2, 0x00020000};
        typedef __attribute__((address_space(3))) float lds_f;
        lds_f* wo = (lds_f*)s_red + wave * 28 + (r < 3 ? (c & 3) * 6 + r * 2 : 24);
        lds_f* fo = (lds_f*)s_red + (lane & 7) * 28 + 24;
        lds_f* go = (lds_f*)s_red + (lane & 7) * 28 + min(lane >> 3, 5) * 4;
        vlds_u32* dw = (vlds_u32*)(s_done + wave);
        vlds_u32* dr = (vlds_u32*)(s_done + (lane & 7));
        unsigned oo = (lane & 7) == 0 && lane < 48 ? lane : OOB, eo = bid == 0 && lane < 4 ? lane * 2 : OOB;
        asm volatile("" : "+s"(inbox), "+s"(obase), "+v"(xo), "+v"(yo), "+v"(sel), "+v"(fl), "+s"(rout), "+v"(wo), "+v"(fo), "+v"(go), "+v"(oo), "+v"(eo), "+v"(dw), "+v"(dr));
        const unsigned o = __builtin_amdgcn_readfirstlane(origin) - (bid >= 240 ? (7 - wave) * 900 + (bid < 248 ? 6000 : 0) : 3328 + (14 - (bid >> 4)) * 750);
        while ((int)((now32() << 8) - o) < pace.rd) __builtin_amdgcn_s_sleep(1);
        PIN();
        u32x4 x, y;
        unsigned same;
        do {
            x = __builtin_amdgcn_raw_buffer_load_b128(rw_, xo, inbox, DEV | NT);
            y = __builtin_amdgcn_raw_buffer_load_b128(rw_, yo, inbox, DEV | NT);
            same = now32() - tstart > SPIN_LIMIT ? ~0u : 0xffffu;
            asm volatile("" : "+s"(same));
            PIN();
        } while (__builtin_amdgcn_ballot_w64(((x[3] ^ tag) | (y[3] ^ tag)) > same) != 0);
#define DPP(c) " " c " row_mask:0xf bank_mask:0xf\n\t"
        asm volatile(
            "v_mov_b32_dpp v54, %3" DPP("quad_perm:[1,0,3,2]") "v_mov_b32_dpp v55, %7" DPP("quad_perm:[1,0,3,2]")
            "v_perm_b32 v54, v54, %3, %8\n\tv_perm_b32 v55, v55, %7, %8\n\t"
            "v_max_f32 v56, v54, v55\n\t"
            "v_lshlrev_b32 v40, 16, %0\n\tv_and_b32 v41, 0xffff0000, %0\n\t"
            "v_max_f32_dpp v56, v56, v56" DPP("row_ror:8")
            "v_lshlrev_b32 v42, 16, %1\n\tv_and_b32 v43, 0xffff0000, %1\n\t"
            "v_max_f32_dpp v56, v56, v56" DPP("row_ror:4")
            "v_lshlrev_b32 v44, 16, %2\n\tv_and_b32 v45, 0xffff0000, %2\n\t"
            "v_max_f32_dpp v56, v56, v56 row_bcast:15 row_mask:0xa bank_mask:0xf\n\t"
            "v_lshlrev_b32 v48, 16, %4\n\tv_and_b32 v49, 0xffff0000, %4\n\t"
            "v_max_f32_dpp v56, v56, v56 row_bcast:31 row_mask:0xc bank_mask:0xf\n\t"
            "v_lshlrev_b32 v50, 16, %5\n\t"
            "v_readlane_b32 vcc_lo, v56, 63\n\t"
            "v_and_b32 v51, 0xffff0000, %5\n\tv_lshlrev_b32 v52, 16, %6\n\t"
            "v_subrev_f32 v54, vcc_lo, v54\n\tv_subrev_f32 v55, vcc_lo, v55\n\t"
            "v_exp_f32 v54, v54\n\tv_exp_f32 v55, v55\n\t"
            "v_and_b32 v53, 0xffff0000, %6\n\t"
            "v_pk_mul_f32 v[40:41], v[40:41], v[54:55] op_sel_hi:[1,0]\n\t"
            "v_pk_mul_f32 v[44:45], v[44:45], v[54:55] op_sel_hi:[1,0]\n\t"
            "v_pk_mul_f32 v[42:43], v[42:43], v[54:55] op_sel_hi:[1,0]\n\t"
            "v_pk_fma_f32 v[40:41], v[48:49], v[54:55], v[40:41] op_sel:[0,1,0]\n\t"
            "v_pk_fma_f32 v[44:45], v[52:53], v[54:55], v[44:45] op_sel:[0,1,0]\n\t"
            "v_pk_fma_f32 v[42:43], v[50:51], v[54:55], v[42:43] op_sel:[0,1,0]\n\t"
            "v_add_f32 v46, v54, v55\n\t"
            "v_ldexp_f32 v47, vcc_lo, -4\n\t"
            "v_permlane32_swap_b32 v40, v44\n\tv_permlane32_swap_b32 v41, v45\n\tv_permlane32_swap_b32 v42, v46\n\tv_permlane32_swap_b32 v43, v47\n\t"
            "v_pk_add_f32 v[40:41], v[40:41], v[44:45]\n\tv_pk_add_f32 v[42:43], v[42:43], v[46:47]\n\t"
            "s_nop 1\n\t"
            "v_permlane16_swap_b32 v40, v42\n\tv_permlane16_swap_b32 v41, v43\n\t"
            "v_add_f32 v40, v40, v42\n\tv_add_f32 v41, v41, v43\n\t"
            "s_nop 0\n\t"
            "v_add_f32_dpp v40, v40, v40" DPP("row_ror:8") "v_add_f32_dpp v41, v41, v41" DPP("row_ror:8")
            "s_nop 0\n\t"
            "v_add_f32_dpp v40, v40, v40" DPP("row_ror:4") "v_add_f32_dpp v41, v41, v41" DPP("row_ror:4")
            "ds_write_b64 %9, v[40:41]\n\tds_write_b32 %10, %11"
            :: "v"(x[0]), "v"(x[1]), "v"(x[2]), "v"(x[3]), "v"(y[0]), "v"(y[1]), "v"(y[2]), "v"(y[3]), "v"(sel), "v"(wo), "v"(dw), "v"(fl)
            : "v40", "v41", "v42", "v43", "v44", "v45", "v46", "v47", "v48", "v49", "v50", "v51", "v52", "v53", "v54", "v55", "v56", "vcc", "memory");
        PIN();
        const unsigned fin = *dr;
        PIN();
        f32x2 sm = *(__attribute__((address_space(3))) f32x2*)fo;
        const f32x4 ov = *(__attribute__((address_space(3))) f32x4*)go;
        f32x2 o01 = {ov[0], ov[1]}, o23 = {ov[2], ov[3]};
        asm volatile("" : "+v"(sm), "+v"(o01), "+v"(o23));
        if (__builtin_amdgcn_ballot_w64(fin != 3 * NI) == 0) {
#define ADD4(c) "v_add_f32_dpp v40, v40, v40" DPP(c) "v_add_f32_dpp v41, v41, v41" DPP(c) "v_add_f32_dpp v42, v42, v42" DPP(c) "v_add_f32_dpp v43, v43, v43" DPP(c)
            asm volatile(
                "v_max_f32_dpp v46, %3, %3" DPP("quad_perm:[1,0,3,2]") "s_nop 1\n\t"
                "v_max_f32_dpp v46, v46, v46" DPP("quad_perm:[2,3,0,1]") "s_nop 1\n\t"
                "v_max_f32_dpp v46, v46, v46" DPP("row_half_mirror")
                "v_sub_f32 v46, %3, v46\n\tv_exp_f32 v46, v46\n\ts_nop 0\n\t"
                "v_mul_f32 v44, %2, v46\n\t"
                "v_pk_mul_f32 v[40:41], %4, v[46:47] op_sel_hi:[1,0]\n\tv_pk_mul_f32 v[42:43], %5, v[46:47] op_sel_hi:[1,0]\n\t"
                "v_add_f32_dpp v44, v44, v44" DPP("quad_perm:[1,0,3,2]") ADD4("quad_perm:[1,0,3,2]")
                "v_add_f32_dpp v44, v44, v44" DPP("quad_perm:[2,3,0,1]") ADD4("quad_perm:[2,3,0,1]")
                "v_add_f32_dpp v44, v44, v44" DPP("row_half_mirror") "v_rcp_f32 v44, v44\n\t" ADD4("row_half_mirror")
                "v_pk_mul_f32 v[40:41], v[40:41], v[44:45] op_sel_hi:[1,0]\n\tv_pk_mul_f32 v[42:43], v[42:43], v[44:45] op_sel_hi:[1,0]\n\t"
                "v_cvt_pk_bf16_f32 v40, v40, v41\n\tv_cvt_pk_bf16_f32 v41, v42, v43\n\t"
                "buffer_store_dwordx2 v[40:41], %0, %1, %6 offen"
                :: "v"(oo), "s"(rout), "v"(sm[0]), "v"(sm[1]), "v"(o01), "v"(o23), "s"(obase)
                : "v40", "v41", "v42", "v43", "v44", "v45", "v46", "v47", "memory");
            if (bid == 0) __builtin_amdgcn_raw_buffer_store_b16((unsigned short)(tag >> 16), rw_, eo, WS_EPOCH, DEV);
        }
    }
    __builtin_amdgcn_endpgm();
}

extern "C" void kernel_launch(void* const* d_in, const int* in_sizes, int n_in,
                              void* d_out, int out_size, void* d_ws, size_t ws_size,
                              hipStream_t stream) {
    const float t_us = 100.6f, start_us = 0.2f, seam_us = 0.1f, scales_us = 1.15f;
    const float end_us = start_us + scales_us + 2 * seam_us + t_us * (129.f / 128.f);
    Pace pace;
    pace.step = (unsigned)(t_us * 25600.f / 128.f);
    pace.start = (int)(start_us * 25600.f);
    pace.offset = (int)(scales_us * 25600.f) | (int)(seam_us * 6400.f) << 16;
    pace.bias = (int)(0.052f * 25600.f);
    pace.rd = (int)((end_us + 1.46f) * 25600.f);
    pace.pub = (int)((end_us + 0.46f) * 25600.f);
    kv_sweep_kernel<<<NWG, 576, 0, stream>>>(d_in[0], d_in[1], d_in[2], d_in[3], d_in[4], d_in[5], d_in[6], d_in[7],
        d_in[8], d_in[9], d_in[10], d_ws, d_out, pace);
}
```
